```python
import torch

def _softmax(x, axis=-1):
    return torch.softmax(x, dim=axis)

def _gelu(x):
    return torch.nn.functional.gelu(x, approximate='tanh')

def _silu(x):
    return torch.nn.functional.silu(x)

def _sigmoid(x):
    return torch.sigmoid(x)

def _layernorm(x, axis=-1, eps=1e-05):
    m = x.mean(dim=axis, keepdim=True)
    v = x.var(dim=axis, keepdim=True, unbiased=False)
    return (x - m) / torch.sqrt(v + eps)

def _scatter_add(tgt, idx, src, axis=0):
    return tgt.clone().index_add_(axis, idx, src)

def _segment_sum(src, seg, n):
    out = torch.zeros((n,) + src.shape[1:], dtype=src.dtype)
    return out.index_add_(0, seg, src)

def _pad(x, pw):
    flat = tuple((v for pair in reversed(pw) for v in pair))
    return torch.nn.functional.pad(x, flat)
kernel_name = 'adaLN_cond__mha_block_cached__dense_activation_silu__prompt'

def setup_inputs():

    _w = lambda a: (lambda t: t.to(torch.bfloat16) if t.is_floating_point() else t)(torch.as_tensor(a))

    def _A_setup():
        B, Sq, D = (1, 2048, 576)
        H, Sk, Dh = (32, 2048, 128)
        return {'x': _w((lambda *s: torch.randn(*s, dtype=torch.bfloat16) * 0.1)(B, Sq, D)), 'k_cache': _w((lambda *s: torch.randn(*s, dtype=torch.bfloat16) * 0.8)(B, H, Sk, Dh)), 'v_cache': _w((lambda *s: torch.randn(*s, dtype=torch.bfloat16) * 3.2)(B, H, Sk, Dh)), 'wq': _w((lambda *s: torch.randn(*s, dtype=torch.bfloat16) * 0.8)(D, H * Dh) * D ** (-0.5)), 'wo': _w((lambda *s: torch.randn(*s, dtype=torch.bfloat16) * 3.2)(H * Dh, D) * (H * Dh) ** (-0.5))}
    _A_in = _A_setup()

    def _B_setup():
        B, S, D, F = (1, 2048, 576, 4608)
        return {'x': _w((lambda *s: torch.randn(*s, dtype=torch.bfloat16) * 0.1)(B, S, D)), 'w1': _w((lambda *s: torch.randn(*s, dtype=torch.bfloat16) * 3.2)(D, F) * D ** (-0.5)), 'w2': _w((lambda *s: torch.randn(*s, dtype=torch.bfloat16) * 3.2)(F, D) * F ** (-0.5))}
    _B_in = _B_setup()
    return {'A_x': _A_in['x'], 'A_k_cache': _A_in['k_cache'], 'A_v_cache': _A_in['v_cache'], 'A_wq': _A_in['wq'], 'A_wo': _A_in['wo'], 'B_x': _B_in['x'], 'B_w1': _B_in['w1'], 'B_w2': _B_in['w2'], 'c': _w((lambda *s: torch.randn(*s, dtype=torch.bfloat16) * 0.2)(1, 576) * 8.0), 'w_mod': _w((lambda *s: torch.randn(*s, dtype=torch.bfloat16) * 0.2)(576, 6 * 576) * 576 ** (-0.5) * 8.0)}

def reference(A_x, A_k_cache, A_v_cache, A_wq, A_wo, B_x, B_w1, B_w2, c, w_mod):

    def _A_ref(x, k_cache, v_cache, wq, wo):
        B, Sq, D = x.shape
        H, Dh = (32, 128)
        q = (lambda x, *d: x.permute(*d))((x @ wq).reshape(B, Sq, H, Dh), 0, 2, 1, 3)
        scores = torch.einsum('bhqd,bhkd->bhqk', q, k_cache) * Dh ** (-0.5)
        p = _softmax(scores, axis=-1)
        o = torch.einsum('bhqk,bhkd->bhqd', p, v_cache)
        return (lambda x, *d: x.permute(*d))(o, 0, 2, 1, 3).reshape(B, Sq, H * Dh) @ wo

    def _B_ref(x, w1, w2):
        h = torch.einsum('bsd,df->bsf', x, w1)
        h = _silu(h)
        out = torch.einsum('bsf,fd->bsd', h, w2)
        return out
    x = A_x
    mod = (_silu(c) @ w_mod).reshape(c.shape[0], 6, 1, -1)
    sa, ca, ga, sb, cb, gb = (mod[:, 0].clone(), mod[:, 1].clone(), mod[:, 2].clone(), mod[:, 3].clone(), mod[:, 4].clone(), mod[:, 5].clone())
    h = _layernorm(x, axis=-1) * (1 + ca) + sa
    x = x + ga * _A_ref(h, A_k_cache, A_v_cache, A_wq, A_wo)
    h = _layernorm(x, axis=-1) * (1 + cb) + sb
    x = x + gb * _B_ref(h, B_w1, B_w2)
    return x
```

```cpp
#include <hip/hip_runtime.h>
#include <hip/hip_bf16.h>
#include <stdint.h>
typedef unsigned short u16;
__device__ __forceinline__ float bf2f(u16 v){ return __uint_as_float(((unsigned)v)<<16); }
__device__ __forceinline__ u16 f2bf(float f){ unsigned u=__float_as_uint(f); u += 0x7fffu + ((u>>16)&1u); return (u16)(u>>16); }
__device__ __forceinline__ float rbf(float f){ return bf2f(f2bf(f)); }
constexpr int S=2048, D=576, H=32, DH=128, F=4608;

__global__ void k_mod(const u16* c, const u16* wmod, float* mod){
  int j = blockIdx.x*blockDim.x+threadIdx.x; if(j>=6*D) return;
  float acc=0;
  for(int i=0;i<D;i++){ float cv=bf2f(c[i]); float s=rbf(cv/(1.f+__expf(-cv))); acc += s*bf2f(wmod[(size_t)i*6*D+j]); }
  mod[j]=rbf(acc);
}
__global__ void k_ln(const u16* x, const float* sh, const float* sc, u16* h){
  __shared__ float red[256];
  int row=blockIdx.x, t=threadIdx.x;
  float s=0; for(int i=t;i<D;i+=256) s+=bf2f(x[(size_t)row*D+i]);
  red[t]=s; __syncthreads(); for(int o=128;o>0;o>>=1){ if(t<o) red[t]+=red[t+o]; __syncthreads(); }
  float m=red[0]/D; __syncthreads();
  float v=0; for(int i=t;i<D;i+=256){ float d=bf2f(x[(size_t)row*D+i])-m; v+=d*d; }
  red[t]=v; __syncthreads(); for(int o=128;o>0;o>>=1){ if(t<o) red[t]+=red[t+o]; __syncthreads(); }
  float r=rsqrtf(red[0]/D+1e-5f);
  for(int i=t;i<D;i+=256){ float xn=(bf2f(x[(size_t)row*D+i])-m)*r; h[(size_t)row*D+i]=f2bf(xn*(1.f+sc[i])+sh[i]); }
}
__global__ void k_gemm(const u16* A, const u16* B, u16* C, int M, int N, int K, int mode, const u16* res, const float* g){
  __shared__ float As[32][33], Bs[32][33];
  int tx=threadIdx.x&31, ty=threadIdx.x>>5;
  int m0=blockIdx.y*32, n0=blockIdx.x*32;
  float acc[4]={0,0,0,0};
  for(int k0=0;k0<K;k0+=32){
    for(int i=0;i<4;i++){ int r=ty+8*i; As[r][tx]=bf2f(A[(size_t)(m0+r)*K+k0+tx]); Bs[r][tx]=bf2f(B[(size_t)(k0+r)*N+n0+tx]); }
    __syncthreads();
    for(int k=0;k<32;k++){ float b=Bs[k][tx]; for(int i=0;i<4;i++) acc[i]+=As[ty+8*i][k]*b; }
    __syncthreads();
  }
  for(int i=0;i<4;i++){ int r=m0+ty+8*i, cidx=n0+tx; float v=acc[i];
    if(mode==1){ v=rbf(v); v=v/(1.f+__expf(-v)); }
    else if(mode==2){ v=bf2f(res[(size_t)r*N+cidx])+g[cidx]*rbf(v); }
    C[(size_t)r*N+cidx]=f2bf(v); }
}
__global__ void k_attn(const u16* q, const u16* kc, const u16* vc, u16* o){
  __shared__ float p[4][S];
  __shared__ float qs[4][DH];
  int w=threadIdx.x>>6, l=threadIdx.x&63;
  int idx=blockIdx.x*4+w; int h=idx/S, row=idx%S;
  qs[w][l]=bf2f(q[(size_t)row*H*DH+h*DH+l]); qs[w][l+64]=bf2f(q[(size_t)row*H*DH+h*DH+l+64]);
  __syncthreads();
  float mx=-1e30f;
  for(int k=l;k<S;k+=64){ const u16* kr=kc+((size_t)h*S+k)*DH; float s=0; for(int d=0;d<DH;d++) s+=qs[w][d]*bf2f(kr[d]); s*=0.08838834764f; p[w][k]=s; mx=fmaxf(mx,s); }
  for(int o2=32;o2>0;o2>>=1) mx=fmaxf(mx,__shfl_xor(mx,o2));
  float sum=0; for(int k=l;k<S;k+=64){ float e=__expf(p[w][k]-mx); p[w][k]=e; sum+=e; }
  for(int o2=32;o2>0;o2>>=1) sum+=__shfl_xor(sum,o2);
  __syncthreads();
  float a0=0,a1=0;
  for(int k=0;k<S;k++){ const u16* vr=vc+((size_t)h*S+k)*DH; float pk=p[w][k]; a0+=pk*bf2f(vr[l]); a1+=pk*bf2f(vr[l+64]); }
  o[(size_t)row*H*DH+h*DH+l]=f2bf(a0/sum); o[(size_t)row*H*DH+h*DH+l+64]=f2bf(a1/sum);
}
extern "C" void kernel_launch(void* const* d_in, const int* in_sizes, int n_in, void* d_out, int out_size, void* d_ws, size_t ws_size, hipStream_t stream){
  const u16* x=(const u16*)d_in[0]; const u16* kc=(const u16*)d_in[1]; const u16* vc=(const u16*)d_in[2];
  const u16* wq=(const u16*)d_in[3]; const u16* wo=(const u16*)d_in[4]; const u16* w1=(const u16*)d_in[6]; const u16* w2=(const u16*)d_in[7];
  const u16* c=(const u16*)d_in[8]; const u16* wmod=(const u16*)d_in[9];
  char* ws=(char*)d_ws;
  float* mod=(float*)ws; u16* h1=(u16*)(ws+(1<<20)); u16* q=(u16*)(ws+(8<<20)); u16* o=(u16*)(ws+(32<<20)); u16* x1=(u16*)(ws+(56<<20)); u16* h2=(u16*)(ws+(64<<20)); u16* u=(u16*)(ws+(72<<20));
  k_mod<<<(6*D+63)/64,64,0,stream>>>(c,wmod,mod);
  k_ln<<<S,256,0,stream>>>(x,mod,mod+D,h1);
  k_gemm<<<dim3(H*DH/32,S/32),256,0,stream>>>(h1,wq,q,S,H*DH,D,0,nullptr,nullptr);
  k_attn<<<H*S/4,256,0,stream>>>(q,kc,vc,o);
  k_gemm<<<dim3(D/32,S/32),256,0,stream>>>(o,wo,x1,S,D,H*DH,2,x,mod+2*D);
  k_ln<<<S,256,0,stream>>>(x1,mod+3*D,mod+4*D,h2);
  k_gemm<<<dim3(F/32,S/32),256,0,stream>>>(h2,w1,u,S,F,D,1,nullptr,nullptr);
  k_gemm<<<dim3(D/32,S/32),256,0,stream>>>(u,w2,(u16*)d_out,S,D,F,2,x1,mod+5*D);
}
```

```cpp
#include <hip/hip_runtime.h>
#include <hip/hip_bf16.h>
#include <stdint.h>
typedef unsigned short u16;
typedef unsigned u32x4 __attribute__((ext_vector_type(4)));
typedef float f32x2 __attribute__((ext_vector_type(2)));
typedef __bf16 bf16x2_t __attribute__((ext_vector_type(2)));
constexpr int S=2048, D=576, H=32, DH=128, F=4608;
constexpr size_t WS_MOD=0, WS_COEF=32768, WS_FLAG=49152, WS_EPOCH=53248, WS_CNT=65536, WS_H1F=1u<<20, WS_OF=8u<<20, WS_P1=32u<<20, WS_H2F=48u<<20, WS_UF=64u<<20, WS_P2=96u<<20, WS_DBG=128u<<20;

__global__ __launch_bounds__(256,1) __attribute__((aligned(4096))) void k_mega(char* __restrict__ ws, const u16* __restrict__ kc, const u16* __restrict__ vc, const u16* __restrict__ wq, const u16* __restrict__ wo,
                                                const u16* __restrict__ w1, const u16* __restrict__ w2, const u16* __restrict__ x, u16* __restrict__ out,
                                                const u16* __restrict__ cvec, const u16* __restrict__ wmod){
  extern __shared__ __attribute__((aligned(16))) char lds[];
  const unsigned wid=__builtin_amdgcn_readfirstlane(threadIdx.x>>6);
  const unsigned bid=blockIdx.x, xcd=bid&7, j=bid>>3;
  const unsigned split=xcd>>1, rb=(xcd&1)*8+(j>>2), ct=j&3;
  const unsigned rbw=(xcd&1)*4+(j>>3), c=(xcd>>1)*8+(j&7);
  const unsigned ldsb=(unsigned)(size_t)lds;
  asm volatile(
".p2align 7\n"
"s_mov_b64 s[60:61], %0\n"
"s_and_b32 s61, s61, 0xffff\n"
"s_mov_b32 s62, 268435456\n"
"s_mov_b32 s63, 0x20000\n"
"s_mov_b32 s68, %9\n"
"s_mov_b32 s69, %10\n"
"v_mbcnt_lo_u32_b32 v236, -1, 0\n"
"v_mbcnt_hi_u32_b32 v236, -1, v236\n"
"v_lshlrev_b32 v230, 4, v236\n"
"s_mov_b64 s[48:49], %17\n"
"s_and_b32 s49, s49, 0xffff\n"
"s_mov_b32 s50, 1152\n"
"s_mov_b32 s51, 0x20000\n"
"s_mov_b64 s[44:45], %18\n"
"s_and_b32 s45, s45, 0xffff\n"
"s_mov_b32 s46, 3981312\n"
"s_mov_b32 s47, 0x20000\n"
"v_and_b32 v101, 7, v236\n"
"v_lshrrev_b32 v102, 3, v236\n"
"s_and_b32 s73, %16, 3\n"
"s_lshr_b32 s74, %16, 2\n"
"s_mov_b32 s72, 57344\n"
"buffer_load_dword v100, off, s[60:63], s72 sc1\n"
"s_mul_i32 s72, s73, 288\n"
"s_mul_i32 s98, %9, 72\n"
"s_add_u32 s72, s72, s98\n"
"v_lshlrev_b32 v104, 1, v102\n"
"buffer_load_ushort v32, v104, s[48:51], s72 offen offset:0\n"
"buffer_load_ushort v33, v104, s[48:51], s72 offen offset:16\n"
"buffer_load_ushort v34, v104, s[48:51], s72 offen offset:32\n"
"buffer_load_ushort v35, v104, s[48:51], s72 offen offset:48\n"
"buffer_load_ushort v36, v104, s[48:51], s72 offen offset:64\n"
"s_mul_i32 s72, s73, 995328\n"
"s_mul_i32 s98, %9, 248832\n"
"s_add_u32 s72, s72, s98\n"
"s_mul_i32 s98, s74, 128\n"
"s_add_u32 s72, s72, s98\n"
"v_mul_u32_u24 v103, 6912, v102\n"
"v_lshl_add_u32 v103, v101, 4, v103\n"
"buffer_load_dwordx4 v[12:15], v103, s[44:47], s72 offen\n"
"s_add_u32 s72, s72, 55296\n"
"buffer_load_dwordx4 v[16:19], v103, s[44:47], s72 offen\n"
"s_add_u32 s72, s72, 55296\n"
"buffer_load_dwordx4 v[20:23], v103, s[44:47], s72 offen\n"
"s_add_u32 s72, s72, 55296\n"
"buffer_load_dwordx4 v[24:27], v103, s[44:47], s72 offen\n"
"s_add_u32 s72, s72, 55296\n"
"buffer_load_dwordx4 v[28:31], v103, s[44:47], s72 offen\n"
"s_mov_b64 s[56:57], %7\n"
"s_and_b32 s57, s57, 0xffff\n"
"s_mov_b32 s58, 2359296\n"
"s_mov_b32 s59, 0x20000\n"
"v_mul_u32_u24 v105, 1152, v101\n"
"v_lshl_add_u32 v105, v102, 4, v105\n"
"s_mul_i32 s72, %16, 9216\n"
"s_mul_i32 s98, %9, 128\n"
"s_add_u32 s72, s72, s98\n"
"buffer_load_dwordx4 v[0:3], v105, s[56:59], s72 offen offset:0\n"
"buffer_load_dwordx4 v[4:7], v105, s[56:59], s72 offen offset:512\n"
"buffer_load_dwordx4 v[8:11], v105, s[56:59], s72 offen offset:1024\n"
"s_getpc_b64 s[76:77]\n"
"L_at_pc:\n"
"s_and_b32 s77, s77, 0xffff\n"
"s_mov_b32 s78, L_at_end-L_at_pc\n"
"s_mov_b32 s79, 0x20000\n"
"s_lshr_b32 s72, %16, 3\n"
"s_mul_i32 s73, s72, 4096\n"
"s_mul_i32 s98, %9, 1024\n"
"s_add_u32 s73, s73, s98\n"
"buffer_load_dwordx4 a[0:3], v230, s[76:79], s73 offen\n"
"s_lshl_b32 s72, s68, 2\n"
"s_add_u32 s73, s69, 161792\n"
"s_add_u32 s72, s72, s73\n"
"v_lshlrev_b32 v90, 4, v101\n"
"v_add_u32 v91, s72, v90\n"
"v_add_u32 v90, s73, v90\n"
"s_lshl_b32 s72, s68, 8\n"
"s_add_u32 s72, s72, s73\n"
"v_lshlrev_b32 v92, 5, v101\n"
"v_add_u32 v92, s72, v92\n"
"v_and_b32 v93, 15, v236\n"
"v_lshlrev_b32 v93, 4, v93\n"
"v_add_u32 v94, s73, v93\n"
"s_and_b32 s73, %16, 3\n"
"s_lshr_b32 s72, %16, 2\n"
"s_mul_i32 s86, s72, 256\n"
"s_mul_i32 s98, s73, 16384\n"
"s_add_u32 s86, s86, s98\n"
"s_add_u32 s86, s86, 131072\n"
"s_cmp_eq_u32 s68, 0\n"
"s_cselect_b32 s74, -1, 0\n"
"v_cmp_gt_u32 vcc, 4, v102\n"
"s_mov_b32 s72, 0xbfb8aa3b\n"
"s_waitcnt vmcnt(9)\n"
"v_readfirstlane_b32 s91, v100\n"
"v_lshlrev_b32 v32, 16, v32\n"
"v_lshlrev_b32 v33, 16, v33\n"
"v_lshlrev_b32 v34, 16, v34\n"
"v_lshlrev_b32 v35, 16, v35\n"
"v_lshlrev_b32 v36, 16, v36\n"
"v_cndmask_b32 v36, 0, v36, vcc\n"
"v_mul_f32 v66, s72, v32\n"
"v_mul_f32 v67, s72, v33\n"
"v_mul_f32 v68, s72, v34\n"
"v_mul_f32 v69, s72, v35\n"
"v_mul_f32 v70, s72, v36\n"
"v_exp_f32 v66, v66\n"
"v_exp_f32 v67, v67\n"
"v_exp_f32 v68, v68\n"
"v_exp_f32 v69, v69\n"
"v_exp_f32 v70, v70\n"
"s_add_u32 s91, s91, 1\n"
"v_add_f32 v66, 1.0, v66\n"
"v_add_f32 v67, 1.0, v67\n"
"v_add_f32 v68, 1.0, v68\n"
"v_add_f32 v69, 1.0, v69\n"
"v_add_f32 v70, 1.0, v70\n"
"v_rcp_f32 v66, v66\n"
"v_rcp_f32 v67, v67\n"
"v_rcp_f32 v68, v68\n"
"v_rcp_f32 v69, v69\n"
"v_rcp_f32 v70, v70\n"
"s_nop 0\n"
"v_mul_f32 v66, v66, v32\n"
"v_mul_f32 v67, v67, v33\n"
"v_mul_f32 v68, v68, v34\n"
"v_mul_f32 v69, v69, v35\n"
"v_mul_f32 v70, v70, v36\n"
"v_cvt_pk_bf16_f32 v66, v66, 0\n"
"v_cvt_pk_bf16_f32 v67, v67, 0\n"
"v_cvt_pk_bf16_f32 v68, v68, 0\n"
"v_cvt_pk_bf16_f32 v69, v69, 0\n"
"v_cvt_pk_bf16_f32 v70, v70, 0\n"
"v_lshlrev_b32 v32, 16, v66\n"
"v_lshlrev_b32 v33, 16, v67\n"
"v_lshlrev_b32 v34, 16, v68\n"
"v_lshlrev_b32 v35, 16, v69\n"
"v_lshlrev_b32 v36, 16, v70\n"
"s_waitcnt vmcnt(4)\n"
"v_dot2_f32_bf16 v72, v12, v66, 0\n"
"v_dot2_f32_bf16 v73, v12, v32, 0\n"
"v_dot2_f32_bf16 v74, v13, v66, 0\n"
"v_dot2_f32_bf16 v75, v13, v32, 0\n"
"v_dot2_f32_bf16 v76, v14, v66, 0\n"
"v_dot2_f32_bf16 v77, v14, v32, 0\n"
"v_dot2_f32_bf16 v78, v15, v66, 0\n"
"v_dot2_f32_bf16 v79, v15, v32, 0\n"
"v_dot2_f32_bf16 v72, v16, v67, v72\n"
"v_dot2_f32_bf16 v73, v16, v33, v73\n"
"v_dot2_f32_bf16 v74, v17, v67, v74\n"
"v_dot2_f32_bf16 v75, v17, v33, v75\n"
"v_dot2_f32_bf16 v76, v18, v67, v76\n"
"v_dot2_f32_bf16 v77, v18, v33, v77\n"
"v_dot2_f32_bf16 v78, v19, v67, v78\n"
"v_dot2_f32_bf16 v79, v19, v33, v79\n"
"v_dot2_f32_bf16 v72, v20, v68, v72\n"
"v_dot2_f32_bf16 v73, v20, v34, v73\n"
"v_dot2_f32_bf16 v74, v21, v68, v74\n"
"v_dot2_f32_bf16 v75, v21, v34, v75\n"
"v_dot2_f32_bf16 v76, v22, v68, v76\n"
"v_dot2_f32_bf16 v77, v22, v34, v77\n"
"v_dot2_f32_bf16 v78, v23, v68, v78\n"
"v_dot2_f32_bf16 v79, v23, v34, v79\n"
"v_dot2_f32_bf16 v72, v24, v69, v72\n"
"v_dot2_f32_bf16 v73, v24, v35, v73\n"
"v_dot2_f32_bf16 v74, v25, v69, v74\n"
"v_dot2_f32_bf16 v75, v25, v35, v75\n"
"v_dot2_f32_bf16 v76, v26, v69, v76\n"
"v_dot2_f32_bf16 v77, v26, v35, v77\n"
"v_dot2_f32_bf16 v78, v27, v69, v78\n"
"v_dot2_f32_bf16 v79, v27, v35, v79\n"
"v_dot2_f32_bf16 v72, v28, v70, v72\n"
"v_dot2_f32_bf16 v73, v28, v36, v73\n"
"v_dot2_f32_bf16 v74, v29, v70, v74\n"
"v_dot2_f32_bf16 v75, v29, v36, v75\n"
"v_dot2_f32_bf16 v76, v30, v70, v76\n"
"v_dot2_f32_bf16 v77, v30, v36, v77\n"
"v_dot2_f32_bf16 v78, v31, v70, v78\n"
"v_dot2_f32_bf16 v79, v31, v36, v79\n"
"s_nop 1\n"
"s_nop 1\n"
"v_add_f32_dpp v72, v72, v72 row_ror:8 row_mask:0xf bank_mask:0xf\n"
"v_add_f32_dpp v73, v73, v73 row_ror:8 row_mask:0xf bank_mask:0xf\n"
"v_add_f32_dpp v74, v74, v74 row_ror:8 row_mask:0xf bank_mask:0xf\n"
"v_add_f32_dpp v75, v75, v75 row_ror:8 row_mask:0xf bank_mask:0xf\n"
"v_add_f32_dpp v76, v76, v76 row_ror:8 row_mask:0xf bank_mask:0xf\n"
"v_add_f32_dpp v77, v77, v77 row_ror:8 row_mask:0xf bank_mask:0xf\n"
"v_add_f32_dpp v78, v78, v78 row_ror:8 row_mask:0xf bank_mask:0xf\n"
"v_add_f32_dpp v79, v79, v79 row_ror:8 row_mask:0xf bank_mask:0xf\n"
"v_mov_b32 v80, v72\n"
"v_mov_b32 v81, v73\n"
"v_mov_b32 v82, v74\n"
"v_mov_b32 v83, v75\n"
"v_mov_b32 v84, v76\n"
"v_mov_b32 v85, v77\n"
"v_mov_b32 v86, v78\n"
"v_mov_b32 v87, v79\n"
"s_nop 1\n"
"v_permlane16_swap_b32 v80, v72\n"
"v_permlane16_swap_b32 v81, v73\n"
"v_permlane16_swap_b32 v82, v74\n"
"v_permlane16_swap_b32 v83, v75\n"
"v_permlane16_swap_b32 v84, v76\n"
"v_permlane16_swap_b32 v85, v77\n"
"v_permlane16_swap_b32 v86, v78\n"
"v_permlane16_swap_b32 v87, v79\n"
"s_nop 1\n"
"v_add_f32 v72, v72, v80\n"
"v_add_f32 v73, v73, v81\n"
"v_add_f32 v74, v74, v82\n"
"v_add_f32 v75, v75, v83\n"
"v_add_f32 v76, v76, v84\n"
"v_add_f32 v77, v77, v85\n"
"v_add_f32 v78, v78, v86\n"
"v_add_f32 v79, v79, v87\n"
"v_mov_b32 v80, v72\n"
"v_mov_b32 v81, v73\n"
"v_mov_b32 v82, v74\n"
"v_mov_b32 v83, v75\n"
"v_mov_b32 v84, v76\n"
"v_mov_b32 v85, v77\n"
"v_mov_b32 v86, v78\n"
"v_mov_b32 v87, v79\n"
"s_nop 1\n"
"v_permlane32_swap_b32 v80, v72\n"
"v_permlane32_swap_b32 v81, v73\n"
"v_permlane32_swap_b32 v82, v74\n"
"v_permlane32_swap_b32 v83, v75\n"
"v_permlane32_swap_b32 v84, v76\n"
"v_permlane32_swap_b32 v85, v77\n"
"v_permlane32_swap_b32 v86, v78\n"
"v_permlane32_swap_b32 v87, v79\n"
"s_nop 1\n"
"v_add_f32 v72, v72, v80\n"
"v_add_f32 v73, v73, v81\n"
"v_add_f32 v74, v74, v82\n"
"v_add_f32 v75, v75, v83\n"
"v_add_f32 v76, v76, v84\n"
"v_add_f32 v77, v77, v85\n"
"v_add_f32 v78, v78, v86\n"
"v_add_f32 v79, v79, v87\n"
"ds_write_b128 v92, v[72:75] offset:1024\n"
"ds_write_b128 v92, v[76:79] offset:1040\n"
"s_waitcnt lgkmcnt(0)\n"
"s_barrier\n"
"s_cmp_eq_u32 s68, 0\n"
"s_cbranch_scc0 L_po_w01\n"
"ds_read_b128 v[12:15], v94 offset:1024\n"
"ds_read_b128 v[16:19], v94 offset:1280\n"
"ds_read_b128 v[20:23], v94 offset:1536\n"
"ds_read_b128 v[24:27], v94 offset:1792\n"
"s_waitcnt lgkmcnt(0)\n"
"v_pk_add_f32 v[12:13], v[12:13], v[16:17]\n"
"v_pk_add_f32 v[14:15], v[14:15], v[18:19]\n"
"v_pk_add_f32 v[20:21], v[20:21], v[24:25]\n"
"v_pk_add_f32 v[22:23], v[22:23], v[26:27]\n"
"v_pk_add_f32 v[12:13], v[12:13], v[20:21]\n"
"v_pk_add_f32 v[14:15], v[14:15], v[22:23]\n"
"v_add_u32 v15, 0x80, v15\n"
"s_mov_b32 s73, 117835009\n"
"v_perm_b32 v12, v12, v15, s73\n"
"s_mov_b32 s73, 117835010\n"
"v_perm_b32 v13, v13, v15, s73\n"
"s_mov_b32 s73, 117835011\n"
"v_perm_b32 v14, v14, v15, s73\n"
"v_mov_b32 v15, s91\n"
"s_mov_b64 exec, 0xffff\n"
"buffer_store_dwordx4 v[12:15], v93, s[60:63], s86 offen sc1\n"
"s_mov_b64 exec, -1\n"
"s_cmp_eq_u32 %16, 255\n"
"s_cbranch_scc0 L_p0_nz\n"
"v_lshlrev_b32 v95, 4, v230\n"
"v_mov_b32 v96, 0\n"
"s_mov_b32 s72, 65536\n"
"s_mov_b32 s73, 81920\n"
"buffer_store_dword v96, v95, s[60:63], s72 offen sc1\n"
"buffer_store_dword v96, v95, s[60:63], s73 offen sc1\n"
"s_mov_b32 s72, 327680\n"
"buffer_store_dword v96, v95, s[60:63], s72 offen sc1\n"
"s_mov_b32 s72, 344064\n"
"buffer_store_dword v96, v95, s[60:63], s72 offen sc1\n"
"s_mov_b32 s72, 360448\n"
"buffer_store_dword v96, v95, s[60:63], s72 offen sc1\n"
"s_mov_b32 s72, 376832\n"
"buffer_store_dword v96, v95, s[60:63], s72 offen sc1\n"
"s_mov_b32 s72, 393216\n"
"buffer_store_dword v96, v95, s[60:63], s72 offen sc1\n"
"s_mov_b32 s72, 409600\n"
"buffer_store_dword v96, v95, s[60:63], s72 offen sc1\n"
"L_p0_nz:\n"
"L_po_w01:\n"
"s_mul_i32 s72, %16, 18432\n"
"s_mul_i32 s98, %9, 4608\n"
"s_add_u32 s72, s72, s98\n"
"v_add_u32 v107, s72, v230\n"
"global_load_dwordx4 a[4:7], v107, %3 offset:0\n"
"global_load_dwordx4 a[4:7], v107, %3 offset:1024\n"
"global_load_dwordx4 a[4:7], v107, %3 offset:2048\n"
"global_load_dwordx4 a[4:7], v107, %3 offset:3072\n"
"v_add_u32 v107, 3584, v107\n"
"global_load_dwordx4 a[4:7], v107, %3\n"
"v_lshlrev_b32 v225, 3, v230\n"
"v_add_u32 v226, 32768, v225\n"
"v_add_u32 v227, 40960, v225\n"
"v_min_u32 v227, 41856, v227\n"
"v_and_b32 v224, 896, v225\n"
"s_mul_i32 s72, %14, 4096\n"
"s_mul_i32 s98, %9, 1024\n"
"s_add_u32 s72, s72, s98\n"
"v_add_u32 v224, s72, v224\n"
"s_mov_b32 s94, 262144\n"
"s_mul_i32 s95, %9, 8192\n"
"s_add_u32 s95, s95, 262144\n"
"buffer_load_dword v218, v224, s[60:63], s94 offen sc1\n"
"s_mov_b64 s[52:53], %3\n"
"s_mul_i32 s99, %15, 256\n"
"s_add_u32 s52, s52, s99\n"
"s_addc_u32 s53, s53, 0\n"
"s_and_b32 s53, s53, 0xffff\n"
"s_mov_b32 s54, 4718592\n"
"s_mov_b32 s55, 0x20000\n"
"s_lshl_b32 s84, s68, 11\n"
"s_add_u32 s84, s84, s69\n"
"v_lshrrev_b32 v160, 4, v236\n"
"v_lshrrev_b32 v161, 5, v236\n"
"v_lshlrev_b32 v161, 3, v161\n"
"v_and_or_b32 v161, v160, 1, v161\n"
"v_and_b32 v162, 15, v236\n"
"v_xor_b32 v161, v161, v162\n"
"v_lshlrev_b32 v160, 13, v160\n"
"v_lshl_add_u32 v231, v161, 4, v160\n"
"s_lshl_b32 s72, s68, 16\n"
"v_add_u32 v231, s72, v231\n"
"v_add_u32 v232, 31744, v231\n"
"v_lshrrev_b32 v160, 4, v236\n"
"v_bfe_u32 v161, v236, 2, 2\n"
"v_lshl_add_u32 v160, v160, 3, v161\n"
"v_lshlrev_b32 v160, 8, v160\n"
"v_add_u32 v160, s69, v160\n"
"v_and_b32 v162, 8, v236\n"
"v_and_or_b32 v162, v161, 1, v162\n"
"v_and_b32 v163, 3, v236\n"
"v_lshl_or_b32 v162, v163, 1, v162\n"
"v_xor_b32 v163, 0, v162\n"
"v_lshl_add_u32 v196, v163, 4, v160\n"
"v_add_u32 v200, 49152, v196\n"
"v_add_u32 v204, 98304, v196\n"
"v_xor_b32 v163, 1, v162\n"
"v_lshl_add_u32 v197, v163, 4, v160\n"
"v_add_u32 v201, 49152, v197\n"
"v_add_u32 v205, 98304, v197\n"
"v_xor_b32 v163, 8, v162\n"
"v_lshl_add_u32 v198, v163, 4, v160\n"
"v_add_u32 v202, 49152, v198\n"
"v_add_u32 v206, 98304, v198\n"
"v_xor_b32 v163, 9, v162\n"
"v_lshl_add_u32 v199, v163, 4, v160\n"
"v_add_u32 v203, 49152, v199\n"
"v_add_u32 v207, 98304, v199\n"
"s_mul_i32 s87, %14, 294912\n"
"s_mul_i32 s98, %9, 73728\n"
"s_add_u32 s87, s87, s98\n"
"s_add_u32 s87, s87, 1048576\n"
"s_lshl_b32 s72, s68, 10\n"
"v_add_u32 v164, s72, v230\n"
"s_add_u32 s74, s69, 156672\n"
"v_add_u32 v212, 0, v164\n"
"v_min_u32 v212, 4592, v212\n"
"v_cmp_gt_u32 vcc, 2304, v212\n"
"v_subrev_u32 v208, 2304, v212\n"
"s_nop 0\n"
"v_cndmask_b32 v208, v208, v212, vcc\n"
"v_cndmask_b32 v210, 1.0, 0, vcc\n"
"v_cndmask_b32 v160, 0, 32, vcc\n"
"v_lshrrev_b32 v161, 5, v208\n"
"v_lshl_add_u32 v160, v161, 6, v160\n"
"v_and_b32 v208, 16, v208\n"
"v_add3_u32 v208, v208, v160, s74\n"
"v_add_u32 v213, 4096, v164\n"
"v_min_u32 v213, 4592, v213\n"
"v_cmp_gt_u32 vcc, 2304, v213\n"
"v_subrev_u32 v209, 2304, v213\n"
"s_nop 0\n"
"v_cndmask_b32 v209, v209, v213, vcc\n"
"v_cndmask_b32 v211, 1.0, 0, vcc\n"
"v_cndmask_b32 v160, 0, 32, vcc\n"
"v_lshrrev_b32 v161, 5, v209\n"
"v_lshl_add_u32 v160, v161, 6, v160\n"
"v_and_b32 v209, 16, v209\n"
"v_add3_u32 v209, v209, v160, s74\n"
"v_lshrrev_b32 v160, 3, v236\n"
"s_lshl_b32 s72, s68, 3\n"
"v_add_u32 v160, s72, v160\n"
"v_lshlrev_b32 v216, 6, v160\n"
"v_add_u32 v216, s74, v216\n"
"v_and_b32 v161, 7, v236\n"
"v_lshlrev_b32 v161, 4, v161\n"
"v_lshl_add_u32 v217, v160, 8, v161\n"
"s_mov_b64 s[44:45], %1\n"
"s_mul_i32 s99, %15, 524288\n"
"s_add_u32 s44, s44, s99\n"
"s_addc_u32 s45, s45, 0\n"
"s_and_b32 s45, s45, 0xffff\n"
"s_mov_b32 s46, 524288\n"
"s_mov_b32 s47, 0x20000\n"
"s_mov_b64 s[48:49], %2\n"
"s_mul_i32 s99, %15, 524288\n"
"s_add_u32 s48, s48, s99\n"
"s_addc_u32 s49, s49, 0\n"
"s_and_b32 s49, s49, 0xffff\n"
"s_mov_b32 s50, 524288\n"
"s_mov_b32 s51, 0x20000\n"
"s_mul_i32 s72, s68, 4160\n"
"s_add_u32 s76, s69, s72\n"
"s_mul_i32 s72, s68, 4224\n"
"s_add_u32 s77, s69, s72\n"
"s_add_u32 s77, s77, 66560\n"
"s_lshl_b32 s78, s68, 12\n"
"s_mov_b32 s79, s78\n"
"s_mov_b32 s81, 1736441856\n"
"s_mov_b32 s80, 6\n"
"v_and_b32 v160, 15, v236\n"
"v_lshrrev_b32 v161, 4, v236\n"
"v_lshlrev_b32 v162, 1, v160\n"
"v_subrev_u32 v164, 7, v162\n"
"v_subrev_u32 v165, 16, v162\n"
"v_cmp_lt_u32 vcc, 3, v160\n"
"s_nop 1\n"
"v_cndmask_b32 v162, v162, v164, vcc\n"
"v_cmp_lt_u32 vcc, 11, v160\n"
"s_nop 1\n"
"v_cndmask_b32 v162, v162, v165, vcc\n"
"v_mul_u32_u24 v228, 1040, v162\n"
"v_lshl_add_u32 v228, v161, 5, v228\n"
"v_add_u32 v228, s69, v228\n"
"v_lshrrev_b32 v163, 2, v236\n"
"v_lshlrev_b32 v162, 1, v163\n"
"v_subrev_u32 v164, 7, v162\n"
"v_subrev_u32 v165, 16, v162\n"
"v_cmp_lt_u32 vcc, 3, v163\n"
"s_nop 1\n"
"v_cndmask_b32 v162, v162, v164, vcc\n"
"v_cmp_lt_u32 vcc, 11, v163\n"
"s_nop 1\n"
"v_cndmask_b32 v162, v162, v165, vcc\n"
"v_mul_u32_u24 v229, 1056, v162\n"
"v_and_b32 v163, 3, v236\n"
"v_lshl_add_u32 v229, v163, 3, v229\n"
"v_add_u32 v229, s69, v229\n"
"v_add_u32 v229, 66560, v229\n"
"v_mov_b32 v164, 0x3f803f80\n"
"v_cmp_eq_u32 vcc, 0, v163\n"
"s_nop 1\n"
"v_cndmask_b32 v164, 0, v164, vcc\n"
"v_accvgpr_write_b32 a160, v164\n"
"v_accvgpr_write_b32 a161, v164\n"
"v_accvgpr_write_b32 a162, v164\n"
"v_accvgpr_write_b32 a163, v164\n"
"s_cmp_eq_u32 s68, 0\n"
"s_cselect_b32 s74, -1, 0\n"
"s_waitcnt vmcnt(7)\n"
"v_and_b32 v8, s74, v8\n"
"v_and_b32 v9, s74, v9\n"
"v_and_b32 v10, s74, v10\n"
"v_and_b32 v11, s74, v11\n"
"v_lshlrev_b32 v40, 16, v0\n"
"v_and_b32 v41, 0xffff0000, v0\n"
"v_lshlrev_b32 v42, 16, v1\n"
"v_and_b32 v43, 0xffff0000, v1\n"
"v_lshlrev_b32 v44, 16, v2\n"
"v_and_b32 v45, 0xffff0000, v2\n"
"v_lshlrev_b32 v46, 16, v3\n"
"v_and_b32 v47, 0xffff0000, v3\n"
"v_lshlrev_b32 v48, 16, v4\n"
"v_and_b32 v49, 0xffff0000, v4\n"
"v_lshlrev_b32 v50, 16, v5\n"
"v_and_b32 v51, 0xffff0000, v5\n"
"v_lshlrev_b32 v52, 16, v6\n"
"v_and_b32 v53, 0xffff0000, v6\n"
"v_lshlrev_b32 v54, 16, v7\n"
"v_and_b32 v55, 0xffff0000, v7\n"
"v_lshlrev_b32 v56, 16, v8\n"
"v_and_b32 v57, 0xffff0000, v8\n"
"v_lshlrev_b32 v58, 16, v9\n"
"v_and_b32 v59, 0xffff0000, v9\n"
"v_lshlrev_b32 v60, 16, v10\n"
"v_and_b32 v61, 0xffff0000, v10\n"
"v_lshlrev_b32 v62, 16, v11\n"
"v_and_b32 v63, 0xffff0000, v11\n"
"v_pk_add_f32 v[64:65], v[40:41], v[42:43]\n"
"v_pk_add_f32 v[64:65], v[64:65], v[44:45]\n"
"v_pk_add_f32 v[64:65], v[64:65], v[46:47]\n"
"v_pk_add_f32 v[64:65], v[64:65], v[48:49]\n"
"v_pk_add_f32 v[64:65], v[64:65], v[50:51]\n"
"v_pk_add_f32 v[64:65], v[64:65], v[52:53]\n"
"v_pk_add_f32 v[64:65], v[64:65], v[54:55]\n"
"v_pk_add_f32 v[64:65], v[64:65], v[56:57]\n"
"v_pk_add_f32 v[64:65], v[64:65], v[58:59]\n"
"v_pk_add_f32 v[64:65], v[64:65], v[60:61]\n"
"v_pk_add_f32 v[64:65], v[64:65], v[62:63]\n"
"v_add_f32 v64, v64, v65\n"
"s_mov_b32 s72, 0x3c000000\n"
"s_cmp_eq_u32 s68, 0\n"
"s_cselect_b32 s72, 0x3baaaaab, s72\n"
"s_nop 1\n"
"v_add_f32_dpp v64, v64, v64 row_ror:8 row_mask:0xf bank_mask:0xf\n"
"v_mov_b32 v80, v64\n"
"s_nop 1\n"
"v_permlane16_swap_b32 v80, v64\n"
"s_nop 1\n"
"v_add_f32 v64, v64, v80\n"
"v_mov_b32 v80, v64\n"
"s_nop 1\n"
"v_permlane32_swap_b32 v80, v64\n"
"s_nop 1\n"
"v_add_f32 v64, v64, v80\n"
"v_mul_f32 v66, s72, v64\n"
"v_pk_add_f32 v[40:41], v[40:41], v[66:67] op_sel_hi:[1,0] neg_lo:[0,1] neg_hi:[0,1]\n"
"v_pk_add_f32 v[42:43], v[42:43], v[66:67] op_sel_hi:[1,0] neg_lo:[0,1] neg_hi:[0,1]\n"
"v_pk_add_f32 v[44:45], v[44:45], v[66:67] op_sel_hi:[1,0] neg_lo:[0,1] neg_hi:[0,1]\n"
"v_pk_add_f32 v[46:47], v[46:47], v[66:67] op_sel_hi:[1,0] neg_lo:[0,1] neg_hi:[0,1]\n"
"v_pk_add_f32 v[48:49], v[48:49], v[66:67] op_sel_hi:[1,0] neg_lo:[0,1] neg_hi:[0,1]\n"
"v_pk_add_f32 v[50:51], v[50:51], v[66:67] op_sel_hi:[1,0] neg_lo:[0,1] neg_hi:[0,1]\n"
"v_pk_add_f32 v[52:53], v[52:53], v[66:67] op_sel_hi:[1,0] neg_lo:[0,1] neg_hi:[0,1]\n"
"v_pk_add_f32 v[54:55], v[54:55], v[66:67] op_sel_hi:[1,0] neg_lo:[0,1] neg_hi:[0,1]\n"
"v_pk_add_f32 v[56:57], v[56:57], v[66:67] op_sel_hi:[1,0] neg_lo:[0,1] neg_hi:[0,1]\n"
"v_pk_add_f32 v[58:59], v[58:59], v[66:67] op_sel_hi:[1,0] neg_lo:[0,1] neg_hi:[0,1]\n"
"v_pk_add_f32 v[60:61], v[60:61], v[66:67] op_sel_hi:[1,0] neg_lo:[0,1] neg_hi:[0,1]\n"
"v_pk_add_f32 v[62:63], v[62:63], v[66:67] op_sel_hi:[1,0] neg_lo:[0,1] neg_hi:[0,1]\n"
"v_and_b32 v56, s74, v56\n"
"v_and_b32 v57, s74, v57\n"
"v_and_b32 v58, s74, v58\n"
"v_and_b32 v59, s74, v59\n"
"v_and_b32 v60, s74, v60\n"
"v_and_b32 v61, s74, v61\n"
"v_and_b32 v62, s74, v62\n"
"v_and_b32 v63, s74, v63\n"
"v_pk_mul_f32 v[68:69], v[40:41], v[40:41]\n"
"v_pk_fma_f32 v[68:69], v[42:43], v[42:43], v[68:69]\n"
"v_pk_fma_f32 v[68:69], v[44:45], v[44:45], v[68:69]\n"
"v_pk_fma_f32 v[68:69], v[46:47], v[46:47], v[68:69]\n"
"v_pk_fma_f32 v[68:69], v[48:49], v[48:49], v[68:69]\n"
"v_pk_fma_f32 v[68:69], v[50:51], v[50:51], v[68:69]\n"
"v_pk_fma_f32 v[68:69], v[52:53], v[52:53], v[68:69]\n"
"v_pk_fma_f32 v[68:69], v[54:55], v[54:55], v[68:69]\n"
"v_pk_fma_f32 v[68:69], v[56:57], v[56:57], v[68:69]\n"
"v_pk_fma_f32 v[68:69], v[58:59], v[58:59], v[68:69]\n"
"v_pk_fma_f32 v[68:69], v[60:61], v[60:61], v[68:69]\n"
"v_pk_fma_f32 v[68:69], v[62:63], v[62:63], v[68:69]\n"
"v_add_f32 v68, v68, v69\n"
"s_nop 1\n"
"v_add_f32_dpp v68, v68, v68 row_ror:8 row_mask:0xf bank_mask:0xf\n"
"v_mov_b32 v80, v68\n"
"s_nop 1\n"
"v_permlane16_swap_b32 v80, v68\n"
"s_nop 1\n"
"v_add_f32 v68, v68, v80\n"
"v_mov_b32 v80, v68\n"
"s_nop 1\n"
"v_permlane32_swap_b32 v80, v68\n"
"s_nop 1\n"
"v_add_f32 v68, v68, v80\n"
"ds_write_b32 v91, v64\n"
"ds_write_b32 v91, v68 offset:128\n"
"s_waitcnt lgkmcnt(0)\n"
"s_barrier\n"
"ds_read_b128 v[80:83], v90\n"
"ds_read_b128 v[84:87], v90 offset:128\n"
"v_mov_b32 v72, 0x3727c5ac\n"
"s_mov_b32 s73, 0x3ae38e39\n"
"s_waitcnt lgkmcnt(0)\n"
"v_add_f32 v70, v80, v81\n"
"v_add_f32 v71, v82, v83\n"
"v_add_f32 v70, v70, v71\n"
"v_mul_f32 v70, s73, v70\n"
"v_sub_f32 v66, v66, v70\n"
"v_fmac_f32 v80, 3275751424, v70\n"
"v_mul_f32 v80, v80, v80\n"
"v_fmac_f32 v84, 1001040555, v80\n"
"v_fmac_f32 v81, 3271557120, v70\n"
"v_mul_f32 v81, v81, v81\n"
"v_fmac_f32 v85, 1006632960, v81\n"
"v_fmac_f32 v82, 3271557120, v70\n"
"v_mul_f32 v82, v82, v82\n"
"v_fmac_f32 v86, 1006632960, v82\n"
"v_fmac_f32 v83, 3271557120, v70\n"
"v_mul_f32 v83, v83, v83\n"
"v_fmac_f32 v87, 1006632960, v83\n"
"v_add_f32 v84, v84, v85\n"
"v_add_f32 v86, v86, v87\n"
"v_add_f32 v84, v84, v86\n"
"v_fma_f32 v80, v84, s73, v72\n"
"v_rsq_f32 v80, v80\n"
"s_nop 0\n"
"v_mul_f32 v66, v66, v80\n"
"v_pk_fma_f32 v[40:41], v[40:41], v[80:81], v[66:67] op_sel_hi:[1,0,0]\n"
"v_pk_fma_f32 v[42:43], v[42:43], v[80:81], v[66:67] op_sel_hi:[1,0,0]\n"
"v_pk_fma_f32 v[44:45], v[44:45], v[80:81], v[66:67] op_sel_hi:[1,0,0]\n"
"v_pk_fma_f32 v[46:47], v[46:47], v[80:81], v[66:67] op_sel_hi:[1,0,0]\n"
"v_pk_fma_f32 v[48:49], v[48:49], v[80:81], v[66:67] op_sel_hi:[1,0,0]\n"
"v_pk_fma_f32 v[50:51], v[50:51], v[80:81], v[66:67] op_sel_hi:[1,0,0]\n"
"v_pk_fma_f32 v[52:53], v[52:53], v[80:81], v[66:67] op_sel_hi:[1,0,0]\n"
"v_pk_fma_f32 v[54:55], v[54:55], v[80:81], v[66:67] op_sel_hi:[1,0,0]\n"
"v_pk_fma_f32 v[56:57], v[56:57], v[80:81], v[66:67] op_sel_hi:[1,0,0]\n"
"v_pk_fma_f32 v[58:59], v[58:59], v[80:81], v[66:67] op_sel_hi:[1,0,0]\n"
"v_pk_fma_f32 v[60:61], v[60:61], v[80:81], v[66:67] op_sel_hi:[1,0,0]\n"
"v_pk_fma_f32 v[62:63], v[62:63], v[80:81], v[66:67] op_sel_hi:[1,0,0]\n"
"s_add_u32 m0, s84, 0\n"
"s_mov_b32 s88, 0\n"
"buffer_load_dwordx4 v231, s[52:55], s88 offen offset:0 lds\n"
"buffer_load_dwordx4 v232, s[52:55], s88 offen offset:1024 lds\n"
"s_add_u32 m0, s84, 8192\n"
"s_mov_b32 s85, 262144\n"
"buffer_load_dwordx4 v231, s[52:55], s85 offen offset:0 lds\n"
"buffer_load_dwordx4 v232, s[52:55], s85 offen offset:1024 lds\n"
"s_mov_b32 s97, 20000\n"
"L_po_poll2:\n"
"s_mov_b32 s73, 131072\n"
"buffer_load_dwordx4 v[128:131], v212, s[60:63], s73 offen sc1\n"
"s_mov_b32 s73, 147456\n"
"buffer_load_dwordx4 v[132:135], v212, s[60:63], s73 offen sc1\n"
"s_mov_b32 s73, 163840\n"
"buffer_load_dwordx4 v[136:139], v212, s[60:63], s73 offen sc1\n"
"s_mov_b32 s73, 180224\n"
"buffer_load_dwordx4 v[140:143], v212, s[60:63], s73 offen sc1\n"
"s_mov_b32 s73, 131072\n"
"buffer_load_dwordx4 v[144:147], v213, s[60:63], s73 offen sc1\n"
"s_mov_b32 s73, 147456\n"
"buffer_load_dwordx4 v[148:151], v213, s[60:63], s73 offen sc1\n"
"s_mov_b32 s73, 163840\n"
"buffer_load_dwordx4 v[152:155], v213, s[60:63], s73 offen sc1\n"
"s_mov_b32 s73, 180224\n"
"buffer_load_dwordx4 v[156:159], v213, s[60:63], s73 offen sc1\n"
"s_waitcnt vmcnt(0)\n"
"v_xor_b32 v131, s91, v131\n"
"v_xor_b32 v135, s91, v135\n"
"v_xor_b32 v139, s91, v139\n"
"v_xor_b32 v143, s91, v143\n"
"v_xor_b32 v147, s91, v147\n"
"v_xor_b32 v151, s91, v151\n"
"v_xor_b32 v155, s91, v155\n"
"v_xor_b32 v159, s91, v159\n"
"v_or3_b32 v218, v131, v135, v139\n"
"v_or3_b32 v219, v143, v147, v151\n"
"v_or3_b32 v218, v218, v155, v159\n"
"v_or_b32 v218, v218, v219\n"
"v_cmp_ne_u32 vcc, 0, v218\n"
"s_nop 1\n"
"s_cbranch_vccz L_po_polld3\n"
"s_sleep 2\n"
"s_sub_u32 s97, s97, 1\n"
"s_cbranch_scc0 L_po_poll2\n"
"L_po_polld3:\n"
"s_lshr_b32 s72, %16, 1\n"
"s_and_b32 s73, %16, 1\n"
"s_mul_i32 s74, s72, 18432\n"
"s_mul_i32 s98, s73, 128\n"
"s_add_u32 s74, s74, s98\n"
"s_add_u32 s74, s74, 1048576\n"
"s_mov_b32 s72, 0x3e0293ee\n"
"s_mov_b32 s98, 0x0c04000c\n"
"s_mov_b32 s99, 0x0402010c\n"
"v_perm_b32 v131, v129, v128, s98\n"
"v_perm_b32 v131, v130, v131, s99\n"
"v_perm_b32 v135, v133, v132, s98\n"
"v_perm_b32 v135, v134, v135, s99\n"
"v_perm_b32 v139, v137, v136, s98\n"
"v_perm_b32 v139, v138, v139, s99\n"
"v_perm_b32 v143, v141, v140, s98\n"
"v_perm_b32 v143, v142, v143, s99\n"
"v_pk_add_f32 v[128:129], v[128:129], v[132:133]\n"
"v_pk_add_f32 v[136:137], v[136:137], v[140:141]\n"
"v_pk_add_f32 v[128:129], v[128:129], v[136:137]\n"
"v_cvt_pk_bf16_f32 v132, v128, v129\n"
"v_lshlrev_b32 v128, 16, v132\n"
"v_and_b32 v129, 0xffff0000, v132\n"
"v_pk_add_f32 v[130:131], v[130:131], v[134:135]\n"
"v_pk_add_f32 v[138:139], v[138:139], v[142:143]\n"
"v_pk_add_f32 v[130:131], v[130:131], v[138:139]\n"
"v_cvt_pk_bf16_f32 v132, v130, v131\n"
"v_lshlrev_b32 v130, 16, v132\n"
"v_and_b32 v131, 0xffff0000, v132\n"
"v_add_f32 v128, v210, v128\n"
"v_add_f32 v129, v210, v129\n"
"v_add_f32 v130, v210, v130\n"
"v_add_f32 v131, v210, v131\n"
"v_mul_f32 v128, s72, v128\n"
"v_mul_f32 v129, s72, v129\n"
"v_mul_f32 v130, s72, v130\n"
"v_mul_f32 v131, s72, v131\n"
"ds_write_b128 v208, v[128:131]\n"
"s_mov_b32 s98, 0x0c04000c\n"
"s_mov_b32 s99, 0x0402010c\n"
"v_perm_b32 v147, v145, v144, s98\n"
"v_perm_b32 v147, v146, v147, s99\n"
"v_perm_b32 v151, v149, v148, s98\n"
"v_perm_b32 v151, v150, v151, s99\n"
"v_perm_b32 v155, v153, v152, s98\n"
"v_perm_b32 v155, v154, v155, s99\n"
"v_perm_b32 v159, v157, v156, s98\n"
"v_perm_b32 v159, v158, v159, s99\n"
"v_pk_add_f32 v[144:145], v[144:145], v[148:149]\n"
"v_pk_add_f32 v[152:153], v[152:153], v[156:157]\n"
"v_pk_add_f32 v[144:145], v[144:145], v[152:153]\n"
"v_cvt_pk_bf16_f32 v148, v144, v145\n"
"v_lshlrev_b32 v144, 16, v148\n"
"v_and_b32 v145, 0xffff0000, v148\n"
"v_pk_add_f32 v[146:147], v[146:147], v[150:151]\n"
"v_pk_add_f32 v[154:155], v[154:155], v[158:159]\n"
"v_pk_add_f32 v[146:147], v[146:147], v[154:155]\n"
"v_cvt_pk_bf16_f32 v148, v146, v147\n"
"v_lshlrev_b32 v146, 16, v148\n"
"v_and_b32 v147, 0xffff0000, v148\n"
"v_add_f32 v144, v211, v144\n"
"v_add_f32 v145, v211, v145\n"
"v_add_f32 v146, v211, v146\n"
"v_add_f32 v147, v211, v147\n"
"v_mul_f32 v144, s72, v144\n"
"v_mul_f32 v145, s72, v145\n"
"v_mul_f32 v146, s72, v146\n"
"v_mul_f32 v147, s72, v147\n"
"ds_write_b128 v209, v[144:147]\n"
"s_waitcnt lgkmcnt(0)\n"
"s_barrier\n"
"ds_read_b128 v[128:131], v216 offset:0\n"
"ds_read_b128 v[132:135], v216 offset:16\n"
"ds_read_b128 v[136:139], v216 offset:32\n"
"ds_read_b128 v[140:143], v216 offset:48\n"
"ds_read_b128 v[144:147], v216 offset:2048\n"
"ds_read_b128 v[148:151], v216 offset:2064\n"
"ds_read_b128 v[152:155], v216 offset:2080\n"
"ds_read_b128 v[156:159], v216 offset:2096\n"
"ds_read_b128 v[160:163], v216 offset:4096\n"
"ds_read_b128 v[164:167], v216 offset:4112\n"
"ds_read_b128 v[168:171], v216 offset:4128\n"
"ds_read_b128 v[172:175], v216 offset:4144\n"
"s_waitcnt lgkmcnt(8)\n"
"v_pk_fma_f32 v[40:41], v[40:41], v[128:129], v[136:137]\n"
"v_cvt_pk_bf16_f32 v0, v40, v41\n"
"v_pk_fma_f32 v[42:43], v[42:43], v[130:131], v[138:139]\n"
"v_cvt_pk_bf16_f32 v1, v42, v43\n"
"v_pk_fma_f32 v[44:45], v[44:45], v[132:133], v[140:141]\n"
"v_cvt_pk_bf16_f32 v2, v44, v45\n"
"v_pk_fma_f32 v[46:47], v[46:47], v[134:135], v[142:143]\n"
"v_cvt_pk_bf16_f32 v3, v46, v47\n"
"s_add_u32 s73, s74, 0\n"
"buffer_store_dwordx4 v[0:3], v217, s[60:63], s73 offen sc1\n"
"s_waitcnt lgkmcnt(4)\n"
"v_pk_fma_f32 v[48:49], v[48:49], v[144:145], v[152:153]\n"
"v_cvt_pk_bf16_f32 v4, v48, v49\n"
"v_pk_fma_f32 v[50:51], v[50:51], v[146:147], v[154:155]\n"
"v_cvt_pk_bf16_f32 v5, v50, v51\n"
"v_pk_fma_f32 v[52:53], v[52:53], v[148:149], v[156:157]\n"
"v_cvt_pk_bf16_f32 v6, v52, v53\n"
"v_pk_fma_f32 v[54:55], v[54:55], v[150:151], v[158:159]\n"
"v_cvt_pk_bf16_f32 v7, v54, v55\n"
"s_add_u32 s73, s74, 8192\n"
"buffer_store_dwordx4 v[4:7], v217, s[60:63], s73 offen sc1\n"
"s_waitcnt lgkmcnt(0)\n"
"s_cmp_eq_u32 s68, 0\n"
"s_cbranch_scc0 L_po_w04\n"
"v_pk_fma_f32 v[56:57], v[56:57], v[160:161], v[168:169]\n"
"v_cvt_pk_bf16_f32 v8, v56, v57\n"
"v_pk_fma_f32 v[58:59], v[58:59], v[162:163], v[170:171]\n"
"v_cvt_pk_bf16_f32 v9, v58, v59\n"
"v_pk_fma_f32 v[60:61], v[60:61], v[164:165], v[172:173]\n"
"v_cvt_pk_bf16_f32 v10, v60, v61\n"
"v_pk_fma_f32 v[62:63], v[62:63], v[166:167], v[174:175]\n"
"v_cvt_pk_bf16_f32 v11, v62, v63\n"
"s_add_u32 s73, s74, 16384\n"
"buffer_store_dwordx4 v[8:11], v217, s[60:63], s73 offen sc1\n"
"L_po_w04:\n"
"s_waitcnt vmcnt(0)\n"
"s_barrier\n"
"s_cmp_eq_u32 s68, 0\n"
"s_cbranch_scc0 L_po_w05\n"
"v_mov_b32 v96, s91\n"
"s_mul_i32 s72, %16, 128\n"
"s_add_u32 s72, s72, 262144\n"
"s_mov_b64 exec, 1\n"
"buffer_store_dword v96, off, s[60:63], s72 sc1\n"
"s_mov_b64 exec, -1\n"
"L_po_w05:\n"
"s_add_u32 m0, s84, 16384\n"
"s_mov_b32 s88, 524288\n"
"buffer_load_dwordx4 v231, s[52:55], s88 offen offset:0 lds\n"
"buffer_load_dwordx4 v232, s[52:55], s88 offen offset:1024 lds\n"
"s_add_u32 m0, s84, 24576\n"
"s_mov_b32 s85, 786432\n"
"buffer_load_dwordx4 v231, s[52:55], s85 offen offset:0 lds\n"
"buffer_load_dwordx4 v232, s[52:55], s85 offen offset:1024 lds\n"
"s_add_u32 m0, s84, 32768\n"
"s_mov_b32 s88, 1048576\n"
"buffer_load_dwordx4 v231, s[52:55], s88 offen offset:0 lds\n"
"buffer_load_dwordx4 v232, s[52:55], s88 offen offset:1024 lds\n"
"s_add_u32 m0, s84, 40960\n"
"s_mov_b32 s85, 1310720\n"
"buffer_load_dwordx4 v231, s[52:55], s85 offen offset:0 lds\n"
"buffer_load_dwordx4 v232, s[52:55], s85 offen offset:1024 lds\n"
"s_add_u32 m0, s84, 49152\n"
"s_mov_b32 s88, 1572864\n"
"buffer_load_dwordx4 v231, s[52:55], s88 offen offset:0 lds\n"
"buffer_load_dwordx4 v232, s[52:55], s88 offen offset:1024 lds\n"
"s_add_u32 m0, s84, 57344\n"
"s_mov_b32 s85, 1835008\n"
"buffer_load_dwordx4 v231, s[52:55], s85 offen offset:0 lds\n"
"buffer_load_dwordx4 v232, s[52:55], s85 offen offset:1024 lds\n"
"s_cmp_eq_u32 s68, 3\n"
"s_cbranch_scc0 L_ts0_0\n"
"s_mov_b64 exec, 0\n"
"s_branch L_tc0_0\n"
"L_tb0_0:\n"
"s_mov_b64 exec, -1\n"
"L_ts0_0:\n"
"s_cmp_eq_u32 s68, 2\n"
"s_cbranch_scc0 L_ts0_1\n"
"s_mov_b64 exec, 0\n"
"s_branch L_tc0_1\n"
"L_tb0_1:\n"
"s_mov_b64 exec, -1\n"
"L_ts0_1:\n"
"s_cmp_eq_u32 s68, 1\n"
"s_cbranch_scc0 L_ts0_2\n"
"s_mov_b64 exec, 0\n"
"s_branch L_tc0_2\n"
"L_tb0_2:\n"
"s_mov_b64 exec, -1\n"
"L_ts0_2:\n"
"s_cmp_eq_u32 s68, 0\n"
"s_cbranch_scc0 L_ts0_3\n"
"s_mov_b64 exec, 0\n"
"s_branch L_tc0_3\n"
"L_tb0_3:\n"
"s_mov_b64 exec, -1\n"
"L_ts0_3:\n"
"s_mov_b32 s97, 20000\n"
"L_po_poll6:\n"
"buffer_load_dword v218, v224, s[60:63], s94 offen sc1\n"
"s_waitcnt vmcnt(0)\n"
"v_xor_b32 v218, s91, v218\n"
"v_cmp_ne_u32 vcc, 0, v218\n"
"s_nop 1\n"
"s_cbranch_vccz L_po_polld7\n"
"s_sleep 2\n"
"s_sub_u32 s97, s97, 1\n"
"s_cbranch_scc0 L_po_poll6\n"
"L_po_polld7:\n"
"s_add_u32 s89, s87, 0\n"
"buffer_load_dwordx4 a[0:3], v230, s[60:63], s89 offen offset:0 sc1\n"
"s_add_u32 s90, s87, 18432\n"
"buffer_load_dwordx4 a[4:7], v230, s[60:63], s90 offen offset:0 sc1\n"
"s_add_u32 s92, s87, 36864\n"
"buffer_load_dwordx4 a[8:11], v230, s[60:63], s92 offen offset:0 sc1\n"
"s_add_u32 s96, s87, 55296\n"
"buffer_load_dwordx4 a[12:15], v230, s[60:63], s96 offen offset:0 sc1\n"
"buffer_load_dwordx4 a[16:19], v230, s[60:63], s89 offen offset:1024 sc1\n"
"buffer_load_dwordx4 a[20:23], v230, s[60:63], s90 offen offset:1024 sc1\n"
"buffer_load_dwordx4 a[24:27], v230, s[60:63], s92 offen offset:1024 sc1\n"
"buffer_load_dwordx4 a[28:31], v230, s[60:63], s96 offen offset:1024 sc1\n"
"ds_read_b64_tr_b16 v[128:129], v196 offset:0\n"
"ds_read_b64_tr_b16 v[130:131], v196 offset:1024\n"
"ds_read_b64_tr_b16 v[132:133], v196 offset:8192\n"
"ds_read_b64_tr_b16 v[134:135], v196 offset:9216\n"
"ds_read_b64_tr_b16 v[136:137], v196 offset:8\n"
"ds_read_b64_tr_b16 v[138:139], v196 offset:1032\n"
"ds_read_b64_tr_b16 v[140:141], v196 offset:8200\n"
"ds_read_b64_tr_b16 v[142:143], v196 offset:9224\n"
"s_waitcnt vmcnt(0) lgkmcnt(4)\n"
"v_mfma_f32_16x16x32_bf16 v[0:3], v[128:131], a[0:3], 0\n"
"buffer_load_dwordx4 a[32:35], v230, s[60:63], s89 offen offset:2048 sc1\n"
"v_mfma_f32_16x16x32_bf16 v[0:3], v[132:135], a[16:19], v[0:3]\n"
"s_nop 0\n"
"ds_read_b64_tr_b16 v[144:145], v197 offset:0\n"
"ds_read_b64_tr_b16 v[146:147], v197 offset:1024\n"
"v_mfma_f32_16x16x32_bf16 v[32:35], v[128:131], a[4:7], 0\n"
"v_mfma_f32_16x16x32_bf16 v[32:35], v[132:135], a[20:23], v[32:35]\n"
"buffer_load_dwordx4 a[36:39], v230, s[60:63], s90 offen offset:2048 sc1\n"
"v_mfma_f32_16x16x32_bf16 v[64:67], v[128:131], a[8:11], 0\n"
"s_nop 0\n"
"ds_read_b64_tr_b16 v[148:149], v197 offset:8192\n"
"ds_read_b64_tr_b16 v[150:151], v197 offset:9216\n"
"v_mfma_f32_16x16x32_bf16 v[64:67], v[132:135], a[24:27], v[64:67]\n"
"v_mfma_f32_16x16x32_bf16 v[96:99], v[128:131], a[12:15], 0\n"
"v_mfma_f32_16x16x32_bf16 v[96:99], v[132:135], a[28:31], v[96:99]\n"
"s_waitcnt lgkmcnt(4)\n"
"v_mfma_f32_16x16x32_bf16 v[4:7], v[136:139], a[0:3], 0\n"
"buffer_load_dwordx4 a[40:43], v230, s[60:63], s92 offen offset:2048 sc1\n"
"v_mfma_f32_16x16x32_bf16 v[4:7], v[140:143], a[16:19], v[4:7]\n"
"s_nop 0\n"
"ds_read_b64_tr_b16 v[128:129], v197 offset:8\n"
"ds_read_b64_tr_b16 v[130:131], v197 offset:1032\n"
"v_mfma_f32_16x16x32_bf16 v[36:39], v[136:139], a[4:7], 0\n"
"v_mfma_f32_16x16x32_bf16 v[36:39], v[140:143], a[20:23], v[36:39]\n"
"s_nop 0\n"
"ds_read_b64_tr_b16 v[132:133], v197 offset:8200\n"
"ds_read_b64_tr_b16 v[134:135], v197 offset:9224\n"
"v_mfma_f32_16x16x32_bf16 v[68:71], v[136:139], a[8:11], 0\n"
"v_mfma_f32_16x16x32_bf16 v[68:71], v[140:143], a[24:27], v[68:71]\n"
"buffer_load_dwordx4 a[44:47], v230, s[60:63], s96 offen offset:2048 sc1\n"
"v_mfma_f32_16x16x32_bf16 v[100:103], v[136:139], a[12:15], 0\n"
"v_mfma_f32_16x16x32_bf16 v[100:103], v[140:143], a[28:31], v[100:103]\n"
"s_waitcnt lgkmcnt(4)\n"
"v_mfma_f32_16x16x32_bf16 v[8:11], v[144:147], a[0:3], 0\n"
"s_nop 0\n"
"ds_read_b64_tr_b16 v[136:137], v198 offset:0\n"
"ds_read_b64_tr_b16 v[138:139], v198 offset:1024\n"
"v_mfma_f32_16x16x32_bf16 v[8:11], v[148:151], a[16:19], v[8:11]\n"
"s_lshr_b32 s73, %16, 3\n"
"s_mul_i32 s72, %11, 1179648\n"
"v_mfma_f32_16x16x32_bf16 v[40:43], v[144:147], a[4:7], 0\n"
"buffer_load_dwordx4 a[48:51], v230, s[60:63], s89 offen offset:3072 sc1\n"
"v_mfma_f32_16x16x32_bf16 v[40:43], v[148:151], a[20:23], v[40:43]\n"
"s_nop 0\n"
"ds_read_b64_tr_b16 v[140:141], v198 offset:8192\n"
"ds_read_b64_tr_b16 v[142:143], v198 offset:9216\n"
"v_mfma_f32_16x16x32_bf16 v[72:75], v[144:147], a[8:11], 0\n"
"s_mul_i32 s98, s73, 36864\n"
"s_add_u32 s72, s72, s98\n"
"v_mfma_f32_16x16x32_bf16 v[72:75], v[148:151], a[24:27], v[72:75]\n"
"s_mul_i32 s98, %9, 9216\n"
"s_add_u32 s72, s72, s98\n"
"v_mfma_f32_16x16x32_bf16 v[104:107], v[144:147], a[12:15], 0\n"
"v_add_u32 v246, s72, v230\n"
"s_mul_i32 s72, %16, 20736\n"
"v_mfma_f32_16x16x32_bf16 v[104:107], v[148:151], a[28:31], v[104:107]\n"
"s_waitcnt lgkmcnt(4)\n"
"s_mul_i32 s98, %9, 5184\n"
"s_add_u32 s72, s72, s98\n"
"v_mfma_f32_16x16x32_bf16 v[12:15], v[128:131], a[0:3], 0\n"
"buffer_load_dwordx4 a[52:55], v230, s[60:63], s90 offen offset:3072 sc1\n"
"v_mfma_f32_16x16x32_bf16 v[12:15], v[132:135], a[16:19], v[12:15]\n"
"s_nop 0\n"
"ds_read_b64_tr_b16 v[144:145], v198 offset:8\n"
"ds_read_b64_tr_b16 v[146:147], v198 offset:1032\n"
"v_mfma_f32_16x16x32_bf16 v[44:47], v[128:131], a[4:7], 0\n"
"v_add_u32 v247, s72, v230\n"
"s_mul_i32 s93, %12, 147456\n"
"v_mfma_f32_16x16x32_bf16 v[44:47], v[132:135], a[20:23], v[44:47]\n"
"s_nop 0\n"
"ds_read_b64_tr_b16 v[148:149], v198 offset:8200\n"
"ds_read_b64_tr_b16 v[150:151], v198 offset:9224\n"
"v_mfma_f32_16x16x32_bf16 v[76:79], v[128:131], a[8:11], 0\n"
"buffer_load_dwordx4 a[56:59], v230, s[60:63], s92 offen offset:3072 sc1\n"
"v_mfma_f32_16x16x32_bf16 v[76:79], v[132:135], a[24:27], v[76:79]\n"
"s_mul_i32 s98, %11, 36864\n"
"s_add_u32 s93, s93, s98\n"
"v_mfma_f32_16x16x32_bf16 v[108:111], v[128:131], a[12:15], 0\n"
"s_mul_i32 s98, %13, 4608\n"
"s_add_u32 s93, s93, s98\n"
"v_mfma_f32_16x16x32_bf16 v[108:111], v[132:135], a[28:31], v[108:111]\n"
"s_waitcnt lgkmcnt(4)\n"
"v_lshrrev_b32 v252, 4, v230\n"
"v_lshrrev_b32 v249, 2, v252\n"
"v_mfma_f32_16x16x32_bf16 v[16:19], v[136:139], a[0:3], 0\n"
"s_nop 0\n"
"ds_read_b64_tr_b16 v[128:129], v199 offset:0\n"
"ds_read_b64_tr_b16 v[130:131], v199 offset:1024\n"
"v_mfma_f32_16x16x32_bf16 v[16:19], v[140:143], a[16:19], v[16:19]\n"
"buffer_load_dwordx4 a[60:63], v230, s[60:63], s96 offen offset:3072 sc1\n"
"v_mfma_f32_16x16x32_bf16 v[48:51], v[136:139], a[4:7], 0\n"
"s_add_u32 s89, s87, 4096\n"
"s_lshl_b32 s72, s68, 8\n"
"v_lshlrev_b32 v243, 4, v249\n"
"v_mfma_f32_16x16x32_bf16 v[48:51], v[140:143], a[20:23], v[48:51]\n"
"s_nop 0\n"
"ds_read_b64_tr_b16 v[132:133], v199 offset:8192\n"
"ds_read_b64_tr_b16 v[134:135], v199 offset:9216\n"
"v_mfma_f32_16x16x32_bf16 v[80:83], v[136:139], a[8:11], 0\n"
"v_add_u32 v243, s72, v243\n"
"v_and_b32 v250, 3, v252\n"
"v_mfma_f32_16x16x32_bf16 v[80:83], v[140:143], a[24:27], v[80:83]\n"
"v_mul_u32_u24 v244, 1152, v250\n"
"v_lshrrev_b32 v251, 1, v243\n"
"v_mfma_f32_16x16x32_bf16 v[112:115], v[136:139], a[12:15], 0\n"
"buffer_load_dwordx4 a[64:67], v230, s[60:63], s89 offen offset:0 sc1\n"
"v_mfma_f32_16x16x32_bf16 v[112:115], v[140:143], a[28:31], v[112:115]\n"
"s_waitcnt lgkmcnt(4)\n"
"v_add_u32 v244, v244, v251\n"
"v_bfe_u32 v251, v252, 4, 1\n"
"v_mfma_f32_16x16x32_bf16 v[20:23], v[144:147], a[0:3], 0\n"
"s_add_u32 s90, s87, 22528\n"
"ds_read_b64_tr_b16 v[136:137], v199 offset:8\n"
"ds_read_b64_tr_b16 v[138:139], v199 offset:1032\n"
"v_mfma_f32_16x16x32_bf16 v[20:23], v[148:151], a[16:19], v[20:23]\n"
"v_mul_u32_u24 v245, 18432, v251\n"
"v_lshrrev_b32 v251, 5, v252\n"
"v_mfma_f32_16x16x32_bf16 v[52:55], v[144:147], a[4:7], 0\n"
"v_lshl_add_u32 v245, v251, 10, v245\n"
"v_bfe_u32 v251, v252, 2, 2\n"
"v_mfma_f32_16x16x32_bf16 v[52:55], v[148:151], a[20:23], v[52:55]\n"
"buffer_load_dwordx4 a[68:71], v230, s[60:63], s90 offen offset:0 sc1\n"
"v_mfma_f32_16x16x32_bf16 v[84:87], v[144:147], a[8:11], 0\n"
"s_add_u32 s92, s87, 40960\n"
"ds_read_b64_tr_b16 v[140:141], v199 offset:8200\n"
"ds_read_b64_tr_b16 v[142:143], v199 offset:9224\n"
"v_mfma_f32_16x16x32_bf16 v[84:87], v[148:151], a[24:27], v[84:87]\n"
"v_lshl_add_u32 v245, v251, 8, v245\n"
"v_lshl_add_u32 v245, v250, 4, v245\n"
"v_mfma_f32_16x16x32_bf16 v[116:119], v[144:147], a[12:15], 0\n"
"s_lshl_b32 s72, s68, 6\n"
"v_add_u32 v185, s72, v252\n"
"v_mfma_f32_16x16x32_bf16 v[116:119], v[148:151], a[28:31], v[116:119]\n"
"s_waitcnt vmcnt(27) lgkmcnt(4)\n"
"s_barrier\n"
"v_mfma_f32_16x16x32_bf16 v[24:27], v[128:131], a[0:3], 0\n"
"buffer_load_dwordx4 a[72:75], v230, s[60:63], s92 offen offset:0 sc1\n"
"v_mfma_f32_16x16x32_bf16 v[24:27], v[132:135], a[16:19], v[24:27]\n"
"s_add_u32 s96, s87, 59392\n"
"ds_read_b64_tr_b16 v[144:145], v196 offset:16384\n"
"ds_read_b64_tr_b16 v[146:147], v196 offset:17408\n"
"v_mfma_f32_16x16x32_bf16 v[56:59], v[128:131], a[4:7], 0\n"
"s_mov_b32 s73, 1152\n"
"v_add_u32 v186, 0, v185\n"
"v_mfma_f32_16x16x32_bf16 v[56:59], v[132:135], a[20:23], v[56:59]\n"
"s_nop 0\n"
"ds_read_b64_tr_b16 v[148:149], v196 offset:24576\n"
"ds_read_b64_tr_b16 v[150:151], v196 offset:25600\n"
"v_mfma_f32_16x16x32_bf16 v[88:91], v[128:131], a[8:11], 0\n"
"v_mul_u32_u24 v187, 58255, v186\n"
"v_lshrrev_b32 v187, 20, v187\n"
"v_mfma_f32_16x16x32_bf16 v[88:91], v[132:135], a[24:27], v[88:91]\n"
"buffer_load_dwordx4 a[76:79], v230, s[60:63], s96 offen offset:0 sc1\n"
"v_mfma_f32_16x16x32_bf16 v[120:123], v[128:131], a[12:15], 0\n"
"v_mul_u32_u24 v188, 18, v187\n"
"v_sub_u32 v186, v186, v188\n"
"v_mfma_f32_16x16x32_bf16 v[120:123], v[132:135], a[28:31], v[120:123]\n"
"s_waitcnt lgkmcnt(4)\n"
"v_mul_u32_u24 v187, s73, v187\n"
"v_lshl_add_u32 v186, v186, 4, v187\n"
"v_mfma_f32_16x16x32_bf16 v[28:31], v[136:139], a[0:3], 0\n"
"s_nop 0\n"
"ds_read_b64_tr_b16 v[128:129], v196 offset:16392\n"
"ds_read_b64_tr_b16 v[130:131], v196 offset:17416\n"
"v_mfma_f32_16x16x32_bf16 v[28:31], v[140:143], a[16:19], v[28:31]\n"
"v_accvgpr_write_b32 a164, v186\n"
"v_add_u32 v186, 256, v185\n"
"v_mfma_f32_16x16x32_bf16 v[60:63], v[136:139], a[4:7], 0\n"
"buffer_load_dwordx4 a[80:83], v230, s[60:63], s89 offen offset:1024 sc1\n"
"v_mfma_f32_16x16x32_bf16 v[60:63], v[140:143], a[20:23], v[60:63]\n"
"s_nop 0\n"
"ds_read_b64_tr_b16 v[132:133], v196 offset:24584\n"
"ds_read_b64_tr_b16 v[134:135], v196 offset:25608\n"
"v_mfma_f32_16x16x32_bf16 v[92:95], v[136:139], a[8:11], 0\n"
"v_mul_u32_u24 v187, 58255, v186\n"
"v_lshrrev_b32 v187, 20, v187\n"
"v_mfma_f32_16x16x32_bf16 v[92:95], v[140:143], a[24:27], v[92:95]\n"
"v_mul_u32_u24 v188, 18, v187\n"
"v_sub_u32 v186, v186, v188\n"
"v_mfma_f32_16x16x32_bf16 v[124:127], v[136:139], a[12:15], 0\n"
"v_mul_u32_u24 v187, s73, v187\n"
"v_lshl_add_u32 v186, v186, 4, v187\n"
"v_mfma_f32_16x16x32_bf16 v[124:127], v[140:143], a[28:31], v[124:127]\n"
"s_waitcnt vmcnt(5) lgkmcnt(4)\n"
"v_accvgpr_write_b32 a165, v186\n"
"v_add_u32 v186, 512, v185\n"
"v_mfma_f32_16x16x32_bf16 v[0:3], v[144:147], a[32:35], v[0:3]\n"
"buffer_load_dwordx4 a[84:87], v230, s[60:63], s90 offen offset:1024 sc1\n"
"v_mfma_f32_16x16x32_bf16 v[0:3], v[148:151], a[48:51], v[0:3]\n"
"s_nop 0\n"
"ds_read_b64_tr_b16 v[136:137], v197 offset:16384\n"
"ds_read_b64_tr_b16 v[138:139], v197 offset:17408\n"
"v_mfma_f32_16x16x32_bf16 v[32:35], v[144:147], a[36:39], v[32:35]\n"
"v_mul_u32_u24 v187, 58255, v186\n"
"v_lshrrev_b32 v187, 20, v187\n"
"v_mfma_f32_16x16x32_bf16 v[32:35], v[148:151], a[52:55], v[32:35]\n"
"s_nop 0\n"
"ds_read_b64_tr_b16 v[140:141], v197 offset:24576\n"
"ds_read_b64_tr_b16 v[142:143], v197 offset:25600\n"
"v_mfma_f32_16x16x32_bf16 v[64:67], v[144:147], a[40:43], v[64:67]\n"
"buffer_load_dwordx4 a[88:91], v230, s[60:63], s92 offen offset:1024 sc1\n"
"v_mfma_f32_16x16x32_bf16 v[64:67], v[148:151], a[56:59], v[64:67]\n"
"v_mul_u32_u24 v188, 18, v187\n"
"v_sub_u32 v186, v186, v188\n"
"v_mfma_f32_16x16x32_bf16 v[96:99], v[144:147], a[44:47], v[96:99]\n"
"v_mul_u32_u24 v187, s73, v187\n"
"v_lshl_add_u32 v186, v186, 4, v187\n"
"v_mfma_f32_16x16x32_bf16 v[96:99], v[148:151], a[60:63], v[96:99]\n"
"s_waitcnt lgkmcnt(4)\n"
"v_accvgpr_write_b32 a166, v186\n"
"v_add_u32 v186, 768, v185\n"
"v_mfma_f32_16x16x32_bf16 v[4:7], v[128:131], a[32:35], v[4:7]\n"
"s_nop 0\n"
"ds_read_b64_tr_b16 v[144:145], v197 offset:16392\n"
"ds_read_b64_tr_b16 v[146:147], v197 offset:17416\n"
"v_mfma_f32_16x16x32_bf16 v[4:7], v[132:135], a[48:51], v[4:7]\n"
"buffer_load_dwordx4 a[92:95], v230, s[60:63], s96 offen offset:1024 sc1\n"
"v_mfma_f32_16x16x32_bf16 v[36:39], v[128:131], a[36:39], v[36:39]\n"
"v_mul_u32_u24 v187, 58255, v186\n"
"v_lshrrev_b32 v187, 20, v187\n"
"v_mfma_f32_16x16x32_bf16 v[36:39], v[132:135], a[52:55], v[36:39]\n"
"s_nop 0\n"
"ds_read_b64_tr_b16 v[148:149], v197 offset:24584\n"
"ds_read_b64_tr_b16 v[150:151], v197 offset:25608\n"
"v_mfma_f32_16x16x32_bf16 v[68:71], v[128:131], a[40:43], v[68:71]\n"
"v_mul_u32_u24 v188, 18, v187\n"
"v_sub_u32 v186, v186, v188\n"
"v_mfma_f32_16x16x32_bf16 v[68:71], v[132:135], a[56:59], v[68:71]\n"
"v_mul_u32_u24 v187, s73, v187\n"
"v_lshl_add_u32 v186, v186, 4, v187\n"
"v_mfma_f32_16x16x32_bf16 v[100:103], v[128:131], a[44:47], v[100:103]\n"
"buffer_load_dwordx4 a[96:99], v230, s[60:63], s89 offen offset:2048 sc1\n"
"v_mfma_f32_16x16x32_bf16 v[100:103], v[132:135], a[60:63], v[100:103]\n"
"s_waitcnt lgkmcnt(4)\n"
"v_accvgpr_write_b32 a167, v186\n"
"v_add_u32 v186, 1024, v185\n"
"v_mfma_f32_16x16x32_bf16 v[8:11], v[136:139], a[32:35], v[8:11]\n"
"s_nop 0\n"
"ds_read_b64_tr_b16 v[128:129], v198 offset:16384\n"
"ds_read_b64_tr_b16 v[130:131], v198 offset:17408\n"
"v_mfma_f32_16x16x32_bf16 v[8:11], v[140:143], a[48:51], v[8:11]\n"
"v_mul_u32_u24 v187, 58255, v186\n"
"v_lshrrev_b32 v187, 20, v187\n"
"v_mfma_f32_16x16x32_bf16 v[40:43], v[136:139], a[36:39], v[40:43]\n"
"v_mul_u32_u24 v188, 18, v187\n"
"v_sub_u32 v186, v186, v188\n"
"v_mfma_f32_16x16x32_bf16 v[40:43], v[140:143], a[52:55], v[40:43]\n"
"buffer_load_dwordx4 a[100:103], v230, s[60:63], s90 offen offset:2048 sc1\n"
"v_mfma_f32_16x16x32_bf16 v[72:75], v[136:139], a[40:43], v[72:75]\n"
"s_nop 0\n"
"ds_read_b64_tr_b16 v[132:133], v198 offset:24576\n"
"ds_read_b64_tr_b16 v[134:135], v198 offset:25600\n"
"v_mfma_f32_16x16x32_bf16 v[72:75], v[140:143], a[56:59], v[72:75]\n"
"v_mul_u32_u24 v187, s73, v187\n"
"v_lshl_add_u32 v186, v186, 4, v187\n"
"v_mfma_f32_16x16x32_bf16 v[104:107], v[136:139], a[44:47], v[104:107]\n"
"v_accvgpr_write_b32 a168, v186\n"
"v_add_u32 v186, 1280, v185\n"
"v_mfma_f32_16x16x32_bf16 v[104:107], v[140:143], a[60:63], v[104:107]\n"
"s_waitcnt lgkmcnt(4)\n"
"v_mul_u32_u24 v187, 58255, v186\n"
"v_lshrrev_b32 v187, 20, v187\n"
"v_mfma_f32_16x16x32_bf16 v[12:15], v[144:147], a[32:35], v[12:15]\n"
"buffer_load_dwordx4 a[104:107], v230, s[60:63], s92 offen offset:2048 sc1\n"
"v_mfma_f32_16x16x32_bf16 v[12:15], v[148:151], a[48:51], v[12:15]\n"
"s_nop 0\n"
"ds_read_b64_tr_b16 v[136:137], v198 offset:16392\n"
"ds_read_b64_tr_b16 v[138:139], v198 offset:17416\n"
"v_mfma_f32_16x16x32_bf16 v[44:47], v[144:147], a[36:39], v[44:47]\n"
"v_mul_u32_u24 v188, 18, v187\n"
"v_sub_u32 v186, v186, v188\n"
"v_mfma_f32_16x16x32_bf16 v[44:47], v[148:151], a[52:55], v[44:47]\n"
"s_nop 0\n"
"ds_read_b64_tr_b16 v[140:141], v198 offset:24584\n"
"ds_read_b64_tr_b16 v[142:143], v198 offset:25608\n"
"v_mfma_f32_16x16x32_bf16 v[76:79], v[144:147], a[40:43], v[76:79]\n"
"v_mul_u32_u24 v187, s73, v187\n"
"v_lshl_add_u32 v186, v186, 4, v187\n"
"v_mfma_f32_16x16x32_bf16 v[76:79], v[148:151], a[56:59], v[76:79]\n"
"buffer_load_dwordx4 a[108:111], v230, s[60:63], s96 offen offset:2048 sc1\n"
"v_mfma_f32_16x16x32_bf16 v[108:111], v[144:147], a[44:47], v[108:111]\n"
"v_accvgpr_write_b32 a169, v186\n"
"v_add_u32 v186, 1536, v185\n"
"v_mfma_f32_16x16x32_bf16 v[108:111], v[148:151], a[60:63], v[108:111]\n"
"s_waitcnt lgkmcnt(4)\n"
"v_mul_u32_u24 v187, 58255, v186\n"
"v_lshrrev_b32 v187, 20, v187\n"
"v_mfma_f32_16x16x32_bf16 v[16:19], v[128:131], a[32:35], v[16:19]\n"
"s_nop 0\n"
"ds_read_b64_tr_b16 v[144:145], v199 offset:16384\n"
"ds_read_b64_tr_b16 v[146:147], v199 offset:17408\n"
"v_mfma_f32_16x16x32_bf16 v[16:19], v[132:135], a[48:51], v[16:19]\n"
"v_mul_u32_u24 v188, 18, v187\n"
"v_sub_u32 v186, v186, v188\n"
"v_mfma_f32_16x16x32_bf16 v[48:51], v[128:131], a[36:39], v[48:51]\n"
"buffer_load_dwordx4 a[112:115], v230, s[60:63], s89 offen offset:3072 sc1\n"
"v_mfma_f32_16x16x32_bf16 v[48:51], v[132:135], a[52:55], v[48:51]\n"
"s_nop 0\n"
"ds_read_b64_tr_b16 v[148:149], v199 offset:24576\n"
"ds_read_b64_tr_b16 v[150:151], v199 offset:25600\n"
"v_mfma_f32_16x16x32_bf16 v[80:83], v[128:131], a[40:43], v[80:83]\n"
"v_mul_u32_u24 v187, s73, v187\n"
"v_lshl_add_u32 v186, v186, 4, v187\n"
"v_mfma_f32_16x16x32_bf16 v[80:83], v[132:135], a[56:59], v[80:83]\n"
"v_accvgpr_write_b32 a170, v186\n"
"v_add_u32 v186, 1792, v185\n"
"v_mfma_f32_16x16x32_bf16 v[112:115], v[128:131], a[44:47], v[112:115]\n"
"v_mul_u32_u24 v187, 58255, v186\n"
"v_lshrrev_b32 v187, 20, v187\n"
"v_mfma_f32_16x16x32_bf16 v[112:115], v[132:135], a[60:63], v[112:115]\n"
"s_waitcnt lgkmcnt(4)\n"
"v_mul_u32_u24 v188, 18, v187\n"
"v_sub_u32 v186, v186, v188\n"
"v_mfma_f32_16x16x32_bf16 v[20:23], v[136:139], a[32:35], v[20:23]\n"
"buffer_load_dwordx4 a[116:119], v230, s[60:63], s90 offen offset:3072 sc1\n"
"v_mfma_f32_16x16x32_bf16 v[20:23], v[140:143], a[48:51], v[20:23]\n"
"s_nop 0\n"
"ds_read_b64_tr_b16 v[128:129], v199 offset:16392\n"
"ds_read_b64_tr_b16 v[130:131], v199 offset:17416\n"
"v_mfma_f32_16x16x32_bf16 v[52:55], v[136:139], a[36:39], v[52:55]\n"
"v_mul_u32_u24 v187, s73, v187\n"
"v_lshl_add_u32 v186, v186, 4, v187\n"
"v_mfma_f32_16x16x32_bf16 v[52:55], v[140:143], a[52:55], v[52:55]\n"
"s_nop 0\n"
"ds_read_b64_tr_b16 v[132:133], v199 offset:24584\n"
"ds_read_b64_tr_b16 v[134:135], v199 offset:25608\n"
"v_mfma_f32_16x16x32_bf16 v[84:87], v[136:139], a[40:43], v[84:87]\n"
"buffer_load_dwordx4 a[120:123], v230, s[60:63], s92 offen offset:3072 sc1\n"
"v_mfma_f32_16x16x32_bf16 v[84:87], v[140:143], a[56:59], v[84:87]\n"
"v_accvgpr_write_b32 a171, v186\n"
"v_add_u32 v186, 2048, v185\n"
"v_mfma_f32_16x16x32_bf16 v[116:119], v[136:139], a[44:47], v[116:119]\n"
"v_mul_u32_u24 v187, 58255, v186\n"
"v_lshrrev_b32 v187, 20, v187\n"
"v_mfma_f32_16x16x32_bf16 v[116:119], v[140:143], a[60:63], v[116:119]\n"
"s_waitcnt vmcnt(36) lgkmcnt(4)\n"
"s_barrier\n"
"v_mfma_f32_16x16x32_bf16 v[24:27], v[144:147], a[32:35], v[24:27]\n"
"s_nop 0\n"
"ds_read_b64_tr_b16 v[136:137], v196 offset:32768\n"
"ds_read_b64_tr_b16 v[138:139], v196 offset:33792\n"
"v_mfma_f32_16x16x32_bf16 v[24:27], v[148:151], a[48:51], v[24:27]\n"
"buffer_load_dwordx4 a[124:127], v230, s[60:63], s96 offen offset:3072 sc1\n"
"v_mfma_f32_16x16x32_bf16 v[56:59], v[144:147], a[36:39], v[56:59]\n"
"s_add_u32 m0, s84, 65536\n"
"v_mul_u32_u24 v188, 18, v187\n"
"v_sub_u32 v186, v186, v188\n"
"v_mfma_f32_16x16x32_bf16 v[56:59], v[148:151], a[52:55], v[56:59]\n"
"s_mov_b32 s88, 2097152\n"
"ds_read_b64_tr_b16 v[140:141], v196 offset:40960\n"
"ds_read_b64_tr_b16 v[142:143], v196 offset:41984\n"
"v_mfma_f32_16x16x32_bf16 v[88:91], v[144:147], a[40:43], v[88:91]\n"
"v_mul_u32_u24 v187, s73, v187\n"
"v_lshl_add_u32 v186, v186, 4, v187\n"
"v_mfma_f32_16x16x32_bf16 v[88:91], v[148:151], a[56:59], v[88:91]\n"
"v_accvgpr_write_b32 a172, v186\n"
"s_lshl_b32 s72, s68, 6\n"
"v_mfma_f32_16x16x32_bf16 v[120:123], v[144:147], a[44:47], v[120:123]\n"
"buffer_load_dwordx4 v231, s[52:55], s88 offen offset:0 lds\n"
"v_mfma_f32_16x16x32_bf16 v[120:123], v[148:151], a[60:63], v[120:123]\n"
"s_waitcnt lgkmcnt(4)\n"
"s_barrier\n"
"v_mfma_f32_16x16x32_bf16 v[28:31], v[128:131], a[32:35], v[28:31]\n"
"s_nop 0\n"
"ds_read_b64_tr_b16 v[144:145], v196 offset:32776\n"
"ds_read_b64_tr_b16 v[146:147], v196 offset:33800\n"
"v_mfma_f32_16x16x32_bf16 v[28:31], v[132:135], a[48:51], v[28:31]\n"
"v_add_u32 v185, s72, v252\n"
"s_mov_b32 s73, 9216\n"
"v_mfma_f32_16x16x32_bf16 v[60:63], v[128:131], a[36:39], v[60:63]\n"
"v_add_u32 v186, 0, v185\n"
"v_mul_u32_u24 v187, 58255, v186\n"
"v_mfma_f32_16x16x32_bf16 v[60:63], v[132:135], a[52:55], v[60:63]\n"
"buffer_load_dwordx4 v232, s[52:55], s88 offen offset:1024 lds\n"
"v_mfma_f32_16x16x32_bf16 v[92:95], v[128:131], a[40:43], v[92:95]\n"
"s_add_u32 m0, s84, 73728\n"
"ds_read_b64_tr_b16 v[148:149], v196 offset:40968\n"
"ds_read_b64_tr_b16 v[150:151], v196 offset:41992\n"
"v_mfma_f32_16x16x32_bf16 v[92:95], v[132:135], a[56:59], v[92:95]\n"
"s_mov_b32 s85, 2359296\n"
"v_lshrrev_b32 v187, 20, v187\n"
"v_mul_u32_u24 v188, 18, v187\n"
"v_mfma_f32_16x16x32_bf16 v[124:127], v[128:131], a[44:47], v[124:127]\n"
"v_sub_u32 v186, v186, v188\n"
"v_mul_u32_u24 v187, s73, v187\n"
"v_mfma_f32_16x16x32_bf16 v[124:127], v[132:135], a[60:63], v[124:127]\n"
"s_waitcnt vmcnt(10) lgkmcnt(4)\n"
"v_lshl_add_u32 v186, v186, 4, v187\n"
"v_accvgpr_write_b32 a173, v186\n"
"v_mfma_f32_16x16x32_bf16 v[0:3], v[136:139], a[64:67], v[0:3]\n"
"buffer_load_dwordx4 v231, s[52:55], s85 offen offset:0 lds\n"
"v_mfma_f32_16x16x32_bf16 v[0:3], v[140:143], a[80:83], v[0:3]\n"
"s_nop 0\n"
"ds_read_b64_tr_b16 v[128:129], v197 offset:32768\n"
"ds_read_b64_tr_b16 v[130:131], v197 offset:33792\n"
"v_mfma_f32_16x16x32_bf16 v[32:35], v[136:139], a[68:71], v[32:35]\n"
"v_add_u32 v186, 256, v185\n"
"v_mul_u32_u24 v187, 58255, v186\n"
"v_mfma_f32_16x16x32_bf16 v[32:35], v[140:143], a[84:87], v[32:35]\n"
"s_nop 0\n"
"ds_read_b64_tr_b16 v[132:133], v197 offset:40960\n"
"ds_read_b64_tr_b16 v[134:135], v197 offset:41984\n"
"v_mfma_f32_16x16x32_bf16 v[64:67], v[136:139], a[72:75], v[64:67]\n"
"L_tc0_0:\n"
"s_cbranch_execz L_tp0_0_1\n"
"v_lshrrev_b32 v187, 20, v187\n"
"v_mul_u32_u24 v188, 18, v187\n"
"v_mfma_f32_16x16x32_bf16 v[64:67], v[140:143], a[88:91], v[64:67]\n"
"buffer_load_dwordx4 v232, s[52:55], s85 offen offset:1024 lds\n"
"v_mfma_f32_16x16x32_bf16 v[96:99], v[136:139], a[76:79], v[96:99]\n"
"s_add_u32 s89, s87, 8192\n"
"v_sub_u32 v186, v186, v188\n"
"v_mul_u32_u24 v187, s73, v187\n"
"v_mfma_f32_16x16x32_bf16 v[96:99], v[140:143], a[92:95], v[96:99]\n"
"s_waitcnt lgkmcnt(4)\n"
"v_lshl_add_u32 v186, v186, 4, v187\n"
"v_accvgpr_write_b32 a174, v186\n"
"v_mfma_f32_16x16x32_bf16 v[4:7], v[144:147], a[64:67], v[4:7]\n"
"s_nop 0\n"
"ds_read_b64_tr_b16 v[136:137], v197 offset:32776\n"
"ds_read_b64_tr_b16 v[138:139], v197 offset:33800\n"
"v_mfma_f32_16x16x32_bf16 v[4:7], v[148:151], a[80:83], v[4:7]\n"
"v_add_u32 v186, 512, v185\n"
"v_mul_u32_u24 v187, 58255, v186\n"
"v_mfma_f32_16x16x32_bf16 v[36:39], v[144:147], a[68:71], v[36:39]\n"
"buffer_load_dwordx4 a[128:131], v230, s[60:63], s89 offen offset:0 sc1\n"
"v_mfma_f32_16x16x32_bf16 v[36:39], v[148:151], a[84:87], v[36:39]\n"
"s_add_u32 s90, s87, 26624\n"
"ds_read_b64_tr_b16 v[140:141], v197 offset:40968\n"
"ds_read_b64_tr_b16 v[142:143], v197 offset:41992\n"
"v_mfma_f32_16x16x32_bf16 v[68:71], v[144:147], a[72:75], v[68:71]\n"
"v_lshrrev_b32 v187, 20, v187\n"
"v_mul_u32_u24 v188, 18, v187\n"
"v_mfma_f32_16x16x32_bf16 v[68:71], v[148:151], a[88:91], v[68:71]\n"
"v_sub_u32 v186, v186, v188\n"
"v_mul_u32_u24 v187, s73, v187\n"
"v_mfma_f32_16x16x32_bf16 v[100:103], v[144:147], a[76:79], v[100:103]\n"
"v_lshl_add_u32 v186, v186, 4, v187\n"
"v_accvgpr_write_b32 a175, v186\n"
"v_mfma_f32_16x16x32_bf16 v[100:103], v[148:151], a[92:95], v[100:103]\n"
"s_waitcnt lgkmcnt(4)\n"
"v_add_u32 v186, 768, v185\n"
"v_mul_u32_u24 v187, 58255, v186\n"
"v_mfma_f32_16x16x32_bf16 v[8:11], v[128:131], a[64:67], v[8:11]\n"
"buffer_load_dwordx4 a[132:135], v230, s[60:63], s90 offen offset:0 sc1\n"
"v_mfma_f32_16x16x32_bf16 v[8:11], v[132:135], a[80:83], v[8:11]\n"
"s_add_u32 s92, s87, 45056\n"
"ds_read_b64_tr_b16 v[144:145], v198 offset:32768\n"
"ds_read_b64_tr_b16 v[146:147], v198 offset:33792\n"
"v_mfma_f32_16x16x32_bf16 v[40:43], v[128:131], a[68:71], v[40:43]\n"
"v_lshrrev_b32 v187, 20, v187\n"
"v_mul_u32_u24 v188, 18, v187\n"
"v_mfma_f32_16x16x32_bf16 v[40:43], v[132:135], a[84:87], v[40:43]\n"
"s_nop 0\n"
"ds_read_b64_tr_b16 v[148:149], v198 offset:40960\n"
"ds_read_b64_tr_b16 v[150:151], v198 offset:41984\n"
"v_mfma_f32_16x16x32_bf16 v[72:75], v[128:131], a[72:75], v[72:75]\n"
"buffer_load_dwordx4 a[136:139], v230, s[60:63], s92 offen offset:0 sc1\n"
"v_mfma_f32_16x16x32_bf16 v[72:75], v[132:135], a[88:91], v[72:75]\n"
"s_add_u32 s96, s87, 63488\n"
"v_sub_u32 v186, v186, v188\n"
"v_mul_u32_u24 v187, s73, v187\n"
"v_mfma_f32_16x16x32_bf16 v[104:107], v[128:131], a[76:79], v[104:107]\n"
"v_lshl_add_u32 v186, v186, 4, v187\n"
"v_accvgpr_write_b32 a176, v186\n"
"v_mfma_f32_16x16x32_bf16 v[104:107], v[132:135], a[92:95], v[104:107]\n"
"s_waitcnt lgkmcnt(4)\n"
"v_add_u32 v186, 1024, v185\n"
"v_mul_u32_u24 v187, 58255, v186\n"
"v_mfma_f32_16x16x32_bf16 v[12:15], v[136:139], a[64:67], v[12:15]\n"
"s_nop 0\n"
"ds_read_b64_tr_b16 v[128:129], v198 offset:32776\n"
"ds_read_b64_tr_b16 v[130:131], v198 offset:33800\n"
"v_mfma_f32_16x16x32_bf16 v[12:15], v[140:143], a[80:83], v[12:15]\n"
"buffer_load_dwordx4 a[140:143], v230, s[60:63], s96 offen offset:0 sc1\n"
"v_mfma_f32_16x16x32_bf16 v[44:47], v[136:139], a[68:71], v[44:47]\n"
"v_lshrrev_b32 v187, 20, v187\n"
"v_mul_u32_u24 v188, 18, v187\n"
"v_mfma_f32_16x16x32_bf16 v[44:47], v[140:143], a[84:87], v[44:47]\n"
"s_nop 0\n"
"ds_read_b64_tr_b16 v[132:133], v198 offset:40968\n"
"ds_read_b64_tr_b16 v[134:135], v198 offset:41992\n"
"v_mfma_f32_16x16x32_bf16 v[76:79], v[136:139], a[72:75], v[76:79]\n"
"v_sub_u32 v186, v186, v188\n"
"v_mul_u32_u24 v187, s73, v187\n"
"v_mfma_f32_16x16x32_bf16 v[76:79], v[140:143], a[88:91], v[76:79]\n"
"v_lshl_add_u32 v186, v186, 4, v187\n"
"v_accvgpr_write_b32 a177, v186\n"
"v_mfma_f32_16x16x32_bf16 v[108:111], v[136:139], a[76:79], v[108:111]\n"
"buffer_load_dwordx4 a[144:147], v230, s[60:63], s89 offen offset:1024 sc1\n"
"v_mfma_f32_16x16x32_bf16 v[108:111], v[140:143], a[92:95], v[108:111]\n"
"s_waitcnt lgkmcnt(4)\n"
"v_add_u32 v186, 1280, v185\n"
"v_mul_u32_u24 v187, 58255, v186\n"
"v_mfma_f32_16x16x32_bf16 v[16:19], v[144:147], a[64:67], v[16:19]\n"
"s_nop 0\n"
"ds_read_b64_tr_b16 v[136:137], v199 offset:32768\n"
"ds_read_b64_tr_b16 v[138:139], v199 offset:33792\n"
"v_mfma_f32_16x16x32_bf16 v[16:19], v[148:151], a[80:83], v[16:19]\n"
"v_lshrrev_b32 v187, 20, v187\n"
"v_mul_u32_u24 v188, 18, v187\n"
"v_mfma_f32_16x16x32_bf16 v[48:51], v[144:147], a[68:71], v[48:51]\n"
"v_sub_u32 v186, v186, v188\n"
"v_mul_u32_u24 v187, s73, v187\n"
"v_mfma_f32_16x16x32_bf16 v[48:51], v[148:151], a[84:87], v[48:51]\n"
"buffer_load_dwordx4 a[148:151], v230, s[60:63], s90 offen offset:1024 sc1\n"
"v_mfma_f32_16x16x32_bf16 v[80:83], v[144:147], a[72:75], v[80:83]\n"
"s_nop 0\n"
"ds_read_b64_tr_b16 v[140:141], v199 offset:40960\n"
"ds_read_b64_tr_b16 v[142:143], v199 offset:41984\n"
"v_mfma_f32_16x16x32_bf16 v[80:83], v[148:151], a[88:91], v[80:83]\n"
"v_lshl_add_u32 v186, v186, 4, v187\n"
"v_accvgpr_write_b32 a178, v186\n"
"v_mfma_f32_16x16x32_bf16 v[112:115], v[144:147], a[76:79], v[112:115]\n"
"v_add_u32 v186, 1536, v185\n"
"v_mul_u32_u24 v187, 58255, v186\n"
"v_mfma_f32_16x16x32_bf16 v[112:115], v[148:151], a[92:95], v[112:115]\n"
"s_waitcnt lgkmcnt(4)\n"
"v_lshrrev_b32 v187, 20, v187\n"
"v_mul_u32_u24 v188, 18, v187\n"
"v_mfma_f32_16x16x32_bf16 v[20:23], v[128:131], a[64:67], v[20:23]\n"
"buffer_load_dwordx4 a[152:155], v230, s[60:63], s92 offen offset:1024 sc1\n"
"v_mfma_f32_16x16x32_bf16 v[20:23], v[132:135], a[80:83], v[20:23]\n"
"s_nop 0\n"
"ds_read_b64_tr_b16 v[144:145], v199 offset:32776\n"
"ds_read_b64_tr_b16 v[146:147], v199 offset:33800\n"
"v_mfma_f32_16x16x32_bf16 v[52:55], v[128:131], a[68:71], v[52:55]\n"
"v_sub_u32 v186, v186, v188\n"
"v_mul_u32_u24 v187, s73, v187\n"
"v_mfma_f32_16x16x32_bf16 v[52:55], v[132:135], a[84:87], v[52:55]\n"
"s_nop 0\n"
"ds_read_b64_tr_b16 v[148:149], v199 offset:40968\n"
"ds_read_b64_tr_b16 v[150:151], v199 offset:41992\n"
"v_mfma_f32_16x16x32_bf16 v[84:87], v[128:131], a[72:75], v[84:87]\n"
"v_lshl_add_u32 v186, v186, 4, v187\n"
"v_accvgpr_write_b32 a179, v186\n"
"v_mfma_f32_16x16x32_bf16 v[84:87], v[132:135], a[88:91], v[84:87]\n"
"buffer_load_dwordx4 a[156:159], v230, s[60:63], s96 offen offset:1024 sc1\n"
"v_mfma_f32_16x16x32_bf16 v[116:119], v[128:131], a[76:79], v[116:119]\n"
"s_add_u32 m0, s84, 81920\n"
"v_add_u32 v186, 1792, v185\n"
"v_mul_u32_u24 v187, 58255, v186\n"
"v_mfma_f32_16x16x32_bf16 v[116:119], v[132:135], a[92:95], v[116:119]\n"
"s_waitcnt vmcnt(45) lgkmcnt(4)\n"
"s_barrier\n"
"v_mfma_f32_16x16x32_bf16 v[24:27], v[136:139], a[64:67], v[24:27]\n"
"s_mov_b32 s88, 2621440\n"
"ds_read_b64_tr_b16 v[128:129], v200 offset:0\n"
"ds_read_b64_tr_b16 v[130:131], v200 offset:1024\n"
"v_mfma_f32_16x16x32_bf16 v[24:27], v[140:143], a[80:83], v[24:27]\n"
"v_lshrrev_b32 v187, 20, v187\n"
"v_mul_u32_u24 v188, 18, v187\n"
"v_mfma_f32_16x16x32_bf16 v[56:59], v[136:139], a[68:71], v[56:59]\n"
"buffer_load_dwordx4 v231, s[52:55], s88 offen offset:0 lds\n"
"v_mfma_f32_16x16x32_bf16 v[56:59], v[140:143], a[84:87], v[56:59]\n"
"s_nop 0\n"
"ds_read_b64_tr_b16 v[132:133], v200 offset:8192\n"
"ds_read_b64_tr_b16 v[134:135], v200 offset:9216\n"
"v_mfma_f32_16x16x32_bf16 v[88:91], v[136:139], a[72:75], v[88:91]\n"
"v_sub_u32 v186, v186, v188\n"
"v_mul_u32_u24 v187, s73, v187\n"
"v_mfma_f32_16x16x32_bf16 v[88:91], v[140:143], a[88:91], v[88:91]\n"
"v_lshl_add_u32 v186, v186, 4, v187\n"
"v_accvgpr_write_b32 a180, v186\n"
"v_mfma_f32_16x16x32_bf16 v[120:123], v[136:139], a[76:79], v[120:123]\n"
"v_add_u32 v186, 2048, v185\n"
"v_mul_u32_u24 v187, 58255, v186\n"
"v_mfma_f32_16x16x32_bf16 v[120:123], v[140:143], a[92:95], v[120:123]\n"
"s_waitcnt lgkmcnt(4)\n"
"v_lshrrev_b32 v187, 20, v187\n"
"v_mul_u32_u24 v188, 18, v187\n"
"v_mfma_f32_16x16x32_bf16 v[28:31], v[144:147], a[64:67], v[28:31]\n"
"buffer_load_dwordx4 v232, s[52:55], s88 offen offset:1024 lds\n"
"v_mfma_f32_16x16x32_bf16 v[28:31], v[148:151], a[80:83], v[28:31]\n"
"s_add_u32 m0, s84, 90112\n"
"ds_read_b64_tr_b16 v[136:137], v200 offset:8\n"
"ds_read_b64_tr_b16 v[138:139], v200 offset:1032\n"
"v_mfma_f32_16x16x32_bf16 v[60:63], v[144:147], a[68:71], v[60:63]\n"
"s_mov_b32 s85, 2883584\n"
"v_sub_u32 v186, v186, v188\n"
"v_mul_u32_u24 v187, s73, v187\n"
"v_mfma_f32_16x16x32_bf16 v[60:63], v[148:151], a[84:87], v[60:63]\n"
"s_nop 0\n"
"ds_read_b64_tr_b16 v[140:141], v200 offset:8200\n"
"ds_read_b64_tr_b16 v[142:143], v200 offset:9224\n"
"v_mfma_f32_16x16x32_bf16 v[92:95], v[144:147], a[72:75], v[92:95]\n"
"buffer_load_dwordx4 v231, s[52:55], s85 offen offset:0 lds\n"
"v_mfma_f32_16x16x32_bf16 v[92:95], v[148:151], a[88:91], v[92:95]\n"
"v_lshl_add_u32 v186, v186, 4, v187\n"
"v_accvgpr_write_b32 a181, v186\n"
"v_mfma_f32_16x16x32_bf16 v[124:127], v[144:147], a[76:79], v[124:127]\n"
"s_lshl_b32 s72, s68, 6\n"
"v_add_u32 v185, s72, v252\n"
"v_mfma_f32_16x16x32_bf16 v[124:127], v[148:151], a[92:95], v[124:127]\n"
"s_waitcnt vmcnt(15) lgkmcnt(4)\n"
"s_mov_b32 s73, 1152\n"
"v_add_u32 v186, 0, v185\n"
"v_mfma_f32_16x16x32_bf16 v[0:3], v[128:131], a[96:99], v[0:3]\n"
"s_nop 0\n"
"ds_read_b64_tr_b16 v[144:145], v201 offset:0\n"
"ds_read_b64_tr_b16 v[146:147], v201 offset:1024\n"
"v_mfma_f32_16x16x32_bf16 v[0:3], v[132:135], a[112:115], v[0:3]\n"
"buffer_load_dwordx4 v232, s[52:55], s85 offen offset:1024 lds\n"
"v_mfma_f32_16x16x32_bf16 v[32:35], v[128:131], a[100:103], v[32:35]\n"
"v_mul_u32_u24 v187, 58255, v186\n"
"v_lshrrev_b32 v187, 20, v187\n"
"v_mfma_f32_16x16x32_bf16 v[32:35], v[132:135], a[116:119], v[32:35]\n"
"s_nop 0\n"
"ds_read_b64_tr_b16 v[148:149], v201 offset:8192\n"
"ds_read_b64_tr_b16 v[150:151], v201 offset:9216\n"
"v_mfma_f32_16x16x32_bf16 v[64:67], v[128:131], a[104:107], v[64:67]\n"
"v_mul_u32_u24 v188, 18, v187\n"
"v_sub_u32 v186, v186, v188\n"
"v_mfma_f32_16x16x32_bf16 v[64:67], v[132:135], a[120:123], v[64:67]\n"
"v_lshrrev_b32 v188, 6, v187\n"
"v_mul_u32_u24 v188, 96, v188\n"
"v_mfma_f32_16x16x32_bf16 v[96:99], v[128:131], a[108:111], v[96:99]\n"
"buffer_load_dwordx4 v[152:155], v230, s[60:63], s89 offen offset:2048 sc1\n"
"v_mfma_f32_16x16x32_bf16 v[96:99], v[132:135], a[124:127], v[96:99]\n"
"s_waitcnt lgkmcnt(4)\n"
"v_add_u32 v187, v187, v188\n"
"v_mul_u32_u24 v187, s73, v187\n"
"v_mfma_f32_16x16x32_bf16 v[4:7], v[136:139], a[96:99], v[4:7]\n"
"s_nop 0\n"
"ds_read_b64_tr_b16 v[128:129], v201 offset:8\n"
"ds_read_b64_tr_b16 v[130:131], v201 offset:1032\n"
"v_mfma_f32_16x16x32_bf16 v[4:7], v[140:143], a[112:115], v[4:7]\n"
"v_lshl_add_u32 v186, v186, 4, v187\n"
"v_accvgpr_write_b32 a182, v186\n"
"v_mfma_f32_16x16x32_bf16 v[36:39], v[136:139], a[100:103], v[36:39]\n"
"v_add_u32 v186, 256, v185\n"
"v_mul_u32_u24 v187, 58255, v186\n"
"v_mfma_f32_16x16x32_bf16 v[36:39], v[140:143], a[116:119], v[36:39]\n"
"buffer_load_dwordx4 v[156:159], v230, s[60:63], s90 offen offset:2048 sc1\n"
"v_mfma_f32_16x16x32_bf16 v[68:71], v[136:139], a[104:107], v[68:71]\n"
"s_nop 0\n"
"ds_read_b64_tr_b16 v[132:133], v201 offset:8200\n"
"ds_read_b64_tr_b16 v[134:135], v201 offset:9224\n"
"v_mfma_f32_16x16x32_bf16 v[68:71], v[140:143], a[120:123], v[68:71]\n"
"v_lshrrev_b32 v187, 20, v187\n"
"v_mul_u32_u24 v188, 18, v187\n"
"v_mfma_f32_16x16x32_bf16 v[100:103], v[136:139], a[108:111], v[100:103]\n"
"v_sub_u32 v186, v186, v188\n"
"v_lshrrev_b32 v188, 6, v187\n"
"v_mfma_f32_16x16x32_bf16 v[100:103], v[140:143], a[124:127], v[100:103]\n"
"s_waitcnt lgkmcnt(4)\n"
"v_mul_u32_u24 v188, 96, v188\n"
"v_add_u32 v187, v187, v188\n"
"v_mfma_f32_16x16x32_bf16 v[8:11], v[144:147], a[96:99], v[8:11]\n"
"buffer_load_dwordx4 v[160:163], v230, s[60:63], s92 offen offset:2048 sc1\n"
"v_mfma_f32_16x16x32_bf16 v[8:11], v[148:151], a[112:115], v[8:11]\n"
"s_nop 0\n"
"ds_read_b64_tr_b16 v[136:137], v202 offset:0\n"
"ds_read_b64_tr_b16 v[138:139], v202 offset:1024\n"
"v_mfma_f32_16x16x32_bf16 v[40:43], v[144:147], a[100:103], v[40:43]\n"
"v_mul_u32_u24 v187, s73, v187\n"
"v_lshl_add_u32 v186, v186, 4, v187\n"
"v_mfma_f32_16x16x32_bf16 v[40:43], v[148:151], a[116:119], v[40:43]\n"
"s_nop 0\n"
"ds_read_b64_tr_b16 v[140:141], v202 offset:8192\n"
"ds_read_b64_tr_b16 v[142:143], v202 offset:9216\n"
"v_mfma_f32_16x16x32_bf16 v[72:75], v[144:147], a[104:107], v[72:75]\n"
"v_accvgpr_write_b32 a183, v186\n"
"v_add_u32 v186, 512, v185\n"
"v_mfma_f32_16x16x32_bf16 v[72:75], v[148:151], a[120:123], v[72:75]\n"
"buffer_load_dwordx4 v[164:167], v230, s[60:63], s96 offen offset:2048 sc1\n"
"v_mfma_f32_16x16x32_bf16 v[104:107], v[144:147], a[108:111], v[104:107]\n"
"v_mul_u32_u24 v187, 58255, v186\n"
"v_lshrrev_b32 v187, 20, v187\n"
"v_mfma_f32_16x16x32_bf16 v[104:107], v[148:151], a[124:127], v[104:107]\n"
"s_waitcnt lgkmcnt(4)\n"
"v_mul_u32_u24 v188, 18, v187\n"
"v_sub_u32 v186, v186, v188\n"
"v_mfma_f32_16x16x32_bf16 v[12:15], v[128:131], a[96:99], v[12:15]\n"
"s_nop 0\n"
"ds_read_b64_tr_b16 v[144:145], v202 offset:8\n"
"ds_read_b64_tr_b16 v[146:147], v202 offset:1032\n"
"v_mfma_f32_16x16x32_bf16 v[12:15], v[132:135], a[112:115], v[12:15]\n"
"v_lshrrev_b32 v188, 6, v187\n"
"v_mul_u32_u24 v188, 96, v188\n"
"v_mfma_f32_16x16x32_bf16 v[44:47], v[128:131], a[100:103], v[44:47]\n"
"buffer_load_dwordx4 v[168:171], v230, s[60:63], s89 offen offset:3072 sc1\n"
"v_mfma_f32_16x16x32_bf16 v[44:47], v[132:135], a[116:119], v[44:47]\n"
"s_nop 0\n"
"ds_read_b64_tr_b16 v[148:149], v202 offset:8200\n"
"ds_read_b64_tr_b16 v[150:151], v202 offset:9224\n"
"v_mfma_f32_16x16x32_bf16 v[76:79], v[128:131], a[104:107], v[76:79]\n"
"v_add_u32 v187, v187, v188\n"
"v_mul_u32_u24 v187, s73, v187\n"
"v_mfma_f32_16x16x32_bf16 v[76:79], v[132:135], a[120:123], v[76:79]\n"
"v_lshl_add_u32 v186, v186, 4, v187\n"
"v_accvgpr_write_b32 a184, v186\n"
"v_mfma_f32_16x16x32_bf16 v[108:111], v[128:131], a[108:111], v[108:111]\n"
"v_add_u32 v186, 768, v185\n"
"v_mul_u32_u24 v187, 58255, v186\n"
"v_mfma_f32_16x16x32_bf16 v[108:111], v[132:135], a[124:127], v[108:111]\n"
"s_waitcnt lgkmcnt(4)\n"
"v_lshrrev_b32 v187, 20, v187\n"
"v_mul_u32_u24 v188, 18, v187\n"
"v_mfma_f32_16x16x32_bf16 v[16:19], v[136:139], a[96:99], v[16:19]\n"
"buffer_load_dwordx4 v[172:175], v230, s[60:63], s90 offen offset:3072 sc1\n"
"v_mfma_f32_16x16x32_bf16 v[16:19], v[140:143], a[112:115], v[16:19]\n"
"s_nop 0\n"
"ds_read_b64_tr_b16 v[128:129], v203 offset:0\n"
"ds_read_b64_tr_b16 v[130:131], v203 offset:1024\n"
"v_mfma_f32_16x16x32_bf16 v[48:51], v[136:139], a[100:103], v[48:51]\n"
"v_sub_u32 v186, v186, v188\n"
"v_lshrrev_b32 v188, 6, v187\n"
"v_mfma_f32_16x16x32_bf16 v[48:51], v[140:143], a[116:119], v[48:51]\n"
"s_nop 0\n"
"ds_read_b64_tr_b16 v[132:133], v203 offset:8192\n"
"ds_read_b64_tr_b16 v[134:135], v203 offset:9216\n"
"v_mfma_f32_16x16x32_bf16 v[80:83], v[136:139], a[104:107], v[80:83]\n"
"buffer_load_dwordx4 v[176:179], v230, s[60:63], s92 offen offset:3072 sc1\n"
"v_mfma_f32_16x16x32_bf16 v[80:83], v[140:143], a[120:123], v[80:83]\n"
"v_mul_u32_u24 v188, 96, v188\n"
"v_add_u32 v187, v187, v188\n"
"v_mfma_f32_16x16x32_bf16 v[112:115], v[136:139], a[108:111], v[112:115]\n"
"v_mul_u32_u24 v187, s73, v187\n"
"v_lshl_add_u32 v186, v186, 4, v187\n"
"v_mfma_f32_16x16x32_bf16 v[112:115], v[140:143], a[124:127], v[112:115]\n"
"s_waitcnt lgkmcnt(4)\n"
"v_accvgpr_write_b32 a185, v186\n"
"v_add_u32 v186, 1024, v185\n"
"v_mfma_f32_16x16x32_bf16 v[20:23], v[144:147], a[96:99], v[20:23]\n"
"s_nop 0\n"
"ds_read_b64_tr_b16 v[136:137], v203 offset:8\n"
"ds_read_b64_tr_b16 v[138:139], v203 offset:1032\n"
"v_mfma_f32_16x16x32_bf16 v[20:23], v[148:151], a[112:115], v[20:23]\n"
"buffer_load_dwordx4 v[180:183], v230, s[60:63], s96 offen offset:3072 sc1\n"
"v_mfma_f32_16x16x32_bf16 v[52:55], v[144:147], a[100:103], v[52:55]\n"
"s_add_u32 m0, s84, 98304\n"
"v_mul_u32_u24 v187, 58255, v186\n"
"v_lshrrev_b32 v187, 20, v187\n"
"v_mfma_f32_16x16x32_bf16 v[52:55], v[148:151], a[116:119], v[52:55]\n"
"s_mov_b32 s88, 3145728\n"
"ds_read_b64_tr_b16 v[140:141], v203 offset:8200\n"
"ds_read_b64_tr_b16 v[142:143], v203 offset:9224\n"
"v_mfma_f32_16x16x32_bf16 v[84:87], v[144:147], a[104:107], v[84:87]\n"
"v_mul_u32_u24 v188, 18, v187\n"
"v_sub_u32 v186, v186, v188\n"
"v_mfma_f32_16x16x32_bf16 v[84:87], v[148:151], a[120:123], v[84:87]\n"
"v_lshrrev_b32 v188, 6, v187\n"
"v_mul_u32_u24 v188, 96, v188\n"
"v_mfma_f32_16x16x32_bf16 v[116:119], v[144:147], a[108:111], v[116:119]\n"
"buffer_load_dwordx4 v231, s[52:55], s88 offen offset:0 lds\n"
"v_mfma_f32_16x16x32_bf16 v[116:119], v[148:151], a[124:127], v[116:119]\n"
"s_waitcnt vmcnt(21) lgkmcnt(4)\n"
"s_barrier\n"
"v_mfma_f32_16x16x32_bf16 v[24:27], v[128:131], a[96:99], v[24:27]\n"
"s_nop 0\n"
"ds_read_b64_tr_b16 v[144:145], v200 offset:16384\n"
"ds_read_b64_tr_b16 v[146:147], v200 offset:17408\n"
"v_mfma_f32_16x16x32_bf16 v[24:27], v[132:135], a[112:115], v[24:27]\n"
"v_add_u32 v187, v187, v188\n"
"v_mul_u32_u24 v187, s73, v187\n"
"v_mfma_f32_16x16x32_bf16 v[56:59], v[128:131], a[100:103], v[56:59]\n"
"v_lshl_add_u32 v186, v186, 4, v187\n"
"v_accvgpr_write_b32 a186, v186\n"
"v_mfma_f32_16x16x32_bf16 v[56:59], v[132:135], a[116:119], v[56:59]\n"
"buffer_load_dwordx4 v232, s[52:55], s88 offen offset:1024 lds\n"
"v_mfma_f32_16x16x32_bf16 v[88:91], v[128:131], a[104:107], v[88:91]\n"
"s_add_u32 m0, s84, 106496\n"
"ds_read_b64_tr_b16 v[148:149], v200 offset:24576\n"
"ds_read_b64_tr_b16 v[150:151], v200 offset:25600\n"
"v_mfma_f32_16x16x32_bf16 v[88:91], v[132:135], a[120:123], v[88:91]\n"
"s_mov_b32 s85, 3407872\n"
"v_add_u32 v186, 1280, v185\n"
"v_mul_u32_u24 v187, 58255, v186\n"
"v_mfma_f32_16x16x32_bf16 v[120:123], v[128:131], a[108:111], v[120:123]\n"
"v_lshrrev_b32 v187, 20, v187\n"
"v_mul_u32_u24 v188, 18, v187\n"
"v_mfma_f32_16x16x32_bf16 v[120:123], v[132:135], a[124:127], v[120:123]\n"
"s_waitcnt lgkmcnt(4)\n"
"v_sub_u32 v186, v186, v188\n"
"v_lshrrev_b32 v188, 6, v187\n"
"v_mfma_f32_16x16x32_bf16 v[28:31], v[136:139], a[96:99], v[28:31]\n"
"buffer_load_dwordx4 v231, s[52:55], s85 offen offset:0 lds\n"
"v_mfma_f32_16x16x32_bf16 v[28:31], v[140:143], a[112:115], v[28:31]\n"
"s_nop 0\n"
"ds_read_b64_tr_b16 v[128:129], v200 offset:16392\n"
"ds_read_b64_tr_b16 v[130:131], v200 offset:17416\n"
"v_mfma_f32_16x16x32_bf16 v[60:63], v[136:139], a[100:103], v[60:63]\n"
"v_mul_u32_u24 v188, 96, v188\n"
"v_add_u32 v187, v187, v188\n"
"v_mfma_f32_16x16x32_bf16 v[60:63], v[140:143], a[116:119], v[60:63]\n"
"s_nop 0\n"
"ds_read_b64_tr_b16 v[132:133], v200 offset:24584\n"
"ds_read_b64_tr_b16 v[134:135], v200 offset:25608\n"
"v_mfma_f32_16x16x32_bf16 v[92:95], v[136:139], a[104:107], v[92:95]\n"
"v_mul_u32_u24 v187, s73, v187\n"
"v_lshl_add_u32 v186, v186, 4, v187\n"
"v_mfma_f32_16x16x32_bf16 v[92:95], v[140:143], a[120:123], v[92:95]\n"
"buffer_load_dwordx4 v232, s[52:55], s85 offen offset:1024 lds\n"
"v_mfma_f32_16x16x32_bf16 v[124:127], v[136:139], a[108:111], v[124:127]\n"
"s_add_u32 s89, s87, 12288\n"
"v_accvgpr_write_b32 a187, v186\n"
"v_add_u32 v186, 1536, v185\n"
"v_mfma_f32_16x16x32_bf16 v[124:127], v[140:143], a[124:127], v[124:127]\n"
"s_waitcnt vmcnt(16) lgkmcnt(4)\n"
"v_mul_u32_u24 v187, 58255, v186\n"
"v_lshrrev_b32 v187, 20, v187\n"
"v_mfma_f32_16x16x32_bf16 v[0:3], v[144:147], a[128:131], v[0:3]\n"
"s_nop 0\n"
"ds_read_b64_tr_b16 v[136:137], v201 offset:16384\n"
"ds_read_b64_tr_b16 v[138:139], v201 offset:17408\n"
"v_mfma_f32_16x16x32_bf16 v[0:3], v[148:151], a[144:147], v[0:3]\n"
"v_mul_u32_u24 v188, 18, v187\n"
"v_sub_u32 v186, v186, v188\n"
"v_mfma_f32_16x16x32_bf16 v[32:35], v[144:147], a[132:135], v[32:35]\n"
"buffer_load_dwordx4 a[0:3], v230, s[60:63], s89 offen offset:0 sc1\n"
"v_mfma_f32_16x16x32_bf16 v[32:35], v[148:151], a[148:151], v[32:35]\n"
"s_add_u32 s90, s87, 30720\n"
"ds_read_b64_tr_b16 v[140:141], v201 offset:24576\n"
"ds_read_b64_tr_b16 v[142:143], v201 offset:25600\n"
"v_mfma_f32_16x16x32_bf16 v[64:67], v[144:147], a[136:139], v[64:67]\n"
"v_lshrrev_b32 v188, 6, v187\n"
"v_mul_u32_u24 v188, 96, v188\n"
"v_mfma_f32_16x16x32_bf16 v[64:67], v[148:151], a[152:155], v[64:67]\n"
"v_add_u32 v187, v187, v188\n"
"v_mul_u32_u24 v187, s73, v187\n"
"v_mfma_f32_16x16x32_bf16 v[96:99], v[144:147], a[140:143], v[96:99]\n"
"v_lshl_add_u32 v186, v186, 4, v187\n"
"v_accvgpr_write_b32 a188, v186\n"
"v_mfma_f32_16x16x32_bf16 v[96:99], v[148:151], a[156:159], v[96:99]\n"
"s_waitcnt lgkmcnt(4)\n"
"v_add_u32 v186, 1792, v185\n"
"v_mul_u32_u24 v187, 58255, v186\n"
"v_mfma_f32_16x16x32_bf16 v[4:7], v[128:131], a[128:131], v[4:7]\n"
"buffer_load_dwordx4 a[4:7], v230, s[60:63], s90 offen offset:0 sc1\n"
"v_mfma_f32_16x16x32_bf16 v[4:7], v[132:135], a[144:147], v[4:7]\n"
"s_add_u32 s92, s87, 49152\n"
"ds_read_b64_tr_b16 v[144:145], v201 offset:16392\n"
"ds_read_b64_tr_b16 v[146:147], v201 offset:17416\n"
"v_mfma_f32_16x16x32_bf16 v[36:39], v[128:131], a[132:135], v[36:39]\n"
"v_lshrrev_b32 v187, 20, v187\n"
"v_mul_u32_u24 v188, 18, v187\n"
"v_mfma_f32_16x16x32_bf16 v[36:39], v[132:135], a[148:151], v[36:39]\n"
"s_nop 0\n"
"ds_read_b64_tr_b16 v[148:149], v201 offset:24584\n"
"ds_read_b64_tr_b16 v[150:151], v201 offset:25608\n"
"v_mfma_f32_16x16x32_bf16 v[68:71], v[128:131], a[136:139], v[68:71]\n"
"buffer_load_dwordx4 a[8:11], v230, s[60:63], s92 offen offset:0 sc1\n"
"v_mfma_f32_16x16x32_bf16 v[68:71], v[132:135], a[152:155], v[68:71]\n"
"s_add_u32 s96, s87, 67584\n"
"v_sub_u32 v186, v186, v188\n"
"v_lshrrev_b32 v188, 6, v187\n"
"v_mfma_f32_16x16x32_bf16 v[100:103], v[128:131], a[140:143], v[100:103]\n"
"v_mul_u32_u24 v188, 96, v188\n"
"v_add_u32 v187, v187, v188\n"
"v_mfma_f32_16x16x32_bf16 v[100:103], v[132:135], a[156:159], v[100:103]\n"
"s_waitcnt lgkmcnt(4)\n"
"v_mul_u32_u24 v187, s73, v187\n"
"v_lshl_add_u32 v186, v186, 4, v187\n"
"v_mfma_f32_16x16x32_bf16 v[8:11], v[136:139], a[128:131], v[8:11]\n"
"s_nop 0\n"
"ds_read_b64_tr_b16 v[128:129], v202 offset:16384\n"
"ds_read_b64_tr_b16 v[130:131], v202 offset:17408\n"
"v_mfma_f32_16x16x32_bf16 v[8:11], v[140:143], a[144:147], v[8:11]\n"
"buffer_load_dwordx4 a[12:15], v230, s[60:63], s96 offen offset:0 sc1\n"
"v_mfma_f32_16x16x32_bf16 v[40:43], v[136:139], a[132:135], v[40:43]\n"
"v_accvgpr_write_b32 a189, v186\n"
"v_add_u32 v186, 2048, v185\n"
"v_mfma_f32_16x16x32_bf16 v[40:43], v[140:143], a[148:151], v[40:43]\n"
"s_nop 0\n"
"ds_read_b64_tr_b16 v[132:133], v202 offset:24576\n"
"ds_read_b64_tr_b16 v[134:135], v202 offset:25600\n"
"v_mfma_f32_16x16x32_bf16 v[72:75], v[136:139], a[136:139], v[72:75]\n"
"v_mul_u32_u24 v187, 58255, v186\n"
"v_lshrrev_b32 v187, 20, v187\n"
"v_mfma_f32_16x16x32_bf16 v[72:75], v[140:143], a[152:155], v[72:75]\n"
"v_mul_u32_u24 v188, 18, v187\n"
"v_sub_u32 v186, v186, v188\n"
"v_mfma_f32_16x16x32_bf16 v[104:107], v[136:139], a[140:143], v[104:107]\n"
"buffer_load_dwordx4 a[16:19], v230, s[60:63], s89 offen offset:1024 sc1\n"
"v_mfma_f32_16x16x32_bf16 v[104:107], v[140:143], a[156:159], v[104:107]\n"
"s_waitcnt lgkmcnt(4)\n"
"v_lshrrev_b32 v188, 6, v187\n"
"v_mul_u32_u24 v188, 96, v188\n"
"v_mfma_f32_16x16x32_bf16 v[12:15], v[144:147], a[128:131], v[12:15]\n"
"s_nop 0\n"
"ds_read_b64_tr_b16 v[136:137], v202 offset:16392\n"
"ds_read_b64_tr_b16 v[138:139], v202 offset:17416\n"
"v_mfma_f32_16x16x32_bf16 v[12:15], v[148:151], a[144:147], v[12:15]\n"
"v_add_u32 v187, v187, v188\n"
"v_mul_u32_u24 v187, s73, v187\n"
"v_mfma_f32_16x16x32_bf16 v[44:47], v[144:147], a[132:135], v[44:47]\n"
"v_lshl_add_u32 v186, v186, 4, v187\n"
"v_accvgpr_write_b32 a190, v186\n"
"v_mfma_f32_16x16x32_bf16 v[44:47], v[148:151], a[148:151], v[44:47]\n"
"buffer_load_dwordx4 a[20:23], v230, s[60:63], s90 offen offset:1024 sc1\n"
"v_mfma_f32_16x16x32_bf16 v[76:79], v[144:147], a[136:139], v[76:79]\n"
"s_nop 0\n"
"ds_read_b64_tr_b16 v[140:141], v202 offset:24584\n"
"ds_read_b64_tr_b16 v[142:143], v202 offset:25608\n"
"v_mfma_f32_16x16x32_bf16 v[76:79], v[148:151], a[152:155], v[76:79]\n"
"v_mfma_f32_16x16x32_bf16 v[108:111], v[144:147], a[140:143], v[108:111]\n"
"v_mfma_f32_16x16x32_bf16 v[108:111], v[148:151], a[156:159], v[108:111]\n"
"s_waitcnt lgkmcnt(4)\n"
"v_mfma_f32_16x16x32_bf16 v[16:19], v[128:131], a[128:131], v[16:19]\n"
"buffer_load_dwordx4 a[24:27], v230, s[60:63], s92 offen offset:1024 sc1\n"
"v_mfma_f32_16x16x32_bf16 v[16:19], v[132:135], a[144:147], v[16:19]\n"
"s_nop 0\n"
"ds_read_b64_tr_b16 v[144:145], v203 offset:16384\n"
"ds_read_b64_tr_b16 v[146:147], v203 offset:17408\n"
"v_mfma_f32_16x16x32_bf16 v[48:51], v[128:131], a[132:135], v[48:51]\n"
"v_mfma_f32_16x16x32_bf16 v[48:51], v[132:135], a[148:151], v[48:51]\n"
"s_nop 0\n"
"ds_read_b64_tr_b16 v[148:149], v203 offset:24576\n"
"ds_read_b64_tr_b16 v[150:151], v203 offset:25600\n"
"v_mfma_f32_16x16x32_bf16 v[80:83], v[128:131], a[136:139], v[80:83]\n"
"v_mfma_f32_16x16x32_bf16 v[80:83], v[132:135], a[152:155], v[80:83]\n"
"buffer_load_dwordx4 a[28:31], v230, s[60:63], s96 offen offset:1024 sc1\n"
"v_mfma_f32_16x16x32_bf16 v[112:115], v[128:131], a[140:143], v[112:115]\n"
"s_add_u32 m0, s84, 114688\n"
"v_mfma_f32_16x16x32_bf16 v[112:115], v[132:135], a[156:159], v[112:115]\n"
"s_waitcnt lgkmcnt(4)\n"
"v_mfma_f32_16x16x32_bf16 v[20:23], v[136:139], a[128:131], v[20:23]\n"
"s_mov_b32 s88, 3670016\n"
"ds_read_b64_tr_b16 v[128:129], v203 offset:16392\n"
"ds_read_b64_tr_b16 v[130:131], v203 offset:17416\n"
"v_mfma_f32_16x16x32_bf16 v[20:23], v[140:143], a[144:147], v[20:23]\n"
"v_mfma_f32_16x16x32_bf16 v[52:55], v[136:139], a[132:135], v[52:55]\n"
"buffer_load_dwordx4 v231, s[52:55], s88 offen offset:0 lds\n"
"v_mfma_f32_16x16x32_bf16 v[52:55], v[140:143], a[148:151], v[52:55]\n"
"s_nop 0\n"
"ds_read_b64_tr_b16 v[132:133], v203 offset:24584\n"
"ds_read_b64_tr_b16 v[134:135], v203 offset:25608\n"
"v_mfma_f32_16x16x32_bf16 v[84:87], v[136:139], a[136:139], v[84:87]\n"
"v_mfma_f32_16x16x32_bf16 v[84:87], v[140:143], a[152:155], v[84:87]\n"
"v_mfma_f32_16x16x32_bf16 v[116:119], v[136:139], a[140:143], v[116:119]\n"
"v_mfma_f32_16x16x32_bf16 v[116:119], v[140:143], a[156:159], v[116:119]\n"
"s_waitcnt vmcnt(21) lgkmcnt(4)\n"
"s_barrier\n"
"v_mfma_f32_16x16x32_bf16 v[24:27], v[144:147], a[128:131], v[24:27]\n"
"buffer_load_dwordx4 v232, s[52:55], s88 offen offset:1024 lds\n"
"v_mfma_f32_16x16x32_bf16 v[24:27], v[148:151], a[144:147], v[24:27]\n"
"s_add_u32 m0, s84, 122880\n"
"ds_read_b64_tr_b16 v[136:137], v200 offset:32768\n"
"ds_read_b64_tr_b16 v[138:139], v200 offset:33792\n"
"v_mfma_f32_16x16x32_bf16 v[56:59], v[144:147], a[132:135], v[56:59]\n"
"s_mov_b32 s85, 3932160\n"
"v_mfma_f32_16x16x32_bf16 v[56:59], v[148:151], a[148:151], v[56:59]\n"
"s_nop 0\n"
"ds_read_b64_tr_b16 v[140:141], v200 offset:40960\n"
"ds_read_b64_tr_b16 v[142:143], v200 offset:41984\n"
"v_mfma_f32_16x16x32_bf16 v[88:91], v[144:147], a[136:139], v[88:91]\n"
"buffer_load_dwordx4 v231, s[52:55], s85 offen offset:0 lds\n"
"v_mfma_f32_16x16x32_bf16 v[88:91], v[148:151], a[152:155], v[88:91]\n"
"v_mfma_f32_16x16x32_bf16 v[120:123], v[144:147], a[140:143], v[120:123]\n"
"v_mfma_f32_16x16x32_bf16 v[120:123], v[148:151], a[156:159], v[120:123]\n"
"s_waitcnt lgkmcnt(4)\n"
"v_mfma_f32_16x16x32_bf16 v[28:31], v[128:131], a[128:131], v[28:31]\n"
"s_nop 0\n"
"ds_read_b64_tr_b16 v[144:145], v200 offset:32776\n"
"ds_read_b64_tr_b16 v[146:147], v200 offset:33800\n"
"v_mfma_f32_16x16x32_bf16 v[28:31], v[132:135], a[144:147], v[28:31]\n"
"buffer_load_dwordx4 v232, s[52:55], s85 offen offset:1024 lds\n"
"v_mfma_f32_16x16x32_bf16 v[60:63], v[128:131], a[132:135], v[60:63]\n"
"s_add_u32 m0, s84, 131072\n"
"v_mfma_f32_16x16x32_bf16 v[60:63], v[132:135], a[148:151], v[60:63]\n"
"s_mov_b32 s88, 4194304\n"
"ds_read_b64_tr_b16 v[148:149], v200 offset:40968\n"
"ds_read_b64_tr_b16 v[150:151], v200 offset:41992\n"
"v_mfma_f32_16x16x32_bf16 v[92:95], v[128:131], a[136:139], v[92:95]\n"
"v_mfma_f32_16x16x32_bf16 v[92:95], v[132:135], a[152:155], v[92:95]\n"
"v_mfma_f32_16x16x32_bf16 v[124:127], v[128:131], a[140:143], v[124:127]\n"
"buffer_load_dwordx4 v231, s[52:55], s88 offen offset:0 lds\n"
"v_mfma_f32_16x16x32_bf16 v[124:127], v[132:135], a[156:159], v[124:127]\n"
"s_waitcnt vmcnt(17) lgkmcnt(4)\n"
"v_mfma_f32_16x16x32_bf16 v[0:3], v[136:139], v[152:155], v[0:3]\n"
"s_nop 0\n"
"ds_read_b64_tr_b16 v[128:129], v201 offset:32768\n"
"ds_read_b64_tr_b16 v[130:131], v201 offset:33792\n"
"v_mfma_f32_16x16x32_bf16 v[0:3], v[140:143], v[168:171], v[0:3]\n"
"v_mfma_f32_16x16x32_bf16 v[32:35], v[136:139], v[156:159], v[32:35]\n"
"v_mfma_f32_16x16x32_bf16 v[32:35], v[140:143], v[172:175], v[32:35]\n"
"buffer_load_dwordx4 v232, s[52:55], s88 offen offset:1024 lds\n"
"v_mfma_f32_16x16x32_bf16 v[64:67], v[136:139], v[160:163], v[64:67]\n"
"s_add_u32 m0, s84, 139264\n"
"ds_read_b64_tr_b16 v[132:133], v201 offset:40960\n"
"ds_read_b64_tr_b16 v[134:135], v201 offset:41984\n"
"v_mfma_f32_16x16x32_bf16 v[64:67], v[140:143], v[176:179], v[64:67]\n"
"s_mov_b32 s85, 4456448\n"
"v_mfma_f32_16x16x32_bf16 v[96:99], v[136:139], v[164:167], v[96:99]\n"
"v_mfma_f32_16x16x32_bf16 v[96:99], v[140:143], v[180:183], v[96:99]\n"
"s_waitcnt lgkmcnt(4)\n"
"v_mfma_f32_16x16x32_bf16 v[4:7], v[144:147], v[152:155], v[4:7]\n"
"buffer_load_dwordx4 v231, s[52:55], s85 offen offset:0 lds\n"
"v_mfma_f32_16x16x32_bf16 v[4:7], v[148:151], v[168:171], v[4:7]\n"
"s_nop 0\n"
"ds_read_b64_tr_b16 v[136:137], v201 offset:32776\n"
"ds_read_b64_tr_b16 v[138:139], v201 offset:33800\n"
"v_mfma_f32_16x16x32_bf16 v[36:39], v[144:147], v[156:159], v[36:39]\n"
"v_mfma_f32_16x16x32_bf16 v[36:39], v[148:151], v[172:175], v[36:39]\n"
"L_tc0_1:\n"
"s_cbranch_execz L_tp0_1_1\n"
"ds_read_b64_tr_b16 v[140:141], v201 offset:40968\n"
"ds_read_b64_tr_b16 v[142:143], v201 offset:41992\n"
"v_mfma_f32_16x16x32_bf16 v[68:71], v[144:147], v[160:163], v[68:71]\n"
"v_mfma_f32_16x16x32_bf16 v[68:71], v[148:151], v[176:179], v[68:71]\n"
"buffer_load_dwordx4 v232, s[52:55], s85 offen offset:1024 lds\n"
"v_mfma_f32_16x16x32_bf16 v[100:103], v[144:147], v[164:167], v[100:103]\n"
"v_mfma_f32_16x16x32_bf16 v[100:103], v[148:151], v[180:183], v[100:103]\n"
"s_waitcnt lgkmcnt(4)\n"
"v_mfma_f32_16x16x32_bf16 v[8:11], v[128:131], v[152:155], v[8:11]\n"
"s_nop 0\n"
"ds_read_b64_tr_b16 v[144:145], v202 offset:32768\n"
"ds_read_b64_tr_b16 v[146:147], v202 offset:33792\n"
"v_mfma_f32_16x16x32_bf16 v[8:11], v[132:135], v[168:171], v[8:11]\n"
"v_mfma_f32_16x16x32_bf16 v[40:43], v[128:131], v[156:159], v[40:43]\n"
"buffer_load_dwordx4 a[32:35], v230, s[60:63], s89 offen offset:2048 sc1\n"
"v_mfma_f32_16x16x32_bf16 v[40:43], v[132:135], v[172:175], v[40:43]\n"
"s_nop 0\n"
"ds_read_b64_tr_b16 v[148:149], v202 offset:40960\n"
"ds_read_b64_tr_b16 v[150:151], v202 offset:41984\n"
"v_mfma_f32_16x16x32_bf16 v[72:75], v[128:131], v[160:163], v[72:75]\n"
"v_mfma_f32_16x16x32_bf16 v[72:75], v[132:135], v[176:179], v[72:75]\n"
"v_mfma_f32_16x16x32_bf16 v[104:107], v[128:131], v[164:167], v[104:107]\n"
"v_mfma_f32_16x16x32_bf16 v[104:107], v[132:135], v[180:183], v[104:107]\n"
"s_waitcnt lgkmcnt(4)\n"
"v_mfma_f32_16x16x32_bf16 v[12:15], v[136:139], v[152:155], v[12:15]\n"
"buffer_load_dwordx4 a[36:39], v230, s[60:63], s90 offen offset:2048 sc1\n"
"v_mfma_f32_16x16x32_bf16 v[12:15], v[140:143], v[168:171], v[12:15]\n"
"s_nop 0\n"
"ds_read_b64_tr_b16 v[128:129], v202 offset:32776\n"
"ds_read_b64_tr_b16 v[130:131], v202 offset:33800\n"
"v_mfma_f32_16x16x32_bf16 v[44:47], v[136:139], v[156:159], v[44:47]\n"
"v_mfma_f32_16x16x32_bf16 v[44:47], v[140:143], v[172:175], v[44:47]\n"
"s_nop 0\n"
"ds_read_b64_tr_b16 v[132:133], v202 offset:40968\n"
"ds_read_b64_tr_b16 v[134:135], v202 offset:41992\n"
"v_mfma_f32_16x16x32_bf16 v[76:79], v[136:139], v[160:163], v[76:79]\n"
"buffer_load_dwordx4 a[40:43], v230, s[60:63], s92 offen offset:2048 sc1\n"
"v_mfma_f32_16x16x32_bf16 v[76:79], v[140:143], v[176:179], v[76:79]\n"
"v_mfma_f32_16x16x32_bf16 v[108:111], v[136:139], v[164:167], v[108:111]\n"
"v_mfma_f32_16x16x32_bf16 v[108:111], v[140:143], v[180:183], v[108:111]\n"
"s_waitcnt lgkmcnt(4)\n"
"v_mfma_f32_16x16x32_bf16 v[16:19], v[144:147], v[152:155], v[16:19]\n"
"s_nop 0\n"
"ds_read_b64_tr_b16 v[136:137], v203 offset:32768\n"
"ds_read_b64_tr_b16 v[138:139], v203 offset:33792\n"
"v_mfma_f32_16x16x32_bf16 v[16:19], v[148:151], v[168:171], v[16:19]\n"
"buffer_load_dwordx4 a[44:47], v230, s[60:63], s96 offen offset:2048 sc1\n"
"v_mfma_f32_16x16x32_bf16 v[48:51], v[144:147], v[156:159], v[48:51]\n"
"v_mfma_f32_16x16x32_bf16 v[48:51], v[148:151], v[172:175], v[48:51]\n"
"s_nop 0\n"
"ds_read_b64_tr_b16 v[140:141], v203 offset:40960\n"
"ds_read_b64_tr_b16 v[142:143], v203 offset:41984\n"
"v_mfma_f32_16x16x32_bf16 v[80:83], v[144:147], v[160:163], v[80:83]\n"
"v_mfma_f32_16x16x32_bf16 v[80:83], v[148:151], v[176:179], v[80:83]\n"
"v_mfma_f32_16x16x32_bf16 v[112:115], v[144:147], v[164:167], v[112:115]\n"
"buffer_load_dwordx4 a[48:51], v230, s[60:63], s89 offen offset:3072 sc1\n"
"v_mfma_f32_16x16x32_bf16 v[112:115], v[148:151], v[180:183], v[112:115]\n"
"s_waitcnt lgkmcnt(4)\n"
"v_mfma_f32_16x16x32_bf16 v[20:23], v[128:131], v[152:155], v[20:23]\n"
"s_nop 0\n"
"ds_read_b64_tr_b16 v[144:145], v203 offset:32776\n"
"ds_read_b64_tr_b16 v[146:147], v203 offset:33800\n"
"v_mfma_f32_16x16x32_bf16 v[20:23], v[132:135], v[168:171], v[20:23]\n"
"v_mfma_f32_16x16x32_bf16 v[52:55], v[128:131], v[156:159], v[52:55]\n"
"v_mfma_f32_16x16x32_bf16 v[52:55], v[132:135], v[172:175], v[52:55]\n"
"buffer_load_dwordx4 a[52:55], v230, s[60:63], s90 offen offset:3072 sc1\n"
"v_mfma_f32_16x16x32_bf16 v[84:87], v[128:131], v[160:163], v[84:87]\n"
"s_nop 0\n"
"ds_read_b64_tr_b16 v[148:149], v203 offset:40968\n"
"ds_read_b64_tr_b16 v[150:151], v203 offset:41992\n"
"v_mfma_f32_16x16x32_bf16 v[84:87], v[132:135], v[176:179], v[84:87]\n"
"v_mfma_f32_16x16x32_bf16 v[116:119], v[128:131], v[164:167], v[116:119]\n"
"v_mfma_f32_16x16x32_bf16 v[116:119], v[132:135], v[180:183], v[116:119]\n"
"s_waitcnt vmcnt(22) lgkmcnt(4)\n"
"s_barrier\n"
"v_mfma_f32_16x16x32_bf16 v[24:27], v[136:139], v[152:155], v[24:27]\n"
"buffer_load_dwordx4 a[56:59], v230, s[60:63], s92 offen offset:3072 sc1\n"
"v_mfma_f32_16x16x32_bf16 v[24:27], v[140:143], v[168:171], v[24:27]\n"
"s_nop 0\n"
"ds_read_b64_tr_b16 v[128:129], v204 offset:0\n"
"ds_read_b64_tr_b16 v[130:131], v204 offset:1024\n"
"v_mfma_f32_16x16x32_bf16 v[56:59], v[136:139], v[156:159], v[56:59]\n"
"v_mfma_f32_16x16x32_bf16 v[56:59], v[140:143], v[172:175], v[56:59]\n"
"s_nop 0\n"
"ds_read_b64_tr_b16 v[132:133], v204 offset:8192\n"
"ds_read_b64_tr_b16 v[134:135], v204 offset:9216\n"
"v_mfma_f32_16x16x32_bf16 v[88:91], v[136:139], v[160:163], v[88:91]\n"
"v_mfma_f32_16x16x32_bf16 v[88:91], v[140:143], v[176:179], v[88:91]\n"
"buffer_load_dwordx4 a[60:63], v230, s[60:63], s96 offen offset:3072 sc1\n"
"v_mfma_f32_16x16x32_bf16 v[120:123], v[136:139], v[164:167], v[120:123]\n"
"s_add_u32 s89, s87, 16384\n"
"v_mfma_f32_16x16x32_bf16 v[120:123], v[140:143], v[180:183], v[120:123]\n"
"s_waitcnt lgkmcnt(4)\n"
"v_mfma_f32_16x16x32_bf16 v[28:31], v[144:147], v[152:155], v[28:31]\n"
"s_nop 0\n"
"ds_read_b64_tr_b16 v[136:137], v204 offset:8\n"
"ds_read_b64_tr_b16 v[138:139], v204 offset:1032\n"
"v_mfma_f32_16x16x32_bf16 v[28:31], v[148:151], v[168:171], v[28:31]\n"
"v_mfma_f32_16x16x32_bf16 v[60:63], v[144:147], v[156:159], v[60:63]\n"
"buffer_load_dwordx4 a[64:67], v230, s[60:63], s89 offen offset:0 sc1\n"
"v_mfma_f32_16x16x32_bf16 v[60:63], v[148:151], v[172:175], v[60:63]\n"
"s_add_u32 s90, s87, 34816\n"
"ds_read_b64_tr_b16 v[140:141], v204 offset:8200\n"
"ds_read_b64_tr_b16 v[142:143], v204 offset:9224\n"
"v_mfma_f32_16x16x32_bf16 v[92:95], v[144:147], v[160:163], v[92:95]\n"
"v_mfma_f32_16x16x32_bf16 v[92:95], v[148:151], v[176:179], v[92:95]\n"
"v_mfma_f32_16x16x32_bf16 v[124:127], v[144:147], v[164:167], v[124:127]\n"
"v_mfma_f32_16x16x32_bf16 v[124:127], v[148:151], v[180:183], v[124:127]\n"
"s_waitcnt vmcnt(17) lgkmcnt(4)\n"
"v_mfma_f32_16x16x32_bf16 v[0:3], v[128:131], a[0:3], v[0:3]\n"
"buffer_load_dwordx4 a[68:71], v230, s[60:63], s90 offen offset:0 sc1\n"
"v_mfma_f32_16x16x32_bf16 v[0:3], v[132:135], a[16:19], v[0:3]\n"
"s_add_u32 s92, s87, 53248\n"
"ds_read_b64_tr_b16 v[144:145], v205 offset:0\n"
"ds_read_b64_tr_b16 v[146:147], v205 offset:1024\n"
"v_mfma_f32_16x16x32_bf16 v[32:35], v[128:131], a[4:7], v[32:35]\n"
"v_mfma_f32_16x16x32_bf16 v[32:35], v[132:135], a[20:23], v[32:35]\n"
"s_nop 0\n"
"ds_read_b64_tr_b16 v[148:149], v205 offset:8192\n"
"ds_read_b64_tr_b16 v[150:151], v205 offset:9216\n"
"v_mfma_f32_16x16x32_bf16 v[64:67], v[128:131], a[8:11], v[64:67]\n"
"buffer_load_dwordx4 a[72:75], v230, s[60:63], s92 offen offset:0 sc1\n"
"v_mfma_f32_16x16x32_bf16 v[64:67], v[132:135], a[24:27], v[64:67]\n"
"s_add_u32 s96, s87, 71680\n"
"v_mfma_f32_16x16x32_bf16 v[96:99], v[128:131], a[12:15], v[96:99]\n"
"v_mfma_f32_16x16x32_bf16 v[96:99], v[132:135], a[28:31], v[96:99]\n"
"s_waitcnt lgkmcnt(4)\n"
"v_mfma_f32_16x16x32_bf16 v[4:7], v[136:139], a[0:3], v[4:7]\n"
"s_nop 0\n"
"ds_read_b64_tr_b16 v[128:129], v205 offset:8\n"
"ds_read_b64_tr_b16 v[130:131], v205 offset:1032\n"
"v_mfma_f32_16x16x32_bf16 v[4:7], v[140:143], a[16:19], v[4:7]\n"
"buffer_load_dwordx4 a[76:79], v230, s[60:63], s96 offen offset:0 sc1\n"
"v_mfma_f32_16x16x32_bf16 v[36:39], v[136:139], a[4:7], v[36:39]\n"
"v_mfma_f32_16x16x32_bf16 v[36:39], v[140:143], a[20:23], v[36:39]\n"
"s_nop 0\n"
"ds_read_b64_tr_b16 v[132:133], v205 offset:8200\n"
"ds_read_b64_tr_b16 v[134:135], v205 offset:9224\n"
"v_mfma_f32_16x16x32_bf16 v[68:71], v[136:139], a[8:11], v[68:71]\n"
"v_mfma_f32_16x16x32_bf16 v[68:71], v[140:143], a[24:27], v[68:71]\n"
"v_mfma_f32_16x16x32_bf16 v[100:103], v[136:139], a[12:15], v[100:103]\n"
"buffer_load_dwordx4 a[80:83], v230, s[60:63], s89 offen offset:1024 sc1\n"
"v_mfma_f32_16x16x32_bf16 v[100:103], v[140:143], a[28:31], v[100:103]\n"
"s_waitcnt lgkmcnt(4)\n"
"v_mfma_f32_16x16x32_bf16 v[8:11], v[144:147], a[0:3], v[8:11]\n"
"s_nop 0\n"
"ds_read_b64_tr_b16 v[136:137], v206 offset:0\n"
"ds_read_b64_tr_b16 v[138:139], v206 offset:1024\n"
"v_mfma_f32_16x16x32_bf16 v[8:11], v[148:151], a[16:19], v[8:11]\n"
"v_mfma_f32_16x16x32_bf16 v[40:43], v[144:147], a[4:7], v[40:43]\n"
"v_mfma_f32_16x16x32_bf16 v[40:43], v[148:151], a[20:23], v[40:43]\n"
"buffer_load_dwordx4 a[84:87], v230, s[60:63], s90 offen offset:1024 sc1\n"
"v_mfma_f32_16x16x32_bf16 v[72:75], v[144:147], a[8:11], v[72:75]\n"
"s_nop 0\n"
"ds_read_b64_tr_b16 v[140:141], v206 offset:8192\n"
"ds_read_b64_tr_b16 v[142:143], v206 offset:9216\n"
"v_mfma_f32_16x16x32_bf16 v[72:75], v[148:151], a[24:27], v[72:75]\n"
"v_mfma_f32_16x16x32_bf16 v[104:107], v[144:147], a[12:15], v[104:107]\n"
"v_mfma_f32_16x16x32_bf16 v[104:107], v[148:151], a[28:31], v[104:107]\n"
"s_waitcnt lgkmcnt(4)\n"
"v_mfma_f32_16x16x32_bf16 v[12:15], v[128:131], a[0:3], v[12:15]\n"
"buffer_load_dwordx4 a[88:91], v230, s[60:63], s92 offen offset:1024 sc1\n"
"v_mfma_f32_16x16x32_bf16 v[12:15], v[132:135], a[16:19], v[12:15]\n"
"s_nop 0\n"
"ds_read_b64_tr_b16 v[144:145], v206 offset:8\n"
"ds_read_b64_tr_b16 v[146:147], v206 offset:1032\n"
"v_mfma_f32_16x16x32_bf16 v[44:47], v[128:131], a[4:7], v[44:47]\n"
"v_mfma_f32_16x16x32_bf16 v[44:47], v[132:135], a[20:23], v[44:47]\n"
"s_nop 0\n"
"ds_read_b64_tr_b16 v[148:149], v206 offset:8200\n"
"ds_read_b64_tr_b16 v[150:151], v206 offset:9224\n"
"v_mfma_f32_16x16x32_bf16 v[76:79], v[128:131], a[8:11], v[76:79]\n"
"v_mfma_f32_16x16x32_bf16 v[76:79], v[132:135], a[24:27], v[76:79]\n"
"buffer_load_dwordx4 a[92:95], v230, s[60:63], s96 offen offset:1024 sc1\n"
"v_mfma_f32_16x16x32_bf16 v[108:111], v[128:131], a[12:15], v[108:111]\n"
"v_mfma_f32_16x16x32_bf16 v[108:111], v[132:135], a[28:31], v[108:111]\n"
"s_waitcnt lgkmcnt(4)\n"
"v_mfma_f32_16x16x32_bf16 v[16:19], v[136:139], a[0:3], v[16:19]\n"
"s_nop 0\n"
"ds_read_b64_tr_b16 v[128:129], v207 offset:0\n"
"ds_read_b64_tr_b16 v[130:131], v207 offset:1024\n"
"v_mfma_f32_16x16x32_bf16 v[16:19], v[140:143], a[16:19], v[16:19]\n"
"v_mfma_f32_16x16x32_bf16 v[48:51], v[136:139], a[4:7], v[48:51]\n"
"buffer_load_dword v192, v225, s[60:63], s95 offen sc1\n"
"v_mfma_f32_16x16x32_bf16 v[48:51], v[140:143], a[20:23], v[48:51]\n"
"s_add_u32 m0, s76, 0\n"
"ds_read_b64_tr_b16 v[132:133], v207 offset:8192\n"
"ds_read_b64_tr_b16 v[134:135], v207 offset:9216\n"
"v_mfma_f32_16x16x32_bf16 v[80:83], v[136:139], a[8:11], v[80:83]\n"
"v_mfma_f32_16x16x32_bf16 v[80:83], v[140:143], a[24:27], v[80:83]\n"
"v_mfma_f32_16x16x32_bf16 v[112:115], v[136:139], a[12:15], v[112:115]\n"
"v_mfma_f32_16x16x32_bf16 v[112:115], v[140:143], a[28:31], v[112:115]\n"
"s_waitcnt lgkmcnt(4)\n"
"v_mfma_f32_16x16x32_bf16 v[20:23], v[144:147], a[0:3], v[20:23]\n"
"buffer_load_dwordx4 v230, s[44:47], s78 offen offset:0 lds\n"
"v_mfma_f32_16x16x32_bf16 v[20:23], v[148:151], a[16:19], v[20:23]\n"
"s_add_u32 m0, s76, 16\n"
"ds_read_b64_tr_b16 v[136:137], v207 offset:8\n"
"ds_read_b64_tr_b16 v[138:139], v207 offset:1032\n"
"v_mfma_f32_16x16x32_bf16 v[52:55], v[144:147], a[4:7], v[52:55]\n"
"v_mfma_f32_16x16x32_bf16 v[52:55], v[148:151], a[20:23], v[52:55]\n"
"s_nop 0\n"
"ds_read_b64_tr_b16 v[140:141], v207 offset:8200\n"
"ds_read_b64_tr_b16 v[142:143], v207 offset:9224\n"
"v_mfma_f32_16x16x32_bf16 v[84:87], v[144:147], a[8:11], v[84:87]\n"
"buffer_load_dwordx4 v230, s[44:47], s78 offen offset:1024 lds\n"
"v_mfma_f32_16x16x32_bf16 v[84:87], v[148:151], a[24:27], v[84:87]\n"
"s_add_u32 m0, s76, 32\n"
"v_mfma_f32_16x16x32_bf16 v[116:119], v[144:147], a[12:15], v[116:119]\n"
"v_mfma_f32_16x16x32_bf16 v[116:119], v[148:151], a[28:31], v[116:119]\n"
"s_waitcnt vmcnt(23) lgkmcnt(4)\n"
"s_barrier\n"
"v_mfma_f32_16x16x32_bf16 v[24:27], v[128:131], a[0:3], v[24:27]\n"
"s_nop 0\n"
"ds_read_b64_tr_b16 v[144:145], v204 offset:16384\n"
"ds_read_b64_tr_b16 v[146:147], v204 offset:17408\n"
"v_mfma_f32_16x16x32_bf16 v[24:27], v[132:135], a[16:19], v[24:27]\n"
"buffer_load_dwordx4 v230, s[44:47], s78 offen offset:2048 lds\n"
"v_mfma_f32_16x16x32_bf16 v[56:59], v[128:131], a[4:7], v[56:59]\n"
"s_add_u32 m0, s76, 48\n"
"v_mfma_f32_16x16x32_bf16 v[56:59], v[132:135], a[20:23], v[56:59]\n"
"s_nop 0\n"
"ds_read_b64_tr_b16 v[148:149], v204 offset:24576\n"
"ds_read_b64_tr_b16 v[150:151], v204 offset:25600\n"
"v_mfma_f32_16x16x32_bf16 v[88:91], v[128:131], a[8:11], v[88:91]\n"
"v_mfma_f32_16x16x32_bf16 v[88:91], v[132:135], a[24:27], v[88:91]\n"
"v_mfma_f32_16x16x32_bf16 v[120:123], v[128:131], a[12:15], v[120:123]\n"
"buffer_load_dwordx4 v230, s[44:47], s78 offen offset:3072 lds\n"
"v_mfma_f32_16x16x32_bf16 v[120:123], v[132:135], a[28:31], v[120:123]\n"
"s_waitcnt vmcnt(21) lgkmcnt(4)\n"
"s_barrier\n"
"v_mfma_f32_16x16x32_bf16 v[28:31], v[136:139], a[0:3], v[28:31]\n"
"s_add_u32 s78, s78, 16384\n"
"ds_read_b64_tr_b16 v[128:129], v204 offset:32768\n"
"ds_read_b64_tr_b16 v[130:131], v204 offset:33792\n"
"v_mfma_f32_16x16x32_bf16 v[28:31], v[140:143], a[16:19], v[28:31]\n"
"s_add_u32 m0, s76, 16640\n"
"v_mfma_f32_16x16x32_bf16 v[60:63], v[136:139], a[4:7], v[60:63]\n"
"v_mfma_f32_16x16x32_bf16 v[60:63], v[140:143], a[20:23], v[60:63]\n"
"buffer_load_dwordx4 v230, s[44:47], s78 offen offset:0 lds\n"
"v_mfma_f32_16x16x32_bf16 v[92:95], v[136:139], a[8:11], v[92:95]\n"
"s_add_u32 m0, s76, 16656\n"
"ds_read_b64_tr_b16 v[132:133], v204 offset:40960\n"
"ds_read_b64_tr_b16 v[134:135], v204 offset:41984\n"
"v_mfma_f32_16x16x32_bf16 v[92:95], v[140:143], a[24:27], v[92:95]\n"
"v_mfma_f32_16x16x32_bf16 v[124:127], v[136:139], a[12:15], v[124:127]\n"
"v_mfma_f32_16x16x32_bf16 v[124:127], v[140:143], a[28:31], v[124:127]\n"
"s_waitcnt vmcnt(14) lgkmcnt(4)\n"
"v_mfma_f32_16x16x32_bf16 v[0:3], v[144:147], a[32:35], v[0:3]\n"
"buffer_load_dwordx4 v230, s[44:47], s78 offen offset:1024 lds\n"
"v_mfma_f32_16x16x32_bf16 v[0:3], v[148:151], a[48:51], v[0:3]\n"
"s_add_u32 m0, s76, 16672\n"
"ds_read_b64_tr_b16 v[136:137], v204 offset:16392\n"
"ds_read_b64_tr_b16 v[138:139], v204 offset:17416\n"
"v_mfma_f32_16x16x32_bf16 v[32:35], v[144:147], a[36:39], v[32:35]\n"
"v_mfma_f32_16x16x32_bf16 v[32:35], v[148:151], a[52:55], v[32:35]\n"
"s_nop 0\n"
"ds_read_b64_tr_b16 v[140:141], v204 offset:24584\n"
"ds_read_b64_tr_b16 v[142:143], v204 offset:25608\n"
"v_mfma_f32_16x16x32_bf16 v[64:67], v[144:147], a[40:43], v[64:67]\n"
"v_mfma_f32_16x16x32_bf16 v[64:67], v[148:151], a[56:59], v[64:67]\n"
"buffer_load_dwordx4 v230, s[44:47], s78 offen offset:2048 lds\n"
"v_mfma_f32_16x16x32_bf16 v[96:99], v[144:147], a[44:47], v[96:99]\n"
"s_add_u32 m0, s76, 16688\n"
"v_mfma_f32_16x16x32_bf16 v[96:99], v[148:151], a[60:63], v[96:99]\n"
"s_waitcnt vmcnt(8) lgkmcnt(4)\n"
"v_mfma_f32_16x16x32_bf16 v[0:3], v[128:131], a[64:67], v[0:3]\n"
"s_nop 0\n"
"ds_read_b64_tr_b16 v[144:145], v204 offset:32776\n"
"ds_read_b64_tr_b16 v[146:147], v204 offset:33800\n"
"v_mfma_f32_16x16x32_bf16 v[0:3], v[132:135], a[80:83], v[0:3]\n"
"v_mfma_f32_16x16x32_bf16 v[32:35], v[128:131], a[68:71], v[32:35]\n"
"buffer_load_dwordx4 v230, s[44:47], s78 offen offset:3072 lds\n"
"v_mfma_f32_16x16x32_bf16 v[32:35], v[132:135], a[84:87], v[32:35]\n"
"s_add_u32 s78, s78, 16384\n"
"ds_read_b64_tr_b16 v[148:149], v204 offset:40968\n"
"ds_read_b64_tr_b16 v[150:151], v204 offset:41992\n"
"v_mfma_f32_16x16x32_bf16 v[64:67], v[128:131], a[72:75], v[64:67]\n"
"s_add_u32 m0, s76, 33280\n"
"v_mfma_f32_16x16x32_bf16 v[64:67], v[132:135], a[88:91], v[64:67]\n"
"v_mfma_f32_16x16x32_bf16 v[96:99], v[128:131], a[76:79], v[96:99]\n"
"v_mfma_f32_16x16x32_bf16 v[96:99], v[132:135], a[92:95], v[96:99]\n"
"s_waitcnt lgkmcnt(4)\n"
"v_mfma_f32_16x16x32_bf16 v[4:7], v[136:139], a[32:35], v[4:7]\n"
"buffer_load_dwordx4 v230, s[44:47], s78 offen offset:0 lds\n"
"v_mfma_f32_16x16x32_bf16 v[4:7], v[140:143], a[48:51], v[4:7]\n"
"s_add_u32 m0, s76, 33296\n"
"ds_read_b64_tr_b16 v[128:129], v205 offset:16384\n"
"ds_read_b64_tr_b16 v[130:131], v205 offset:17408\n"
"v_mfma_f32_16x16x32_bf16 v[36:39], v[136:139], a[36:39], v[36:39]\n"
"v_mfma_f32_16x16x32_bf16 v[36:39], v[140:143], a[52:55], v[36:39]\n"
"s_nop 0\n"
"ds_read_b64_tr_b16 v[132:133], v205 offset:24576\n"
"ds_read_b64_tr_b16 v[134:135], v205 offset:25600\n"
"v_mfma_f32_16x16x32_bf16 v[68:71], v[136:139], a[40:43], v[68:71]\n"
"buffer_load_dwordx4 v230, s[44:47], s78 offen offset:1024 lds\n"
"v_mfma_f32_16x16x32_bf16 v[68:71], v[140:143], a[56:59], v[68:71]\n"
"s_add_u32 m0, s76, 33312\n"
"v_mfma_f32_16x16x32_bf16 v[100:103], v[136:139], a[44:47], v[100:103]\n"
"v_mfma_f32_16x16x32_bf16 v[100:103], v[140:143], a[60:63], v[100:103]\n"
"s_waitcnt lgkmcnt(4)\n"
"v_mfma_f32_16x16x32_bf16 v[4:7], v[144:147], a[64:67], v[4:7]\n"
"s_nop 0\n"
"ds_read_b64_tr_b16 v[136:137], v205 offset:32768\n"
"ds_read_b64_tr_b16 v[138:139], v205 offset:33792\n"
"v_mfma_f32_16x16x32_bf16 v[4:7], v[148:151], a[80:83], v[4:7]\n"
"buffer_load_dwordx4 v230, s[44:47], s78 offen offset:2048 lds\n"
"v_mfma_f32_16x16x32_bf16 v[36:39], v[144:147], a[68:71], v[36:39]\n"
"s_add_u32 m0, s76, 33328\n"
"v_mfma_f32_16x16x32_bf16 v[36:39], v[148:151], a[84:87], v[36:39]\n"
"s_nop 0\n"
"ds_read_b64_tr_b16 v[140:141], v205 offset:40960\n"
"ds_read_b64_tr_b16 v[142:143], v205 offset:41984\n"
"v_mfma_f32_16x16x32_bf16 v[68:71], v[144:147], a[72:75], v[68:71]\n"
"v_mfma_f32_16x16x32_bf16 v[68:71], v[148:151], a[88:91], v[68:71]\n"
"v_mfma_f32_16x16x32_bf16 v[100:103], v[144:147], a[76:79], v[100:103]\n"
"buffer_load_dwordx4 v230, s[44:47], s78 offen offset:3072 lds\n"
"v_mfma_f32_16x16x32_bf16 v[100:103], v[148:151], a[92:95], v[100:103]\n"
"s_waitcnt lgkmcnt(4)\n"
"v_mfma_f32_16x16x32_bf16 v[8:11], v[128:131], a[32:35], v[8:11]\n"
"s_add_u32 s78, s78, 16384\n"
"ds_read_b64_tr_b16 v[144:145], v205 offset:16392\n"
"ds_read_b64_tr_b16 v[146:147], v205 offset:17416\n"
"v_cvt_pk_bf16_f32 v0, v0, v1\n"
"v_mfma_f32_16x16x32_bf16 v[8:11], v[132:135], a[48:51], v[8:11]\n"
"s_add_u32 m0, s77, 0\n"
"v_cvt_pk_bf16_f32 v1, v2, v3\n"
"v_cvt_pk_bf16_f32 v2, v4, v5\n"
"v_cvt_pk_bf16_f32 v3, v6, v7\n"
"v_mfma_f32_16x16x32_bf16 v[40:43], v[128:131], a[36:39], v[40:43]\n"
"v_accvgpr_write_b32 a192, v0\n"
"v_accvgpr_write_b32 a193, v1\n"
"v_accvgpr_write_b32 a194, v2\n"
"v_mfma_f32_16x16x32_bf16 v[40:43], v[132:135], a[52:55], v[40:43]\n"
"buffer_load_dwordx4 v230, s[48:51], s79 offen offset:0 lds\n"
"v_accvgpr_write_b32 a195, v3\n"
"v_mfma_f32_16x16x32_bf16 v[72:75], v[128:131], a[40:43], v[72:75]\n"
"s_add_u32 m0, s77, 32\n"
"ds_read_b64_tr_b16 v[148:149], v205 offset:24584\n"
"ds_read_b64_tr_b16 v[150:151], v205 offset:25608\n"
"v_mfma_f32_16x16x32_bf16 v[72:75], v[132:135], a[56:59], v[72:75]\n"
"v_cvt_pk_bf16_f32 v32, v32, v33\n"
"v_cvt_pk_bf16_f32 v33, v34, v35\n"
"v_mfma_f32_16x16x32_bf16 v[104:107], v[128:131], a[44:47], v[104:107]\n"
"v_cvt_pk_bf16_f32 v34, v36, v37\n"
"v_cvt_pk_bf16_f32 v35, v38, v39\n"
"v_mfma_f32_16x16x32_bf16 v[104:107], v[132:135], a[60:63], v[104:107]\n"
"s_waitcnt lgkmcnt(4)\n"
"v_accvgpr_write_b32 a208, v32\n"
"v_accvgpr_write_b32 a209, v33\n"
"v_mfma_f32_16x16x32_bf16 v[8:11], v[136:139], a[64:67], v[8:11]\n"
"buffer_load_dwordx4 v230, s[48:51], s79 offen offset:1024 lds\n"
"v_mfma_f32_16x16x32_bf16 v[8:11], v[140:143], a[80:83], v[8:11]\n"
"s_add_u32 m0, s77, 64\n"
"ds_read_b64_tr_b16 v[128:129], v205 offset:32776\n"
"ds_read_b64_tr_b16 v[130:131], v205 offset:33800\n"
"v_mfma_f32_16x16x32_bf16 v[40:43], v[136:139], a[68:71], v[40:43]\n"
"v_accvgpr_write_b32 a210, v34\n"
"v_accvgpr_write_b32 a211, v35\n"
"v_mfma_f32_16x16x32_bf16 v[40:43], v[140:143], a[84:87], v[40:43]\n"
"s_nop 0\n"
"ds_read_b64_tr_b16 v[132:133], v205 offset:40968\n"
"ds_read_b64_tr_b16 v[134:135], v205 offset:41992\n"
"v_mfma_f32_16x16x32_bf16 v[72:75], v[136:139], a[72:75], v[72:75]\n"
"v_cvt_pk_bf16_f32 v64, v64, v65\n"
"v_cvt_pk_bf16_f32 v65, v66, v67\n"
"v_mfma_f32_16x16x32_bf16 v[72:75], v[140:143], a[88:91], v[72:75]\n"
"buffer_load_dwordx4 v230, s[48:51], s79 offen offset:2048 lds\n"
"v_mfma_f32_16x16x32_bf16 v[104:107], v[136:139], a[76:79], v[104:107]\n"
"s_add_u32 m0, s77, 96\n"
"v_cvt_pk_bf16_f32 v66, v68, v69\n"
"v_cvt_pk_bf16_f32 v67, v70, v71\n"
"v_mfma_f32_16x16x32_bf16 v[104:107], v[140:143], a[92:95], v[104:107]\n"
"s_waitcnt lgkmcnt(4)\n"
"v_accvgpr_write_b32 a224, v64\n"
"v_accvgpr_write_b32 a225, v65\n"
"v_mfma_f32_16x16x32_bf16 v[12:15], v[144:147], a[32:35], v[12:15]\n"
"s_nop 0\n"
"ds_read_b64_tr_b16 v[136:137], v206 offset:16384\n"
"ds_read_b64_tr_b16 v[138:139], v206 offset:17408\n"
"v_mfma_f32_16x16x32_bf16 v[12:15], v[148:151], a[48:51], v[12:15]\n"
"v_accvgpr_write_b32 a226, v66\n"
"v_accvgpr_write_b32 a227, v67\n"
"v_mfma_f32_16x16x32_bf16 v[44:47], v[144:147], a[36:39], v[44:47]\n"
"buffer_load_dwordx4 v230, s[48:51], s79 offen offset:3072 lds\n"
"v_mfma_f32_16x16x32_bf16 v[44:47], v[148:151], a[52:55], v[44:47]\n"
"s_add_u32 s79, s79, 16384\n"
"ds_read_b64_tr_b16 v[140:141], v206 offset:24576\n"
"ds_read_b64_tr_b16 v[142:143], v206 offset:25600\n"
"v_mfma_f32_16x16x32_bf16 v[76:79], v[144:147], a[40:43], v[76:79]\n"
"s_add_u32 m0, s77, 16896\n"
"v_cvt_pk_bf16_f32 v96, v96, v97\n"
"v_cvt_pk_bf16_f32 v97, v98, v99\n"
"v_mfma_f32_16x16x32_bf16 v[76:79], v[148:151], a[56:59], v[76:79]\n"
"v_cvt_pk_bf16_f32 v98, v100, v101\n"
"v_cvt_pk_bf16_f32 v99, v102, v103\n"
"v_mfma_f32_16x16x32_bf16 v[108:111], v[144:147], a[44:47], v[108:111]\n"
"v_accvgpr_write_b32 a240, v96\n"
"v_accvgpr_write_b32 a241, v97\n"
"v_mfma_f32_16x16x32_bf16 v[108:111], v[148:151], a[60:63], v[108:111]\n"
"s_waitcnt lgkmcnt(4)\n"
"v_accvgpr_write_b32 a242, v98\n"
"v_accvgpr_write_b32 a243, v99\n"
"v_mfma_f32_16x16x32_bf16 v[12:15], v[128:131], a[64:67], v[12:15]\n"
"buffer_load_dwordx4 v230, s[48:51], s79 offen offset:0 lds\n"
"v_mfma_f32_16x16x32_bf16 v[12:15], v[132:135], a[80:83], v[12:15]\n"
"s_add_u32 m0, s77, 16928\n"
"ds_read_b64_tr_b16 v[144:145], v206 offset:32768\n"
"ds_read_b64_tr_b16 v[146:147], v206 offset:33792\n"
"v_mfma_f32_16x16x32_bf16 v[44:47], v[128:131], a[68:71], v[44:47]\n"
"v_mfma_f32_16x16x32_bf16 v[44:47], v[132:135], a[84:87], v[44:47]\n"
"s_nop 0\n"
"ds_read_b64_tr_b16 v[148:149], v206 offset:40960\n"
"ds_read_b64_tr_b16 v[150:151], v206 offset:41984\n"
"v_mfma_f32_16x16x32_bf16 v[76:79], v[128:131], a[72:75], v[76:79]\n"
"buffer_load_dwordx4 v230, s[48:51], s79 offen offset:1024 lds\n"
"v_mfma_f32_16x16x32_bf16 v[76:79], v[132:135], a[88:91], v[76:79]\n"
"s_add_u32 m0, s77, 16960\n"
"v_mfma_f32_16x16x32_bf16 v[108:111], v[128:131], a[76:79], v[108:111]\n"
"v_mfma_f32_16x16x32_bf16 v[108:111], v[132:135], a[92:95], v[108:111]\n"
"s_waitcnt lgkmcnt(4)\n"
"v_mfma_f32_16x16x32_bf16 v[16:19], v[136:139], a[32:35], v[16:19]\n"
"s_nop 0\n"
"ds_read_b64_tr_b16 v[128:129], v206 offset:16392\n"
"ds_read_b64_tr_b16 v[130:131], v206 offset:17416\n"
"v_cvt_pk_bf16_f32 v8, v8, v9\n"
"v_mfma_f32_16x16x32_bf16 v[16:19], v[140:143], a[48:51], v[16:19]\n"
"buffer_load_dwordx4 v230, s[48:51], s79 offen offset:2048 lds\n"
"v_cvt_pk_bf16_f32 v9, v10, v11\n"
"v_mfma_f32_16x16x32_bf16 v[48:51], v[136:139], a[36:39], v[48:51]\n"
"s_add_u32 m0, s77, 16992\n"
"v_cvt_pk_bf16_f32 v10, v12, v13\n"
"v_cvt_pk_bf16_f32 v11, v14, v15\n"
"v_accvgpr_write_b32 a196, v8\n"
"v_mfma_f32_16x16x32_bf16 v[48:51], v[140:143], a[52:55], v[48:51]\n"
"s_nop 0\n"
"ds_read_b64_tr_b16 v[132:133], v206 offset:24584\n"
"ds_read_b64_tr_b16 v[134:135], v206 offset:25608\n"
"v_accvgpr_write_b32 a197, v9\n"
"v_mfma_f32_16x16x32_bf16 v[80:83], v[136:139], a[40:43], v[80:83]\n"
"v_accvgpr_write_b32 a198, v10\n"
"v_accvgpr_write_b32 a199, v11\n"
"v_mfma_f32_16x16x32_bf16 v[80:83], v[140:143], a[56:59], v[80:83]\n"
"v_cvt_pk_bf16_f32 v40, v40, v41\n"
"v_cvt_pk_bf16_f32 v41, v42, v43\n"
"v_mfma_f32_16x16x32_bf16 v[112:115], v[136:139], a[44:47], v[112:115]\n"
"buffer_load_dwordx4 v230, s[48:51], s79 offen offset:3072 lds\n"
"v_mfma_f32_16x16x32_bf16 v[112:115], v[140:143], a[60:63], v[112:115]\n"
"s_waitcnt lgkmcnt(4)\n"
"v_cvt_pk_bf16_f32 v42, v44, v45\n"
"v_cvt_pk_bf16_f32 v43, v46, v47\n"
"v_mfma_f32_16x16x32_bf16 v[16:19], v[144:147], a[64:67], v[16:19]\n"
"s_add_u32 s79, s79, 16384\n"
"ds_read_b64_tr_b16 v[136:137], v206 offset:32776\n"
"ds_read_b64_tr_b16 v[138:139], v206 offset:33800\n"
"v_mfma_f32_16x16x32_bf16 v[16:19], v[148:151], a[80:83], v[16:19]\n"
"v_accvgpr_write_b32 a212, v40\n"
"v_accvgpr_write_b32 a213, v41\n"
"v_mfma_f32_16x16x32_bf16 v[48:51], v[144:147], a[68:71], v[48:51]\n"
"v_accvgpr_write_b32 a214, v42\n"
"v_accvgpr_write_b32 a215, v43\n"
"v_mfma_f32_16x16x32_bf16 v[48:51], v[148:151], a[84:87], v[48:51]\n"
"s_nop 0\n"
"ds_read_b64_tr_b16 v[140:141], v206 offset:40968\n"
"ds_read_b64_tr_b16 v[142:143], v206 offset:41992\n"
"v_mfma_f32_16x16x32_bf16 v[80:83], v[144:147], a[72:75], v[80:83]\n"
"v_cvt_pk_bf16_f32 v72, v72, v73\n"
"v_cvt_pk_bf16_f32 v73, v74, v75\n"
"v_mfma_f32_16x16x32_bf16 v[80:83], v[148:151], a[88:91], v[80:83]\n"
"v_cvt_pk_bf16_f32 v74, v76, v77\n"
"v_cvt_pk_bf16_f32 v75, v78, v79\n"
"v_mfma_f32_16x16x32_bf16 v[112:115], v[144:147], a[76:79], v[112:115]\n"
"v_accvgpr_write_b32 a228, v72\n"
"v_accvgpr_write_b32 a229, v73\n"
"v_mfma_f32_16x16x32_bf16 v[112:115], v[148:151], a[92:95], v[112:115]\n"
"s_waitcnt lgkmcnt(4)\n"
"v_accvgpr_write_b32 a230, v74\n"
"v_accvgpr_write_b32 a231, v75\n"
"v_mfma_f32_16x16x32_bf16 v[20:23], v[128:131], a[32:35], v[20:23]\n"
"s_nop 0\n"
"ds_read_b64_tr_b16 v[144:145], v207 offset:16384\n"
"ds_read_b64_tr_b16 v[146:147], v207 offset:17408\n"
"v_mfma_f32_16x16x32_bf16 v[20:23], v[132:135], a[48:51], v[20:23]\n"
"v_cvt_pk_bf16_f32 v104, v104, v105\n"
"v_cvt_pk_bf16_f32 v105, v106, v107\n"
"v_mfma_f32_16x16x32_bf16 v[52:55], v[128:131], a[36:39], v[52:55]\n"
"v_cvt_pk_bf16_f32 v106, v108, v109\n"
"v_cvt_pk_bf16_f32 v107, v110, v111\n"
"v_mfma_f32_16x16x32_bf16 v[52:55], v[132:135], a[52:55], v[52:55]\n"
"s_nop 0\n"
"ds_read_b64_tr_b16 v[148:149], v207 offset:24576\n"
"ds_read_b64_tr_b16 v[150:151], v207 offset:25600\n"
"v_mfma_f32_16x16x32_bf16 v[84:87], v[128:131], a[40:43], v[84:87]\n"
"v_accvgpr_write_b32 a244, v104\n"
"v_accvgpr_write_b32 a245, v105\n"
"v_mfma_f32_16x16x32_bf16 v[84:87], v[132:135], a[56:59], v[84:87]\n"
"v_accvgpr_write_b32 a246, v106\n"
"v_accvgpr_write_b32 a247, v107\n"
"v_mfma_f32_16x16x32_bf16 v[116:119], v[128:131], a[44:47], v[116:119]\n"
"v_mfma_f32_16x16x32_bf16 v[116:119], v[132:135], a[60:63], v[116:119]\n"
"s_waitcnt lgkmcnt(4)\n"
"v_mfma_f32_16x16x32_bf16 v[20:23], v[136:139], a[64:67], v[20:23]\n"
"s_nop 0\n"
"ds_read_b64_tr_b16 v[128:129], v207 offset:32768\n"
"ds_read_b64_tr_b16 v[130:131], v207 offset:33792\n"
"v_mfma_f32_16x16x32_bf16 v[20:23], v[140:143], a[80:83], v[20:23]\n"
"v_mfma_f32_16x16x32_bf16 v[52:55], v[136:139], a[68:71], v[52:55]\n"
"v_mfma_f32_16x16x32_bf16 v[52:55], v[140:143], a[84:87], v[52:55]\n"
"s_nop 0\n"
"ds_read_b64_tr_b16 v[132:133], v207 offset:40960\n"
"ds_read_b64_tr_b16 v[134:135], v207 offset:41984\n"
"v_mfma_f32_16x16x32_bf16 v[84:87], v[136:139], a[72:75], v[84:87]\n"
"v_mfma_f32_16x16x32_bf16 v[84:87], v[140:143], a[88:91], v[84:87]\n"
"v_mfma_f32_16x16x32_bf16 v[116:119], v[136:139], a[76:79], v[116:119]\n"
"v_mfma_f32_16x16x32_bf16 v[116:119], v[140:143], a[92:95], v[116:119]\n"
"s_waitcnt lgkmcnt(4)\n"
"v_mfma_f32_16x16x32_bf16 v[24:27], v[144:147], a[32:35], v[24:27]\n"
"s_nop 0\n"
"ds_read_b64_tr_b16 v[136:137], v207 offset:16392\n"
"ds_read_b64_tr_b16 v[138:139], v207 offset:17416\n"
"v_cvt_pk_bf16_f32 v16, v16, v17\n"
"v_mfma_f32_16x16x32_bf16 v[24:27], v[148:151], a[48:51], v[24:27]\n"
"v_cvt_pk_bf16_f32 v17, v18, v19\n"
"v_cvt_pk_bf16_f32 v18, v20, v21\n"
"v_cvt_pk_bf16_f32 v19, v22, v23\n"
"v_mfma_f32_16x16x32_bf16 v[56:59], v[144:147], a[36:39], v[56:59]\n"
"v_accvgpr_write_b32 a200, v16\n"
"v_accvgpr_write_b32 a201, v17\n"
"v_accvgpr_write_b32 a202, v18\n"
"v_mfma_f32_16x16x32_bf16 v[56:59], v[148:151], a[52:55], v[56:59]\n"
"s_nop 0\n"
"ds_read_b64_tr_b16 v[140:141], v207 offset:24584\n"
"ds_read_b64_tr_b16 v[142:143], v207 offset:25608\n"
"v_accvgpr_write_b32 a203, v19\n"
"v_mfma_f32_16x16x32_bf16 v[88:91], v[144:147], a[40:43], v[88:91]\n"
"v_cvt_pk_bf16_f32 v48, v48, v49\n"
"v_cvt_pk_bf16_f32 v49, v50, v51\n"
"v_mfma_f32_16x16x32_bf16 v[88:91], v[148:151], a[56:59], v[88:91]\n"
"v_cvt_pk_bf16_f32 v50, v52, v53\n"
"v_cvt_pk_bf16_f32 v51, v54, v55\n"
"v_mfma_f32_16x16x32_bf16 v[120:123], v[144:147], a[44:47], v[120:123]\n"
"v_accvgpr_write_b32 a216, v48\n"
"v_accvgpr_write_b32 a217, v49\n"
"v_mfma_f32_16x16x32_bf16 v[120:123], v[148:151], a[60:63], v[120:123]\n"
"s_waitcnt lgkmcnt(4)\n"
"v_accvgpr_write_b32 a218, v50\n"
"v_accvgpr_write_b32 a219, v51\n"
"v_mfma_f32_16x16x32_bf16 v[24:27], v[128:131], a[64:67], v[24:27]\n"
"s_nop 0\n"
"ds_read_b64_tr_b16 v[144:145], v207 offset:32776\n"
"ds_read_b64_tr_b16 v[146:147], v207 offset:33800\n"
"v_mfma_f32_16x16x32_bf16 v[24:27], v[132:135], a[80:83], v[24:27]\n"
"L_tc0_2:\n"
"s_cbranch_execz L_tp0_2_1\n"
"v_cvt_pk_bf16_f32 v80, v80, v81\n"
"v_cvt_pk_bf16_f32 v81, v82, v83\n"
"v_mfma_f32_16x16x32_bf16 v[56:59], v[128:131], a[68:71], v[56:59]\n"
"v_cvt_pk_bf16_f32 v82, v84, v85\n"
"v_cvt_pk_bf16_f32 v83, v86, v87\n"
"v_mfma_f32_16x16x32_bf16 v[56:59], v[132:135], a[84:87], v[56:59]\n"
"s_nop 0\n"
"ds_read_b64_tr_b16 v[148:149], v207 offset:40968\n"
"ds_read_b64_tr_b16 v[150:151], v207 offset:41992\n"
"v_mfma_f32_16x16x32_bf16 v[88:91], v[128:131], a[72:75], v[88:91]\n"
"v_accvgpr_write_b32 a232, v80\n"
"v_accvgpr_write_b32 a233, v81\n"
"v_mfma_f32_16x16x32_bf16 v[88:91], v[132:135], a[88:91], v[88:91]\n"
"v_accvgpr_write_b32 a234, v82\n"
"v_accvgpr_write_b32 a235, v83\n"
"v_mfma_f32_16x16x32_bf16 v[120:123], v[128:131], a[76:79], v[120:123]\n"
"v_cvt_pk_bf16_f32 v112, v112, v113\n"
"v_cvt_pk_bf16_f32 v113, v114, v115\n"
"v_mfma_f32_16x16x32_bf16 v[120:123], v[132:135], a[92:95], v[120:123]\n"
"s_waitcnt lgkmcnt(4)\n"
"s_barrier\n"
"v_mfma_f32_16x16x32_bf16 v[28:31], v[136:139], a[32:35], v[28:31]\n"
"s_add_u32 m0, s77, 33792\n"
"v_cvt_pk_bf16_f32 v114, v116, v117\n"
"v_cvt_pk_bf16_f32 v115, v118, v119\n"
"v_mfma_f32_16x16x32_bf16 v[28:31], v[140:143], a[48:51], v[28:31]\n"
"buffer_load_dwordx4 v230, s[48:51], s79 offen offset:0 lds\n"
"v_mfma_f32_16x16x32_bf16 v[60:63], v[136:139], a[36:39], v[60:63]\n"
"s_add_u32 m0, s77, 33824\n"
"v_accvgpr_write_b32 a248, v112\n"
"v_accvgpr_write_b32 a249, v113\n"
"v_mfma_f32_16x16x32_bf16 v[60:63], v[140:143], a[52:55], v[60:63]\n"
"v_accvgpr_write_b32 a250, v114\n"
"v_accvgpr_write_b32 a251, v115\n"
"v_mfma_f32_16x16x32_bf16 v[92:95], v[136:139], a[40:43], v[92:95]\n"
"buffer_load_dwordx4 v230, s[48:51], s79 offen offset:1024 lds\n"
"v_mfma_f32_16x16x32_bf16 v[92:95], v[140:143], a[56:59], v[92:95]\n"
"s_add_u32 m0, s77, 33856\n"
"v_mfma_f32_16x16x32_bf16 v[124:127], v[136:139], a[44:47], v[124:127]\n"
"v_mfma_f32_16x16x32_bf16 v[124:127], v[140:143], a[60:63], v[124:127]\n"
"s_waitcnt lgkmcnt(0)\n"
"v_mfma_f32_16x16x32_bf16 v[28:31], v[144:147], a[64:67], v[28:31]\n"
"v_mfma_f32_16x16x32_bf16 v[28:31], v[148:151], a[80:83], v[28:31]\n"
"buffer_load_dwordx4 v230, s[48:51], s79 offen offset:2048 lds\n"
"v_mfma_f32_16x16x32_bf16 v[60:63], v[144:147], a[68:71], v[60:63]\n"
"s_add_u32 m0, s77, 33888\n"
"v_mfma_f32_16x16x32_bf16 v[60:63], v[148:151], a[84:87], v[60:63]\n"
"v_mfma_f32_16x16x32_bf16 v[92:95], v[144:147], a[72:75], v[92:95]\n"
"v_mfma_f32_16x16x32_bf16 v[92:95], v[148:151], a[88:91], v[92:95]\n"
"v_mfma_f32_16x16x32_bf16 v[124:127], v[144:147], a[76:79], v[124:127]\n"
"buffer_load_dwordx4 v230, s[48:51], s79 offen offset:3072 lds\n"
"v_mfma_f32_16x16x32_bf16 v[124:127], v[148:151], a[92:95], v[124:127]\n"
"s_add_u32 s79, s79, 16384\n"
"s_waitcnt vmcnt(8) lgkmcnt(0)\n"
"s_barrier\n"
"ds_read_b128 a[64:67], v228 offset:0\n"
"ds_read_b128 a[68:71], v228 offset:16\n"
"ds_read_b128 a[72:75], v228 offset:128\n"
"ds_read_b128 a[76:79], v228 offset:144\n"
"ds_read_b128 a[80:83], v228 offset:256\n"
"ds_read_b128 a[84:87], v228 offset:272\n"
"ds_read_b128 a[88:91], v228 offset:384\n"
"ds_read_b128 a[92:95], v228 offset:400\n"
"s_nop 1\n"
"v_cvt_pk_bf16_f32 v24, v24, v25\n"
"v_cvt_pk_bf16_f32 v25, v26, v27\n"
"v_cvt_pk_bf16_f32 v26, v28, v29\n"
"v_cvt_pk_bf16_f32 v27, v30, v31\n"
"v_accvgpr_write_b32 a204, v24\n"
"v_accvgpr_write_b32 a205, v25\n"
"v_accvgpr_write_b32 a206, v26\n"
"v_accvgpr_write_b32 a207, v27\n"
"v_cvt_pk_bf16_f32 v56, v56, v57\n"
"v_cvt_pk_bf16_f32 v57, v58, v59\n"
"v_cvt_pk_bf16_f32 v58, v60, v61\n"
"v_cvt_pk_bf16_f32 v59, v62, v63\n"
"v_accvgpr_write_b32 a220, v56\n"
"v_accvgpr_write_b32 a221, v57\n"
"v_accvgpr_write_b32 a222, v58\n"
"v_accvgpr_write_b32 a223, v59\n"
"v_cvt_pk_bf16_f32 v88, v88, v89\n"
"v_cvt_pk_bf16_f32 v89, v90, v91\n"
"v_cvt_pk_bf16_f32 v90, v92, v93\n"
"v_cvt_pk_bf16_f32 v91, v94, v95\n"
"v_accvgpr_write_b32 a236, v88\n"
"v_accvgpr_write_b32 a237, v89\n"
"v_accvgpr_write_b32 a238, v90\n"
"v_accvgpr_write_b32 a239, v91\n"
"v_cvt_pk_bf16_f32 v120, v120, v121\n"
"v_cvt_pk_bf16_f32 v121, v122, v123\n"
"v_cvt_pk_bf16_f32 v122, v124, v125\n"
"v_cvt_pk_bf16_f32 v123, v126, v127\n"
"v_accvgpr_write_b32 a252, v120\n"
"v_accvgpr_write_b32 a253, v121\n"
"v_accvgpr_write_b32 a254, v122\n"
"v_accvgpr_write_b32 a255, v123\n"
"s_mov_b32 s97, 20000\n"
"s_waitcnt vmcnt(24)\n"
"L_p1_poll:\n"
"v_cmp_ne_u32 vcc, s91, v192\n"
"s_nop 1\n"
"s_cbranch_vccz L_p1_done\n"
"s_sleep 2\n"
"buffer_load_dword v192, v225, s[60:63], s95 offen sc1\n"
"s_waitcnt vmcnt(0)\n"
"s_sub_u32 s97, s97, 1\n"
"s_cbranch_scc0 L_p1_poll\n"
"L_p1_done:\n"
"s_waitcnt lgkmcnt(0)\n"
"s_barrier\n"
"s_mul_i32 s72, %16, 4\n"
"s_mul_i32 s98, %9, 1\n"
"s_add_u32 s72, s72, s98\n"
"s_cmp_eq_u32 s72, 0\n"
"s_cbranch_scc0 L_p0_nb\n"
"v_mov_b32 v193, s91\n"
"s_mov_b32 s72, 57344\n"
"buffer_store_dword v193, off, s[60:63], s72 sc1\n"
"L_p0_nb:\n"
"v_mfma_f32_16x16x32_bf16 v[128:131], a[64:67], a[192:195], 0\n"
"s_lshl_b32 s72, s68, 10\n"
"v_add_u32 v48, s72, v230\n"
"v_mfma_f32_16x16x32_bf16 v[128:131], a[68:71], a[196:199], v[128:131]\n"
"v_add_u32 v231, 4608, v48\n"
"v_min_u32 v231, 13808, v231\n"
"v_mfma_f32_16x16x32_bf16 v[128:131], a[72:75], a[200:203], v[128:131]\n"
"v_add_u32 v232, 8704, v48\n"
"v_min_u32 v232, 13808, v232\n"
"v_mfma_f32_16x16x32_bf16 v[128:131], a[76:79], a[204:207], v[128:131]\n"
"v_add_u32 v234, 12800, v48\n"
"v_min_u32 v234, 13808, v234\n"
"v_mfma_f32_16x16x32_bf16 v[132:135], a[80:83], a[192:195], 0\n"
"v_mfma_f32_16x16x32_bf16 v[132:135], a[84:87], a[196:199], v[132:135]\n"
"v_mfma_f32_16x16x32_bf16 v[132:135], a[88:91], a[200:203], v[132:135]\n"
"v_mfma_f32_16x16x32_bf16 v[132:135], a[92:95], a[204:207], v[132:135]\n"
"v_mfma_f32_16x16x32_bf16 v[136:139], a[64:67], a[208:211], 0\n"
"v_mfma_f32_16x16x32_bf16 v[136:139], a[68:71], a[212:215], v[136:139]\n"
"v_mfma_f32_16x16x32_bf16 v[136:139], a[72:75], a[216:219], v[136:139]\n"
"v_mfma_f32_16x16x32_bf16 v[136:139], a[76:79], a[220:223], v[136:139]\n"
"v_mfma_f32_16x16x32_bf16 v[140:143], a[80:83], a[208:211], 0\n"
"v_mfma_f32_16x16x32_bf16 v[140:143], a[84:87], a[212:215], v[140:143]\n"
"v_mfma_f32_16x16x32_bf16 v[140:143], a[88:91], a[216:219], v[140:143]\n"
"v_mfma_f32_16x16x32_bf16 v[140:143], a[92:95], a[220:223], v[140:143]\n"
"v_mfma_f32_16x16x32_bf16 v[144:147], a[64:67], a[224:227], 0\n"
"v_mfma_f32_16x16x32_bf16 v[144:147], a[68:71], a[228:231], v[144:147]\n"
"v_mfma_f32_16x16x32_bf16 v[144:147], a[72:75], a[232:235], v[144:147]\n"
"v_mfma_f32_16x16x32_bf16 v[144:147], a[76:79], a[236:239], v[144:147]\n"
"v_mfma_f32_16x16x32_bf16 v[148:151], a[80:83], a[224:227], 0\n"
"v_mfma_f32_16x16x32_bf16 v[148:151], a[84:87], a[228:231], v[148:151]\n"
"v_mfma_f32_16x16x32_bf16 v[148:151], a[88:91], a[232:235], v[148:151]\n"
"v_mfma_f32_16x16x32_bf16 v[148:151], a[92:95], a[236:239], v[148:151]\n"
"v_mfma_f32_16x16x32_bf16 v[152:155], a[64:67], a[240:243], 0\n"
"v_mfma_f32_16x16x32_bf16 v[152:155], a[68:71], a[244:247], v[152:155]\n"
"v_mfma_f32_16x16x32_bf16 v[152:155], a[72:75], a[248:251], v[152:155]\n"
"v_mfma_f32_16x16x32_bf16 v[152:155], a[76:79], a[252:255], v[152:155]\n"
"v_mfma_f32_16x16x32_bf16 v[156:159], a[80:83], a[240:243], 0\n"
"v_mfma_f32_16x16x32_bf16 v[156:159], a[84:87], a[244:247], v[156:159]\n"
"v_mfma_f32_16x16x32_bf16 v[156:159], a[88:91], a[248:251], v[156:159]\n"
"v_mfma_f32_16x16x32_bf16 v[156:159], a[92:95], a[252:255], v[156:159]\n"
"s_nop 7\n"
"v_max3_f32 v209, v128, v129, v130\n"
"v_max3_f32 v209, v209, v131, v132\n"
"v_max3_f32 v209, v209, v133, v134\n"
"v_max_f32 v209, v209, v135\n"
"v_max3_f32 v213, v136, v137, v138\n"
"v_max3_f32 v213, v213, v139, v140\n"
"v_max3_f32 v213, v213, v141, v142\n"
"v_max_f32 v213, v213, v143\n"
"v_max3_f32 v217, v144, v145, v146\n"
"v_max3_f32 v217, v217, v147, v148\n"
"v_max3_f32 v217, v217, v149, v150\n"
"v_max_f32 v217, v217, v151\n"
"v_max3_f32 v221, v152, v153, v154\n"
"v_max3_f32 v221, v221, v155, v156\n"
"v_max3_f32 v221, v221, v157, v158\n"
"v_max_f32 v221, v221, v159\n"
"v_mov_b32 v210, v209\n"
"v_mov_b32 v214, v213\n"
"v_mov_b32 v218, v217\n"
"v_mov_b32 v222, v221\n"
"s_nop 1\n"
"v_permlane16_swap_b32 v210, v209\n"
"v_permlane16_swap_b32 v214, v213\n"
"v_permlane16_swap_b32 v218, v217\n"
"v_permlane16_swap_b32 v222, v221\n"
"s_nop 1\n"
"v_max_f32 v209, v209, v210\n"
"v_max_f32 v213, v213, v214\n"
"v_max_f32 v217, v217, v218\n"
"v_max_f32 v221, v221, v222\n"
"v_mov_b32 v210, v209\n"
"v_mov_b32 v214, v213\n"
"v_mov_b32 v218, v217\n"
"v_mov_b32 v222, v221\n"
"s_nop 1\n"
"v_permlane32_swap_b32 v210, v209\n"
"v_permlane32_swap_b32 v214, v213\n"
"v_permlane32_swap_b32 v218, v217\n"
"v_permlane32_swap_b32 v222, v221\n"
"s_nop 1\n"
"v_max_f32 v209, v209, v210\n"
"v_max_f32 v213, v213, v214\n"
"v_max_f32 v217, v217, v218\n"
"v_max_f32 v221, v221, v222\n"
"v_sub_f32 v128, v128, v209\n"
"v_sub_f32 v129, v129, v209\n"
"v_sub_f32 v130, v130, v209\n"
"v_sub_f32 v131, v131, v209\n"
"v_sub_f32 v132, v132, v209\n"
"v_sub_f32 v133, v133, v209\n"
"v_sub_f32 v134, v134, v209\n"
"v_sub_f32 v135, v135, v209\n"
"v_sub_f32 v192, 0, v209\n"
"v_sub_f32 v193, 0, v209\n"
"v_sub_f32 v194, 0, v209\n"
"v_sub_f32 v195, 0, v209\n"
"v_sub_f32 v136, v136, v213\n"
"v_sub_f32 v137, v137, v213\n"
"v_sub_f32 v138, v138, v213\n"
"v_sub_f32 v139, v139, v213\n"
"v_sub_f32 v140, v140, v213\n"
"v_sub_f32 v141, v141, v213\n"
"v_sub_f32 v142, v142, v213\n"
"v_sub_f32 v143, v143, v213\n"
"v_sub_f32 v196, 0, v213\n"
"v_sub_f32 v197, 0, v213\n"
"v_sub_f32 v198, 0, v213\n"
"v_sub_f32 v199, 0, v213\n"
"v_sub_f32 v144, v144, v217\n"
"v_sub_f32 v145, v145, v217\n"
"v_sub_f32 v146, v146, v217\n"
"v_sub_f32 v147, v147, v217\n"
"v_sub_f32 v148, v148, v217\n"
"v_sub_f32 v149, v149, v217\n"
"v_sub_f32 v150, v150, v217\n"
"v_sub_f32 v151, v151, v217\n"
"v_sub_f32 v200, 0, v217\n"
"v_sub_f32 v201, 0, v217\n"
"v_sub_f32 v202, 0, v217\n"
"v_sub_f32 v203, 0, v217\n"
"v_sub_f32 v152, v152, v221\n"
"v_sub_f32 v153, v153, v221\n"
"v_sub_f32 v154, v154, v221\n"
"v_sub_f32 v155, v155, v221\n"
"v_sub_f32 v156, v156, v221\n"
"v_sub_f32 v157, v157, v221\n"
"v_sub_f32 v158, v158, v221\n"
"v_sub_f32 v159, v159, v221\n"
"v_sub_f32 v204, 0, v221\n"
"v_sub_f32 v205, 0, v221\n"
"v_sub_f32 v206, 0, v221\n"
"v_sub_f32 v207, 0, v221\n"
"v_mov_b32 v208, 0\n"
"v_mov_b32 v209, 0\n"
"v_mov_b32 v210, 0\n"
"v_mov_b32 v211, 0\n"
"v_mov_b32 v212, 0\n"
"v_mov_b32 v213, 0\n"
"v_mov_b32 v214, 0\n"
"v_mov_b32 v215, 0\n"
"v_mov_b32 v216, 0\n"
"v_mov_b32 v217, 0\n"
"v_mov_b32 v218, 0\n"
"v_mov_b32 v219, 0\n"
"v_mov_b32 v220, 0\n"
"v_mov_b32 v221, 0\n"
"v_mov_b32 v222, 0\n"
"v_mov_b32 v223, 0\n"
"v_exp_f32 v128, v128\n"
"v_exp_f32 v129, v129\n"
"v_exp_f32 v130, v130\n"
"v_exp_f32 v131, v131\n"
"v_exp_f32 v132, v132\n"
"v_exp_f32 v133, v133\n"
"v_exp_f32 v134, v134\n"
"v_exp_f32 v135, v135\n"
"s_nop 0\n"
"v_cvt_pk_bf16_f32 v128, v128, v129\n"
"v_cvt_pk_bf16_f32 v129, v130, v131\n"
"v_cvt_pk_bf16_f32 v130, v132, v133\n"
"v_cvt_pk_bf16_f32 v131, v134, v135\n"
"s_nop 0\n"
"v_exp_f32 v136, v136\n"
"v_mfma_f32_16x16x32_bf16 v[208:211], a[160:163], v[128:131], v[208:211]\n"
"v_exp_f32 v137, v137\n"
"s_nop 0\n"
"ds_read_b128 a[96:99], v228 offset:512\n"
"v_add_f32 v212, v212, v136\n"
"s_nop 0\n"
"v_exp_f32 v138, v138\n"
"s_nop 0\n"
"ds_read_b128 a[100:103], v228 offset:528\n"
"v_add_f32 v212, v212, v137\n"
"s_nop 0\n"
"ds_read_b128 a[104:107], v228 offset:640\n"
"v_cvt_pk_bf16_f32 v136, v136, v137\n"
"s_nop 0\n"
"s_add_u32 m0, s76, 49920\n"
"v_exp_f32 v139, v139\n"
"s_nop 0\n"
"ds_read_b128 a[108:111], v228 offset:656\n"
"v_add_f32 v212, v212, v138\n"
"s_nop 0\n"
"v_exp_f32 v140, v140\n"
"s_nop 0\n"
"ds_read_b128 a[112:115], v228 offset:768\n"
"v_add_f32 v212, v212, v139\n"
"s_nop 0\n"
"buffer_load_dwordx4 v230, s[44:47], s78 offen offset:0 lds\n"
"s_nop 0\n"
"ds_read_b128 a[116:119], v228 offset:784\n"
"v_cvt_pk_bf16_f32 v137, v138, v139\n"
"s_nop 0\n"
"v_exp_f32 v141, v141\n"
"s_nop 0\n"
"ds_read_b128 a[120:123], v228 offset:896\n"
"v_add_f32 v212, v212, v140\n"
"s_nop 0\n"
"v_exp_f32 v142, v142\n"
"s_nop 0\n"
"ds_read_b128 a[124:127], v228 offset:912\n"
"v_add_f32 v212, v212, v141\n"
"s_nop 0\n"
"ds_read_b64_tr_b16 a[128:129], v229 offset:0\n"
"v_cvt_pk_bf16_f32 v138, v140, v141\n"
"s_nop 0\n"
"v_exp_f32 v143, v143\n"
"s_nop 0\n"
"ds_read_b64_tr_b16 a[130:131], v229 offset:256\n"
"v_add_f32 v212, v212, v142\n"
"s_nop 0\n"
"ds_read_b64_tr_b16 a[132:133], v229 offset:32\n"
"v_add_f32 v212, v212, v143\n"
"s_nop 0\n"
"ds_read_b64_tr_b16 a[134:135], v229 offset:288\n"
"v_cvt_pk_bf16_f32 v139, v142, v143\n"
"s_nop 0\n"
"v_exp_f32 v144, v144\n"
"s_nop 0\n"
"s_add_u32 m0, s76, 49936\n"
"v_exp_f32 v145, v145\n"
"s_nop 0\n"
"v_exp_f32 v146, v146\n"
"s_nop 0\n"
"ds_read_b64_tr_b16 a[136:137], v229 offset:64\n"
"v_cvt_pk_bf16_f32 v144, v144, v145\n"
"s_nop 0\n"
"v_exp_f32 v147, v147\n"
"s_nop 0\n"
"buffer_load_dwordx4 v230, s[44:47], s78 offen offset:1024 lds\n"
"s_nop 0\n"
"v_exp_f32 v148, v148\n"
"s_nop 0\n"
"ds_read_b64_tr_b16 a[138:139], v229 offset:320\n"
"v_cvt_pk_bf16_f32 v145, v146, v147\n"
"s_nop 0\n"
"v_exp_f32 v149, v149\n"
"s_nop 0\n"
"v_exp_f32 v150, v150\n"
"s_nop 0\n"
"ds_read_b64_tr_b16 a[140:141], v229 offset:96\n"
"v_cvt_pk_bf16_f32 v146, v148, v149\n"
"s_nop 0\n"
"s_waitcnt lgkmcnt(7)\n"
"v_exp_f32 v151, v151\n"
"v_mfma_f32_16x16x32_bf16 v[160:163], a[96:99], a[192:195], v[192:195]\n"
"ds_read_b64_tr_b16 a[142:143], v229 offset:352\n"
"v_cvt_pk_bf16_f32 v147, v150, v151\n"
"v_mfma_f32_16x16x32_bf16 v[160:163], a[100:103], a[196:199], v[160:163]\n"
"v_exp_f32 v152, v152\n"
"v_mfma_f32_16x16x32_bf16 v[160:163], a[104:107], a[200:203], v[160:163]\n"
"v_exp_f32 v153, v153\n"
"v_mfma_f32_16x16x32_bf16 v[160:163], a[108:111], a[204:207], v[160:163]\n"
"v_exp_f32 v154, v154\n"
"v_mfma_f32_16x16x32_bf16 v[216:219], a[160:163], v[144:147], v[216:219]\n"
"ds_read_b64_tr_b16 a[144:145], v229 offset:128\n"
"v_cvt_pk_bf16_f32 v152, v152, v153\n"
"v_mfma_f32_16x16x32_bf16 v[164:167], a[112:115], a[192:195], v[192:195]\n"
"s_add_u32 m0, s76, 49952\n"
"v_exp_f32 v155, v155\n"
"v_mfma_f32_16x16x32_bf16 v[164:167], a[116:119], a[196:199], v[164:167]\n"
"v_exp_f32 v156, v156\n"
"v_mfma_f32_16x16x32_bf16 v[164:167], a[120:123], a[200:203], v[164:167]\n"
"ds_read_b64_tr_b16 a[146:147], v229 offset:384\n"
"v_cvt_pk_bf16_f32 v153, v154, v155\n"
"v_mfma_f32_16x16x32_bf16 v[164:167], a[124:127], a[204:207], v[164:167]\n"
"v_exp_f32 v157, v157\n"
"v_mfma_f32_16x16x32_bf16 v[168:171], a[96:99], a[208:211], v[196:199]\n"
"buffer_load_dwordx4 v230, s[44:47], s78 offen offset:2048 lds\n"
"v_mfma_f32_16x16x32_bf16 v[168:171], a[100:103], a[212:215], v[168:171]\n"
"v_exp_f32 v158, v158\n"
"v_mfma_f32_16x16x32_bf16 v[168:171], a[104:107], a[216:219], v[168:171]\n"
"ds_read_b64_tr_b16 a[148:149], v229 offset:160\n"
"v_cvt_pk_bf16_f32 v154, v156, v157\n"
"v_mfma_f32_16x16x32_bf16 v[168:171], a[108:111], a[220:223], v[168:171]\n"
"v_exp_f32 v159, v159\n"
"v_mfma_f32_16x16x32_bf16 v[172:175], a[112:115], a[208:211], v[196:199]\n"
"ds_read_b64_tr_b16 a[150:151], v229 offset:416\n"
"v_cvt_pk_bf16_f32 v155, v158, v159\n"
"v_mfma_f32_16x16x32_bf16 v[172:175], a[116:119], a[212:215], v[172:175]\n"
"v_exp_f32 v160, v160\n"
"v_mfma_f32_16x16x32_bf16 v[172:175], a[120:123], a[216:219], v[172:175]\n"
"v_exp_f32 v161, v161\n"
"v_mfma_f32_16x16x32_bf16 v[172:175], a[124:127], a[220:223], v[172:175]\n"
"v_exp_f32 v162, v162\n"
"v_mfma_f32_16x16x32_bf16 v[220:223], a[160:163], v[152:155], v[220:223]\n"
"ds_read_b64_tr_b16 a[152:153], v229 offset:192\n"
"v_cvt_pk_bf16_f32 v160, v160, v161\n"
"v_mfma_f32_16x16x32_bf16 v[176:179], a[96:99], a[224:227], v[200:203]\n"
"v_exp_f32 v163, v163\n"
"v_mfma_f32_16x16x32_bf16 v[176:179], a[100:103], a[228:231], v[176:179]\n"
"v_exp_f32 v164, v164\n"
"v_mfma_f32_16x16x32_bf16 v[176:179], a[104:107], a[232:235], v[176:179]\n"
"ds_read_b64_tr_b16 a[154:155], v229 offset:448\n"
"v_cvt_pk_bf16_f32 v161, v162, v163\n"
"v_mfma_f32_16x16x32_bf16 v[176:179], a[108:111], a[236:239], v[176:179]\n"
"s_add_u32 m0, s76, 49968\n"
"v_exp_f32 v165, v165\n"
"v_mfma_f32_16x16x32_bf16 v[180:183], a[112:115], a[224:227], v[200:203]\n"
"v_exp_f32 v166, v166\n"
"v_mfma_f32_16x16x32_bf16 v[180:183], a[116:119], a[228:231], v[180:183]\n"
"ds_read_b64_tr_b16 a[156:157], v229 offset:224\n"
"v_cvt_pk_bf16_f32 v162, v164, v165\n"
"v_mfma_f32_16x16x32_bf16 v[180:183], a[120:123], a[232:235], v[180:183]\n"
"v_exp_f32 v167, v167\n"
"v_mfma_f32_16x16x32_bf16 v[180:183], a[124:127], a[236:239], v[180:183]\n"
"buffer_load_dwordx4 v230, s[44:47], s78 offen offset:3072 lds\n"
"v_mfma_f32_16x16x32_bf16 v[184:187], a[96:99], a[240:243], v[204:207]\n"
"s_add_u32 s78, s78, 16384\n"
"ds_read_b64_tr_b16 a[158:159], v229 offset:480\n"
"v_cvt_pk_bf16_f32 v163, v166, v167\n"
"v_mfma_f32_16x16x32_bf16 v[184:187], a[100:103], a[244:247], v[184:187]\n"
"v_mfma_f32_16x16x32_bf16 v[184:187], a[104:107], a[248:251], v[184:187]\n"
"v_mfma_f32_16x16x32_bf16 v[184:187], a[108:111], a[252:255], v[184:187]\n"
"v_mfma_f32_16x16x32_bf16 v[188:191], a[112:115], a[240:243], v[204:207]\n"
"v_mfma_f32_16x16x32_bf16 v[188:191], a[116:119], a[244:247], v[188:191]\n"
"v_mfma_f32_16x16x32_bf16 v[188:191], a[120:123], a[248:251], v[188:191]\n"
"v_mfma_f32_16x16x32_bf16 v[188:191], a[124:127], a[252:255], v[188:191]\n"
"s_waitcnt vmcnt(8) lgkmcnt(0)\n"
"s_barrier\n"
"v_mfma_f32_16x16x32_bf16 v[0:3], a[128:131], v[128:131], 0\n"
"v_exp_f32 v168, v168\n"
"v_mfma_f32_16x16x32_bf16 v[208:211], a[160:163], v[160:163], v[208:211]\n"
"v_exp_f32 v169, v169\n"
"v_mfma_f32_16x16x32_bf16 v[32:35], a[128:131], v[136:139], 0\n"
"ds_read_b128 a[64:67], v228 offset:16640\n"
"v_add_f32 v212, v212, v168\n"
"v_mfma_f32_16x16x32_bf16 v[64:67], a[128:131], v[144:147], 0\n"
"v_exp_f32 v170, v170\n"
"v_mfma_f32_16x16x32_bf16 v[96:99], a[128:131], v[152:155], 0\n"
"ds_read_b128 a[68:71], v228 offset:16656\n"
"v_add_f32 v212, v212, v169\n"
"v_mfma_f32_16x16x32_bf16 v[4:7], a[132:135], v[128:131], 0\n"
"ds_read_b128 a[72:75], v228 offset:16768\n"
"v_cvt_pk_bf16_f32 v168, v168, v169\n"
"v_mfma_f32_16x16x32_bf16 v[36:39], a[132:135], v[136:139], 0\n"
"s_add_u32 m0, s77, 50688\n"
"v_exp_f32 v171, v171\n"
"v_mfma_f32_16x16x32_bf16 v[68:71], a[132:135], v[144:147], 0\n"
"ds_read_b128 a[76:79], v228 offset:16784\n"
"v_add_f32 v212, v212, v170\n"
"v_mfma_f32_16x16x32_bf16 v[100:103], a[132:135], v[152:155], 0\n"
"v_exp_f32 v172, v172\n"
"v_mfma_f32_16x16x32_bf16 v[8:11], a[136:139], v[128:131], 0\n"
"ds_read_b128 a[80:83], v228 offset:16896\n"
"v_add_f32 v212, v212, v171\n"
"v_mfma_f32_16x16x32_bf16 v[40:43], a[136:139], v[136:139], 0\n"
"buffer_load_dwordx4 v230, s[48:51], s79 offen offset:0 lds\n"
"v_mfma_f32_16x16x32_bf16 v[72:75], a[136:139], v[144:147], 0\n"
"ds_read_b128 a[84:87], v228 offset:16912\n"
"v_cvt_pk_bf16_f32 v169, v170, v171\n"
"v_mfma_f32_16x16x32_bf16 v[104:107], a[136:139], v[152:155], 0\n"
"v_exp_f32 v173, v173\n"
"v_mfma_f32_16x16x32_bf16 v[12:15], a[140:143], v[128:131], 0\n"
"ds_read_b128 a[88:91], v228 offset:17024\n"
"v_add_f32 v212, v212, v172\n"
"v_mfma_f32_16x16x32_bf16 v[44:47], a[140:143], v[136:139], 0\n"
"v_exp_f32 v174, v174\n"
"v_mfma_f32_16x16x32_bf16 v[76:79], a[140:143], v[144:147], 0\n"
"ds_read_b128 a[92:95], v228 offset:17040\n"
"v_add_f32 v212, v212, v173\n"
"v_mfma_f32_16x16x32_bf16 v[108:111], a[140:143], v[152:155], 0\n"
"ds_read_b64_tr_b16 a[128:129], v229 offset:512\n"
"v_cvt_pk_bf16_f32 v170, v172, v173\n"
"v_mfma_f32_16x16x32_bf16 v[16:19], a[144:147], v[128:131], 0\n"
"v_exp_f32 v175, v175\n"
"v_mfma_f32_16x16x32_bf16 v[48:51], a[144:147], v[136:139], 0\n"
"ds_read_b64_tr_b16 a[130:131], v229 offset:768\n"
"v_add_f32 v212, v212, v174\n"
"v_mfma_f32_16x16x32_bf16 v[80:83], a[144:147], v[144:147], 0\n"
"ds_read_b64_tr_b16 a[132:133], v229 offset:544\n"
"v_add_f32 v212, v212, v175\n"
"v_mfma_f32_16x16x32_bf16 v[112:115], a[144:147], v[152:155], 0\n"
"ds_read_b64_tr_b16 a[134:135], v229 offset:800\n"
"v_cvt_pk_bf16_f32 v171, v174, v175\n"
"v_mfma_f32_16x16x32_bf16 v[20:23], a[148:151], v[128:131], 0\n"
"v_exp_f32 v176, v176\n"
"v_mfma_f32_16x16x32_bf16 v[52:55], a[148:151], v[136:139], 0\n"
"s_add_u32 m0, s77, 50720\n"
"v_exp_f32 v177, v177\n"
"v_mfma_f32_16x16x32_bf16 v[84:87], a[148:151], v[144:147], 0\n"
"v_exp_f32 v178, v178\n"
"v_mfma_f32_16x16x32_bf16 v[116:119], a[148:151], v[152:155], 0\n"
"ds_read_b64_tr_b16 a[136:137], v229 offset:576\n"
"v_cvt_pk_bf16_f32 v176, v176, v177\n"
"v_mfma_f32_16x16x32_bf16 v[24:27], a[152:155], v[128:131], 0\n"
"v_exp_f32 v179, v179\n"
"v_mfma_f32_16x16x32_bf16 v[56:59], a[152:155], v[136:139], 0\n"
"buffer_load_dwordx4 v230, s[48:51], s79 offen offset:1024 lds\n"
"v_mfma_f32_16x16x32_bf16 v[88:91], a[152:155], v[144:147], 0\n"
"v_exp_f32 v180, v180\n"
"v_mfma_f32_16x16x32_bf16 v[120:123], a[152:155], v[152:155], 0\n"
"ds_read_b64_tr_b16 a[138:139], v229 offset:832\n"
"v_cvt_pk_bf16_f32 v177, v178, v179\n"
"v_mfma_f32_16x16x32_bf16 v[28:31], a[156:159], v[128:131], 0\n"
"v_exp_f32 v181, v181\n"
"v_mfma_f32_16x16x32_bf16 v[60:63], a[156:159], v[136:139], 0\n"
"v_exp_f32 v182, v182\n"
"v_mfma_f32_16x16x32_bf16 v[92:95], a[156:159], v[144:147], 0\n"
"ds_read_b64_tr_b16 a[140:141], v229 offset:608\n"
"v_cvt_pk_bf16_f32 v178, v180, v181\n"
"v_mfma_f32_16x16x32_bf16 v[124:127], a[156:159], v[152:155], 0\n"
"s_waitcnt lgkmcnt(7)\n"
"v_exp_f32 v183, v183\n"
"v_mfma_f32_16x16x32_bf16 v[128:131], a[64:67], a[192:195], v[192:195]\n"
"ds_read_b64_tr_b16 a[142:143], v229 offset:864\n"
"v_cvt_pk_bf16_f32 v179, v182, v183\n"
"v_mfma_f32_16x16x32_bf16 v[128:131], a[68:71], a[196:199], v[128:131]\n"
"v_exp_f32 v184, v184\n"
"v_mfma_f32_16x16x32_bf16 v[128:131], a[72:75], a[200:203], v[128:131]\n"
"v_exp_f32 v185, v185\n"
"v_mfma_f32_16x16x32_bf16 v[128:131], a[76:79], a[204:207], v[128:131]\n"
"v_exp_f32 v186, v186\n"
"v_mfma_f32_16x16x32_bf16 v[216:219], a[160:163], v[176:179], v[216:219]\n"
"ds_read_b64_tr_b16 a[144:145], v229 offset:640\n"
"v_cvt_pk_bf16_f32 v184, v184, v185\n"
"v_mfma_f32_16x16x32_bf16 v[132:135], a[80:83], a[192:195], v[192:195]\n"
"s_add_u32 m0, s77, 50752\n"
"v_exp_f32 v187, v187\n"
"v_mfma_f32_16x16x32_bf16 v[132:135], a[84:87], a[196:199], v[132:135]\n"
"v_exp_f32 v188, v188\n"
"v_mfma_f32_16x16x32_bf16 v[132:135], a[88:91], a[200:203], v[132:135]\n"
"ds_read_b64_tr_b16 a[146:147], v229 offset:896\n"
"v_cvt_pk_bf16_f32 v185, v186, v187\n"
"v_mfma_f32_16x16x32_bf16 v[132:135], a[92:95], a[204:207], v[132:135]\n"
"v_exp_f32 v189, v189\n"
"v_mfma_f32_16x16x32_bf16 v[136:139], a[64:67], a[208:211], v[196:199]\n"
"buffer_load_dwordx4 v230, s[48:51], s79 offen offset:2048 lds\n"
"v_mfma_f32_16x16x32_bf16 v[136:139], a[68:71], a[212:215], v[136:139]\n"
"v_exp_f32 v190, v190\n"
"v_mfma_f32_16x16x32_bf16 v[136:139], a[72:75], a[216:219], v[136:139]\n"
"ds_read_b64_tr_b16 a[148:149], v229 offset:672\n"
"v_cvt_pk_bf16_f32 v186, v188, v189\n"
"v_mfma_f32_16x16x32_bf16 v[136:139], a[76:79], a[220:223], v[136:139]\n"
"v_exp_f32 v191, v191\n"
"v_mfma_f32_16x16x32_bf16 v[140:143], a[80:83], a[208:211], v[196:199]\n"
"ds_read_b64_tr_b16 a[150:151], v229 offset:928\n"
"v_cvt_pk_bf16_f32 v187, v190, v191\n"
"v_mfma_f32_16x16x32_bf16 v[140:143], a[84:87], a[212:215], v[140:143]\n"
"v_exp_f32 v128, v128\n"
"v_mfma_f32_16x16x32_bf16 v[140:143], a[88:91], a[216:219], v[140:143]\n"
"v_exp_f32 v129, v129\n"
"v_mfma_f32_16x16x32_bf16 v[140:143], a[92:95], a[220:223], v[140:143]\n"
"v_exp_f32 v130, v130\n"
"v_mfma_f32_16x16x32_bf16 v[220:223], a[160:163], v[184:187], v[220:223]\n"
"ds_read_b64_tr_b16 a[152:153], v229 offset:704\n"
"v_cvt_pk_bf16_f32 v128, v128, v129\n"
"v_mfma_f32_16x16x32_bf16 v[144:147], a[64:67], a[224:227], v[200:203]\n"
"v_exp_f32 v131, v131\n"
"v_mfma_f32_16x16x32_bf16 v[144:147], a[68:71], a[228:231], v[144:147]\n"
"v_exp_f32 v132, v132\n"
"v_mfma_f32_16x16x32_bf16 v[144:147], a[72:75], a[232:235], v[144:147]\n"
"ds_read_b64_tr_b16 a[154:155], v229 offset:960\n"
"v_cvt_pk_bf16_f32 v129, v130, v131\n"
"v_mfma_f32_16x16x32_bf16 v[144:147], a[76:79], a[236:239], v[144:147]\n"
"s_add_u32 m0, s77, 50784\n"
"v_exp_f32 v133, v133\n"
"v_mfma_f32_16x16x32_bf16 v[148:151], a[80:83], a[224:227], v[200:203]\n"
"v_exp_f32 v134, v134\n"
"v_mfma_f32_16x16x32_bf16 v[148:151], a[84:87], a[228:231], v[148:151]\n"
"ds_read_b64_tr_b16 a[156:157], v229 offset:736\n"
"v_cvt_pk_bf16_f32 v130, v132, v133\n"
"v_mfma_f32_16x16x32_bf16 v[148:151], a[88:91], a[232:235], v[148:151]\n"
"v_exp_f32 v135, v135\n"
"v_mfma_f32_16x16x32_bf16 v[148:151], a[92:95], a[236:239], v[148:151]\n"
"buffer_load_dwordx4 v230, s[48:51], s79 offen offset:3072 lds\n"
"v_mfma_f32_16x16x32_bf16 v[152:155], a[64:67], a[240:243], v[204:207]\n"
"s_add_u32 s79, s79, 16384\n"
"ds_read_b64_tr_b16 a[158:159], v229 offset:992\n"
"v_cvt_pk_bf16_f32 v131, v134, v135\n"
"v_mfma_f32_16x16x32_bf16 v[152:155], a[68:71], a[244:247], v[152:155]\n"
"v_mfma_f32_16x16x32_bf16 v[152:155], a[72:75], a[248:251], v[152:155]\n"
"v_mfma_f32_16x16x32_bf16 v[152:155], a[76:79], a[252:255], v[152:155]\n"
"v_mfma_f32_16x16x32_bf16 v[156:159], a[80:83], a[240:243], v[204:207]\n"
"v_mfma_f32_16x16x32_bf16 v[156:159], a[84:87], a[244:247], v[156:159]\n"
"v_mfma_f32_16x16x32_bf16 v[156:159], a[88:91], a[248:251], v[156:159]\n"
"v_mfma_f32_16x16x32_bf16 v[156:159], a[92:95], a[252:255], v[156:159]\n"
"s_branch L_at_b1\n"
"L_at_b0:\n"
"s_waitcnt vmcnt(12) lgkmcnt(0)\n"
"s_barrier\n"
"v_mfma_f32_16x16x32_bf16 v[0:3], a[128:131], v[128:131], v[0:3]\n"
"v_exp_f32 v168, v168\n"
"v_mfma_f32_16x16x32_bf16 v[208:211], a[160:163], v[160:163], v[208:211]\n"
"v_exp_f32 v169, v169\n"
"v_mfma_f32_16x16x32_bf16 v[32:35], a[128:131], v[136:139], v[32:35]\n"
"ds_read_b128 a[64:67], v228 offset:16640\n"
"v_add_f32 v212, v212, v168\n"
"v_mfma_f32_16x16x32_bf16 v[64:67], a[128:131], v[144:147], v[64:67]\n"
"v_exp_f32 v170, v170\n"
"v_mfma_f32_16x16x32_bf16 v[96:99], a[128:131], v[152:155], v[96:99]\n"
"ds_read_b128 a[68:71], v228 offset:16656\n"
"v_add_f32 v212, v212, v169\n"
"v_mfma_f32_16x16x32_bf16 v[4:7], a[132:135], v[128:131], v[4:7]\n"
"ds_read_b128 a[72:75], v228 offset:16768\n"
"v_cvt_pk_bf16_f32 v168, v168, v169\n"
"v_mfma_f32_16x16x32_bf16 v[36:39], a[132:135], v[136:139], v[36:39]\n"
"s_add_u32 m0, s77, 50688\n"
"v_exp_f32 v171, v171\n"
"v_mfma_f32_16x16x32_bf16 v[68:71], a[132:135], v[144:147], v[68:71]\n"
"ds_read_b128 a[76:79], v228 offset:16784\n"
"v_add_f32 v212, v212, v170\n"
"v_mfma_f32_16x16x32_bf16 v[100:103], a[132:135], v[152:155], v[100:103]\n"
"v_exp_f32 v172, v172\n"
"v_mfma_f32_16x16x32_bf16 v[8:11], a[136:139], v[128:131], v[8:11]\n"
"ds_read_b128 a[80:83], v228 offset:16896\n"
"v_add_f32 v212, v212, v171\n"
"v_mfma_f32_16x16x32_bf16 v[40:43], a[136:139], v[136:139], v[40:43]\n"
"buffer_load_dwordx4 v230, s[48:51], s79 offen offset:0 lds\n"
"v_mfma_f32_16x16x32_bf16 v[72:75], a[136:139], v[144:147], v[72:75]\n"
"ds_read_b128 a[84:87], v228 offset:16912\n"
"v_cvt_pk_bf16_f32 v169, v170, v171\n"
"v_mfma_f32_16x16x32_bf16 v[104:107], a[136:139], v[152:155], v[104:107]\n"
"v_exp_f32 v173, v173\n"
"v_mfma_f32_16x16x32_bf16 v[12:15], a[140:143], v[128:131], v[12:15]\n"
"ds_read_b128 a[88:91], v228 offset:17024\n"
"v_add_f32 v212, v212, v172\n"
"v_mfma_f32_16x16x32_bf16 v[44:47], a[140:143], v[136:139], v[44:47]\n"
"v_exp_f32 v174, v174\n"
"v_mfma_f32_16x16x32_bf16 v[76:79], a[140:143], v[144:147], v[76:79]\n"
"ds_read_b128 a[92:95], v228 offset:17040\n"
"v_add_f32 v212, v212, v173\n"
"v_mfma_f32_16x16x32_bf16 v[108:111], a[140:143], v[152:155], v[108:111]\n"
"ds_read_b64_tr_b16 a[128:129], v229 offset:512\n"
"v_cvt_pk_bf16_f32 v170, v172, v173\n"
"v_mfma_f32_16x16x32_bf16 v[16:19], a[144:147], v[128:131], v[16:19]\n"
"L_tc0_3:\n"
"s_cbranch_execz L_tp0_3_1\n"
"v_exp_f32 v175, v175\n"
"v_mfma_f32_16x16x32_bf16 v[48:51], a[144:147], v[136:139], v[48:51]\n"
"ds_read_b64_tr_b16 a[130:131], v229 offset:768\n"
"v_add_f32 v212, v212, v174\n"
"v_mfma_f32_16x16x32_bf16 v[80:83], a[144:147], v[144:147], v[80:83]\n"
"ds_read_b64_tr_b16 a[132:133], v229 offset:544\n"
"v_add_f32 v212, v212, v175\n"
"v_mfma_f32_16x16x32_bf16 v[112:115], a[144:147], v[152:155], v[112:115]\n"
"ds_read_b64_tr_b16 a[134:135], v229 offset:800\n"
"v_cvt_pk_bf16_f32 v171, v174, v175\n"
"v_mfma_f32_16x16x32_bf16 v[20:23], a[148:151], v[128:131], v[20:23]\n"
"v_exp_f32 v176, v176\n"
"v_mfma_f32_16x16x32_bf16 v[52:55], a[148:151], v[136:139], v[52:55]\n"
"s_add_u32 m0, s77, 50720\n"
"v_exp_f32 v177, v177\n"
"v_mfma_f32_16x16x32_bf16 v[84:87], a[148:151], v[144:147], v[84:87]\n"
"v_exp_f32 v178, v178\n"
"v_mfma_f32_16x16x32_bf16 v[116:119], a[148:151], v[152:155], v[116:119]\n"
"ds_read_b64_tr_b16 a[136:137], v229 offset:576\n"
"v_cvt_pk_bf16_f32 v176, v176, v177\n"
"v_mfma_f32_16x16x32_bf16 v[24:27], a[152:155], v[128:131], v[24:27]\n"
"v_exp_f32 v179, v179\n"
"v_mfma_f32_16x16x32_bf16 v[56:59], a[152:155], v[136:139], v[56:59]\n"
"buffer_load_dwordx4 v230, s[48:51], s79 offen offset:1024 lds\n"
"v_mfma_f32_16x16x32_bf16 v[88:91], a[152:155], v[144:147], v[88:91]\n"
"v_exp_f32 v180, v180\n"
"v_mfma_f32_16x16x32_bf16 v[120:123], a[152:155], v[152:155], v[120:123]\n"
"ds_read_b64_tr_b16 a[138:139], v229 offset:832\n"
"v_cvt_pk_bf16_f32 v177, v178, v179\n"
"v_mfma_f32_16x16x32_bf16 v[28:31], a[156:159], v[128:131], v[28:31]\n"
"v_exp_f32 v181, v181\n"
"v_mfma_f32_16x16x32_bf16 v[60:63], a[156:159], v[136:139], v[60:63]\n"
"v_exp_f32 v182, v182\n"
"v_mfma_f32_16x16x32_bf16 v[92:95], a[156:159], v[144:147], v[92:95]\n"
"ds_read_b64_tr_b16 a[140:141], v229 offset:608\n"
"v_cvt_pk_bf16_f32 v178, v180, v181\n"
"v_mfma_f32_16x16x32_bf16 v[124:127], a[156:159], v[152:155], v[124:127]\n"
"s_waitcnt lgkmcnt(7)\n"
"v_exp_f32 v183, v183\n"
"v_mfma_f32_16x16x32_bf16 v[128:131], a[64:67], a[192:195], v[192:195]\n"
"ds_read_b64_tr_b16 a[142:143], v229 offset:864\n"
"v_cvt_pk_bf16_f32 v179, v182, v183\n"
"v_mfma_f32_16x16x32_bf16 v[128:131], a[68:71], a[196:199], v[128:131]\n"
"v_exp_f32 v184, v184\n"
"v_mfma_f32_16x16x32_bf16 v[128:131], a[72:75], a[200:203], v[128:131]\n"
"v_exp_f32 v185, v185\n"
"v_mfma_f32_16x16x32_bf16 v[128:131], a[76:79], a[204:207], v[128:131]\n"
"v_exp_f32 v186, v186\n"
"v_mfma_f32_16x16x32_bf16 v[216:219], a[160:163], v[176:179], v[216:219]\n"
"ds_read_b64_tr_b16 a[144:145], v229 offset:640\n"
"v_cvt_pk_bf16_f32 v184, v184, v185\n"
"v_mfma_f32_16x16x32_bf16 v[132:135], a[80:83], a[192:195], v[192:195]\n"
"s_add_u32 m0, s77, 50752\n"
"v_exp_f32 v187, v187\n"
"v_mfma_f32_16x16x32_bf16 v[132:135], a[84:87], a[196:199], v[132:135]\n"
"v_exp_f32 v188, v188\n"
"v_mfma_f32_16x16x32_bf16 v[132:135], a[88:91], a[200:203], v[132:135]\n"
"ds_read_b64_tr_b16 a[146:147], v229 offset:896\n"
"v_cvt_pk_bf16_f32 v185, v186, v187\n"
"v_mfma_f32_16x16x32_bf16 v[132:135], a[92:95], a[204:207], v[132:135]\n"
"v_exp_f32 v189, v189\n"
"v_mfma_f32_16x16x32_bf16 v[136:139], a[64:67], a[208:211], v[196:199]\n"
"buffer_load_dwordx4 v230, s[48:51], s79 offen offset:2048 lds\n"
"v_mfma_f32_16x16x32_bf16 v[136:139], a[68:71], a[212:215], v[136:139]\n"
"v_exp_f32 v190, v190\n"
"v_mfma_f32_16x16x32_bf16 v[136:139], a[72:75], a[216:219], v[136:139]\n"
"ds_read_b64_tr_b16 a[148:149], v229 offset:672\n"
"v_cvt_pk_bf16_f32 v186, v188, v189\n"
"v_mfma_f32_16x16x32_bf16 v[136:139], a[76:79], a[220:223], v[136:139]\n"
"v_exp_f32 v191, v191\n"
"v_mfma_f32_16x16x32_bf16 v[140:143], a[80:83], a[208:211], v[196:199]\n"
"ds_read_b64_tr_b16 a[150:151], v229 offset:928\n"
"v_cvt_pk_bf16_f32 v187, v190, v191\n"
"v_mfma_f32_16x16x32_bf16 v[140:143], a[84:87], a[212:215], v[140:143]\n"
"v_exp_f32 v128, v128\n"
"v_mfma_f32_16x16x32_bf16 v[140:143], a[88:91], a[216:219], v[140:143]\n"
"v_exp_f32 v129, v129\n"
"v_mfma_f32_16x16x32_bf16 v[140:143], a[92:95], a[220:223], v[140:143]\n"
"v_exp_f32 v130, v130\n"
"v_mfma_f32_16x16x32_bf16 v[220:223], a[160:163], v[184:187], v[220:223]\n"
"ds_read_b64_tr_b16 a[152:153], v229 offset:704\n"
"v_cvt_pk_bf16_f32 v128, v128, v129\n"
"v_mfma_f32_16x16x32_bf16 v[144:147], a[64:67], a[224:227], v[200:203]\n"
"v_exp_f32 v131, v131\n"
"v_mfma_f32_16x16x32_bf16 v[144:147], a[68:71], a[228:231], v[144:147]\n"
"v_exp_f32 v132, v132\n"
"v_mfma_f32_16x16x32_bf16 v[144:147], a[72:75], a[232:235], v[144:147]\n"
"ds_read_b64_tr_b16 a[154:155], v229 offset:960\n"
"v_cvt_pk_bf16_f32 v129, v130, v131\n"
"v_mfma_f32_16x16x32_bf16 v[144:147], a[76:79], a[236:239], v[144:147]\n"
"s_add_u32 m0, s77, 50784\n"
"v_exp_f32 v133, v133\n"
"v_mfma_f32_16x16x32_bf16 v[148:151], a[80:83], a[224:227], v[200:203]\n"
"v_exp_f32 v134, v134\n"
"v_mfma_f32_16x16x32_bf16 v[148:151], a[84:87], a[228:231], v[148:151]\n"
"ds_read_b64_tr_b16 a[156:157], v229 offset:736\n"
"v_cvt_pk_bf16_f32 v130, v132, v133\n"
"v_mfma_f32_16x16x32_bf16 v[148:151], a[88:91], a[232:235], v[148:151]\n"
"v_exp_f32 v135, v135\n"
"v_mfma_f32_16x16x32_bf16 v[148:151], a[92:95], a[236:239], v[148:151]\n"
"buffer_load_dwordx4 v230, s[48:51], s79 offen offset:3072 lds\n"
"v_mfma_f32_16x16x32_bf16 v[152:155], a[64:67], a[240:243], v[204:207]\n"
"s_add_u32 s79, s79, 16384\n"
"ds_read_b64_tr_b16 a[158:159], v229 offset:992\n"
"v_cvt_pk_bf16_f32 v131, v134, v135\n"
"v_mfma_f32_16x16x32_bf16 v[152:155], a[68:71], a[244:247], v[152:155]\n"
"v_mfma_f32_16x16x32_bf16 v[152:155], a[72:75], a[248:251], v[152:155]\n"
"v_mfma_f32_16x16x32_bf16 v[152:155], a[76:79], a[252:255], v[152:155]\n"
"v_mfma_f32_16x16x32_bf16 v[156:159], a[80:83], a[240:243], v[204:207]\n"
"v_mfma_f32_16x16x32_bf16 v[156:159], a[84:87], a[244:247], v[156:159]\n"
"v_mfma_f32_16x16x32_bf16 v[156:159], a[88:91], a[248:251], v[156:159]\n"
"v_mfma_f32_16x16x32_bf16 v[156:159], a[92:95], a[252:255], v[156:159]\n"
"L_at_b1:\n"
"s_waitcnt lgkmcnt(0)\n"
"v_mfma_f32_16x16x32_bf16 v[0:3], a[128:131], v[160:163], v[0:3]\n"
"v_exp_f32 v136, v136\n"
"v_mfma_f32_16x16x32_bf16 v[208:211], a[160:163], v[128:131], v[208:211]\n"
"v_exp_f32 v137, v137\n"
"v_mfma_f32_16x16x32_bf16 v[32:35], a[128:131], v[168:171], v[32:35]\n"
"ds_read_b128 a[96:99], v228 offset:17152\n"
"v_add_f32 v212, v212, v136\n"
"v_mfma_f32_16x16x32_bf16 v[64:67], a[128:131], v[176:179], v[64:67]\n"
"v_exp_f32 v138, v138\n"
"v_mfma_f32_16x16x32_bf16 v[96:99], a[128:131], v[184:187], v[96:99]\n"
"ds_read_b128 a[100:103], v228 offset:17168\n"
"v_add_f32 v212, v212, v137\n"
"v_mfma_f32_16x16x32_bf16 v[4:7], a[132:135], v[160:163], v[4:7]\n"
"ds_read_b128 a[104:107], v228 offset:17280\n"
"v_cvt_pk_bf16_f32 v136, v136, v137\n"
"v_mfma_f32_16x16x32_bf16 v[36:39], a[132:135], v[168:171], v[36:39]\n"
"s_add_u32 m0, s76, 0\n"
"v_exp_f32 v139, v139\n"
"v_mfma_f32_16x16x32_bf16 v[68:71], a[132:135], v[176:179], v[68:71]\n"
"ds_read_b128 a[108:111], v228 offset:17296\n"
"v_add_f32 v212, v212, v138\n"
"v_mfma_f32_16x16x32_bf16 v[100:103], a[132:135], v[184:187], v[100:103]\n"
"v_exp_f32 v140, v140\n"
"v_mfma_f32_16x16x32_bf16 v[8:11], a[136:139], v[160:163], v[8:11]\n"
"ds_read_b128 a[112:115], v228 offset:17408\n"
"v_add_f32 v212, v212, v139\n"
"v_mfma_f32_16x16x32_bf16 v[40:43], a[136:139], v[168:171], v[40:43]\n"
"buffer_load_dwordx4 v230, s[44:47], s78 offen offset:0 lds\n"
"v_mfma_f32_16x16x32_bf16 v[72:75], a[136:139], v[176:179], v[72:75]\n"
"ds_read_b128 a[116:119], v228 offset:17424\n"
"v_cvt_pk_bf16_f32 v137, v138, v139\n"
"v_mfma_f32_16x16x32_bf16 v[104:107], a[136:139], v[184:187], v[104:107]\n"
"v_exp_f32 v141, v141\n"
"v_mfma_f32_16x16x32_bf16 v[12:15], a[140:143], v[160:163], v[12:15]\n"
"ds_read_b128 a[120:123], v228 offset:17536\n"
"v_add_f32 v212, v212, v140\n"
"v_mfma_f32_16x16x32_bf16 v[44:47], a[140:143], v[168:171], v[44:47]\n"
"v_exp_f32 v142, v142\n"
"v_mfma_f32_16x16x32_bf16 v[76:79], a[140:143], v[176:179], v[76:79]\n"
"ds_read_b128 a[124:127], v228 offset:17552\n"
"v_add_f32 v212, v212, v141\n"
"v_mfma_f32_16x16x32_bf16 v[108:111], a[140:143], v[184:187], v[108:111]\n"
"ds_read_b64_tr_b16 a[128:129], v229 offset:16896\n"
"v_cvt_pk_bf16_f32 v138, v140, v141\n"
"v_mfma_f32_16x16x32_bf16 v[16:19], a[144:147], v[160:163], v[16:19]\n"
"v_exp_f32 v143, v143\n"
"v_mfma_f32_16x16x32_bf16 v[48:51], a[144:147], v[168:171], v[48:51]\n"
"ds_read_b64_tr_b16 a[130:131], v229 offset:17152\n"
"v_add_f32 v212, v212, v142\n"
"v_mfma_f32_16x16x32_bf16 v[80:83], a[144:147], v[176:179], v[80:83]\n"
"ds_read_b64_tr_b16 a[132:133], v229 offset:16928\n"
"v_add_f32 v212, v212, v143\n"
"v_mfma_f32_16x16x32_bf16 v[112:115], a[144:147], v[184:187], v[112:115]\n"
"ds_read_b64_tr_b16 a[134:135], v229 offset:17184\n"
"v_cvt_pk_bf16_f32 v139, v142, v143\n"
"v_mfma_f32_16x16x32_bf16 v[20:23], a[148:151], v[160:163], v[20:23]\n"
"v_exp_f32 v144, v144\n"
"v_mfma_f32_16x16x32_bf16 v[52:55], a[148:151], v[168:171], v[52:55]\n"
"s_add_u32 m0, s76, 16\n"
"v_exp_f32 v145, v145\n"
"v_mfma_f32_16x16x32_bf16 v[84:87], a[148:151], v[176:179], v[84:87]\n"
"v_exp_f32 v146, v146\n"
"v_mfma_f32_16x16x32_bf16 v[116:119], a[148:151], v[184:187], v[116:119]\n"
"ds_read_b64_tr_b16 a[136:137], v229 offset:16960\n"
"v_cvt_pk_bf16_f32 v144, v144, v145\n"
"v_mfma_f32_16x16x32_bf16 v[24:27], a[152:155], v[160:163], v[24:27]\n"
"v_exp_f32 v147, v147\n"
"v_mfma_f32_16x16x32_bf16 v[56:59], a[152:155], v[168:171], v[56:59]\n"
"buffer_load_dwordx4 v230, s[44:47], s78 offen offset:1024 lds\n"
"v_mfma_f32_16x16x32_bf16 v[88:91], a[152:155], v[176:179], v[88:91]\n"
"v_exp_f32 v148, v148\n"
"v_mfma_f32_16x16x32_bf16 v[120:123], a[152:155], v[184:187], v[120:123]\n"
"ds_read_b64_tr_b16 a[138:139], v229 offset:17216\n"
"v_cvt_pk_bf16_f32 v145, v146, v147\n"
"v_mfma_f32_16x16x32_bf16 v[28:31], a[156:159], v[160:163], v[28:31]\n"
"v_exp_f32 v149, v149\n"
"v_mfma_f32_16x16x32_bf16 v[60:63], a[156:159], v[168:171], v[60:63]\n"
"v_exp_f32 v150, v150\n"
"v_mfma_f32_16x16x32_bf16 v[92:95], a[156:159], v[176:179], v[92:95]\n"
"ds_read_b64_tr_b16 a[140:141], v229 offset:16992\n"
"v_cvt_pk_bf16_f32 v146, v148, v149\n"
"v_mfma_f32_16x16x32_bf16 v[124:127], a[156:159], v[184:187], v[124:127]\n"
"s_waitcnt lgkmcnt(7)\n"
"v_exp_f32 v151, v151\n"
"v_mfma_f32_16x16x32_bf16 v[160:163], a[96:99], a[192:195], v[192:195]\n"
"ds_read_b64_tr_b16 a[142:143], v229 offset:17248\n"
"v_cvt_pk_bf16_f32 v147, v150, v151\n"
"v_mfma_f32_16x16x32_bf16 v[160:163], a[100:103], a[196:199], v[160:163]\n"
"v_exp_f32 v152, v152\n"
"v_mfma_f32_16x16x32_bf16 v[160:163], a[104:107], a[200:203], v[160:163]\n"
"v_exp_f32 v153, v153\n"
"v_mfma_f32_16x16x32_bf16 v[160:163], a[108:111], a[204:207], v[160:163]\n"
"v_exp_f32 v154, v154\n"
"v_mfma_f32_16x16x32_bf16 v[216:219], a[160:163], v[144:147], v[216:219]\n"
"ds_read_b64_tr_b16 a[144:145], v229 offset:17024\n"
"v_cvt_pk_bf16_f32 v152, v152, v153\n"
"v_mfma_f32_16x16x32_bf16 v[164:167], a[112:115], a[192:195], v[192:195]\n"
"s_add_u32 m0, s76, 32\n"
"v_exp_f32 v155, v155\n"
"v_mfma_f32_16x16x32_bf16 v[164:167], a[116:119], a[196:199], v[164:167]\n"
"v_exp_f32 v156, v156\n"
"v_mfma_f32_16x16x32_bf16 v[164:167], a[120:123], a[200:203], v[164:167]\n"
"ds_read_b64_tr_b16 a[146:147], v229 offset:17280\n"
"v_cvt_pk_bf16_f32 v153, v154, v155\n"
"v_mfma_f32_16x16x32_bf16 v[164:167], a[124:127], a[204:207], v[164:167]\n"
"v_exp_f32 v157, v157\n"
"v_mfma_f32_16x16x32_bf16 v[168:171], a[96:99], a[208:211], v[196:199]\n"
"buffer_load_dwordx4 v230, s[44:47], s78 offen offset:2048 lds\n"
"v_mfma_f32_16x16x32_bf16 v[168:171], a[100:103], a[212:215], v[168:171]\n"
"v_exp_f32 v158, v158\n"
"v_mfma_f32_16x16x32_bf16 v[168:171], a[104:107], a[216:219], v[168:171]\n"
"ds_read_b64_tr_b16 a[148:149], v229 offset:17056\n"
"v_cvt_pk_bf16_f32 v154, v156, v157\n"
"v_mfma_f32_16x16x32_bf16 v[168:171], a[108:111], a[220:223], v[168:171]\n"
"v_exp_f32 v159, v159\n"
"v_mfma_f32_16x16x32_bf16 v[172:175], a[112:115], a[208:211], v[196:199]\n"
"ds_read_b64_tr_b16 a[150:151], v229 offset:17312\n"
"v_cvt_pk_bf16_f32 v155, v158, v159\n"
"v_mfma_f32_16x16x32_bf16 v[172:175], a[116:119], a[212:215], v[172:175]\n"
"v_exp_f32 v160, v160\n"
"v_mfma_f32_16x16x32_bf16 v[172:175], a[120:123], a[216:219], v[172:175]\n"
"v_exp_f32 v161, v161\n"
"v_mfma_f32_16x16x32_bf16 v[172:175], a[124:127], a[220:223], v[172:175]\n"
"v_exp_f32 v162, v162\n"
"v_mfma_f32_16x16x32_bf16 v[220:223], a[160:163], v[152:155], v[220:223]\n"
"ds_read_b64_tr_b16 a[152:153], v229 offset:17088\n"
"v_cvt_pk_bf16_f32 v160, v160, v161\n"
"v_mfma_f32_16x16x32_bf16 v[176:179], a[96:99], a[224:227], v[200:203]\n"
"v_exp_f32 v163, v163\n"
"v_mfma_f32_16x16x32_bf16 v[176:179], a[100:103], a[228:231], v[176:179]\n"
"v_exp_f32 v164, v164\n"
"v_mfma_f32_16x16x32_bf16 v[176:179], a[104:107], a[232:235], v[176:179]\n"
"ds_read_b64_tr_b16 a[154:155], v229 offset:17344\n"
"v_cvt_pk_bf16_f32 v161, v162, v163\n"
"v_mfma_f32_16x16x32_bf16 v[176:179], a[108:111], a[236:239], v[176:179]\n"
"s_add_u32 m0, s76, 48\n"
"v_exp_f32 v165, v165\n"
"v_mfma_f32_16x16x32_bf16 v[180:183], a[112:115], a[224:227], v[200:203]\n"
"v_exp_f32 v166, v166\n"
"v_mfma_f32_16x16x32_bf16 v[180:183], a[116:119], a[228:231], v[180:183]\n"
"ds_read_b64_tr_b16 a[156:157], v229 offset:17120\n"
"v_cvt_pk_bf16_f32 v162, v164, v165\n"
"v_mfma_f32_16x16x32_bf16 v[180:183], a[120:123], a[232:235], v[180:183]\n"
"v_exp_f32 v167, v167\n"
"v_mfma_f32_16x16x32_bf16 v[180:183], a[124:127], a[236:239], v[180:183]\n"
"buffer_load_dwordx4 v230, s[44:47], s78 offen offset:3072 lds\n"
"v_mfma_f32_16x16x32_bf16 v[184:187], a[96:99], a[240:243], v[204:207]\n"
"s_add_u32 s78, s78, 16384\n"
"ds_read_b64_tr_b16 a[158:159], v229 offset:17376\n"
"v_cvt_pk_bf16_f32 v163, v166, v167\n"
"v_mfma_f32_16x16x32_bf16 v[184:187], a[100:103], a[244:247], v[184:187]\n"
"v_mfma_f32_16x16x32_bf16 v[184:187], a[104:107], a[248:251], v[184:187]\n"
"v_mfma_f32_16x16x32_bf16 v[184:187], a[108:111], a[252:255], v[184:187]\n"
"v_mfma_f32_16x16x32_bf16 v[188:191], a[112:115], a[240:243], v[204:207]\n"
"v_mfma_f32_16x16x32_bf16 v[188:191], a[116:119], a[244:247], v[188:191]\n"
"v_mfma_f32_16x16x32_bf16 v[188:191], a[120:123], a[248:251], v[188:191]\n"
"v_mfma_f32_16x16x32_bf16 v[188:191], a[124:127], a[252:255], v[188:191]\n"
"L_at_b2:\n"
"s_waitcnt vmcnt(12) lgkmcnt(0)\n"
"s_barrier\n"
"v_mfma_f32_16x16x32_bf16 v[0:3], a[128:131], v[128:131], v[0:3]\n"
"v_exp_f32 v168, v168\n"
"v_mfma_f32_16x16x32_bf16 v[208:211], a[160:163], v[160:163], v[208:211]\n"
"v_exp_f32 v169, v169\n"
"v_mfma_f32_16x16x32_bf16 v[32:35], a[128:131], v[136:139], v[32:35]\n"
"ds_read_b128 a[64:67], v228 offset:33280\n"
"v_add_f32 v212, v212, v168\n"
"v_mfma_f32_16x16x32_bf16 v[64:67], a[128:131], v[144:147], v[64:67]\n"
"v_exp_f32 v170, v170\n"
"v_mfma_f32_16x16x32_bf16 v[96:99], a[128:131], v[152:155], v[96:99]\n"
"ds_read_b128 a[68:71], v228 offset:33296\n"
"v_add_f32 v212, v212, v169\n"
"v_mfma_f32_16x16x32_bf16 v[4:7], a[132:135], v[128:131], v[4:7]\n"
"ds_read_b128 a[72:75], v228 offset:33408\n"
"v_cvt_pk_bf16_f32 v168, v168, v169\n"
"v_mfma_f32_16x16x32_bf16 v[36:39], a[132:135], v[136:139], v[36:39]\n"
"s_add_u32 m0, s77, 0\n"
"v_exp_f32 v171, v171\n"
"v_mfma_f32_16x16x32_bf16 v[68:71], a[132:135], v[144:147], v[68:71]\n"
"ds_read_b128 a[76:79], v228 offset:33424\n"
"v_add_f32 v212, v212, v170\n"
"v_mfma_f32_16x16x32_bf16 v[100:103], a[132:135], v[152:155], v[100:103]\n"
"v_exp_f32 v172, v172\n"
"v_mfma_f32_16x16x32_bf16 v[8:11], a[136:139], v[128:131], v[8:11]\n"
"ds_read_b128 a[80:83], v228 offset:33536\n"
"v_add_f32 v212, v212, v171\n"
"v_mfma_f32_16x16x32_bf16 v[40:43], a[136:139], v[136:139], v[40:43]\n"
"buffer_load_dwordx4 v230, s[48:51], s79 offen offset:0 lds\n"
"v_mfma_f32_16x16x32_bf16 v[72:75], a[136:139], v[144:147], v[72:75]\n"
"ds_read_b128 a[84:87], v228 offset:33552\n"
"v_cvt_pk_bf16_f32 v169, v170, v171\n"
"v_mfma_f32_16x16x32_bf16 v[104:107], a[136:139], v[152:155], v[104:107]\n"
"v_exp_f32 v173, v173\n"
"v_mfma_f32_16x16x32_bf16 v[12:15], a[140:143], v[128:131], v[12:15]\n"
"ds_read_b128 a[88:91], v228 offset:33664\n"
"v_add_f32 v212, v212, v172\n"
"v_mfma_f32_16x16x32_bf16 v[44:47], a[140:143], v[136:139], v[44:47]\n"
"v_exp_f32 v174, v174\n"
"v_mfma_f32_16x16x32_bf16 v[76:79], a[140:143], v[144:147], v[76:79]\n"
"ds_read_b128 a[92:95], v228 offset:33680\n"
"v_add_f32 v212, v212, v173\n"
"v_mfma_f32_16x16x32_bf16 v[108:111], a[140:143], v[152:155], v[108:111]\n"
"ds_read_b64_tr_b16 a[128:129], v229 offset:17408\n"
"v_min_u32 v248, 5308400, v247\n"
"v_mfma_f32_16x16x32_bf16 v[16:19], a[144:147], v[128:131], v[16:19]\n"
"ds_read_b64_tr_b16 a[130:131], v229 offset:17664\n"
"v_cvt_pk_bf16_f32 v170, v172, v173\n"
"v_mfma_f32_16x16x32_bf16 v[48:51], a[144:147], v[136:139], v[48:51]\n"
"v_exp_f32 v175, v175\n"
"v_mfma_f32_16x16x32_bf16 v[80:83], a[144:147], v[144:147], v[80:83]\n"
"ds_read_b64_tr_b16 a[132:133], v229 offset:17440\n"
"v_add_f32 v212, v212, v174\n"
"v_mfma_f32_16x16x32_bf16 v[112:115], a[144:147], v[152:155], v[112:115]\n"
"global_load_dwordx4 a[60:63], v248, %5\n"
"v_mfma_f32_16x16x32_bf16 v[20:23], a[148:151], v[128:131], v[20:23]\n"
"ds_read_b64_tr_b16 a[134:135], v229 offset:17696\n"
"v_add_f32 v212, v212, v175\n"
"v_mfma_f32_16x16x32_bf16 v[52:55], a[148:151], v[136:139], v[52:55]\n"
"s_add_u32 m0, s77, 32\n"
"ds_read_b64_tr_b16 a[136:137], v229 offset:17472\n"
"v_cvt_pk_bf16_f32 v171, v174, v175\n"
"v_mfma_f32_16x16x32_bf16 v[84:87], a[148:151], v[144:147], v[84:87]\n"
"v_exp_f32 v176, v176\n"
"v_mfma_f32_16x16x32_bf16 v[116:119], a[148:151], v[152:155], v[116:119]\n"
"v_exp_f32 v177, v177\n"
"v_mfma_f32_16x16x32_bf16 v[24:27], a[152:155], v[128:131], v[24:27]\n"
"v_exp_f32 v178, v178\n"
"v_mfma_f32_16x16x32_bf16 v[56:59], a[152:155], v[136:139], v[56:59]\n"
"buffer_load_dwordx4 v230, s[48:51], s79 offen offset:1024 lds\n"
"v_mfma_f32_16x16x32_bf16 v[88:91], a[152:155], v[144:147], v[88:91]\n"
"ds_read_b64_tr_b16 a[138:139], v229 offset:17728\n"
"v_cvt_pk_bf16_f32 v176, v176, v177\n"
"v_mfma_f32_16x16x32_bf16 v[120:123], a[152:155], v[152:155], v[120:123]\n"
"v_exp_f32 v179, v179\n"
"v_mfma_f32_16x16x32_bf16 v[28:31], a[156:159], v[128:131], v[28:31]\n"
"v_exp_f32 v180, v180\n"
"v_mfma_f32_16x16x32_bf16 v[60:63], a[156:159], v[136:139], v[60:63]\n"
"ds_read_b64_tr_b16 a[140:141], v229 offset:17504\n"
"v_cvt_pk_bf16_f32 v177, v178, v179\n"
"v_mfma_f32_16x16x32_bf16 v[92:95], a[156:159], v[144:147], v[92:95]\n"
"v_exp_f32 v181, v181\n"
"v_mfma_f32_16x16x32_bf16 v[124:127], a[156:159], v[152:155], v[124:127]\n"
"s_waitcnt lgkmcnt(7)\n"
"v_exp_f32 v182, v182\n"
"v_mfma_f32_16x16x32_bf16 v[128:131], a[64:67], a[192:195], v[192:195]\n"
"ds_read_b64_tr_b16 a[142:143], v229 offset:17760\n"
"v_cvt_pk_bf16_f32 v178, v180, v181\n"
"v_mfma_f32_16x16x32_bf16 v[128:131], a[68:71], a[196:199], v[128:131]\n"
"v_exp_f32 v183, v183\n"
"v_mfma_f32_16x16x32_bf16 v[128:131], a[72:75], a[200:203], v[128:131]\n"
"ds_read_b64_tr_b16 a[144:145], v229 offset:17536\n"
"v_add_u32 v247, 704, v247\n"
"v_mfma_f32_16x16x32_bf16 v[128:131], a[76:79], a[204:207], v[128:131]\n"
"ds_read_b64_tr_b16 a[146:147], v229 offset:17792\n"
"v_cvt_pk_bf16_f32 v179, v182, v183\n"
"v_mfma_f32_16x16x32_bf16 v[132:135], a[80:83], a[192:195], v[192:195]\n"
"v_exp_f32 v184, v184\n"
"v_mfma_f32_16x16x32_bf16 v[132:135], a[84:87], a[196:199], v[132:135]\n"
"global_load_dwordx4 a[60:63], v248, %6\n"
"v_mfma_f32_16x16x32_bf16 v[132:135], a[88:91], a[200:203], v[132:135]\n"
"s_add_u32 m0, s77, 64\n"
"v_exp_f32 v185, v185\n"
"v_mfma_f32_16x16x32_bf16 v[132:135], a[92:95], a[204:207], v[132:135]\n"
"v_exp_f32 v186, v186\n"
"v_mfma_f32_16x16x32_bf16 v[216:219], a[160:163], v[176:179], v[216:219]\n"
"ds_read_b64_tr_b16 a[148:149], v229 offset:17568\n"
"v_cvt_pk_bf16_f32 v184, v184, v185\n"
"v_mfma_f32_16x16x32_bf16 v[136:139], a[64:67], a[208:211], v[196:199]\n"
"buffer_load_dwordx4 v230, s[48:51], s79 offen offset:2048 lds\n"
"v_mfma_f32_16x16x32_bf16 v[136:139], a[68:71], a[212:215], v[136:139]\n"
"v_exp_f32 v187, v187\n"
"v_mfma_f32_16x16x32_bf16 v[136:139], a[72:75], a[216:219], v[136:139]\n"
"v_exp_f32 v188, v188\n"
"v_mfma_f32_16x16x32_bf16 v[136:139], a[76:79], a[220:223], v[136:139]\n"
"ds_read_b64_tr_b16 a[150:151], v229 offset:17824\n"
"v_cvt_pk_bf16_f32 v185, v186, v187\n"
"v_mfma_f32_16x16x32_bf16 v[140:143], a[80:83], a[208:211], v[196:199]\n"
"v_exp_f32 v189, v189\n"
"v_mfma_f32_16x16x32_bf16 v[140:143], a[84:87], a[212:215], v[140:143]\n"
"v_exp_f32 v190, v190\n"
"v_mfma_f32_16x16x32_bf16 v[140:143], a[88:91], a[216:219], v[140:143]\n"
"ds_read_b64_tr_b16 a[152:153], v229 offset:17600\n"
"v_cvt_pk_bf16_f32 v186, v188, v189\n"
"v_mfma_f32_16x16x32_bf16 v[140:143], a[92:95], a[220:223], v[140:143]\n"
"v_exp_f32 v191, v191\n"
"v_mfma_f32_16x16x32_bf16 v[144:147], a[64:67], a[224:227], v[200:203]\n"
"ds_read_b64_tr_b16 a[154:155], v229 offset:17856\n"
"v_cvt_pk_bf16_f32 v187, v190, v191\n"
"v_mfma_f32_16x16x32_bf16 v[144:147], a[68:71], a[228:231], v[144:147]\n"
"v_exp_f32 v128, v128\n"
"v_mfma_f32_16x16x32_bf16 v[144:147], a[72:75], a[232:235], v[144:147]\n"
"v_exp_f32 v129, v129\n"
"v_mfma_f32_16x16x32_bf16 v[144:147], a[76:79], a[236:239], v[144:147]\n"
"v_exp_f32 v130, v130\n"
"v_mfma_f32_16x16x32_bf16 v[220:223], a[160:163], v[184:187], v[220:223]\n"
"s_add_u32 m0, s77, 96\n"
"ds_read_b64_tr_b16 a[156:157], v229 offset:17632\n"
"v_cvt_pk_bf16_f32 v128, v128, v129\n"
"v_mfma_f32_16x16x32_bf16 v[148:151], a[80:83], a[224:227], v[200:203]\n"
"v_exp_f32 v131, v131\n"
"v_mfma_f32_16x16x32_bf16 v[148:151], a[84:87], a[228:231], v[148:151]\n"
"v_exp_f32 v132, v132\n"
"v_mfma_f32_16x16x32_bf16 v[148:151], a[88:91], a[232:235], v[148:151]\n"
"ds_read_b64_tr_b16 a[158:159], v229 offset:17888\n"
"v_cvt_pk_bf16_f32 v129, v130, v131\n"
"v_mfma_f32_16x16x32_bf16 v[148:151], a[92:95], a[236:239], v[148:151]\n"
"buffer_load_dwordx4 v230, s[48:51], s79 offen offset:3072 lds\n"
"v_mfma_f32_16x16x32_bf16 v[152:155], a[64:67], a[240:243], v[204:207]\n"
"s_add_u32 s79, s79, 16384\n"
"v_exp_f32 v133, v133\n"
"v_mfma_f32_16x16x32_bf16 v[152:155], a[68:71], a[244:247], v[152:155]\n"
"v_exp_f32 v134, v134\n"
"v_mfma_f32_16x16x32_bf16 v[152:155], a[72:75], a[248:251], v[152:155]\n"
"v_cvt_pk_bf16_f32 v130, v132, v133\n"
"v_mfma_f32_16x16x32_bf16 v[152:155], a[76:79], a[252:255], v[152:155]\n"
"v_exp_f32 v135, v135\n"
"v_mfma_f32_16x16x32_bf16 v[156:159], a[80:83], a[240:243], v[204:207]\n"
"v_cvt_pk_bf16_f32 v131, v134, v135\n"
"v_mfma_f32_16x16x32_bf16 v[156:159], a[84:87], a[244:247], v[156:159]\n"
"v_mfma_f32_16x16x32_bf16 v[156:159], a[88:91], a[248:251], v[156:159]\n"
"v_mfma_f32_16x16x32_bf16 v[156:159], a[92:95], a[252:255], v[156:159]\n"
"L_at_b3:\n"
"s_waitcnt lgkmcnt(0)\n"
"v_mfma_f32_16x16x32_bf16 v[0:3], a[128:131], v[160:163], v[0:3]\n"
"v_exp_f32 v136, v136\n"
"v_mfma_f32_16x16x32_bf16 v[208:211], a[160:163], v[128:131], v[208:211]\n"
"v_exp_f32 v137, v137\n"
"v_mfma_f32_16x16x32_bf16 v[32:35], a[128:131], v[168:171], v[32:35]\n"
"ds_read_b128 a[96:99], v228 offset:33792\n"
"v_add_f32 v212, v212, v136\n"
"v_mfma_f32_16x16x32_bf16 v[64:67], a[128:131], v[176:179], v[64:67]\n"
"v_exp_f32 v138, v138\n"
"v_mfma_f32_16x16x32_bf16 v[96:99], a[128:131], v[184:187], v[96:99]\n"
"ds_read_b128 a[100:103], v228 offset:33808\n"
"v_add_f32 v212, v212, v137\n"
"v_mfma_f32_16x16x32_bf16 v[4:7], a[132:135], v[160:163], v[4:7]\n"
"ds_read_b128 a[104:107], v228 offset:33920\n"
"v_cvt_pk_bf16_f32 v136, v136, v137\n"
"v_mfma_f32_16x16x32_bf16 v[36:39], a[132:135], v[168:171], v[36:39]\n"
"s_add_u32 m0, s76, 16640\n"
"v_exp_f32 v139, v139\n"
"v_mfma_f32_16x16x32_bf16 v[68:71], a[132:135], v[176:179], v[68:71]\n"
"ds_read_b128 a[108:111], v228 offset:33936\n"
"v_add_f32 v212, v212, v138\n"
"v_mfma_f32_16x16x32_bf16 v[100:103], a[132:135], v[184:187], v[100:103]\n"
"v_exp_f32 v140, v140\n"
"v_mfma_f32_16x16x32_bf16 v[8:11], a[136:139], v[160:163], v[8:11]\n"
"ds_read_b128 a[112:115], v228 offset:34048\n"
"v_add_f32 v212, v212, v139\n"
"v_mfma_f32_16x16x32_bf16 v[40:43], a[136:139], v[168:171], v[40:43]\n"
"buffer_load_dwordx4 v230, s[44:47], s78 offen offset:0 lds\n"
"v_mfma_f32_16x16x32_bf16 v[72:75], a[136:139], v[176:179], v[72:75]\n"
"ds_read_b128 a[116:119], v228 offset:34064\n"
"v_cvt_pk_bf16_f32 v137, v138, v139\n"
"v_mfma_f32_16x16x32_bf16 v[104:107], a[136:139], v[184:187], v[104:107]\n"
"v_exp_f32 v141, v141\n"
"v_mfma_f32_16x16x32_bf16 v[12:15], a[140:143], v[160:163], v[12:15]\n"
"ds_read_b128 a[120:123], v228 offset:34176\n"
"v_add_f32 v212, v212, v140\n"
"v_mfma_f32_16x16x32_bf16 v[44:47], a[140:143], v[168:171], v[44:47]\n"
"v_exp_f32 v142, v142\n"
"v_mfma_f32_16x16x32_bf16 v[76:79], a[140:143], v[176:179], v[76:79]\n"
"ds_read_b128 a[124:127], v228 offset:34192\n"
"v_add_f32 v212, v212, v141\n"
"v_mfma_f32_16x16x32_bf16 v[108:111], a[140:143], v[184:187], v[108:111]\n"
"ds_read_b64_tr_b16 a[128:129], v229 offset:33792\n"
"v_cvt_pk_bf16_f32 v138, v140, v141\n"
"v_mfma_f32_16x16x32_bf16 v[16:19], a[144:147], v[160:163], v[16:19]\n"
"v_exp_f32 v143, v143\n"
"v_mfma_f32_16x16x32_bf16 v[48:51], a[144:147], v[168:171], v[48:51]\n"
"ds_read_b64_tr_b16 a[130:131], v229 offset:34048\n"
"v_add_f32 v212, v212, v142\n"
"v_mfma_f32_16x16x32_bf16 v[80:83], a[144:147], v[176:179], v[80:83]\n"
"ds_read_b64_tr_b16 a[132:133], v229 offset:33824\n"
"v_add_f32 v212, v212, v143\n"
"v_mfma_f32_16x16x32_bf16 v[112:115], a[144:147], v[184:187], v[112:115]\n"
"ds_read_b64_tr_b16 a[134:135], v229 offset:34080\n"
"v_cvt_pk_bf16_f32 v139, v142, v143\n"
"v_mfma_f32_16x16x32_bf16 v[20:23], a[148:151], v[160:163], v[20:23]\n"
"v_exp_f32 v144, v144\n"
"v_mfma_f32_16x16x32_bf16 v[52:55], a[148:151], v[168:171], v[52:55]\n"
"s_add_u32 m0, s76, 16656\n"
"v_exp_f32 v145, v145\n"
"v_mfma_f32_16x16x32_bf16 v[84:87], a[148:151], v[176:179], v[84:87]\n"
"v_exp_f32 v146, v146\n"
"v_mfma_f32_16x16x32_bf16 v[116:119], a[148:151], v[184:187], v[116:119]\n"
"ds_read_b64_tr_b16 a[136:137], v229 offset:33856\n"
"v_cvt_pk_bf16_f32 v144, v144, v145\n"
"v_mfma_f32_16x16x32_bf16 v[24:27], a[152:155], v[160:163], v[24:27]\n"
"v_exp_f32 v147, v147\n"
"v_mfma_f32_16x16x32_bf16 v[56:59], a[152:155], v[168:171], v[56:59]\n"
"buffer_load_dwordx4 v230, s[44:47], s78 offen offset:1024 lds\n"
"v_mfma_f32_16x16x32_bf16 v[88:91], a[152:155], v[176:179], v[88:91]\n"
"v_exp_f32 v148, v148\n"
"v_mfma_f32_16x16x32_bf16 v[120:123], a[152:155], v[184:187], v[120:123]\n"
"ds_read_b64_tr_b16 a[138:139], v229 offset:34112\n"
"v_cvt_pk_bf16_f32 v145, v146, v147\n"
"v_mfma_f32_16x16x32_bf16 v[28:31], a[156:159], v[160:163], v[28:31]\n"
"v_exp_f32 v149, v149\n"
"v_mfma_f32_16x16x32_bf16 v[60:63], a[156:159], v[168:171], v[60:63]\n"
"v_exp_f32 v150, v150\n"
"v_mfma_f32_16x16x32_bf16 v[92:95], a[156:159], v[176:179], v[92:95]\n"
"ds_read_b64_tr_b16 a[140:141], v229 offset:33888\n"
"v_cvt_pk_bf16_f32 v146, v148, v149\n"
"v_mfma_f32_16x16x32_bf16 v[124:127], a[156:159], v[184:187], v[124:127]\n"
"s_waitcnt lgkmcnt(7)\n"
"v_exp_f32 v151, v151\n"
"v_mfma_f32_16x16x32_bf16 v[160:163], a[96:99], a[192:195], v[192:195]\n"
"ds_read_b64_tr_b16 a[142:143], v229 offset:34144\n"
"v_cvt_pk_bf16_f32 v147, v150, v151\n"
"v_mfma_f32_16x16x32_bf16 v[160:163], a[100:103], a[196:199], v[160:163]\n"
"v_exp_f32 v152, v152\n"
"v_mfma_f32_16x16x32_bf16 v[160:163], a[104:107], a[200:203], v[160:163]\n"
"v_exp_f32 v153, v153\n"
"v_mfma_f32_16x16x32_bf16 v[160:163], a[108:111], a[204:207], v[160:163]\n"
"v_exp_f32 v154, v154\n"
"v_mfma_f32_16x16x32_bf16 v[216:219], a[160:163], v[144:147], v[216:219]\n"
"ds_read_b64_tr_b16 a[144:145], v229 offset:33920\n"
"v_cvt_pk_bf16_f32 v152, v152, v153\n"
"v_mfma_f32_16x16x32_bf16 v[164:167], a[112:115], a[192:195], v[192:195]\n"
"s_add_u32 m0, s76, 16672\n"
"v_exp_f32 v155, v155\n"
"v_mfma_f32_16x16x32_bf16 v[164:167], a[116:119], a[196:199], v[164:167]\n"
"v_exp_f32 v156, v156\n"
"v_mfma_f32_16x16x32_bf16 v[164:167], a[120:123], a[200:203], v[164:167]\n"
"ds_read_b64_tr_b16 a[146:147], v229 offset:34176\n"
"v_cvt_pk_bf16_f32 v153, v154, v155\n"
"v_mfma_f32_16x16x32_bf16 v[164:167], a[124:127], a[204:207], v[164:167]\n"
"v_exp_f32 v157, v157\n"
"v_mfma_f32_16x16x32_bf16 v[168:171], a[96:99], a[208:211], v[196:199]\n"
"buffer_load_dwordx4 v230, s[44:47], s78 offen offset:2048 lds\n"
"v_mfma_f32_16x16x32_bf16 v[168:171], a[100:103], a[212:215], v[168:171]\n"
"v_exp_f32 v158, v158\n"
"v_mfma_f32_16x16x32_bf16 v[168:171], a[104:107], a[216:219], v[168:171]\n"
"ds_read_b64_tr_b16 a[148:149], v229 offset:33952\n"
"v_cvt_pk_bf16_f32 v154, v156, v157\n"
"v_mfma_f32_16x16x32_bf16 v[168:171], a[108:111], a[220:223], v[168:171]\n"
"v_exp_f32 v159, v159\n"
"v_mfma_f32_16x16x32_bf16 v[172:175], a[112:115], a[208:211], v[196:199]\n"
"ds_read_b64_tr_b16 a[150:151], v229 offset:34208\n"
"v_cvt_pk_bf16_f32 v155, v158, v159\n"
"v_mfma_f32_16x16x32_bf16 v[172:175], a[116:119], a[212:215], v[172:175]\n"
"v_exp_f32 v160, v160\n"
"v_mfma_f32_16x16x32_bf16 v[172:175], a[120:123], a[216:219], v[172:175]\n"
"v_exp_f32 v161, v161\n"
"v_mfma_f32_16x16x32_bf16 v[172:175], a[124:127], a[220:223], v[172:175]\n"
"v_exp_f32 v162, v162\n"
"v_mfma_f32_16x16x32_bf16 v[220:223], a[160:163], v[152:155], v[220:223]\n"
"ds_read_b64_tr_b16 a[152:153], v229 offset:33984\n"
"v_cvt_pk_bf16_f32 v160, v160, v161\n"
"v_mfma_f32_16x16x32_bf16 v[176:179], a[96:99], a[224:227], v[200:203]\n"
"v_exp_f32 v163, v163\n"
"v_mfma_f32_16x16x32_bf16 v[176:179], a[100:103], a[228:231], v[176:179]\n"
"v_exp_f32 v164, v164\n"
"v_mfma_f32_16x16x32_bf16 v[176:179], a[104:107], a[232:235], v[176:179]\n"
"ds_read_b64_tr_b16 a[154:155], v229 offset:34240\n"
"v_cvt_pk_bf16_f32 v161, v162, v163\n"
"v_mfma_f32_16x16x32_bf16 v[176:179], a[108:111], a[236:239], v[176:179]\n"
"s_add_u32 m0, s76, 16688\n"
"v_exp_f32 v165, v165\n"
"v_mfma_f32_16x16x32_bf16 v[180:183], a[112:115], a[224:227], v[200:203]\n"
"v_exp_f32 v166, v166\n"
"v_mfma_f32_16x16x32_bf16 v[180:183], a[116:119], a[228:231], v[180:183]\n"
"ds_read_b64_tr_b16 a[156:157], v229 offset:34016\n"
"v_cvt_pk_bf16_f32 v162, v164, v165\n"
"v_mfma_f32_16x16x32_bf16 v[180:183], a[120:123], a[232:235], v[180:183]\n"
"v_exp_f32 v167, v167\n"
"v_mfma_f32_16x16x32_bf16 v[180:183], a[124:127], a[236:239], v[180:183]\n"
"buffer_load_dwordx4 v230, s[44:47], s78 offen offset:3072 lds\n"
"v_mfma_f32_16x16x32_bf16 v[184:187], a[96:99], a[240:243], v[204:207]\n"
"s_add_u32 s78, s78, 16384\n"
"ds_read_b64_tr_b16 a[158:159], v229 offset:34272\n"
"v_cvt_pk_bf16_f32 v163, v166, v167\n"
"v_mfma_f32_16x16x32_bf16 v[184:187], a[100:103], a[244:247], v[184:187]\n"
"v_mfma_f32_16x16x32_bf16 v[184:187], a[104:107], a[248:251], v[184:187]\n"
"v_mfma_f32_16x16x32_bf16 v[184:187], a[108:111], a[252:255], v[184:187]\n"
"v_mfma_f32_16x16x32_bf16 v[188:191], a[112:115], a[240:243], v[204:207]\n"
"v_mfma_f32_16x16x32_bf16 v[188:191], a[116:119], a[244:247], v[188:191]\n"
"v_mfma_f32_16x16x32_bf16 v[188:191], a[120:123], a[248:251], v[188:191]\n"
"v_mfma_f32_16x16x32_bf16 v[188:191], a[124:127], a[252:255], v[188:191]\n"
"L_at_b4:\n"
"s_waitcnt vmcnt(12) lgkmcnt(0)\n"
"s_barrier\n"
"v_mfma_f32_16x16x32_bf16 v[0:3], a[128:131], v[128:131], v[0:3]\n"
"L_tp0_0_1:\n"
"s_cbranch_execz L_tb0_0\n"
"v_exp_f32 v168, v168\n"
"v_mfma_f32_16x16x32_bf16 v[208:211], a[160:163], v[160:163], v[208:211]\n"
"v_exp_f32 v169, v169\n"
"v_mfma_f32_16x16x32_bf16 v[32:35], a[128:131], v[136:139], v[32:35]\n"
"ds_read_b128 a[64:67], v228 offset:49920\n"
"v_add_f32 v212, v212, v168\n"
"v_mfma_f32_16x16x32_bf16 v[64:67], a[128:131], v[144:147], v[64:67]\n"
"v_exp_f32 v170, v170\n"
"v_mfma_f32_16x16x32_bf16 v[96:99], a[128:131], v[152:155], v[96:99]\n"
"ds_read_b128 a[68:71], v228 offset:49936\n"
"v_add_f32 v212, v212, v169\n"
"v_mfma_f32_16x16x32_bf16 v[4:7], a[132:135], v[128:131], v[4:7]\n"
"ds_read_b128 a[72:75], v228 offset:50048\n"
"v_cvt_pk_bf16_f32 v168, v168, v169\n"
"v_mfma_f32_16x16x32_bf16 v[36:39], a[132:135], v[136:139], v[36:39]\n"
"s_add_u32 m0, s77, 16896\n"
"v_exp_f32 v171, v171\n"
"v_mfma_f32_16x16x32_bf16 v[68:71], a[132:135], v[144:147], v[68:71]\n"
"ds_read_b128 a[76:79], v228 offset:50064\n"
"v_add_f32 v212, v212, v170\n"
"v_mfma_f32_16x16x32_bf16 v[100:103], a[132:135], v[152:155], v[100:103]\n"
"v_exp_f32 v172, v172\n"
"v_mfma_f32_16x16x32_bf16 v[8:11], a[136:139], v[128:131], v[8:11]\n"
"ds_read_b128 a[80:83], v228 offset:50176\n"
"v_add_f32 v212, v212, v171\n"
"v_mfma_f32_16x16x32_bf16 v[40:43], a[136:139], v[136:139], v[40:43]\n"
"buffer_load_dwordx4 v230, s[48:51], s79 offen offset:0 lds\n"
"v_mfma_f32_16x16x32_bf16 v[72:75], a[136:139], v[144:147], v[72:75]\n"
"ds_read_b128 a[84:87], v228 offset:50192\n"
"v_cvt_pk_bf16_f32 v169, v170, v171\n"
"v_mfma_f32_16x16x32_bf16 v[104:107], a[136:139], v[152:155], v[104:107]\n"
"v_exp_f32 v173, v173\n"
"v_mfma_f32_16x16x32_bf16 v[12:15], a[140:143], v[128:131], v[12:15]\n"
"ds_read_b128 a[88:91], v228 offset:50304\n"
"v_add_f32 v212, v212, v172\n"
"v_mfma_f32_16x16x32_bf16 v[44:47], a[140:143], v[136:139], v[44:47]\n"
"v_exp_f32 v174, v174\n"
"v_mfma_f32_16x16x32_bf16 v[76:79], a[140:143], v[144:147], v[76:79]\n"
"ds_read_b128 a[92:95], v228 offset:50320\n"
"v_add_f32 v212, v212, v173\n"
"v_mfma_f32_16x16x32_bf16 v[108:111], a[140:143], v[152:155], v[108:111]\n"
"ds_read_b64_tr_b16 a[128:129], v229 offset:34304\n"
"v_cvt_pk_bf16_f32 v170, v172, v173\n"
"v_mfma_f32_16x16x32_bf16 v[16:19], a[144:147], v[128:131], v[16:19]\n"
"v_exp_f32 v175, v175\n"
"v_mfma_f32_16x16x32_bf16 v[48:51], a[144:147], v[136:139], v[48:51]\n"
"ds_read_b64_tr_b16 a[130:131], v229 offset:34560\n"
"v_add_f32 v212, v212, v174\n"
"v_mfma_f32_16x16x32_bf16 v[80:83], a[144:147], v[144:147], v[80:83]\n"
"ds_read_b64_tr_b16 a[132:133], v229 offset:34336\n"
"v_add_f32 v212, v212, v175\n"
"v_mfma_f32_16x16x32_bf16 v[112:115], a[144:147], v[152:155], v[112:115]\n"
"ds_read_b64_tr_b16 a[134:135], v229 offset:34592\n"
"v_cvt_pk_bf16_f32 v171, v174, v175\n"
"v_mfma_f32_16x16x32_bf16 v[20:23], a[148:151], v[128:131], v[20:23]\n"
"v_exp_f32 v176, v176\n"
"v_mfma_f32_16x16x32_bf16 v[52:55], a[148:151], v[136:139], v[52:55]\n"
"s_add_u32 m0, s77, 16928\n"
"v_exp_f32 v177, v177\n"
"v_mfma_f32_16x16x32_bf16 v[84:87], a[148:151], v[144:147], v[84:87]\n"
"v_exp_f32 v178, v178\n"
"v_mfma_f32_16x16x32_bf16 v[116:119], a[148:151], v[152:155], v[116:119]\n"
"ds_read_b64_tr_b16 a[136:137], v229 offset:34368\n"
"v_cvt_pk_bf16_f32 v176, v176, v177\n"
"v_mfma_f32_16x16x32_bf16 v[24:27], a[152:155], v[128:131], v[24:27]\n"
"v_exp_f32 v179, v179\n"
"v_mfma_f32_16x16x32_bf16 v[56:59], a[152:155], v[136:139], v[56:59]\n"
"buffer_load_dwordx4 v230, s[48:51], s79 offen offset:1024 lds\n"
"v_mfma_f32_16x16x32_bf16 v[88:91], a[152:155], v[144:147], v[88:91]\n"
"v_exp_f32 v180, v180\n"
"v_mfma_f32_16x16x32_bf16 v[120:123], a[152:155], v[152:155], v[120:123]\n"
"ds_read_b64_tr_b16 a[138:139], v229 offset:34624\n"
"v_cvt_pk_bf16_f32 v177, v178, v179\n"
"v_mfma_f32_16x16x32_bf16 v[28:31], a[156:159], v[128:131], v[28:31]\n"
"v_exp_f32 v181, v181\n"
"v_mfma_f32_16x16x32_bf16 v[60:63], a[156:159], v[136:139], v[60:63]\n"
"v_exp_f32 v182, v182\n"
"v_mfma_f32_16x16x32_bf16 v[92:95], a[156:159], v[144:147], v[92:95]\n"
"ds_read_b64_tr_b16 a[140:141], v229 offset:34400\n"
"v_cvt_pk_bf16_f32 v178, v180, v181\n"
"v_mfma_f32_16x16x32_bf16 v[124:127], a[156:159], v[152:155], v[124:127]\n"
"s_waitcnt lgkmcnt(7)\n"
"v_exp_f32 v183, v183\n"
"v_mfma_f32_16x16x32_bf16 v[128:131], a[64:67], a[192:195], v[192:195]\n"
"ds_read_b64_tr_b16 a[142:143], v229 offset:34656\n"
"v_cvt_pk_bf16_f32 v179, v182, v183\n"
"v_mfma_f32_16x16x32_bf16 v[128:131], a[68:71], a[196:199], v[128:131]\n"
"v_exp_f32 v184, v184\n"
"v_mfma_f32_16x16x32_bf16 v[128:131], a[72:75], a[200:203], v[128:131]\n"
"v_exp_f32 v185, v185\n"
"v_mfma_f32_16x16x32_bf16 v[128:131], a[76:79], a[204:207], v[128:131]\n"
"v_exp_f32 v186, v186\n"
"v_mfma_f32_16x16x32_bf16 v[216:219], a[160:163], v[176:179], v[216:219]\n"
"ds_read_b64_tr_b16 a[144:145], v229 offset:34432\n"
"v_cvt_pk_bf16_f32 v184, v184, v185\n"
"v_mfma_f32_16x16x32_bf16 v[132:135], a[80:83], a[192:195], v[192:195]\n"
"s_add_u32 m0, s77, 16960\n"
"v_exp_f32 v187, v187\n"
"v_mfma_f32_16x16x32_bf16 v[132:135], a[84:87], a[196:199], v[132:135]\n"
"v_exp_f32 v188, v188\n"
"v_mfma_f32_16x16x32_bf16 v[132:135], a[88:91], a[200:203], v[132:135]\n"
"ds_read_b64_tr_b16 a[146:147], v229 offset:34688\n"
"v_cvt_pk_bf16_f32 v185, v186, v187\n"
"v_mfma_f32_16x16x32_bf16 v[132:135], a[92:95], a[204:207], v[132:135]\n"
"v_exp_f32 v189, v189\n"
"v_mfma_f32_16x16x32_bf16 v[136:139], a[64:67], a[208:211], v[196:199]\n"
"buffer_load_dwordx4 v230, s[48:51], s79 offen offset:2048 lds\n"
"v_mfma_f32_16x16x32_bf16 v[136:139], a[68:71], a[212:215], v[136:139]\n"
"v_exp_f32 v190, v190\n"
"v_mfma_f32_16x16x32_bf16 v[136:139], a[72:75], a[216:219], v[136:139]\n"
"ds_read_b64_tr_b16 a[148:149], v229 offset:34464\n"
"v_cvt_pk_bf16_f32 v186, v188, v189\n"
"v_mfma_f32_16x16x32_bf16 v[136:139], a[76:79], a[220:223], v[136:139]\n"
"v_exp_f32 v191, v191\n"
"v_mfma_f32_16x16x32_bf16 v[140:143], a[80:83], a[208:211], v[196:199]\n"
"ds_read_b64_tr_b16 a[150:151], v229 offset:34720\n"
"v_cvt_pk_bf16_f32 v187, v190, v191\n"
"v_mfma_f32_16x16x32_bf16 v[140:143], a[84:87], a[212:215], v[140:143]\n"
"v_exp_f32 v128, v128\n"
"v_mfma_f32_16x16x32_bf16 v[140:143], a[88:91], a[216:219], v[140:143]\n"
"v_exp_f32 v129, v129\n"
"v_mfma_f32_16x16x32_bf16 v[140:143], a[92:95], a[220:223], v[140:143]\n"
"v_exp_f32 v130, v130\n"
"v_mfma_f32_16x16x32_bf16 v[220:223], a[160:163], v[184:187], v[220:223]\n"
"ds_read_b64_tr_b16 a[152:153], v229 offset:34496\n"
"v_cvt_pk_bf16_f32 v128, v128, v129\n"
"v_mfma_f32_16x16x32_bf16 v[144:147], a[64:67], a[224:227], v[200:203]\n"
"v_exp_f32 v131, v131\n"
"v_mfma_f32_16x16x32_bf16 v[144:147], a[68:71], a[228:231], v[144:147]\n"
"v_exp_f32 v132, v132\n"
"v_mfma_f32_16x16x32_bf16 v[144:147], a[72:75], a[232:235], v[144:147]\n"
"ds_read_b64_tr_b16 a[154:155], v229 offset:34752\n"
"v_cvt_pk_bf16_f32 v129, v130, v131\n"
"v_mfma_f32_16x16x32_bf16 v[144:147], a[76:79], a[236:239], v[144:147]\n"
"s_add_u32 m0, s77, 16992\n"
"v_exp_f32 v133, v133\n"
"v_mfma_f32_16x16x32_bf16 v[148:151], a[80:83], a[224:227], v[200:203]\n"
"v_exp_f32 v134, v134\n"
"v_mfma_f32_16x16x32_bf16 v[148:151], a[84:87], a[228:231], v[148:151]\n"
"ds_read_b64_tr_b16 a[156:157], v229 offset:34528\n"
"v_cvt_pk_bf16_f32 v130, v132, v133\n"
"v_mfma_f32_16x16x32_bf16 v[148:151], a[88:91], a[232:235], v[148:151]\n"
"v_exp_f32 v135, v135\n"
"v_mfma_f32_16x16x32_bf16 v[148:151], a[92:95], a[236:239], v[148:151]\n"
"buffer_load_dwordx4 v230, s[48:51], s79 offen offset:3072 lds\n"
"v_mfma_f32_16x16x32_bf16 v[152:155], a[64:67], a[240:243], v[204:207]\n"
"s_add_u32 s79, s79, 16384\n"
"ds_read_b64_tr_b16 a[158:159], v229 offset:34784\n"
"v_cvt_pk_bf16_f32 v131, v134, v135\n"
"v_mfma_f32_16x16x32_bf16 v[152:155], a[68:71], a[244:247], v[152:155]\n"
"v_mfma_f32_16x16x32_bf16 v[152:155], a[72:75], a[248:251], v[152:155]\n"
"v_mfma_f32_16x16x32_bf16 v[152:155], a[76:79], a[252:255], v[152:155]\n"
"v_mfma_f32_16x16x32_bf16 v[156:159], a[80:83], a[240:243], v[204:207]\n"
"v_mfma_f32_16x16x32_bf16 v[156:159], a[84:87], a[244:247], v[156:159]\n"
"v_mfma_f32_16x16x32_bf16 v[156:159], a[88:91], a[248:251], v[156:159]\n"
"v_mfma_f32_16x16x32_bf16 v[156:159], a[92:95], a[252:255], v[156:159]\n"
"L_at_b5:\n"
"s_waitcnt lgkmcnt(0)\n"
"v_mfma_f32_16x16x32_bf16 v[0:3], a[128:131], v[160:163], v[0:3]\n"
"v_exp_f32 v136, v136\n"
"v_mfma_f32_16x16x32_bf16 v[208:211], a[160:163], v[128:131], v[208:211]\n"
"v_exp_f32 v137, v137\n"
"v_mfma_f32_16x16x32_bf16 v[32:35], a[128:131], v[168:171], v[32:35]\n"
"ds_read_b128 a[96:99], v228 offset:50432\n"
"v_add_f32 v212, v212, v136\n"
"v_mfma_f32_16x16x32_bf16 v[64:67], a[128:131], v[176:179], v[64:67]\n"
"v_exp_f32 v138, v138\n"
"v_mfma_f32_16x16x32_bf16 v[96:99], a[128:131], v[184:187], v[96:99]\n"
"ds_read_b128 a[100:103], v228 offset:50448\n"
"v_add_f32 v212, v212, v137\n"
"v_mfma_f32_16x16x32_bf16 v[4:7], a[132:135], v[160:163], v[4:7]\n"
"ds_read_b128 a[104:107], v228 offset:50560\n"
"v_cvt_pk_bf16_f32 v136, v136, v137\n"
"v_mfma_f32_16x16x32_bf16 v[36:39], a[132:135], v[168:171], v[36:39]\n"
"s_add_u32 m0, s76, 33280\n"
"v_exp_f32 v139, v139\n"
"v_mfma_f32_16x16x32_bf16 v[68:71], a[132:135], v[176:179], v[68:71]\n"
"ds_read_b128 a[108:111], v228 offset:50576\n"
"v_add_f32 v212, v212, v138\n"
"v_mfma_f32_16x16x32_bf16 v[100:103], a[132:135], v[184:187], v[100:103]\n"
"v_exp_f32 v140, v140\n"
"v_mfma_f32_16x16x32_bf16 v[8:11], a[136:139], v[160:163], v[8:11]\n"
"ds_read_b128 a[112:115], v228 offset:50688\n"
"v_add_f32 v212, v212, v139\n"
"v_mfma_f32_16x16x32_bf16 v[40:43], a[136:139], v[168:171], v[40:43]\n"
"buffer_load_dwordx4 v230, s[44:47], s78 offen offset:0 lds\n"
"v_mfma_f32_16x16x32_bf16 v[72:75], a[136:139], v[176:179], v[72:75]\n"
"ds_read_b128 a[116:119], v228 offset:50704\n"
"v_cvt_pk_bf16_f32 v137, v138, v139\n"
"v_mfma_f32_16x16x32_bf16 v[104:107], a[136:139], v[184:187], v[104:107]\n"
"v_exp_f32 v141, v141\n"
"v_mfma_f32_16x16x32_bf16 v[12:15], a[140:143], v[160:163], v[12:15]\n"
"ds_read_b128 a[120:123], v228 offset:50816\n"
"v_add_f32 v212, v212, v140\n"
"v_mfma_f32_16x16x32_bf16 v[44:47], a[140:143], v[168:171], v[44:47]\n"
"v_exp_f32 v142, v142\n"
"v_mfma_f32_16x16x32_bf16 v[76:79], a[140:143], v[176:179], v[76:79]\n"
"ds_read_b128 a[124:127], v228 offset:50832\n"
"v_add_f32 v212, v212, v141\n"
"v_mfma_f32_16x16x32_bf16 v[108:111], a[140:143], v[184:187], v[108:111]\n"
"ds_read_b64_tr_b16 a[128:129], v229 offset:50688\n"
"v_cvt_pk_bf16_f32 v138, v140, v141\n"
"v_mfma_f32_16x16x32_bf16 v[16:19], a[144:147], v[160:163], v[16:19]\n"
"v_exp_f32 v143, v143\n"
"v_mfma_f32_16x16x32_bf16 v[48:51], a[144:147], v[168:171], v[48:51]\n"
"ds_read_b64_tr_b16 a[130:131], v229 offset:50944\n"
"v_add_f32 v212, v212, v142\n"
"v_mfma_f32_16x16x32_bf16 v[80:83], a[144:147], v[176:179], v[80:83]\n"
"ds_read_b64_tr_b16 a[132:133], v229 offset:50720\n"
"v_add_f32 v212, v212, v143\n"
"v_mfma_f32_16x16x32_bf16 v[112:115], a[144:147], v[184:187], v[112:115]\n"
"ds_read_b64_tr_b16 a[134:135], v229 offset:50976\n"
"v_cvt_pk_bf16_f32 v139, v142, v143\n"
"v_mfma_f32_16x16x32_bf16 v[20:23], a[148:151], v[160:163], v[20:23]\n"
"v_exp_f32 v144, v144\n"
"v_mfma_f32_16x16x32_bf16 v[52:55], a[148:151], v[168:171], v[52:55]\n"
"s_add_u32 m0, s76, 33296\n"
"v_exp_f32 v145, v145\n"
"v_mfma_f32_16x16x32_bf16 v[84:87], a[148:151], v[176:179], v[84:87]\n"
"v_exp_f32 v146, v146\n"
"v_mfma_f32_16x16x32_bf16 v[116:119], a[148:151], v[184:187], v[116:119]\n"
"ds_read_b64_tr_b16 a[136:137], v229 offset:50752\n"
"v_cvt_pk_bf16_f32 v144, v144, v145\n"
"v_mfma_f32_16x16x32_bf16 v[24:27], a[152:155], v[160:163], v[24:27]\n"
"v_exp_f32 v147, v147\n"
"v_mfma_f32_16x16x32_bf16 v[56:59], a[152:155], v[168:171], v[56:59]\n"
"buffer_load_dwordx4 v230, s[44:47], s78 offen offset:1024 lds\n"
"v_mfma_f32_16x16x32_bf16 v[88:91], a[152:155], v[176:179], v[88:91]\n"
"v_exp_f32 v148, v148\n"
"v_mfma_f32_16x16x32_bf16 v[120:123], a[152:155], v[184:187], v[120:123]\n"
"ds_read_b64_tr_b16 a[138:139], v229 offset:51008\n"
"v_cvt_pk_bf16_f32 v145, v146, v147\n"
"v_mfma_f32_16x16x32_bf16 v[28:31], a[156:159], v[160:163], v[28:31]\n"
"v_exp_f32 v149, v149\n"
"v_mfma_f32_16x16x32_bf16 v[60:63], a[156:159], v[168:171], v[60:63]\n"
"v_exp_f32 v150, v150\n"
"v_mfma_f32_16x16x32_bf16 v[92:95], a[156:159], v[176:179], v[92:95]\n"
"ds_read_b64_tr_b16 a[140:141], v229 offset:50784\n"
"v_cvt_pk_bf16_f32 v146, v148, v149\n"
"v_mfma_f32_16x16x32_bf16 v[124:127], a[156:159], v[184:187], v[124:127]\n"
"s_waitcnt lgkmcnt(7)\n"
"v_exp_f32 v151, v151\n"
"v_mfma_f32_16x16x32_bf16 v[160:163], a[96:99], a[192:195], v[192:195]\n"
"ds_read_b64_tr_b16 a[142:143], v229 offset:51040\n"
"v_cvt_pk_bf16_f32 v147, v150, v151\n"
"v_mfma_f32_16x16x32_bf16 v[160:163], a[100:103], a[196:199], v[160:163]\n"
"v_exp_f32 v152, v152\n"
"v_mfma_f32_16x16x32_bf16 v[160:163], a[104:107], a[200:203], v[160:163]\n"
"v_exp_f32 v153, v153\n"
"v_mfma_f32_16x16x32_bf16 v[160:163], a[108:111], a[204:207], v[160:163]\n"
"v_exp_f32 v154, v154\n"
"v_mfma_f32_16x16x32_bf16 v[216:219], a[160:163], v[144:147], v[216:219]\n"
"ds_read_b64_tr_b16 a[144:145], v229 offset:50816\n"
"v_cvt_pk_bf16_f32 v152, v152, v153\n"
"v_mfma_f32_16x16x32_bf16 v[164:167], a[112:115], a[192:195], v[192:195]\n"
"s_add_u32 m0, s76, 33312\n"
"v_exp_f32 v155, v155\n"
"v_mfma_f32_16x16x32_bf16 v[164:167], a[116:119], a[196:199], v[164:167]\n"
"v_exp_f32 v156, v156\n"
"v_mfma_f32_16x16x32_bf16 v[164:167], a[120:123], a[200:203], v[164:167]\n"
"ds_read_b64_tr_b16 a[146:147], v229 offset:51072\n"
"v_cvt_pk_bf16_f32 v153, v154, v155\n"
"v_mfma_f32_16x16x32_bf16 v[164:167], a[124:127], a[204:207], v[164:167]\n"
"v_exp_f32 v157, v157\n"
"v_mfma_f32_16x16x32_bf16 v[168:171], a[96:99], a[208:211], v[196:199]\n"
"buffer_load_dwordx4 v230, s[44:47], s78 offen offset:2048 lds\n"
"v_mfma_f32_16x16x32_bf16 v[168:171], a[100:103], a[212:215], v[168:171]\n"
"v_exp_f32 v158, v158\n"
"v_mfma_f32_16x16x32_bf16 v[168:171], a[104:107], a[216:219], v[168:171]\n"
"ds_read_b64_tr_b16 a[148:149], v229 offset:50848\n"
"v_cvt_pk_bf16_f32 v154, v156, v157\n"
"v_mfma_f32_16x16x32_bf16 v[168:171], a[108:111], a[220:223], v[168:171]\n"
"v_exp_f32 v159, v159\n"
"v_mfma_f32_16x16x32_bf16 v[172:175], a[112:115], a[208:211], v[196:199]\n"
"ds_read_b64_tr_b16 a[150:151], v229 offset:51104\n"
"v_cvt_pk_bf16_f32 v155, v158, v159\n"
"v_mfma_f32_16x16x32_bf16 v[172:175], a[116:119], a[212:215], v[172:175]\n"
"v_exp_f32 v160, v160\n"
"v_mfma_f32_16x16x32_bf16 v[172:175], a[120:123], a[216:219], v[172:175]\n"
"v_exp_f32 v161, v161\n"
"v_mfma_f32_16x16x32_bf16 v[172:175], a[124:127], a[220:223], v[172:175]\n"
"v_exp_f32 v162, v162\n"
"v_mfma_f32_16x16x32_bf16 v[220:223], a[160:163], v[152:155], v[220:223]\n"
"ds_read_b64_tr_b16 a[152:153], v229 offset:50880\n"
"v_cvt_pk_bf16_f32 v160, v160, v161\n"
"v_mfma_f32_16x16x32_bf16 v[176:179], a[96:99], a[224:227], v[200:203]\n"
"v_exp_f32 v163, v163\n"
"v_mfma_f32_16x16x32_bf16 v[176:179], a[100:103], a[228:231], v[176:179]\n"
"v_exp_f32 v164, v164\n"
"v_mfma_f32_16x16x32_bf16 v[176:179], a[104:107], a[232:235], v[176:179]\n"
"ds_read_b64_tr_b16 a[154:155], v229 offset:51136\n"
"v_cvt_pk_bf16_f32 v161, v162, v163\n"
"v_mfma_f32_16x16x32_bf16 v[176:179], a[108:111], a[236:239], v[176:179]\n"
"s_add_u32 m0, s76, 33328\n"
"v_exp_f32 v165, v165\n"
"v_mfma_f32_16x16x32_bf16 v[180:183], a[112:115], a[224:227], v[200:203]\n"
"v_exp_f32 v166, v166\n"
"v_mfma_f32_16x16x32_bf16 v[180:183], a[116:119], a[228:231], v[180:183]\n"
"ds_read_b64_tr_b16 a[156:157], v229 offset:50912\n"
"v_cvt_pk_bf16_f32 v162, v164, v165\n"
"v_mfma_f32_16x16x32_bf16 v[180:183], a[120:123], a[232:235], v[180:183]\n"
"v_exp_f32 v167, v167\n"
"v_mfma_f32_16x16x32_bf16 v[180:183], a[124:127], a[236:239], v[180:183]\n"
"buffer_load_dwordx4 v230, s[44:47], s78 offen offset:3072 lds\n"
"v_mfma_f32_16x16x32_bf16 v[184:187], a[96:99], a[240:243], v[204:207]\n"
"s_add_u32 s78, s78, 16384\n"
"ds_read_b64_tr_b16 a[158:159], v229 offset:51168\n"
"v_cvt_pk_bf16_f32 v163, v166, v167\n"
"v_mfma_f32_16x16x32_bf16 v[184:187], a[100:103], a[244:247], v[184:187]\n"
"v_mfma_f32_16x16x32_bf16 v[184:187], a[104:107], a[248:251], v[184:187]\n"
"v_mfma_f32_16x16x32_bf16 v[184:187], a[108:111], a[252:255], v[184:187]\n"
"v_mfma_f32_16x16x32_bf16 v[188:191], a[112:115], a[240:243], v[204:207]\n"
"v_mfma_f32_16x16x32_bf16 v[188:191], a[116:119], a[244:247], v[188:191]\n"
"v_mfma_f32_16x16x32_bf16 v[188:191], a[120:123], a[248:251], v[188:191]\n"
"v_mfma_f32_16x16x32_bf16 v[188:191], a[124:127], a[252:255], v[188:191]\n"
"L_at_b6:\n"
"s_waitcnt vmcnt(12) lgkmcnt(0)\n"
"s_barrier\n"
"v_mfma_f32_16x16x32_bf16 v[0:3], a[128:131], v[128:131], v[0:3]\n"
"v_exp_f32 v168, v168\n"
"v_mfma_f32_16x16x32_bf16 v[208:211], a[160:163], v[160:163], v[208:211]\n"
"v_exp_f32 v169, v169\n"
"v_mfma_f32_16x16x32_bf16 v[32:35], a[128:131], v[136:139], v[32:35]\n"
"ds_read_b128 a[64:67], v228 offset:0\n"
"v_add_f32 v212, v212, v168\n"
"v_mfma_f32_16x16x32_bf16 v[64:67], a[128:131], v[144:147], v[64:67]\n"
"v_exp_f32 v170, v170\n"
"v_mfma_f32_16x16x32_bf16 v[96:99], a[128:131], v[152:155], v[96:99]\n"
"ds_read_b128 a[68:71], v228 offset:16\n"
"v_add_f32 v212, v212, v169\n"
"v_mfma_f32_16x16x32_bf16 v[4:7], a[132:135], v[128:131], v[4:7]\n"
"ds_read_b128 a[72:75], v228 offset:128\n"
"v_cvt_pk_bf16_f32 v168, v168, v169\n"
"v_mfma_f32_16x16x32_bf16 v[36:39], a[132:135], v[136:139], v[36:39]\n"
"s_add_u32 m0, s77, 33792\n"
"v_exp_f32 v171, v171\n"
"v_mfma_f32_16x16x32_bf16 v[68:71], a[132:135], v[144:147], v[68:71]\n"
"ds_read_b128 a[76:79], v228 offset:144\n"
"v_add_f32 v212, v212, v170\n"
"v_mfma_f32_16x16x32_bf16 v[100:103], a[132:135], v[152:155], v[100:103]\n"
"v_exp_f32 v172, v172\n"
"v_mfma_f32_16x16x32_bf16 v[8:11], a[136:139], v[128:131], v[8:11]\n"
"ds_read_b128 a[80:83], v228 offset:256\n"
"v_add_f32 v212, v212, v171\n"
"v_mfma_f32_16x16x32_bf16 v[40:43], a[136:139], v[136:139], v[40:43]\n"
"buffer_load_dwordx4 v230, s[48:51], s79 offen offset:0 lds\n"
"v_mfma_f32_16x16x32_bf16 v[72:75], a[136:139], v[144:147], v[72:75]\n"
"ds_read_b128 a[84:87], v228 offset:272\n"
"v_cvt_pk_bf16_f32 v169, v170, v171\n"
"v_mfma_f32_16x16x32_bf16 v[104:107], a[136:139], v[152:155], v[104:107]\n"
"v_exp_f32 v173, v173\n"
"v_mfma_f32_16x16x32_bf16 v[12:15], a[140:143], v[128:131], v[12:15]\n"
"ds_read_b128 a[88:91], v228 offset:384\n"
"v_add_f32 v212, v212, v172\n"
"v_mfma_f32_16x16x32_bf16 v[44:47], a[140:143], v[136:139], v[44:47]\n"
"v_exp_f32 v174, v174\n"
"v_mfma_f32_16x16x32_bf16 v[76:79], a[140:143], v[144:147], v[76:79]\n"
"ds_read_b128 a[92:95], v228 offset:400\n"
"v_add_f32 v212, v212, v173\n"
"v_mfma_f32_16x16x32_bf16 v[108:111], a[140:143], v[152:155], v[108:111]\n"
"ds_read_b64_tr_b16 a[128:129], v229 offset:51200\n"
"v_cvt_pk_bf16_f32 v170, v172, v173\n"
"v_mfma_f32_16x16x32_bf16 v[16:19], a[144:147], v[128:131], v[16:19]\n"
"v_exp_f32 v175, v175\n"
"v_mfma_f32_16x16x32_bf16 v[48:51], a[144:147], v[136:139], v[48:51]\n"
"ds_read_b64_tr_b16 a[130:131], v229 offset:51456\n"
"v_add_f32 v212, v212, v174\n"
"v_mfma_f32_16x16x32_bf16 v[80:83], a[144:147], v[144:147], v[80:83]\n"
"ds_read_b64_tr_b16 a[132:133], v229 offset:51232\n"
"v_add_f32 v212, v212, v175\n"
"v_mfma_f32_16x16x32_bf16 v[112:115], a[144:147], v[152:155], v[112:115]\n"
"ds_read_b64_tr_b16 a[134:135], v229 offset:51488\n"
"v_cvt_pk_bf16_f32 v171, v174, v175\n"
"v_mfma_f32_16x16x32_bf16 v[20:23], a[148:151], v[128:131], v[20:23]\n"
"v_exp_f32 v176, v176\n"
"v_mfma_f32_16x16x32_bf16 v[52:55], a[148:151], v[136:139], v[52:55]\n"
"s_add_u32 m0, s77, 33824\n"
"v_exp_f32 v177, v177\n"
"v_mfma_f32_16x16x32_bf16 v[84:87], a[148:151], v[144:147], v[84:87]\n"
"v_exp_f32 v178, v178\n"
"v_mfma_f32_16x16x32_bf16 v[116:119], a[148:151], v[152:155], v[116:119]\n"
"ds_read_b64_tr_b16 a[136:137], v229 offset:51264\n"
"v_cvt_pk_bf16_f32 v176, v176, v177\n"
"v_mfma_f32_16x16x32_bf16 v[24:27], a[152:155], v[128:131], v[24:27]\n"
"v_exp_f32 v179, v179\n"
"v_mfma_f32_16x16x32_bf16 v[56:59], a[152:155], v[136:139], v[56:59]\n"
"buffer_load_dwordx4 v230, s[48:51], s79 offen offset:1024 lds\n"
"v_mfma_f32_16x16x32_bf16 v[88:91], a[152:155], v[144:147], v[88:91]\n"
"v_exp_f32 v180, v180\n"
"v_mfma_f32_16x16x32_bf16 v[120:123], a[152:155], v[152:155], v[120:123]\n"
"ds_read_b64_tr_b16 a[138:139], v229 offset:51520\n"
"v_cvt_pk_bf16_f32 v177, v178, v179\n"
"v_mfma_f32_16x16x32_bf16 v[28:31], a[156:159], v[128:131], v[28:31]\n"
"v_exp_f32 v181, v181\n"
"v_mfma_f32_16x16x32_bf16 v[60:63], a[156:159], v[136:139], v[60:63]\n"
"v_exp_f32 v182, v182\n"
"v_mfma_f32_16x16x32_bf16 v[92:95], a[156:159], v[144:147], v[92:95]\n"
"ds_read_b64_tr_b16 a[140:141], v229 offset:51296\n"
"v_cvt_pk_bf16_f32 v178, v180, v181\n"
"v_mfma_f32_16x16x32_bf16 v[124:127], a[156:159], v[152:155], v[124:127]\n"
"s_waitcnt lgkmcnt(7)\n"
"v_exp_f32 v183, v183\n"
"v_mfma_f32_16x16x32_bf16 v[128:131], a[64:67], a[192:195], v[192:195]\n"
"ds_read_b64_tr_b16 a[142:143], v229 offset:51552\n"
"v_cvt_pk_bf16_f32 v179, v182, v183\n"
"v_mfma_f32_16x16x32_bf16 v[128:131], a[68:71], a[196:199], v[128:131]\n"
"v_exp_f32 v184, v184\n"
"v_mfma_f32_16x16x32_bf16 v[128:131], a[72:75], a[200:203], v[128:131]\n"
"v_exp_f32 v185, v185\n"
"v_mfma_f32_16x16x32_bf16 v[128:131], a[76:79], a[204:207], v[128:131]\n"
"v_exp_f32 v186, v186\n"
"v_mfma_f32_16x16x32_bf16 v[216:219], a[160:163], v[176:179], v[216:219]\n"
"ds_read_b64_tr_b16 a[144:145], v229 offset:51328\n"
"v_cvt_pk_bf16_f32 v184, v184, v185\n"
"v_mfma_f32_16x16x32_bf16 v[132:135], a[80:83], a[192:195], v[192:195]\n"
"s_add_u32 m0, s77, 33856\n"
"v_exp_f32 v187, v187\n"
"v_mfma_f32_16x16x32_bf16 v[132:135], a[84:87], a[196:199], v[132:135]\n"
"v_exp_f32 v188, v188\n"
"v_mfma_f32_16x16x32_bf16 v[132:135], a[88:91], a[200:203], v[132:135]\n"
"ds_read_b64_tr_b16 a[146:147], v229 offset:51584\n"
"v_cvt_pk_bf16_f32 v185, v186, v187\n"
"v_mfma_f32_16x16x32_bf16 v[132:135], a[92:95], a[204:207], v[132:135]\n"
"v_exp_f32 v189, v189\n"
"v_mfma_f32_16x16x32_bf16 v[136:139], a[64:67], a[208:211], v[196:199]\n"
"buffer_load_dwordx4 v230, s[48:51], s79 offen offset:2048 lds\n"
"v_mfma_f32_16x16x32_bf16 v[136:139], a[68:71], a[212:215], v[136:139]\n"
"v_exp_f32 v190, v190\n"
"v_mfma_f32_16x16x32_bf16 v[136:139], a[72:75], a[216:219], v[136:139]\n"
"ds_read_b64_tr_b16 a[148:149], v229 offset:51360\n"
"v_cvt_pk_bf16_f32 v186, v188, v189\n"
"v_mfma_f32_16x16x32_bf16 v[136:139], a[76:79], a[220:223], v[136:139]\n"
"v_exp_f32 v191, v191\n"
"v_mfma_f32_16x16x32_bf16 v[140:143], a[80:83], a[208:211], v[196:199]\n"
"ds_read_b64_tr_b16 a[150:151], v229 offset:51616\n"
"v_cvt_pk_bf16_f32 v187, v190, v191\n"
"v_mfma_f32_16x16x32_bf16 v[140:143], a[84:87], a[212:215], v[140:143]\n"
"v_exp_f32 v128, v128\n"
"v_mfma_f32_16x16x32_bf16 v[140:143], a[88:91], a[216:219], v[140:143]\n"
"v_exp_f32 v129, v129\n"
"v_mfma_f32_16x16x32_bf16 v[140:143], a[92:95], a[220:223], v[140:143]\n"
"v_exp_f32 v130, v130\n"
"v_mfma_f32_16x16x32_bf16 v[220:223], a[160:163], v[184:187], v[220:223]\n"
"ds_read_b64_tr_b16 a[152:153], v229 offset:51392\n"
"v_cvt_pk_bf16_f32 v128, v128, v129\n"
"v_mfma_f32_16x16x32_bf16 v[144:147], a[64:67], a[224:227], v[200:203]\n"
"v_exp_f32 v131, v131\n"
"v_mfma_f32_16x16x32_bf16 v[144:147], a[68:71], a[228:231], v[144:147]\n"
"v_exp_f32 v132, v132\n"
"v_mfma_f32_16x16x32_bf16 v[144:147], a[72:75], a[232:235], v[144:147]\n"
"ds_read_b64_tr_b16 a[154:155], v229 offset:51648\n"
"v_cvt_pk_bf16_f32 v129, v130, v131\n"
"v_mfma_f32_16x16x32_bf16 v[144:147], a[76:79], a[236:239], v[144:147]\n"
"s_add_u32 m0, s77, 33888\n"
"v_exp_f32 v133, v133\n"
"v_mfma_f32_16x16x32_bf16 v[148:151], a[80:83], a[224:227], v[200:203]\n"
"v_exp_f32 v134, v134\n"
"v_mfma_f32_16x16x32_bf16 v[148:151], a[84:87], a[228:231], v[148:151]\n"
"ds_read_b64_tr_b16 a[156:157], v229 offset:51424\n"
"v_cvt_pk_bf16_f32 v130, v132, v133\n"
"v_mfma_f32_16x16x32_bf16 v[148:151], a[88:91], a[232:235], v[148:151]\n"
"v_exp_f32 v135, v135\n"
"v_mfma_f32_16x16x32_bf16 v[148:151], a[92:95], a[236:239], v[148:151]\n"
"buffer_load_dwordx4 v230, s[48:51], s79 offen offset:3072 lds\n"
"v_mfma_f32_16x16x32_bf16 v[152:155], a[64:67], a[240:243], v[204:207]\n"
"s_add_u32 s79, s79, 16384\n"
"ds_read_b64_tr_b16 a[158:159], v229 offset:51680\n"
"v_cvt_pk_bf16_f32 v131, v134, v135\n"
"v_mfma_f32_16x16x32_bf16 v[152:155], a[68:71], a[244:247], v[152:155]\n"
"v_mfma_f32_16x16x32_bf16 v[152:155], a[72:75], a[248:251], v[152:155]\n"
"v_mfma_f32_16x16x32_bf16 v[152:155], a[76:79], a[252:255], v[152:155]\n"
"v_mfma_f32_16x16x32_bf16 v[156:159], a[80:83], a[240:243], v[204:207]\n"
"v_mfma_f32_16x16x32_bf16 v[156:159], a[84:87], a[244:247], v[156:159]\n"
"v_mfma_f32_16x16x32_bf16 v[156:159], a[88:91], a[248:251], v[156:159]\n"
"v_mfma_f32_16x16x32_bf16 v[156:159], a[92:95], a[252:255], v[156:159]\n"
"L_at_b7:\n"
"s_waitcnt lgkmcnt(0)\n"
"v_mfma_f32_16x16x32_bf16 v[0:3], a[128:131], v[160:163], v[0:3]\n"
"v_exp_f32 v136, v136\n"
"v_mfma_f32_16x16x32_bf16 v[208:211], a[160:163], v[128:131], v[208:211]\n"
"v_exp_f32 v137, v137\n"
"v_mfma_f32_16x16x32_bf16 v[32:35], a[128:131], v[168:171], v[32:35]\n"
"ds_read_b128 a[96:99], v228 offset:512\n"
"v_add_f32 v212, v212, v136\n"
"v_mfma_f32_16x16x32_bf16 v[64:67], a[128:131], v[176:179], v[64:67]\n"
"v_exp_f32 v138, v138\n"
"v_mfma_f32_16x16x32_bf16 v[96:99], a[128:131], v[184:187], v[96:99]\n"
"ds_read_b128 a[100:103], v228 offset:528\n"
"v_add_f32 v212, v212, v137\n"
"v_mfma_f32_16x16x32_bf16 v[4:7], a[132:135], v[160:163], v[4:7]\n"
"ds_read_b128 a[104:107], v228 offset:640\n"
"v_cvt_pk_bf16_f32 v136, v136, v137\n"
"v_mfma_f32_16x16x32_bf16 v[36:39], a[132:135], v[168:171], v[36:39]\n"
"s_add_u32 m0, s76, 49920\n"
"v_exp_f32 v139, v139\n"
"v_mfma_f32_16x16x32_bf16 v[68:71], a[132:135], v[176:179], v[68:71]\n"
"ds_read_b128 a[108:111], v228 offset:656\n"
"v_add_f32 v212, v212, v138\n"
"v_mfma_f32_16x16x32_bf16 v[100:103], a[132:135], v[184:187], v[100:103]\n"
"v_exp_f32 v140, v140\n"
"v_mfma_f32_16x16x32_bf16 v[8:11], a[136:139], v[160:163], v[8:11]\n"
"ds_read_b128 a[112:115], v228 offset:768\n"
"v_add_f32 v212, v212, v139\n"
"v_mfma_f32_16x16x32_bf16 v[40:43], a[136:139], v[168:171], v[40:43]\n"
"buffer_load_dwordx4 v230, s[44:47], s78 offen offset:0 lds\n"
"v_mfma_f32_16x16x32_bf16 v[72:75], a[136:139], v[176:179], v[72:75]\n"
"ds_read_b128 a[116:119], v228 offset:784\n"
"v_cvt_pk_bf16_f32 v137, v138, v139\n"
"v_mfma_f32_16x16x32_bf16 v[104:107], a[136:139], v[184:187], v[104:107]\n"
"v_exp_f32 v141, v141\n"
"v_mfma_f32_16x16x32_bf16 v[12:15], a[140:143], v[160:163], v[12:15]\n"
"ds_read_b128 a[120:123], v228 offset:896\n"
"v_add_f32 v212, v212, v140\n"
"v_mfma_f32_16x16x32_bf16 v[44:47], a[140:143], v[168:171], v[44:47]\n"
"v_exp_f32 v142, v142\n"
"v_mfma_f32_16x16x32_bf16 v[76:79], a[140:143], v[176:179], v[76:79]\n"
"ds_read_b128 a[124:127], v228 offset:912\n"
"v_add_f32 v212, v212, v141\n"
"v_mfma_f32_16x16x32_bf16 v[108:111], a[140:143], v[184:187], v[108:111]\n"
"ds_read_b64_tr_b16 a[128:129], v229 offset:0\n"
"v_cvt_pk_bf16_f32 v138, v140, v141\n"
"v_mfma_f32_16x16x32_bf16 v[16:19], a[144:147], v[160:163], v[16:19]\n"
"v_exp_f32 v143, v143\n"
"v_mfma_f32_16x16x32_bf16 v[48:51], a[144:147], v[168:171], v[48:51]\n"
"ds_read_b64_tr_b16 a[130:131], v229 offset:256\n"
"v_add_f32 v212, v212, v142\n"
"v_mfma_f32_16x16x32_bf16 v[80:83], a[144:147], v[176:179], v[80:83]\n"
"ds_read_b64_tr_b16 a[132:133], v229 offset:32\n"
"v_add_f32 v212, v212, v143\n"
"v_mfma_f32_16x16x32_bf16 v[112:115], a[144:147], v[184:187], v[112:115]\n"
"ds_read_b64_tr_b16 a[134:135], v229 offset:288\n"
"v_cvt_pk_bf16_f32 v139, v142, v143\n"
"v_mfma_f32_16x16x32_bf16 v[20:23], a[148:151], v[160:163], v[20:23]\n"
"v_exp_f32 v144, v144\n"
"v_mfma_f32_16x16x32_bf16 v[52:55], a[148:151], v[168:171], v[52:55]\n"
"s_add_u32 m0, s76, 49936\n"
"v_exp_f32 v145, v145\n"
"v_mfma_f32_16x16x32_bf16 v[84:87], a[148:151], v[176:179], v[84:87]\n"
"v_exp_f32 v146, v146\n"
"v_mfma_f32_16x16x32_bf16 v[116:119], a[148:151], v[184:187], v[116:119]\n"
"ds_read_b64_tr_b16 a[136:137], v229 offset:64\n"
"v_cvt_pk_bf16_f32 v144, v144, v145\n"
"v_mfma_f32_16x16x32_bf16 v[24:27], a[152:155], v[160:163], v[24:27]\n"
"v_exp_f32 v147, v147\n"
"v_mfma_f32_16x16x32_bf16 v[56:59], a[152:155], v[168:171], v[56:59]\n"
"buffer_load_dwordx4 v230, s[44:47], s78 offen offset:1024 lds\n"
"v_mfma_f32_16x16x32_bf16 v[88:91], a[152:155], v[176:179], v[88:91]\n"
"v_exp_f32 v148, v148\n"
"v_mfma_f32_16x16x32_bf16 v[120:123], a[152:155], v[184:187], v[120:123]\n"
"ds_read_b64_tr_b16 a[138:139], v229 offset:320\n"
"v_cvt_pk_bf16_f32 v145, v146, v147\n"
"v_mfma_f32_16x16x32_bf16 v[28:31], a[156:159], v[160:163], v[28:31]\n"
"v_exp_f32 v149, v149\n"
"v_mfma_f32_16x16x32_bf16 v[60:63], a[156:159], v[168:171], v[60:63]\n"
"v_exp_f32 v150, v150\n"
"v_mfma_f32_16x16x32_bf16 v[92:95], a[156:159], v[176:179], v[92:95]\n"
"ds_read_b64_tr_b16 a[140:141], v229 offset:96\n"
"v_cvt_pk_bf16_f32 v146, v148, v149\n"
"v_mfma_f32_16x16x32_bf16 v[124:127], a[156:159], v[184:187], v[124:127]\n"
"s_waitcnt lgkmcnt(7)\n"
"v_exp_f32 v151, v151\n"
"v_mfma_f32_16x16x32_bf16 v[160:163], a[96:99], a[192:195], v[192:195]\n"
"ds_read_b64_tr_b16 a[142:143], v229 offset:352\n"
"v_cvt_pk_bf16_f32 v147, v150, v151\n"
"v_mfma_f32_16x16x32_bf16 v[160:163], a[100:103], a[196:199], v[160:163]\n"
"v_exp_f32 v152, v152\n"
"v_mfma_f32_16x16x32_bf16 v[160:163], a[104:107], a[200:203], v[160:163]\n"
"v_exp_f32 v153, v153\n"
"v_mfma_f32_16x16x32_bf16 v[160:163], a[108:111], a[204:207], v[160:163]\n"
"v_exp_f32 v154, v154\n"
"v_mfma_f32_16x16x32_bf16 v[216:219], a[160:163], v[144:147], v[216:219]\n"
"ds_read_b64_tr_b16 a[144:145], v229 offset:128\n"
"v_cvt_pk_bf16_f32 v152, v152, v153\n"
"v_mfma_f32_16x16x32_bf16 v[164:167], a[112:115], a[192:195], v[192:195]\n"
"s_add_u32 m0, s76, 49952\n"
"v_exp_f32 v155, v155\n"
"v_mfma_f32_16x16x32_bf16 v[164:167], a[116:119], a[196:199], v[164:167]\n"
"v_exp_f32 v156, v156\n"
"v_mfma_f32_16x16x32_bf16 v[164:167], a[120:123], a[200:203], v[164:167]\n"
"ds_read_b64_tr_b16 a[146:147], v229 offset:384\n"
"v_cvt_pk_bf16_f32 v153, v154, v155\n"
"v_mfma_f32_16x16x32_bf16 v[164:167], a[124:127], a[204:207], v[164:167]\n"
"v_exp_f32 v157, v157\n"
"v_mfma_f32_16x16x32_bf16 v[168:171], a[96:99], a[208:211], v[196:199]\n"
"buffer_load_dwordx4 v230, s[44:47], s78 offen offset:2048 lds\n"
"v_mfma_f32_16x16x32_bf16 v[168:171], a[100:103], a[212:215], v[168:171]\n"
"v_exp_f32 v158, v158\n"
"v_mfma_f32_16x16x32_bf16 v[168:171], a[104:107], a[216:219], v[168:171]\n"
"ds_read_b64_tr_b16 a[148:149], v229 offset:160\n"
"v_cvt_pk_bf16_f32 v154, v156, v157\n"
"v_mfma_f32_16x16x32_bf16 v[168:171], a[108:111], a[220:223], v[168:171]\n"
"v_exp_f32 v159, v159\n"
"v_mfma_f32_16x16x32_bf16 v[172:175], a[112:115], a[208:211], v[196:199]\n"
"L_tp0_1_1:\n"
"s_cbranch_execz L_tb0_1\n"
"ds_read_b64_tr_b16 a[150:151], v229 offset:416\n"
"v_cvt_pk_bf16_f32 v155, v158, v159\n"
"v_mfma_f32_16x16x32_bf16 v[172:175], a[116:119], a[212:215], v[172:175]\n"
"v_exp_f32 v160, v160\n"
"v_mfma_f32_16x16x32_bf16 v[172:175], a[120:123], a[216:219], v[172:175]\n"
"v_exp_f32 v161, v161\n"
"v_mfma_f32_16x16x32_bf16 v[172:175], a[124:127], a[220:223], v[172:175]\n"
"v_exp_f32 v162, v162\n"
"v_mfma_f32_16x16x32_bf16 v[220:223], a[160:163], v[152:155], v[220:223]\n"
"ds_read_b64_tr_b16 a[152:153], v229 offset:192\n"
"v_cvt_pk_bf16_f32 v160, v160, v161\n"
"v_mfma_f32_16x16x32_bf16 v[176:179], a[96:99], a[224:227], v[200:203]\n"
"v_exp_f32 v163, v163\n"
"v_mfma_f32_16x16x32_bf16 v[176:179], a[100:103], a[228:231], v[176:179]\n"
"v_exp_f32 v164, v164\n"
"v_mfma_f32_16x16x32_bf16 v[176:179], a[104:107], a[232:235], v[176:179]\n"
"ds_read_b64_tr_b16 a[154:155], v229 offset:448\n"
"v_cvt_pk_bf16_f32 v161, v162, v163\n"
"v_mfma_f32_16x16x32_bf16 v[176:179], a[108:111], a[236:239], v[176:179]\n"
"s_add_u32 m0, s76, 49968\n"
"v_exp_f32 v165, v165\n"
"v_mfma_f32_16x16x32_bf16 v[180:183], a[112:115], a[224:227], v[200:203]\n"
"v_exp_f32 v166, v166\n"
"v_mfma_f32_16x16x32_bf16 v[180:183], a[116:119], a[228:231], v[180:183]\n"
"ds_read_b64_tr_b16 a[156:157], v229 offset:224\n"
"v_cvt_pk_bf16_f32 v162, v164, v165\n"
"v_mfma_f32_16x16x32_bf16 v[180:183], a[120:123], a[232:235], v[180:183]\n"
"v_exp_f32 v167, v167\n"
"v_mfma_f32_16x16x32_bf16 v[180:183], a[124:127], a[236:239], v[180:183]\n"
"buffer_load_dwordx4 v230, s[44:47], s78 offen offset:3072 lds\n"
"v_mfma_f32_16x16x32_bf16 v[184:187], a[96:99], a[240:243], v[204:207]\n"
"s_add_u32 s78, s78, 16384\n"
"ds_read_b64_tr_b16 a[158:159], v229 offset:480\n"
"v_cvt_pk_bf16_f32 v163, v166, v167\n"
"v_mfma_f32_16x16x32_bf16 v[184:187], a[100:103], a[244:247], v[184:187]\n"
"v_mfma_f32_16x16x32_bf16 v[184:187], a[104:107], a[248:251], v[184:187]\n"
"v_mfma_f32_16x16x32_bf16 v[184:187], a[108:111], a[252:255], v[184:187]\n"
"v_mfma_f32_16x16x32_bf16 v[188:191], a[112:115], a[240:243], v[204:207]\n"
"v_mfma_f32_16x16x32_bf16 v[188:191], a[116:119], a[244:247], v[188:191]\n"
"v_mfma_f32_16x16x32_bf16 v[188:191], a[120:123], a[248:251], v[188:191]\n"
"v_mfma_f32_16x16x32_bf16 v[188:191], a[124:127], a[252:255], v[188:191]\n"
"s_sub_u32 s80, s80, 1\n"
"s_cbranch_scc0 L_at_b0\n"
"L_cw_post:\n"
"s_waitcnt vmcnt(12) lgkmcnt(0)\n"
"s_barrier\n"
"v_mfma_f32_16x16x32_bf16 v[0:3], a[128:131], v[128:131], v[0:3]\n"
"v_exp_f32 v168, v168\n"
"v_mfma_f32_16x16x32_bf16 v[208:211], a[160:163], v[160:163], v[208:211]\n"
"v_exp_f32 v169, v169\n"
"v_mfma_f32_16x16x32_bf16 v[32:35], a[128:131], v[136:139], v[32:35]\n"
"ds_read_b128 a[64:67], v228 offset:16640\n"
"v_add_f32 v212, v212, v168\n"
"v_mfma_f32_16x16x32_bf16 v[64:67], a[128:131], v[144:147], v[64:67]\n"
"v_exp_f32 v170, v170\n"
"v_mfma_f32_16x16x32_bf16 v[96:99], a[128:131], v[152:155], v[96:99]\n"
"ds_read_b128 a[68:71], v228 offset:16656\n"
"v_add_f32 v212, v212, v169\n"
"v_mfma_f32_16x16x32_bf16 v[4:7], a[132:135], v[128:131], v[4:7]\n"
"ds_read_b128 a[72:75], v228 offset:16768\n"
"v_cvt_pk_bf16_f32 v168, v168, v169\n"
"v_mfma_f32_16x16x32_bf16 v[36:39], a[132:135], v[136:139], v[36:39]\n"
"s_add_u32 m0, s77, 50688\n"
"v_exp_f32 v171, v171\n"
"v_mfma_f32_16x16x32_bf16 v[68:71], a[132:135], v[144:147], v[68:71]\n"
"ds_read_b128 a[76:79], v228 offset:16784\n"
"v_add_f32 v212, v212, v170\n"
"v_mfma_f32_16x16x32_bf16 v[100:103], a[132:135], v[152:155], v[100:103]\n"
"v_exp_f32 v172, v172\n"
"v_mfma_f32_16x16x32_bf16 v[8:11], a[136:139], v[128:131], v[8:11]\n"
"ds_read_b128 a[80:83], v228 offset:16896\n"
"v_add_f32 v212, v212, v171\n"
"v_mfma_f32_16x16x32_bf16 v[40:43], a[136:139], v[136:139], v[40:43]\n"
"buffer_load_dwordx4 v230, s[48:51], s79 offen offset:0 lds\n"
"v_mfma_f32_16x16x32_bf16 v[72:75], a[136:139], v[144:147], v[72:75]\n"
"ds_read_b128 a[84:87], v228 offset:16912\n"
"v_cvt_pk_bf16_f32 v169, v170, v171\n"
"v_mfma_f32_16x16x32_bf16 v[104:107], a[136:139], v[152:155], v[104:107]\n"
"v_exp_f32 v173, v173\n"
"v_mfma_f32_16x16x32_bf16 v[12:15], a[140:143], v[128:131], v[12:15]\n"
"ds_read_b128 a[88:91], v228 offset:17024\n"
"v_add_f32 v212, v212, v172\n"
"v_mfma_f32_16x16x32_bf16 v[44:47], a[140:143], v[136:139], v[44:47]\n"
"v_exp_f32 v174, v174\n"
"v_mfma_f32_16x16x32_bf16 v[76:79], a[140:143], v[144:147], v[76:79]\n"
"ds_read_b128 a[92:95], v228 offset:17040\n"
"v_add_f32 v212, v212, v173\n"
"v_mfma_f32_16x16x32_bf16 v[108:111], a[140:143], v[152:155], v[108:111]\n"
"ds_read_b64_tr_b16 a[128:129], v229 offset:512\n"
"v_cvt_pk_bf16_f32 v170, v172, v173\n"
"v_mfma_f32_16x16x32_bf16 v[16:19], a[144:147], v[128:131], v[16:19]\n"
"v_exp_f32 v175, v175\n"
"v_mfma_f32_16x16x32_bf16 v[48:51], a[144:147], v[136:139], v[48:51]\n"
"ds_read_b64_tr_b16 a[130:131], v229 offset:768\n"
"v_add_f32 v212, v212, v174\n"
"v_mfma_f32_16x16x32_bf16 v[80:83], a[144:147], v[144:147], v[80:83]\n"
"ds_read_b64_tr_b16 a[132:133], v229 offset:544\n"
"v_add_f32 v212, v212, v175\n"
"v_mfma_f32_16x16x32_bf16 v[112:115], a[144:147], v[152:155], v[112:115]\n"
"global_load_dwordx4 a[60:63], v246, %4 offset:0\n"
"v_mfma_f32_16x16x32_bf16 v[20:23], a[148:151], v[128:131], v[20:23]\n"
"ds_read_b64_tr_b16 a[134:135], v229 offset:800\n"
"v_cvt_pk_bf16_f32 v171, v174, v175\n"
"v_mfma_f32_16x16x32_bf16 v[52:55], a[148:151], v[136:139], v[52:55]\n"
"s_add_u32 m0, s77, 50720\n"
"v_exp_f32 v176, v176\n"
"v_mfma_f32_16x16x32_bf16 v[84:87], a[148:151], v[144:147], v[84:87]\n"
"v_exp_f32 v177, v177\n"
"v_mfma_f32_16x16x32_bf16 v[116:119], a[148:151], v[152:155], v[116:119]\n"
"v_exp_f32 v178, v178\n"
"v_mfma_f32_16x16x32_bf16 v[24:27], a[152:155], v[128:131], v[24:27]\n"
"ds_read_b64_tr_b16 a[136:137], v229 offset:576\n"
"v_cvt_pk_bf16_f32 v176, v176, v177\n"
"v_mfma_f32_16x16x32_bf16 v[56:59], a[152:155], v[136:139], v[56:59]\n"
"buffer_load_dwordx4 v230, s[48:51], s79 offen offset:1024 lds\n"
"v_mfma_f32_16x16x32_bf16 v[88:91], a[152:155], v[144:147], v[88:91]\n"
"v_exp_f32 v179, v179\n"
"v_mfma_f32_16x16x32_bf16 v[120:123], a[152:155], v[152:155], v[120:123]\n"
"v_exp_f32 v180, v180\n"
"v_mfma_f32_16x16x32_bf16 v[28:31], a[156:159], v[128:131], v[28:31]\n"
"ds_read_b64_tr_b16 a[138:139], v229 offset:832\n"
"v_cvt_pk_bf16_f32 v177, v178, v179\n"
"v_mfma_f32_16x16x32_bf16 v[60:63], a[156:159], v[136:139], v[60:63]\n"
"v_exp_f32 v181, v181\n"
"v_mfma_f32_16x16x32_bf16 v[92:95], a[156:159], v[144:147], v[92:95]\n"
"v_exp_f32 v182, v182\n"
"v_mfma_f32_16x16x32_bf16 v[124:127], a[156:159], v[152:155], v[124:127]\n"
"s_waitcnt lgkmcnt(6)\n"
"ds_read_b64_tr_b16 a[140:141], v229 offset:608\n"
"v_cvt_pk_bf16_f32 v178, v180, v181\n"
"v_mfma_f32_16x16x32_bf16 v[128:131], a[64:67], a[192:195], v[192:195]\n"
"v_exp_f32 v183, v183\n"
"v_mfma_f32_16x16x32_bf16 v[128:131], a[68:71], a[196:199], v[128:131]\n"
"ds_read_b64_tr_b16 a[142:143], v229 offset:864\n"
"v_cvt_pk_bf16_f32 v179, v182, v183\n"
"v_mfma_f32_16x16x32_bf16 v[128:131], a[72:75], a[200:203], v[128:131]\n"
"v_exp_f32 v184, v184\n"
"v_mfma_f32_16x16x32_bf16 v[128:131], a[76:79], a[204:207], v[128:131]\n"
"v_exp_f32 v185, v185\n"
"v_mfma_f32_16x16x32_bf16 v[216:219], a[160:163], v[176:179], v[216:219]\n"
"v_exp_f32 v186, v186\n"
"v_mfma_f32_16x16x32_bf16 v[132:135], a[80:83], a[192:195], v[192:195]\n"
"global_load_dwordx4 a[60:63], v246, %4 offset:1024\n"
"v_mfma_f32_16x16x32_bf16 v[132:135], a[84:87], a[196:199], v[132:135]\n"
"s_add_u32 m0, s77, 50752\n"
"ds_read_b64_tr_b16 a[144:145], v229 offset:640\n"
"v_cvt_pk_bf16_f32 v184, v184, v185\n"
"v_mfma_f32_16x16x32_bf16 v[132:135], a[88:91], a[200:203], v[132:135]\n"
"v_exp_f32 v187, v187\n"
"v_mfma_f32_16x16x32_bf16 v[132:135], a[92:95], a[204:207], v[132:135]\n"
"v_exp_f32 v188, v188\n"
"v_mfma_f32_16x16x32_bf16 v[136:139], a[64:67], a[208:211], v[196:199]\n"
"buffer_load_dwordx4 v230, s[48:51], s79 offen offset:2048 lds\n"
"v_mfma_f32_16x16x32_bf16 v[136:139], a[68:71], a[212:215], v[136:139]\n"
"ds_read_b64_tr_b16 a[146:147], v229 offset:896\n"
"v_cvt_pk_bf16_f32 v185, v186, v187\n"
"v_mfma_f32_16x16x32_bf16 v[136:139], a[72:75], a[216:219], v[136:139]\n"
"v_exp_f32 v189, v189\n"
"v_mfma_f32_16x16x32_bf16 v[136:139], a[76:79], a[220:223], v[136:139]\n"
"v_exp_f32 v190, v190\n"
"v_mfma_f32_16x16x32_bf16 v[140:143], a[80:83], a[208:211], v[196:199]\n"
"ds_read_b64_tr_b16 a[148:149], v229 offset:672\n"
"v_cvt_pk_bf16_f32 v186, v188, v189\n"
"v_mfma_f32_16x16x32_bf16 v[140:143], a[84:87], a[212:215], v[140:143]\n"
"v_exp_f32 v191, v191\n"
"v_mfma_f32_16x16x32_bf16 v[140:143], a[88:91], a[216:219], v[140:143]\n"
"ds_read_b64_tr_b16 a[150:151], v229 offset:928\n"
"v_cvt_pk_bf16_f32 v187, v190, v191\n"
"v_mfma_f32_16x16x32_bf16 v[140:143], a[92:95], a[220:223], v[140:143]\n"
"v_exp_f32 v128, v128\n"
"v_mfma_f32_16x16x32_bf16 v[220:223], a[160:163], v[184:187], v[220:223]\n"
"v_exp_f32 v129, v129\n"
"v_mfma_f32_16x16x32_bf16 v[144:147], a[64:67], a[224:227], v[200:203]\n"
"v_exp_f32 v130, v130\n"
"v_mfma_f32_16x16x32_bf16 v[144:147], a[68:71], a[228:231], v[144:147]\n"
"ds_read_b64_tr_b16 a[152:153], v229 offset:704\n"
"v_cvt_pk_bf16_f32 v128, v128, v129\n"
"v_mfma_f32_16x16x32_bf16 v[144:147], a[72:75], a[232:235], v[144:147]\n"
"v_exp_f32 v131, v131\n"
"v_mfma_f32_16x16x32_bf16 v[144:147], a[76:79], a[236:239], v[144:147]\n"
"s_add_u32 m0, s77, 50784\n"
"v_exp_f32 v132, v132\n"
"v_mfma_f32_16x16x32_bf16 v[148:151], a[80:83], a[224:227], v[200:203]\n"
"ds_read_b64_tr_b16 a[154:155], v229 offset:960\n"
"v_cvt_pk_bf16_f32 v129, v130, v131\n"
"v_mfma_f32_16x16x32_bf16 v[148:151], a[84:87], a[228:231], v[148:151]\n"
"v_exp_f32 v133, v133\n"
"v_mfma_f32_16x16x32_bf16 v[148:151], a[88:91], a[232:235], v[148:151]\n"
"v_exp_f32 v134, v134\n"
"v_mfma_f32_16x16x32_bf16 v[148:151], a[92:95], a[236:239], v[148:151]\n"
"buffer_load_dwordx4 v230, s[48:51], s79 offen offset:3072 lds\n"
"v_mfma_f32_16x16x32_bf16 v[152:155], a[64:67], a[240:243], v[204:207]\n"
"s_add_u32 s79, s79, 16384\n"
"ds_read_b64_tr_b16 a[156:157], v229 offset:736\n"
"v_cvt_pk_bf16_f32 v130, v132, v133\n"
"v_mfma_f32_16x16x32_bf16 v[152:155], a[68:71], a[244:247], v[152:155]\n"
"ds_read_b64_tr_b16 a[158:159], v229 offset:992\n"
"v_mfma_f32_16x16x32_bf16 v[152:155], a[72:75], a[248:251], v[152:155]\n"
"v_exp_f32 v135, v135\n"
"v_mfma_f32_16x16x32_bf16 v[152:155], a[76:79], a[252:255], v[152:155]\n"
"v_cvt_pk_bf16_f32 v131, v134, v135\n"
"v_mfma_f32_16x16x32_bf16 v[156:159], a[80:83], a[240:243], v[204:207]\n"
"v_mfma_f32_16x16x32_bf16 v[156:159], a[84:87], a[244:247], v[156:159]\n"
"v_mfma_f32_16x16x32_bf16 v[156:159], a[88:91], a[248:251], v[156:159]\n"
"v_mfma_f32_16x16x32_bf16 v[156:159], a[92:95], a[252:255], v[156:159]\n"
"s_waitcnt lgkmcnt(0)\n"
"v_mfma_f32_16x16x32_bf16 v[0:3], a[128:131], v[160:163], v[0:3]\n"
"v_exp_f32 v136, v136\n"
"v_mfma_f32_16x16x32_bf16 v[208:211], a[160:163], v[128:131], v[208:211]\n"
"s_mov_b32 s88, 131072\n"
"v_exp_f32 v137, v137\n"
"v_mfma_f32_16x16x32_bf16 v[32:35], a[128:131], v[168:171], v[32:35]\n"
"ds_read_b128 a[96:99], v228 offset:17152\n"
"v_add_f32 v212, v212, v136\n"
"v_mfma_f32_16x16x32_bf16 v[64:67], a[128:131], v[176:179], v[64:67]\n"
"v_exp_f32 v138, v138\n"
"v_mfma_f32_16x16x32_bf16 v[96:99], a[128:131], v[184:187], v[96:99]\n"
"ds_read_b128 a[100:103], v228 offset:17168\n"
"v_add_f32 v212, v212, v137\n"
"v_mfma_f32_16x16x32_bf16 v[4:7], a[132:135], v[160:163], v[4:7]\n"
"buffer_load_dwordx4 a[12:15], v231, s[60:63], s88 offen sc1\n"
"v_mfma_f32_16x16x32_bf16 v[36:39], a[132:135], v[168:171], v[36:39]\n"
"ds_read_b128 a[104:107], v228 offset:17280\n"
"v_cvt_pk_bf16_f32 v136, v136, v137\n"
"v_mfma_f32_16x16x32_bf16 v[68:71], a[132:135], v[176:179], v[68:71]\n"
"v_exp_f32 v139, v139\n"
"v_mfma_f32_16x16x32_bf16 v[100:103], a[132:135], v[184:187], v[100:103]\n"
"ds_read_b128 a[108:111], v228 offset:17296\n"
"v_add_f32 v212, v212, v138\n"
"v_mfma_f32_16x16x32_bf16 v[8:11], a[136:139], v[160:163], v[8:11]\n"
"v_exp_f32 v140, v140\n"
"v_mfma_f32_16x16x32_bf16 v[40:43], a[136:139], v[168:171], v[40:43]\n"
"ds_read_b128 a[112:115], v228 offset:17408\n"
"v_add_f32 v212, v212, v139\n"
"v_mfma_f32_16x16x32_bf16 v[72:75], a[136:139], v[176:179], v[72:75]\n"
"ds_read_b128 a[116:119], v228 offset:17424\n"
"v_cvt_pk_bf16_f32 v137, v138, v139\n"
"v_mfma_f32_16x16x32_bf16 v[104:107], a[136:139], v[184:187], v[104:107]\n"
"v_exp_f32 v141, v141\n"
"v_mfma_f32_16x16x32_bf16 v[12:15], a[140:143], v[160:163], v[12:15]\n"
"global_load_dwordx4 a[60:63], v246, %4 offset:2048\n"
"v_mfma_f32_16x16x32_bf16 v[44:47], a[140:143], v[168:171], v[44:47]\n"
"ds_read_b128 a[120:123], v228 offset:17536\n"
"v_add_f32 v212, v212, v140\n"
"v_mfma_f32_16x16x32_bf16 v[76:79], a[140:143], v[176:179], v[76:79]\n"
"v_exp_f32 v142, v142\n"
"v_mfma_f32_16x16x32_bf16 v[108:111], a[140:143], v[184:187], v[108:111]\n"
"ds_read_b128 a[124:127], v228 offset:17552\n"
"v_add_f32 v212, v212, v141\n"
"v_mfma_f32_16x16x32_bf16 v[16:19], a[144:147], v[160:163], v[16:19]\n"
"ds_read_b64_tr_b16 a[128:129], v229 offset:16896\n"
"v_cvt_pk_bf16_f32 v138, v140, v141\n"
"v_mfma_f32_16x16x32_bf16 v[48:51], a[144:147], v[168:171], v[48:51]\n"
"v_exp_f32 v143, v143\n"
"v_mfma_f32_16x16x32_bf16 v[80:83], a[144:147], v[176:179], v[80:83]\n"
"ds_read_b64_tr_b16 a[130:131], v229 offset:17152\n"
"v_add_f32 v212, v212, v142\n"
"v_mfma_f32_16x16x32_bf16 v[112:115], a[144:147], v[184:187], v[112:115]\n"
"ds_read_b64_tr_b16 a[132:133], v229 offset:16928\n"
"v_add_f32 v212, v212, v143\n"
"v_mfma_f32_16x16x32_bf16 v[20:23], a[148:151], v[160:163], v[20:23]\n"
"ds_read_b64_tr_b16 a[134:135], v229 offset:17184\n"
"v_cvt_pk_bf16_f32 v139, v142, v143\n"
"v_mfma_f32_16x16x32_bf16 v[52:55], a[148:151], v[168:171], v[52:55]\n"
"buffer_load_dwordx4 a[28:31], v232, s[60:63], s88 offen sc1\n"
"v_mfma_f32_16x16x32_bf16 v[84:87], a[148:151], v[176:179], v[84:87]\n"
"v_exp_f32 v144, v144\n"
"v_mfma_f32_16x16x32_bf16 v[116:119], a[148:151], v[184:187], v[116:119]\n"
"v_exp_f32 v145, v145\n"
"v_mfma_f32_16x16x32_bf16 v[24:27], a[152:155], v[160:163], v[24:27]\n"
"v_exp_f32 v146, v146\n"
"v_mfma_f32_16x16x32_bf16 v[56:59], a[152:155], v[168:171], v[56:59]\n"
"ds_read_b64_tr_b16 a[136:137], v229 offset:16960\n"
"v_cvt_pk_bf16_f32 v144, v144, v145\n"
"v_mfma_f32_16x16x32_bf16 v[88:91], a[152:155], v[176:179], v[88:91]\n"
"v_exp_f32 v147, v147\n"
"v_mfma_f32_16x16x32_bf16 v[120:123], a[152:155], v[184:187], v[120:123]\n"
"v_exp_f32 v148, v148\n"
"v_mfma_f32_16x16x32_bf16 v[28:31], a[156:159], v[160:163], v[28:31]\n"
"ds_read_b64_tr_b16 a[138:139], v229 offset:17216\n"
"v_cvt_pk_bf16_f32 v145, v146, v147\n"
"v_mfma_f32_16x16x32_bf16 v[60:63], a[156:159], v[168:171], v[60:63]\n"
"global_load_dwordx4 a[60:63], v246, %4 offset:3072\n"
"v_mfma_f32_16x16x32_bf16 v[92:95], a[156:159], v[176:179], v[92:95]\n"
"v_exp_f32 v149, v149\n"
"v_mfma_f32_16x16x32_bf16 v[124:127], a[156:159], v[184:187], v[124:127]\n"
"s_waitcnt lgkmcnt(6)\n"
"v_exp_f32 v150, v150\n"
"v_mfma_f32_16x16x32_bf16 v[160:163], a[96:99], a[192:195], v[192:195]\n"
"ds_read_b64_tr_b16 a[140:141], v229 offset:16992\n"
"v_cvt_pk_bf16_f32 v146, v148, v149\n"
"v_mfma_f32_16x16x32_bf16 v[160:163], a[100:103], a[196:199], v[160:163]\n"
"v_exp_f32 v151, v151\n"
"v_mfma_f32_16x16x32_bf16 v[160:163], a[104:107], a[200:203], v[160:163]\n"
"ds_read_b64_tr_b16 a[142:143], v229 offset:17248\n"
"v_cvt_pk_bf16_f32 v147, v150, v151\n"
"v_mfma_f32_16x16x32_bf16 v[160:163], a[108:111], a[204:207], v[160:163]\n"
"v_exp_f32 v152, v152\n"
"v_mfma_f32_16x16x32_bf16 v[216:219], a[160:163], v[144:147], v[216:219]\n"
"v_exp_f32 v153, v153\n"
"v_mfma_f32_16x16x32_bf16 v[164:167], a[112:115], a[192:195], v[192:195]\n"
"v_exp_f32 v154, v154\n"
"v_mfma_f32_16x16x32_bf16 v[164:167], a[116:119], a[196:199], v[164:167]\n"
"buffer_load_dwordx4 a[44:47], v234, s[60:63], s88 offen sc1\n"
"v_mfma_f32_16x16x32_bf16 v[164:167], a[120:123], a[200:203], v[164:167]\n"
"ds_read_b64_tr_b16 a[144:145], v229 offset:17024\n"
"v_cvt_pk_bf16_f32 v152, v152, v153\n"
"v_mfma_f32_16x16x32_bf16 v[164:167], a[124:127], a[204:207], v[164:167]\n"
"v_exp_f32 v155, v155\n"
"v_mfma_f32_16x16x32_bf16 v[168:171], a[96:99], a[208:211], v[196:199]\n"
"v_exp_f32 v156, v156\n"
"v_mfma_f32_16x16x32_bf16 v[168:171], a[100:103], a[212:215], v[168:171]\n"
"ds_read_b64_tr_b16 a[146:147], v229 offset:17280\n"
"v_add_u32 v246, 4096, v246\n"
"v_mfma_f32_16x16x32_bf16 v[168:171], a[104:107], a[216:219], v[168:171]\n"
"ds_read_b64_tr_b16 a[148:149], v229 offset:17056\n"
"v_cvt_pk_bf16_f32 v153, v154, v155\n"
"v_mfma_f32_16x16x32_bf16 v[168:171], a[108:111], a[220:223], v[168:171]\n"
"v_exp_f32 v157, v157\n"
"v_mfma_f32_16x16x32_bf16 v[172:175], a[112:115], a[208:211], v[196:199]\n"
"v_exp_f32 v158, v158\n"
"v_mfma_f32_16x16x32_bf16 v[172:175], a[116:119], a[212:215], v[172:175]\n"
"global_load_dwordx4 a[60:63], v246, %4 offset:0\n"
"v_mfma_f32_16x16x32_bf16 v[172:175], a[120:123], a[216:219], v[172:175]\n"
"ds_read_b64_tr_b16 a[150:151], v229 offset:17312\n"
"v_cvt_pk_bf16_f32 v154, v156, v157\n"
"v_mfma_f32_16x16x32_bf16 v[172:175], a[124:127], a[220:223], v[172:175]\n"
"v_exp_f32 v159, v159\n"
"v_mfma_f32_16x16x32_bf16 v[176:179], a[96:99], a[224:227], v[200:203]\n"
"ds_read_b64_tr_b16 a[152:153], v229 offset:17088\n"
"v_cvt_pk_bf16_f32 v155, v158, v159\n"
"v_mfma_f32_16x16x32_bf16 v[176:179], a[100:103], a[228:231], v[176:179]\n"
"v_exp_f32 v160, v160\n"
"v_mfma_f32_16x16x32_bf16 v[176:179], a[104:107], a[232:235], v[176:179]\n"
"s_mov_b32 s88, 147456\n"
"v_exp_f32 v161, v161\n"
"v_mfma_f32_16x16x32_bf16 v[176:179], a[108:111], a[236:239], v[176:179]\n"
"v_exp_f32 v162, v162\n"
"v_mfma_f32_16x16x32_bf16 v[220:223], a[160:163], v[152:155], v[220:223]\n"
"ds_read_b64_tr_b16 a[154:155], v229 offset:17344\n"
"v_cvt_pk_bf16_f32 v160, v160, v161\n"
"v_mfma_f32_16x16x32_bf16 v[180:183], a[112:115], a[224:227], v[200:203]\n"
"v_exp_f32 v163, v163\n"
"v_mfma_f32_16x16x32_bf16 v[180:183], a[116:119], a[228:231], v[180:183]\n"
"buffer_load_dwordx4 a[16:19], v231, s[60:63], s88 offen sc1\n"
"v_mfma_f32_16x16x32_bf16 v[180:183], a[120:123], a[232:235], v[180:183]\n"
"v_exp_f32 v164, v164\n"
"v_mfma_f32_16x16x32_bf16 v[180:183], a[124:127], a[236:239], v[180:183]\n"
"ds_read_b64_tr_b16 a[156:157], v229 offset:17120\n"
"v_cvt_pk_bf16_f32 v161, v162, v163\n"
"v_mfma_f32_16x16x32_bf16 v[184:187], a[96:99], a[240:243], v[204:207]\n"
"v_exp_f32 v165, v165\n"
"v_mfma_f32_16x16x32_bf16 v[184:187], a[100:103], a[244:247], v[184:187]\n"
"ds_read_b64_tr_b16 a[158:159], v229 offset:17376\n"
"v_mfma_f32_16x16x32_bf16 v[184:187], a[104:107], a[248:251], v[184:187]\n"
"v_exp_f32 v166, v166\n"
"v_mfma_f32_16x16x32_bf16 v[184:187], a[108:111], a[252:255], v[184:187]\n"
"global_load_dwordx4 a[60:63], v246, %4 offset:1024\n"
"v_mfma_f32_16x16x32_bf16 v[188:191], a[112:115], a[240:243], v[204:207]\n"
"v_cvt_pk_bf16_f32 v162, v164, v165\n"
"v_mfma_f32_16x16x32_bf16 v[188:191], a[116:119], a[244:247], v[188:191]\n"
"v_exp_f32 v167, v167\n"
"v_mfma_f32_16x16x32_bf16 v[188:191], a[120:123], a[248:251], v[188:191]\n"
"v_cvt_pk_bf16_f32 v163, v166, v167\n"
"v_mfma_f32_16x16x32_bf16 v[188:191], a[124:127], a[252:255], v[188:191]\n"
"s_waitcnt vmcnt(18) lgkmcnt(0)\n"
"s_barrier\n"
"v_mfma_f32_16x16x32_bf16 v[0:3], a[128:131], v[128:131], v[0:3]\n"
"v_exp_f32 v168, v168\n"
"v_mfma_f32_16x16x32_bf16 v[208:211], a[160:163], v[160:163], v[208:211]\n"
"v_exp_f32 v169, v169\n"
"v_mfma_f32_16x16x32_bf16 v[32:35], a[128:131], v[136:139], v[32:35]\n"
"ds_read_b128 a[64:67], v228 offset:33280\n"
"v_add_f32 v212, v212, v168\n"
"v_mfma_f32_16x16x32_bf16 v[64:67], a[128:131], v[144:147], v[64:67]\n"
"v_exp_f32 v170, v170\n"
"v_mfma_f32_16x16x32_bf16 v[96:99], a[128:131], v[152:155], v[96:99]\n"
"ds_read_b128 a[68:71], v228 offset:33296\n"
"v_add_f32 v212, v212, v169\n"
"v_mfma_f32_16x16x32_bf16 v[4:7], a[132:135], v[128:131], v[4:7]\n"
"buffer_load_dwordx4 a[32:35], v232, s[60:63], s88 offen sc1\n"
"v_mfma_f32_16x16x32_bf16 v[36:39], a[132:135], v[136:139], v[36:39]\n"
"ds_read_b128 a[72:75], v228 offset:33408\n"
"v_cvt_pk_bf16_f32 v168, v168, v169\n"
"v_mfma_f32_16x16x32_bf16 v[68:71], a[132:135], v[144:147], v[68:71]\n"
"v_exp_f32 v171, v171\n"
"v_mfma_f32_16x16x32_bf16 v[100:103], a[132:135], v[152:155], v[100:103]\n"
"ds_read_b128 a[76:79], v228 offset:33424\n"
"v_add_f32 v212, v212, v170\n"
"v_mfma_f32_16x16x32_bf16 v[8:11], a[136:139], v[128:131], v[8:11]\n"
"v_exp_f32 v172, v172\n"
"v_mfma_f32_16x16x32_bf16 v[40:43], a[136:139], v[136:139], v[40:43]\n"
"ds_read_b128 a[80:83], v228 offset:33536\n"
"v_add_f32 v212, v212, v171\n"
"v_mfma_f32_16x16x32_bf16 v[72:75], a[136:139], v[144:147], v[72:75]\n"
"ds_read_b128 a[84:87], v228 offset:33552\n"
"v_cvt_pk_bf16_f32 v169, v170, v171\n"
"v_mfma_f32_16x16x32_bf16 v[104:107], a[136:139], v[152:155], v[104:107]\n"
"v_exp_f32 v173, v173\n"
"v_mfma_f32_16x16x32_bf16 v[12:15], a[140:143], v[128:131], v[12:15]\n"
"global_load_dwordx4 a[60:63], v246, %4 offset:2048\n"
"v_mfma_f32_16x16x32_bf16 v[44:47], a[140:143], v[136:139], v[44:47]\n"
"ds_read_b128 a[88:91], v228 offset:33664\n"
"v_add_f32 v212, v212, v172\n"
"v_mfma_f32_16x16x32_bf16 v[76:79], a[140:143], v[144:147], v[76:79]\n"
"v_exp_f32 v174, v174\n"
"v_mfma_f32_16x16x32_bf16 v[108:111], a[140:143], v[152:155], v[108:111]\n"
"ds_read_b128 a[92:95], v228 offset:33680\n"
"v_add_f32 v212, v212, v173\n"
"v_mfma_f32_16x16x32_bf16 v[16:19], a[144:147], v[128:131], v[16:19]\n"
"ds_read_b64_tr_b16 a[128:129], v229 offset:17408\n"
"v_cvt_pk_bf16_f32 v170, v172, v173\n"
"v_mfma_f32_16x16x32_bf16 v[48:51], a[144:147], v[136:139], v[48:51]\n"
"v_exp_f32 v175, v175\n"
"v_mfma_f32_16x16x32_bf16 v[80:83], a[144:147], v[144:147], v[80:83]\n"
"ds_read_b64_tr_b16 a[130:131], v229 offset:17664\n"
"v_add_f32 v212, v212, v174\n"
"v_mfma_f32_16x16x32_bf16 v[112:115], a[144:147], v[152:155], v[112:115]\n"
"ds_read_b64_tr_b16 a[132:133], v229 offset:17440\n"
"v_add_f32 v212, v212, v175\n"
"v_mfma_f32_16x16x32_bf16 v[20:23], a[148:151], v[128:131], v[20:23]\n"
"ds_read_b64_tr_b16 a[134:135], v229 offset:17696\n"
"v_cvt_pk_bf16_f32 v171, v174, v175\n"
"v_mfma_f32_16x16x32_bf16 v[52:55], a[148:151], v[136:139], v[52:55]\n"
"buffer_load_dwordx4 a[48:51], v234, s[60:63], s88 offen sc1\n"
"v_mfma_f32_16x16x32_bf16 v[84:87], a[148:151], v[144:147], v[84:87]\n"
"v_exp_f32 v176, v176\n"
"v_mfma_f32_16x16x32_bf16 v[116:119], a[148:151], v[152:155], v[116:119]\n"
"v_exp_f32 v177, v177\n"
"v_mfma_f32_16x16x32_bf16 v[24:27], a[152:155], v[128:131], v[24:27]\n"
"v_exp_f32 v178, v178\n"
"v_mfma_f32_16x16x32_bf16 v[56:59], a[152:155], v[136:139], v[56:59]\n"
"ds_read_b64_tr_b16 a[136:137], v229 offset:17472\n"
"v_cvt_pk_bf16_f32 v176, v176, v177\n"
"v_mfma_f32_16x16x32_bf16 v[88:91], a[152:155], v[144:147], v[88:91]\n"
"v_exp_f32 v179, v179\n"
"v_mfma_f32_16x16x32_bf16 v[120:123], a[152:155], v[152:155], v[120:123]\n"
"v_exp_f32 v180, v180\n"
"v_mfma_f32_16x16x32_bf16 v[28:31], a[156:159], v[128:131], v[28:31]\n"
"ds_read_b64_tr_b16 a[138:139], v229 offset:17728\n"
"v_cvt_pk_bf16_f32 v177, v178, v179\n"
"v_mfma_f32_16x16x32_bf16 v[60:63], a[156:159], v[136:139], v[60:63]\n"
"global_load_dwordx4 a[60:63], v246, %4 offset:3072\n"
"v_mfma_f32_16x16x32_bf16 v[92:95], a[156:159], v[144:147], v[92:95]\n"
"v_exp_f32 v181, v181\n"
"v_mfma_f32_16x16x32_bf16 v[124:127], a[156:159], v[152:155], v[124:127]\n"
"s_waitcnt lgkmcnt(6)\n"
"v_exp_f32 v182, v182\n"
"v_mfma_f32_16x16x32_bf16 v[128:131], a[64:67], a[192:195], v[192:195]\n"
"ds_read_b64_tr_b16 a[140:141], v229 offset:17504\n"
"v_cvt_pk_bf16_f32 v178, v180, v181\n"
"v_mfma_f32_16x16x32_bf16 v[128:131], a[68:71], a[196:199], v[128:131]\n"
"v_exp_f32 v183, v183\n"
"v_mfma_f32_16x16x32_bf16 v[128:131], a[72:75], a[200:203], v[128:131]\n"
"s_mov_b32 s88, 163840\n"
"ds_read_b64_tr_b16 a[142:143], v229 offset:17760\n"
"v_cvt_pk_bf16_f32 v179, v182, v183\n"
"v_mfma_f32_16x16x32_bf16 v[128:131], a[76:79], a[204:207], v[128:131]\n"
"v_exp_f32 v184, v184\n"
"v_mfma_f32_16x16x32_bf16 v[216:219], a[160:163], v[176:179], v[216:219]\n"
"v_exp_f32 v185, v185\n"
"v_mfma_f32_16x16x32_bf16 v[132:135], a[80:83], a[192:195], v[192:195]\n"
"v_exp_f32 v186, v186\n"
"v_mfma_f32_16x16x32_bf16 v[132:135], a[84:87], a[196:199], v[132:135]\n"
"buffer_load_dwordx4 a[20:23], v231, s[60:63], s88 offen sc1\n"
"v_mfma_f32_16x16x32_bf16 v[132:135], a[88:91], a[200:203], v[132:135]\n"
"ds_read_b64_tr_b16 a[144:145], v229 offset:17536\n"
"v_cvt_pk_bf16_f32 v184, v184, v185\n"
"v_mfma_f32_16x16x32_bf16 v[132:135], a[92:95], a[204:207], v[132:135]\n"
"v_exp_f32 v187, v187\n"
"v_mfma_f32_16x16x32_bf16 v[136:139], a[64:67], a[208:211], v[196:199]\n"
"v_exp_f32 v188, v188\n"
"v_mfma_f32_16x16x32_bf16 v[136:139], a[68:71], a[212:215], v[136:139]\n"
"ds_read_b64_tr_b16 a[146:147], v229 offset:17792\n"
"v_add_u32 v246, 4096, v246\n"
"v_mfma_f32_16x16x32_bf16 v[136:139], a[72:75], a[216:219], v[136:139]\n"
"ds_read_b64_tr_b16 a[148:149], v229 offset:17568\n"
"v_cvt_pk_bf16_f32 v185, v186, v187\n"
"v_mfma_f32_16x16x32_bf16 v[136:139], a[76:79], a[220:223], v[136:139]\n"
"v_exp_f32 v189, v189\n"
"v_mfma_f32_16x16x32_bf16 v[140:143], a[80:83], a[208:211], v[196:199]\n"
"v_exp_f32 v190, v190\n"
"v_mfma_f32_16x16x32_bf16 v[140:143], a[84:87], a[212:215], v[140:143]\n"
"global_load_dwordx4 a[60:63], v246, %4 offset:0\n"
"v_mfma_f32_16x16x32_bf16 v[140:143], a[88:91], a[216:219], v[140:143]\n"
"ds_read_b64_tr_b16 a[150:151], v229 offset:17824\n"
"v_cvt_pk_bf16_f32 v186, v188, v189\n"
"v_mfma_f32_16x16x32_bf16 v[140:143], a[92:95], a[220:223], v[140:143]\n"
"v_exp_f32 v191, v191\n"
"v_mfma_f32_16x16x32_bf16 v[144:147], a[64:67], a[224:227], v[200:203]\n"
"ds_read_b64_tr_b16 a[152:153], v229 offset:17600\n"
"v_cvt_pk_bf16_f32 v187, v190, v191\n"
"v_mfma_f32_16x16x32_bf16 v[144:147], a[68:71], a[228:231], v[144:147]\n"
"v_exp_f32 v128, v128\n"
"v_mfma_f32_16x16x32_bf16 v[144:147], a[72:75], a[232:235], v[144:147]\n"
"v_exp_f32 v129, v129\n"
"v_mfma_f32_16x16x32_bf16 v[144:147], a[76:79], a[236:239], v[144:147]\n"
"v_exp_f32 v130, v130\n"
"v_mfma_f32_16x16x32_bf16 v[220:223], a[160:163], v[184:187], v[220:223]\n"
"ds_read_b64_tr_b16 a[154:155], v229 offset:17856\n"
"v_cvt_pk_bf16_f32 v128, v128, v129\n"
"v_mfma_f32_16x16x32_bf16 v[148:151], a[80:83], a[224:227], v[200:203]\n"
"v_exp_f32 v131, v131\n"
"v_mfma_f32_16x16x32_bf16 v[148:151], a[84:87], a[228:231], v[148:151]\n"
"buffer_load_dwordx4 a[36:39], v232, s[60:63], s88 offen sc1\n"
"v_mfma_f32_16x16x32_bf16 v[148:151], a[88:91], a[232:235], v[148:151]\n"
"v_exp_f32 v132, v132\n"
"v_mfma_f32_16x16x32_bf16 v[148:151], a[92:95], a[236:239], v[148:151]\n"
"ds_read_b64_tr_b16 a[156:157], v229 offset:17632\n"
"v_cvt_pk_bf16_f32 v129, v130, v131\n"
"v_mfma_f32_16x16x32_bf16 v[152:155], a[64:67], a[240:243], v[204:207]\n"
"v_exp_f32 v133, v133\n"
"v_mfma_f32_16x16x32_bf16 v[152:155], a[68:71], a[244:247], v[152:155]\n"
"ds_read_b64_tr_b16 a[158:159], v229 offset:17888\n"
"v_mfma_f32_16x16x32_bf16 v[152:155], a[72:75], a[248:251], v[152:155]\n"
"v_exp_f32 v134, v134\n"
"v_mfma_f32_16x16x32_bf16 v[152:155], a[76:79], a[252:255], v[152:155]\n"
"buffer_load_dwordx4 a[52:55], v234, s[60:63], s88 offen sc1\n"
"v_mfma_f32_16x16x32_bf16 v[156:159], a[80:83], a[240:243], v[204:207]\n"
"v_cvt_pk_bf16_f32 v130, v132, v133\n"
"v_mfma_f32_16x16x32_bf16 v[156:159], a[84:87], a[244:247], v[156:159]\n"
"v_exp_f32 v135, v135\n"
"v_mfma_f32_16x16x32_bf16 v[156:159], a[88:91], a[248:251], v[156:159]\n"
"v_cvt_pk_bf16_f32 v131, v134, v135\n"
"v_mfma_f32_16x16x32_bf16 v[156:159], a[92:95], a[252:255], v[156:159]\n"
"s_mov_b64 s[52:53], %4\n"
"s_mul_i32 s99, %11, 1179648\n"
"s_mul_i32 s98, %13, 288\n"
"s_add_u32 s99, s99, s98\n"
"s_add_u32 s52, s52, s99\n"
"s_addc_u32 s53, s53, 0\n"
"s_and_b32 s53, s53, 0xffff\n"
"s_mov_b32 s54, 1179648\n"
"s_mov_b32 s55, 0x20000\n"
"s_lshl_b32 s72, s68, 10\n"
"s_add_u32 s71, s69, s72\n"
"s_waitcnt lgkmcnt(0)\n"
"v_mfma_f32_16x16x32_bf16 v[0:3], a[128:131], v[160:163], v[0:3]\n"
"v_exp_f32 v136, v136\n"
"v_mfma_f32_16x16x32_bf16 v[208:211], a[160:163], v[128:131], v[208:211]\n"
"s_mov_b32 s88, 180224\n"
"v_exp_f32 v137, v137\n"
"v_add_f32 v212, v212, v136\n"
"v_mfma_f32_16x16x32_bf16 v[32:35], a[128:131], v[168:171], v[32:35]\n"
"v_exp_f32 v138, v138\n"
"v_add_f32 v212, v212, v137\n"
"v_mfma_f32_16x16x32_bf16 v[64:67], a[128:131], v[176:179], v[64:67]\n"
"ds_read_b128 a[96:99], v228 offset:33792\n"
"v_cvt_pk_bf16_f32 v136, v136, v137\n"
"v_mfma_f32_16x16x32_bf16 v[96:99], a[128:131], v[184:187], v[96:99]\n"
"v_exp_f32 v139, v139\n"
"v_add_f32 v212, v212, v138\n"
"v_mfma_f32_16x16x32_bf16 v[4:7], a[132:135], v[160:163], v[4:7]\n"
"buffer_load_dwordx4 a[24:27], v231, s[60:63], s88 offen sc1\n"
"v_mfma_f32_16x16x32_bf16 v[36:39], a[132:135], v[168:171], v[36:39]\n"
"v_exp_f32 v140, v140\n"
"v_add_f32 v212, v212, v139\n"
"v_mfma_f32_16x16x32_bf16 v[68:71], a[132:135], v[176:179], v[68:71]\n"
"ds_read_b128 a[100:103], v228 offset:33808\n"
"v_cvt_pk_bf16_f32 v137, v138, v139\n"
"v_mfma_f32_16x16x32_bf16 v[100:103], a[132:135], v[184:187], v[100:103]\n"
"v_exp_f32 v141, v141\n"
"v_add_f32 v212, v212, v140\n"
"v_mfma_f32_16x16x32_bf16 v[8:11], a[136:139], v[160:163], v[8:11]\n"
"v_exp_f32 v142, v142\n"
"v_add_f32 v212, v212, v141\n"
"v_mfma_f32_16x16x32_bf16 v[40:43], a[136:139], v[168:171], v[40:43]\n"
"ds_read_b128 a[104:107], v228 offset:33920\n"
"v_cvt_pk_bf16_f32 v138, v140, v141\n"
"v_mfma_f32_16x16x32_bf16 v[72:75], a[136:139], v[176:179], v[72:75]\n"
"v_exp_f32 v143, v143\n"
"v_add_f32 v212, v212, v142\n"
"v_mfma_f32_16x16x32_bf16 v[104:107], a[136:139], v[184:187], v[104:107]\n"
"ds_read_b128 a[108:111], v228 offset:33936\n"
"v_add_f32 v212, v212, v143\n"
"v_mfma_f32_16x16x32_bf16 v[12:15], a[140:143], v[160:163], v[12:15]\n"
"buffer_load_dwordx4 a[40:43], v232, s[60:63], s88 offen sc1\n"
"v_mfma_f32_16x16x32_bf16 v[44:47], a[140:143], v[168:171], v[44:47]\n"
"ds_read_b128 a[112:115], v228 offset:34048\n"
"v_cvt_pk_bf16_f32 v139, v142, v143\n"
"v_mfma_f32_16x16x32_bf16 v[76:79], a[140:143], v[176:179], v[76:79]\n"
"v_exp_f32 v144, v144\n"
"v_mfma_f32_16x16x32_bf16 v[108:111], a[140:143], v[184:187], v[108:111]\n"
"v_exp_f32 v145, v145\n"
"v_mfma_f32_16x16x32_bf16 v[16:19], a[144:147], v[160:163], v[16:19]\n"
"v_exp_f32 v146, v146\n"
"v_mfma_f32_16x16x32_bf16 v[48:51], a[144:147], v[168:171], v[48:51]\n"
"ds_read_b128 a[116:119], v228 offset:34064\n"
"v_cvt_pk_bf16_f32 v144, v144, v145\n"
"v_mfma_f32_16x16x32_bf16 v[80:83], a[144:147], v[176:179], v[80:83]\n"
"v_exp_f32 v147, v147\n"
"v_mfma_f32_16x16x32_bf16 v[112:115], a[144:147], v[184:187], v[112:115]\n"
"ds_read_b128 a[120:123], v228 offset:34176\n"
"v_accvgpr_read_b32 v246, a164\n"
"v_mfma_f32_16x16x32_bf16 v[20:23], a[148:151], v[160:163], v[20:23]\n"
"L_tp0_2_1:\n"
"s_cbranch_execz L_tb0_2\n"
"v_exp_f32 v148, v148\n"
"v_mfma_f32_16x16x32_bf16 v[52:55], a[148:151], v[168:171], v[52:55]\n"
"buffer_load_dwordx4 a[56:59], v234, s[60:63], s88 offen sc1\n"
"v_mfma_f32_16x16x32_bf16 v[84:87], a[148:151], v[176:179], v[84:87]\n"
"ds_read_b128 a[124:127], v228 offset:34192\n"
"v_cvt_pk_bf16_f32 v145, v146, v147\n"
"v_mfma_f32_16x16x32_bf16 v[116:119], a[148:151], v[184:187], v[116:119]\n"
"v_exp_f32 v149, v149\n"
"v_mfma_f32_16x16x32_bf16 v[24:27], a[152:155], v[160:163], v[24:27]\n"
"v_exp_f32 v150, v150\n"
"v_mfma_f32_16x16x32_bf16 v[56:59], a[152:155], v[168:171], v[56:59]\n"
"s_add_u32 s88, s93, 0\n"
"ds_read_b64_tr_b16 a[128:129], v229 offset:33792\n"
"v_cvt_pk_bf16_f32 v146, v148, v149\n"
"v_mfma_f32_16x16x32_bf16 v[88:91], a[152:155], v[176:179], v[88:91]\n"
"v_exp_f32 v151, v151\n"
"v_mfma_f32_16x16x32_bf16 v[120:123], a[152:155], v[184:187], v[120:123]\n"
"ds_read_b64_tr_b16 a[130:131], v229 offset:34048\n"
"v_cvt_pk_bf16_f32 v147, v150, v151\n"
"v_mfma_f32_16x16x32_bf16 v[28:31], a[156:159], v[160:163], v[28:31]\n"
"v_exp_f32 v152, v152\n"
"v_mfma_f32_16x16x32_bf16 v[216:219], a[160:163], v[144:147], v[216:219]\n"
"buffer_load_dwordx2 a[0:1], v244, s[56:59], s88 offen offset:0\n"
"v_mfma_f32_16x16x32_bf16 v[60:63], a[156:159], v[168:171], v[60:63]\n"
"v_exp_f32 v153, v153\n"
"v_mfma_f32_16x16x32_bf16 v[92:95], a[156:159], v[176:179], v[92:95]\n"
"v_exp_f32 v154, v154\n"
"v_mfma_f32_16x16x32_bf16 v[124:127], a[156:159], v[184:187], v[124:127]\n"
"s_waitcnt lgkmcnt(2)\n"
"ds_read_b64_tr_b16 a[132:133], v229 offset:33824\n"
"v_cvt_pk_bf16_f32 v152, v152, v153\n"
"v_mfma_f32_16x16x32_bf16 v[160:163], a[96:99], a[192:195], v[192:195]\n"
"v_exp_f32 v155, v155\n"
"v_mfma_f32_16x16x32_bf16 v[160:163], a[100:103], a[196:199], v[160:163]\n"
"v_exp_f32 v156, v156\n"
"v_mfma_f32_16x16x32_bf16 v[160:163], a[104:107], a[200:203], v[160:163]\n"
"ds_read_b64_tr_b16 a[134:135], v229 offset:34080\n"
"v_cvt_pk_bf16_f32 v153, v154, v155\n"
"v_mfma_f32_16x16x32_bf16 v[160:163], a[108:111], a[204:207], v[160:163]\n"
"v_exp_f32 v157, v157\n"
"v_mfma_f32_16x16x32_bf16 v[164:167], a[112:115], a[192:195], v[192:195]\n"
"s_nop 0\n"
"ds_read_b64_tr_b16 a[136:137], v229 offset:33856\n"
"ds_read_b64_tr_b16 a[138:139], v229 offset:34112\n"
"v_mfma_f32_16x16x32_bf16 v[164:167], a[116:119], a[196:199], v[164:167]\n"
"buffer_load_dwordx2 a[4:5], v244, s[56:59], s88 offen offset:512\n"
"v_mfma_f32_16x16x32_bf16 v[164:167], a[120:123], a[200:203], v[164:167]\n"
"v_exp_f32 v158, v158\n"
"v_mfma_f32_16x16x32_bf16 v[164:167], a[124:127], a[204:207], v[164:167]\n"
"ds_read_b64_tr_b16 a[140:141], v229 offset:33888\n"
"v_cvt_pk_bf16_f32 v154, v156, v157\n"
"v_mfma_f32_16x16x32_bf16 v[168:171], a[96:99], a[208:211], v[196:199]\n"
"v_exp_f32 v159, v159\n"
"v_mfma_f32_16x16x32_bf16 v[168:171], a[100:103], a[212:215], v[168:171]\n"
"ds_read_b64_tr_b16 a[142:143], v229 offset:34144\n"
"v_cvt_pk_bf16_f32 v155, v158, v159\n"
"v_mfma_f32_16x16x32_bf16 v[168:171], a[104:107], a[216:219], v[168:171]\n"
"s_nop 0\n"
"ds_read_b64_tr_b16 a[144:145], v229 offset:33920\n"
"ds_read_b64_tr_b16 a[146:147], v229 offset:34176\n"
"v_mfma_f32_16x16x32_bf16 v[168:171], a[108:111], a[220:223], v[168:171]\n"
"v_exp_f32 v160, v160\n"
"v_mfma_f32_16x16x32_bf16 v[220:223], a[160:163], v[152:155], v[220:223]\n"
"v_exp_f32 v161, v161\n"
"v_mfma_f32_16x16x32_bf16 v[172:175], a[112:115], a[208:211], v[196:199]\n"
"buffer_load_dwordx2 a[8:9], v244, s[56:59], s88 offen offset:1024\n"
"v_mfma_f32_16x16x32_bf16 v[172:175], a[116:119], a[212:215], v[172:175]\n"
"v_exp_f32 v162, v162\n"
"v_mfma_f32_16x16x32_bf16 v[172:175], a[120:123], a[216:219], v[172:175]\n"
"ds_read_b64_tr_b16 a[148:149], v229 offset:33952\n"
"v_cvt_pk_bf16_f32 v160, v160, v161\n"
"v_mfma_f32_16x16x32_bf16 v[172:175], a[124:127], a[220:223], v[172:175]\n"
"v_exp_f32 v163, v163\n"
"v_mfma_f32_16x16x32_bf16 v[176:179], a[96:99], a[224:227], v[200:203]\n"
"v_exp_f32 v164, v164\n"
"v_mfma_f32_16x16x32_bf16 v[176:179], a[100:103], a[228:231], v[176:179]\n"
"s_add_u32 s88, s93, 18432\n"
"ds_read_b64_tr_b16 a[150:151], v229 offset:34208\n"
"v_cvt_pk_bf16_f32 v161, v162, v163\n"
"v_mfma_f32_16x16x32_bf16 v[176:179], a[104:107], a[232:235], v[176:179]\n"
"v_exp_f32 v165, v165\n"
"v_mfma_f32_16x16x32_bf16 v[176:179], a[108:111], a[236:239], v[176:179]\n"
"s_nop 0\n"
"ds_read_b64_tr_b16 a[152:153], v229 offset:33984\n"
"ds_read_b64_tr_b16 a[154:155], v229 offset:34240\n"
"v_mfma_f32_16x16x32_bf16 v[180:183], a[112:115], a[224:227], v[200:203]\n"
"v_exp_f32 v166, v166\n"
"v_mfma_f32_16x16x32_bf16 v[180:183], a[116:119], a[228:231], v[180:183]\n"
"buffer_load_dwordx2 a[2:3], v244, s[56:59], s88 offen offset:0\n"
"v_mfma_f32_16x16x32_bf16 v[180:183], a[120:123], a[232:235], v[180:183]\n"
"ds_read_b64_tr_b16 a[156:157], v229 offset:34016\n"
"v_cvt_pk_bf16_f32 v162, v164, v165\n"
"v_mfma_f32_16x16x32_bf16 v[180:183], a[124:127], a[236:239], v[180:183]\n"
"v_exp_f32 v167, v167\n"
"v_mfma_f32_16x16x32_bf16 v[184:187], a[96:99], a[240:243], v[204:207]\n"
"ds_read_b64_tr_b16 a[158:159], v229 offset:34272\n"
"v_cvt_pk_bf16_f32 v163, v166, v167\n"
"v_mfma_f32_16x16x32_bf16 v[184:187], a[100:103], a[244:247], v[184:187]\n"
"v_accvgpr_read_b32 v247, a165\n"
"v_accvgpr_read_b32 v248, a166\n"
"v_mfma_f32_16x16x32_bf16 v[184:187], a[104:107], a[248:251], v[184:187]\n"
"v_accvgpr_read_b32 v249, a167\n"
"v_accvgpr_read_b32 v250, a168\n"
"v_mfma_f32_16x16x32_bf16 v[184:187], a[108:111], a[252:255], v[184:187]\n"
"buffer_load_dwordx2 a[6:7], v244, s[56:59], s88 offen offset:512\n"
"v_mfma_f32_16x16x32_bf16 v[188:191], a[112:115], a[240:243], v[204:207]\n"
"v_accvgpr_read_b32 v251, a169\n"
"v_accvgpr_read_b32 v252, a170\n"
"v_mfma_f32_16x16x32_bf16 v[188:191], a[116:119], a[244:247], v[188:191]\n"
"v_accvgpr_read_b32 v253, a171\n"
"v_accvgpr_read_b32 v254, a172\n"
"v_mfma_f32_16x16x32_bf16 v[188:191], a[120:123], a[248:251], v[188:191]\n"
"v_mfma_f32_16x16x32_bf16 v[188:191], a[124:127], a[252:255], v[188:191]\n"
"s_nop 0\n"
"s_nop 0\n"
"buffer_load_dwordx2 a[10:11], v244, s[56:59], s88 offen offset:1024\n"
"s_waitcnt vmcnt(25) lgkmcnt(0)\n"
"s_barrier\n"
"v_mfma_f32_16x16x32_bf16 v[0:3], a[128:131], v[128:131], v[0:3]\n"
"v_exp_f32 v168, v168\n"
"v_mfma_f32_16x16x32_bf16 v[208:211], a[160:163], v[160:163], v[208:211]\n"
"s_add_u32 m0, s71, 0\n"
"v_exp_f32 v169, v169\n"
"v_mfma_f32_16x16x32_bf16 v[32:35], a[128:131], v[136:139], v[32:35]\n"
"s_mov_b32 s66, 0\n"
"ds_read_b128 a[64:67], v228 offset:49920\n"
"v_add_f32 v212, v212, v168\n"
"v_mfma_f32_16x16x32_bf16 v[64:67], a[128:131], v[144:147], v[64:67]\n"
"v_exp_f32 v170, v170\n"
"v_mfma_f32_16x16x32_bf16 v[96:99], a[128:131], v[152:155], v[96:99]\n"
"ds_read_b128 a[68:71], v228 offset:49936\n"
"v_add_f32 v212, v212, v169\n"
"v_mfma_f32_16x16x32_bf16 v[4:7], a[132:135], v[128:131], v[4:7]\n"
"buffer_load_dwordx4 v246, s[52:55], s66 offen lds\n"
"v_mfma_f32_16x16x32_bf16 v[36:39], a[132:135], v[136:139], v[36:39]\n"
"ds_read_b128 a[72:75], v228 offset:50048\n"
"v_cvt_pk_bf16_f32 v168, v168, v169\n"
"v_mfma_f32_16x16x32_bf16 v[68:71], a[132:135], v[144:147], v[68:71]\n"
"v_exp_f32 v171, v171\n"
"v_mfma_f32_16x16x32_bf16 v[100:103], a[132:135], v[152:155], v[100:103]\n"
"ds_read_b128 a[76:79], v228 offset:50064\n"
"v_add_f32 v212, v212, v170\n"
"v_mfma_f32_16x16x32_bf16 v[8:11], a[136:139], v[128:131], v[8:11]\n"
"v_exp_f32 v172, v172\n"
"v_mfma_f32_16x16x32_bf16 v[40:43], a[136:139], v[136:139], v[40:43]\n"
"ds_read_b128 a[80:83], v228 offset:50176\n"
"v_add_f32 v212, v212, v171\n"
"v_mfma_f32_16x16x32_bf16 v[72:75], a[136:139], v[144:147], v[72:75]\n"
"ds_read_b128 a[84:87], v228 offset:50192\n"
"v_cvt_pk_bf16_f32 v169, v170, v171\n"
"v_mfma_f32_16x16x32_bf16 v[104:107], a[136:139], v[152:155], v[104:107]\n"
"s_add_u32 m0, s71, 4096\n"
"v_exp_f32 v173, v173\n"
"v_mfma_f32_16x16x32_bf16 v[12:15], a[140:143], v[128:131], v[12:15]\n"
"ds_read_b128 a[88:91], v228 offset:50304\n"
"v_add_f32 v212, v212, v172\n"
"v_mfma_f32_16x16x32_bf16 v[44:47], a[140:143], v[136:139], v[44:47]\n"
"v_exp_f32 v174, v174\n"
"v_mfma_f32_16x16x32_bf16 v[76:79], a[140:143], v[144:147], v[76:79]\n"
"ds_read_b128 a[92:95], v228 offset:50320\n"
"v_add_f32 v212, v212, v173\n"
"v_mfma_f32_16x16x32_bf16 v[108:111], a[140:143], v[152:155], v[108:111]\n"
"buffer_load_dwordx4 v247, s[52:55], s66 offen lds\n"
"v_mfma_f32_16x16x32_bf16 v[16:19], a[144:147], v[128:131], v[16:19]\n"
"ds_read_b64_tr_b16 a[128:129], v229 offset:34304\n"
"v_cvt_pk_bf16_f32 v170, v172, v173\n"
"v_mfma_f32_16x16x32_bf16 v[48:51], a[144:147], v[136:139], v[48:51]\n"
"v_exp_f32 v175, v175\n"
"v_mfma_f32_16x16x32_bf16 v[80:83], a[144:147], v[144:147], v[80:83]\n"
"ds_read_b64_tr_b16 a[130:131], v229 offset:34560\n"
"v_add_f32 v212, v212, v174\n"
"v_mfma_f32_16x16x32_bf16 v[112:115], a[144:147], v[152:155], v[112:115]\n"
"ds_read_b64_tr_b16 a[132:133], v229 offset:34336\n"
"v_add_f32 v212, v212, v175\n"
"v_mfma_f32_16x16x32_bf16 v[20:23], a[148:151], v[128:131], v[20:23]\n"
"ds_read_b64_tr_b16 a[134:135], v229 offset:34592\n"
"v_cvt_pk_bf16_f32 v171, v174, v175\n"
"v_mfma_f32_16x16x32_bf16 v[52:55], a[148:151], v[136:139], v[52:55]\n"
"v_exp_f32 v176, v176\n"
"v_mfma_f32_16x16x32_bf16 v[84:87], a[148:151], v[144:147], v[84:87]\n"
"v_exp_f32 v177, v177\n"
"v_mfma_f32_16x16x32_bf16 v[116:119], a[148:151], v[152:155], v[116:119]\n"
"v_exp_f32 v178, v178\n"
"v_mfma_f32_16x16x32_bf16 v[24:27], a[152:155], v[128:131], v[24:27]\n"
"ds_read_b64_tr_b16 a[136:137], v229 offset:34368\n"
"v_cvt_pk_bf16_f32 v176, v176, v177\n"
"v_mfma_f32_16x16x32_bf16 v[56:59], a[152:155], v[136:139], v[56:59]\n"
"s_add_u32 m0, s71, 8192\n"
"v_exp_f32 v179, v179\n"
"v_mfma_f32_16x16x32_bf16 v[88:91], a[152:155], v[144:147], v[88:91]\n"
"v_exp_f32 v180, v180\n"
"v_mfma_f32_16x16x32_bf16 v[120:123], a[152:155], v[152:155], v[120:123]\n"
"ds_read_b64_tr_b16 a[138:139], v229 offset:34624\n"
"v_cvt_pk_bf16_f32 v177, v178, v179\n"
"v_mfma_f32_16x16x32_bf16 v[28:31], a[156:159], v[128:131], v[28:31]\n"
"v_exp_f32 v181, v181\n"
"v_mfma_f32_16x16x32_bf16 v[60:63], a[156:159], v[136:139], v[60:63]\n"
"buffer_load_dwordx4 v248, s[52:55], s66 offen lds\n"
"v_mfma_f32_16x16x32_bf16 v[92:95], a[156:159], v[144:147], v[92:95]\n"
"v_exp_f32 v182, v182\n"
"v_mfma_f32_16x16x32_bf16 v[124:127], a[156:159], v[152:155], v[124:127]\n"
"s_waitcnt lgkmcnt(6)\n"
"ds_read_b64_tr_b16 a[140:141], v229 offset:34400\n"
"v_cvt_pk_bf16_f32 v178, v180, v181\n"
"v_mfma_f32_16x16x32_bf16 v[128:131], a[64:67], a[192:195], v[192:195]\n"
"v_exp_f32 v183, v183\n"
"v_mfma_f32_16x16x32_bf16 v[128:131], a[68:71], a[196:199], v[128:131]\n"
"ds_read_b64_tr_b16 a[142:143], v229 offset:34656\n"
"v_cvt_pk_bf16_f32 v179, v182, v183\n"
"v_mfma_f32_16x16x32_bf16 v[128:131], a[72:75], a[200:203], v[128:131]\n"
"v_exp_f32 v184, v184\n"
"v_mfma_f32_16x16x32_bf16 v[128:131], a[76:79], a[204:207], v[128:131]\n"
"v_exp_f32 v185, v185\n"
"v_mfma_f32_16x16x32_bf16 v[216:219], a[160:163], v[176:179], v[216:219]\n"
"v_exp_f32 v186, v186\n"
"v_mfma_f32_16x16x32_bf16 v[132:135], a[80:83], a[192:195], v[192:195]\n"
"ds_read_b64_tr_b16 a[144:145], v229 offset:34432\n"
"v_cvt_pk_bf16_f32 v184, v184, v185\n"
"v_mfma_f32_16x16x32_bf16 v[132:135], a[84:87], a[196:199], v[132:135]\n"
"v_exp_f32 v187, v187\n"
"v_mfma_f32_16x16x32_bf16 v[132:135], a[88:91], a[200:203], v[132:135]\n"
"v_exp_f32 v188, v188\n"
"v_mfma_f32_16x16x32_bf16 v[132:135], a[92:95], a[204:207], v[132:135]\n"
"ds_read_b64_tr_b16 a[146:147], v229 offset:34688\n"
"v_cvt_pk_bf16_f32 v185, v186, v187\n"
"v_mfma_f32_16x16x32_bf16 v[136:139], a[64:67], a[208:211], v[196:199]\n"
"v_exp_f32 v189, v189\n"
"v_mfma_f32_16x16x32_bf16 v[136:139], a[68:71], a[212:215], v[136:139]\n"
"s_add_u32 m0, s71, 12288\n"
"v_exp_f32 v190, v190\n"
"v_mfma_f32_16x16x32_bf16 v[136:139], a[72:75], a[216:219], v[136:139]\n"
"ds_read_b64_tr_b16 a[148:149], v229 offset:34464\n"
"v_cvt_pk_bf16_f32 v186, v188, v189\n"
"v_mfma_f32_16x16x32_bf16 v[136:139], a[76:79], a[220:223], v[136:139]\n"
"v_exp_f32 v191, v191\n"
"v_mfma_f32_16x16x32_bf16 v[140:143], a[80:83], a[208:211], v[196:199]\n"
"ds_read_b64_tr_b16 a[150:151], v229 offset:34720\n"
"v_cvt_pk_bf16_f32 v187, v190, v191\n"
"v_mfma_f32_16x16x32_bf16 v[140:143], a[84:87], a[212:215], v[140:143]\n"
"buffer_load_dwordx4 v249, s[52:55], s66 offen lds\n"
"v_mfma_f32_16x16x32_bf16 v[140:143], a[88:91], a[216:219], v[140:143]\n"
"v_exp_f32 v128, v128\n"
"v_mfma_f32_16x16x32_bf16 v[140:143], a[92:95], a[220:223], v[140:143]\n"
"v_exp_f32 v129, v129\n"
"v_mfma_f32_16x16x32_bf16 v[220:223], a[160:163], v[184:187], v[220:223]\n"
"v_exp_f32 v130, v130\n"
"v_mfma_f32_16x16x32_bf16 v[144:147], a[64:67], a[224:227], v[200:203]\n"
"ds_read_b64_tr_b16 a[152:153], v229 offset:34496\n"
"v_cvt_pk_bf16_f32 v128, v128, v129\n"
"v_mfma_f32_16x16x32_bf16 v[144:147], a[68:71], a[228:231], v[144:147]\n"
"v_exp_f32 v131, v131\n"
"v_mfma_f32_16x16x32_bf16 v[144:147], a[72:75], a[232:235], v[144:147]\n"
"v_exp_f32 v132, v132\n"
"v_mfma_f32_16x16x32_bf16 v[144:147], a[76:79], a[236:239], v[144:147]\n"
"ds_read_b64_tr_b16 a[154:155], v229 offset:34752\n"
"v_cvt_pk_bf16_f32 v129, v130, v131\n"
"v_mfma_f32_16x16x32_bf16 v[148:151], a[80:83], a[224:227], v[200:203]\n"
"v_exp_f32 v133, v133\n"
"v_mfma_f32_16x16x32_bf16 v[148:151], a[84:87], a[228:231], v[148:151]\n"
"s_add_u32 m0, s71, 16384\n"
"v_exp_f32 v134, v134\n"
"v_mfma_f32_16x16x32_bf16 v[148:151], a[88:91], a[232:235], v[148:151]\n"
"ds_read_b64_tr_b16 a[156:157], v229 offset:34528\n"
"v_cvt_pk_bf16_f32 v130, v132, v133\n"
"v_mfma_f32_16x16x32_bf16 v[148:151], a[92:95], a[236:239], v[148:151]\n"
"v_exp_f32 v135, v135\n"
"v_mfma_f32_16x16x32_bf16 v[152:155], a[64:67], a[240:243], v[204:207]\n"
"ds_read_b64_tr_b16 a[158:159], v229 offset:34784\n"
"v_cvt_pk_bf16_f32 v131, v134, v135\n"
"v_mfma_f32_16x16x32_bf16 v[152:155], a[68:71], a[244:247], v[152:155]\n"
"buffer_load_dwordx4 v250, s[52:55], s66 offen lds\n"
"v_mfma_f32_16x16x32_bf16 v[152:155], a[72:75], a[248:251], v[152:155]\n"
"v_mfma_f32_16x16x32_bf16 v[152:155], a[76:79], a[252:255], v[152:155]\n"
"v_mfma_f32_16x16x32_bf16 v[156:159], a[80:83], a[240:243], v[204:207]\n"
"v_mfma_f32_16x16x32_bf16 v[156:159], a[84:87], a[244:247], v[156:159]\n"
"v_mfma_f32_16x16x32_bf16 v[156:159], a[88:91], a[248:251], v[156:159]\n"
"v_mfma_f32_16x16x32_bf16 v[156:159], a[92:95], a[252:255], v[156:159]\n"
"s_waitcnt lgkmcnt(0)\n"
"v_mfma_f32_16x16x32_bf16 v[0:3], a[128:131], v[160:163], v[0:3]\n"
"v_exp_f32 v136, v136\n"
"v_mfma_f32_16x16x32_bf16 v[208:211], a[160:163], v[128:131], v[208:211]\n"
"v_exp_f32 v137, v137\n"
"v_mfma_f32_16x16x32_bf16 v[32:35], a[128:131], v[168:171], v[32:35]\n"
"ds_read_b128 a[96:99], v228 offset:50432\n"
"v_add_f32 v212, v212, v136\n"
"v_mfma_f32_16x16x32_bf16 v[64:67], a[128:131], v[176:179], v[64:67]\n"
"v_exp_f32 v138, v138\n"
"v_mfma_f32_16x16x32_bf16 v[96:99], a[128:131], v[184:187], v[96:99]\n"
"ds_read_b128 a[100:103], v228 offset:50448\n"
"v_add_f32 v212, v212, v137\n"
"v_mfma_f32_16x16x32_bf16 v[4:7], a[132:135], v[160:163], v[4:7]\n"
"ds_read_b128 a[104:107], v228 offset:50560\n"
"v_cvt_pk_bf16_f32 v136, v136, v137\n"
"v_mfma_f32_16x16x32_bf16 v[36:39], a[132:135], v[168:171], v[36:39]\n"
"v_exp_f32 v139, v139\n"
"v_mfma_f32_16x16x32_bf16 v[68:71], a[132:135], v[176:179], v[68:71]\n"
"ds_read_b128 a[108:111], v228 offset:50576\n"
"v_add_f32 v212, v212, v138\n"
"v_mfma_f32_16x16x32_bf16 v[100:103], a[132:135], v[184:187], v[100:103]\n"
"v_exp_f32 v140, v140\n"
"v_mfma_f32_16x16x32_bf16 v[8:11], a[136:139], v[160:163], v[8:11]\n"
"s_add_u32 m0, s71, 20480\n"
"ds_read_b128 a[112:115], v228 offset:50688\n"
"v_add_f32 v212, v212, v139\n"
"v_mfma_f32_16x16x32_bf16 v[40:43], a[136:139], v[168:171], v[40:43]\n"
"ds_read_b128 a[116:119], v228 offset:50704\n"
"v_cvt_pk_bf16_f32 v137, v138, v139\n"
"v_mfma_f32_16x16x32_bf16 v[72:75], a[136:139], v[176:179], v[72:75]\n"
"v_exp_f32 v141, v141\n"
"v_mfma_f32_16x16x32_bf16 v[104:107], a[136:139], v[184:187], v[104:107]\n"
"ds_read_b128 a[120:123], v228 offset:50816\n"
"v_add_f32 v212, v212, v140\n"
"v_mfma_f32_16x16x32_bf16 v[12:15], a[140:143], v[160:163], v[12:15]\n"
"buffer_load_dwordx4 v251, s[52:55], s66 offen lds\n"
"v_mfma_f32_16x16x32_bf16 v[44:47], a[140:143], v[168:171], v[44:47]\n"
"v_exp_f32 v142, v142\n"
"v_mfma_f32_16x16x32_bf16 v[76:79], a[140:143], v[176:179], v[76:79]\n"
"ds_read_b128 a[124:127], v228 offset:50832\n"
"v_add_f32 v212, v212, v141\n"
"v_mfma_f32_16x16x32_bf16 v[108:111], a[140:143], v[184:187], v[108:111]\n"
"ds_read_b64_tr_b16 a[128:129], v229 offset:50688\n"
"v_cvt_pk_bf16_f32 v138, v140, v141\n"
"v_mfma_f32_16x16x32_bf16 v[16:19], a[144:147], v[160:163], v[16:19]\n"
"v_exp_f32 v143, v143\n"
"v_mfma_f32_16x16x32_bf16 v[48:51], a[144:147], v[168:171], v[48:51]\n"
"ds_read_b64_tr_b16 a[130:131], v229 offset:50944\n"
"v_add_f32 v212, v212, v142\n"
"v_mfma_f32_16x16x32_bf16 v[80:83], a[144:147], v[176:179], v[80:83]\n"
"ds_read_b64_tr_b16 a[132:133], v229 offset:50720\n"
"v_add_f32 v212, v212, v143\n"
"v_mfma_f32_16x16x32_bf16 v[112:115], a[144:147], v[184:187], v[112:115]\n"
"ds_read_b64_tr_b16 a[134:135], v229 offset:50976\n"
"v_cvt_pk_bf16_f32 v139, v142, v143\n"
"v_mfma_f32_16x16x32_bf16 v[20:23], a[148:151], v[160:163], v[20:23]\n"
"v_exp_f32 v144, v144\n"
"v_mfma_f32_16x16x32_bf16 v[52:55], a[148:151], v[168:171], v[52:55]\n"
"v_exp_f32 v145, v145\n"
"v_mfma_f32_16x16x32_bf16 v[84:87], a[148:151], v[176:179], v[84:87]\n"
"v_exp_f32 v146, v146\n"
"v_mfma_f32_16x16x32_bf16 v[116:119], a[148:151], v[184:187], v[116:119]\n"
"ds_read_b64_tr_b16 a[136:137], v229 offset:50752\n"
"v_cvt_pk_bf16_f32 v144, v144, v145\n"
"v_mfma_f32_16x16x32_bf16 v[24:27], a[152:155], v[160:163], v[24:27]\n"
"v_exp_f32 v147, v147\n"
"v_mfma_f32_16x16x32_bf16 v[56:59], a[152:155], v[168:171], v[56:59]\n"
"s_add_u32 m0, s71, 24576\n"
"v_exp_f32 v148, v148\n"
"v_mfma_f32_16x16x32_bf16 v[88:91], a[152:155], v[176:179], v[88:91]\n"
"ds_read_b64_tr_b16 a[138:139], v229 offset:51008\n"
"v_cvt_pk_bf16_f32 v145, v146, v147\n"
"v_mfma_f32_16x16x32_bf16 v[120:123], a[152:155], v[184:187], v[120:123]\n"
"v_exp_f32 v149, v149\n"
"v_mfma_f32_16x16x32_bf16 v[28:31], a[156:159], v[160:163], v[28:31]\n"
"v_exp_f32 v150, v150\n"
"v_mfma_f32_16x16x32_bf16 v[60:63], a[156:159], v[168:171], v[60:63]\n"
"buffer_load_dwordx4 v252, s[52:55], s66 offen lds\n"
"v_mfma_f32_16x16x32_bf16 v[92:95], a[156:159], v[176:179], v[92:95]\n"
"ds_read_b64_tr_b16 a[140:141], v229 offset:50784\n"
"v_cvt_pk_bf16_f32 v146, v148, v149\n"
"v_mfma_f32_16x16x32_bf16 v[124:127], a[156:159], v[184:187], v[124:127]\n"
"s_waitcnt lgkmcnt(7)\n"
"v_exp_f32 v151, v151\n"
"v_mfma_f32_16x16x32_bf16 v[160:163], a[96:99], a[192:195], v[192:195]\n"
"ds_read_b64_tr_b16 a[142:143], v229 offset:51040\n"
"v_cvt_pk_bf16_f32 v147, v150, v151\n"
"v_mfma_f32_16x16x32_bf16 v[160:163], a[100:103], a[196:199], v[160:163]\n"
"v_exp_f32 v152, v152\n"
"v_mfma_f32_16x16x32_bf16 v[160:163], a[104:107], a[200:203], v[160:163]\n"
"v_exp_f32 v153, v153\n"
"v_mfma_f32_16x16x32_bf16 v[160:163], a[108:111], a[204:207], v[160:163]\n"
"v_exp_f32 v154, v154\n"
"v_mfma_f32_16x16x32_bf16 v[216:219], a[160:163], v[144:147], v[216:219]\n"
"ds_read_b64_tr_b16 a[144:145], v229 offset:50816\n"
"v_cvt_pk_bf16_f32 v152, v152, v153\n"
"v_mfma_f32_16x16x32_bf16 v[164:167], a[112:115], a[192:195], v[192:195]\n"
"v_exp_f32 v155, v155\n"
"v_mfma_f32_16x16x32_bf16 v[164:167], a[116:119], a[196:199], v[164:167]\n"
"v_exp_f32 v156, v156\n"
"v_mfma_f32_16x16x32_bf16 v[164:167], a[120:123], a[200:203], v[164:167]\n"
"ds_read_b64_tr_b16 a[146:147], v229 offset:51072\n"
"v_cvt_pk_bf16_f32 v153, v154, v155\n"
"v_mfma_f32_16x16x32_bf16 v[164:167], a[124:127], a[204:207], v[164:167]\n"
"v_exp_f32 v157, v157\n"
"v_mfma_f32_16x16x32_bf16 v[168:171], a[96:99], a[208:211], v[196:199]\n"
"v_exp_f32 v158, v158\n"
"v_mfma_f32_16x16x32_bf16 v[168:171], a[100:103], a[212:215], v[168:171]\n"
"s_add_u32 m0, s71, 28672\n"
"ds_read_b64_tr_b16 a[148:149], v229 offset:50848\n"
"v_cvt_pk_bf16_f32 v154, v156, v157\n"
"v_mfma_f32_16x16x32_bf16 v[168:171], a[104:107], a[216:219], v[168:171]\n"
"v_exp_f32 v159, v159\n"
"v_mfma_f32_16x16x32_bf16 v[168:171], a[108:111], a[220:223], v[168:171]\n"
"ds_read_b64_tr_b16 a[150:151], v229 offset:51104\n"
"v_cvt_pk_bf16_f32 v155, v158, v159\n"
"v_mfma_f32_16x16x32_bf16 v[172:175], a[112:115], a[208:211], v[196:199]\n"
"v_exp_f32 v160, v160\n"
"v_mfma_f32_16x16x32_bf16 v[172:175], a[116:119], a[212:215], v[172:175]\n"
"buffer_load_dwordx4 v253, s[52:55], s66 offen lds\n"
"v_mfma_f32_16x16x32_bf16 v[172:175], a[120:123], a[216:219], v[172:175]\n"
"v_exp_f32 v161, v161\n"
"v_mfma_f32_16x16x32_bf16 v[172:175], a[124:127], a[220:223], v[172:175]\n"
"v_exp_f32 v162, v162\n"
"v_mfma_f32_16x16x32_bf16 v[220:223], a[160:163], v[152:155], v[220:223]\n"
"ds_read_b64_tr_b16 a[152:153], v229 offset:50880\n"
"v_cvt_pk_bf16_f32 v160, v160, v161\n"
"v_mfma_f32_16x16x32_bf16 v[176:179], a[96:99], a[224:227], v[200:203]\n"
"v_exp_f32 v163, v163\n"
"v_mfma_f32_16x16x32_bf16 v[176:179], a[100:103], a[228:231], v[176:179]\n"
"v_exp_f32 v164, v164\n"
"v_mfma_f32_16x16x32_bf16 v[176:179], a[104:107], a[232:235], v[176:179]\n"
"ds_read_b64_tr_b16 a[154:155], v229 offset:51136\n"
"v_cvt_pk_bf16_f32 v161, v162, v163\n"
"v_mfma_f32_16x16x32_bf16 v[176:179], a[108:111], a[236:239], v[176:179]\n"
"v_exp_f32 v165, v165\n"
"v_mfma_f32_16x16x32_bf16 v[180:183], a[112:115], a[224:227], v[200:203]\n"
"v_exp_f32 v166, v166\n"
"v_mfma_f32_16x16x32_bf16 v[180:183], a[116:119], a[228:231], v[180:183]\n"
"ds_read_b64_tr_b16 a[156:157], v229 offset:50912\n"
"v_cvt_pk_bf16_f32 v162, v164, v165\n"
"v_mfma_f32_16x16x32_bf16 v[180:183], a[120:123], a[232:235], v[180:183]\n"
"v_exp_f32 v167, v167\n"
"v_mfma_f32_16x16x32_bf16 v[180:183], a[124:127], a[236:239], v[180:183]\n"
"s_add_u32 m0, s71, 32768\n"
"ds_read_b64_tr_b16 a[158:159], v229 offset:51168\n"
"v_cvt_pk_bf16_f32 v163, v166, v167\n"
"v_mfma_f32_16x16x32_bf16 v[184:187], a[96:99], a[240:243], v[204:207]\n"
"v_mfma_f32_16x16x32_bf16 v[184:187], a[100:103], a[244:247], v[184:187]\n"
"v_mfma_f32_16x16x32_bf16 v[184:187], a[104:107], a[248:251], v[184:187]\n"
"v_mfma_f32_16x16x32_bf16 v[184:187], a[108:111], a[252:255], v[184:187]\n"
"buffer_load_dwordx4 v254, s[52:55], s66 offen lds\n"
"v_mfma_f32_16x16x32_bf16 v[188:191], a[112:115], a[240:243], v[204:207]\n"
"v_mfma_f32_16x16x32_bf16 v[188:191], a[116:119], a[244:247], v[188:191]\n"
"v_mfma_f32_16x16x32_bf16 v[188:191], a[120:123], a[248:251], v[188:191]\n"
"v_mfma_f32_16x16x32_bf16 v[188:191], a[124:127], a[252:255], v[188:191]\n"
"s_mul_i32 s88, %14, 2097152\n"
"s_mul_i32 s98, %9, 524288\n"
"s_add_u32 s88, s88, s98\n"
"s_mul_i32 s98, %15, 4096\n"
"s_add_u32 s88, s88, s98\n"
"s_add_u32 s88, s88, 8388608\n"
"s_add_u32 s89, s88, 131072\n"
"s_add_u32 s90, s88, 262144\n"
"s_add_u32 s92, s88, 393216\n"
"s_waitcnt vmcnt(9) lgkmcnt(0)\n"
"s_barrier\n"
"v_mfma_f32_16x16x32_bf16 v[208:211], a[160:163], v[160:163], v[208:211]\n"
"s_add_u32 m0, s71, 36864\n"
"v_exp_f32 v168, v168\n"
"v_mfma_f32_16x16x32_bf16 v[0:3], a[128:131], v[128:131], v[0:3]\n"
"s_mov_b32 s66, 147456\n"
"v_exp_f32 v169, v169\n"
"v_mfma_f32_16x16x32_bf16 v[32:35], a[128:131], v[136:139], v[32:35]\n"
"buffer_load_dwordx4 v246, s[52:55], s66 offen lds\n"
"v_mfma_f32_16x16x32_bf16 v[64:67], a[128:131], v[144:147], v[64:67]\n"
"s_add_u32 m0, s71, 40960\n"
"v_add_f32 v212, v212, v168\n"
"v_mfma_f32_16x16x32_bf16 v[96:99], a[128:131], v[152:155], v[96:99]\n"
"s_nop 0\n"
"ds_read_b64_tr_b16 a[128:129], v229 offset:51200\n"
"ds_read_b64_tr_b16 a[130:131], v229 offset:51456\n"
"v_mfma_f32_16x16x32_bf16 v[4:7], a[132:135], v[128:131], v[4:7]\n"
"buffer_load_dwordx4 v247, s[52:55], s66 offen lds\n"
"v_mfma_f32_16x16x32_bf16 v[36:39], a[132:135], v[136:139], v[36:39]\n"
"s_add_u32 m0, s71, 45056\n"
"v_exp_f32 v170, v170\n"
"v_mfma_f32_16x16x32_bf16 v[68:71], a[132:135], v[144:147], v[68:71]\n"
"v_add_f32 v212, v212, v169\n"
"v_cvt_pk_bf16_f32 v168, v168, v169\n"
"v_mfma_f32_16x16x32_bf16 v[100:103], a[132:135], v[152:155], v[100:103]\n"
"buffer_load_dwordx4 v248, s[52:55], s66 offen lds\n"
"v_mfma_f32_16x16x32_bf16 v[8:11], a[136:139], v[128:131], v[8:11]\n"
"s_add_u32 m0, s71, 49152\n"
"ds_read_b64_tr_b16 a[132:133], v229 offset:51232\n"
"ds_read_b64_tr_b16 a[134:135], v229 offset:51488\n"
"v_mfma_f32_16x16x32_bf16 v[40:43], a[136:139], v[136:139], v[40:43]\n"
"v_exp_f32 v171, v171\n"
"v_mfma_f32_16x16x32_bf16 v[72:75], a[136:139], v[144:147], v[72:75]\n"
"buffer_load_dwordx4 v249, s[52:55], s66 offen lds\n"
"v_mfma_f32_16x16x32_bf16 v[104:107], a[136:139], v[152:155], v[104:107]\n"
"s_add_u32 m0, s71, 53248\n"
"ds_read_b64_tr_b16 a[136:137], v229 offset:51264\n"
"ds_read_b64_tr_b16 a[138:139], v229 offset:51520\n"
"v_mfma_f32_16x16x32_bf16 v[12:15], a[140:143], v[128:131], v[12:15]\n"
"v_add_f32 v212, v212, v170\n"
"v_mfma_f32_16x16x32_bf16 v[44:47], a[140:143], v[136:139], v[44:47]\n"
"buffer_load_dwordx4 v250, s[52:55], s66 offen lds\n"
"v_mfma_f32_16x16x32_bf16 v[76:79], a[140:143], v[144:147], v[76:79]\n"
"s_add_u32 m0, s71, 57344\n"
"v_exp_f32 v172, v172\n"
"v_mfma_f32_16x16x32_bf16 v[108:111], a[140:143], v[152:155], v[108:111]\n"
"s_nop 0\n"
"ds_read_b64_tr_b16 a[140:141], v229 offset:51296\n"
"ds_read_b64_tr_b16 a[142:143], v229 offset:51552\n"
"v_mfma_f32_16x16x32_bf16 v[16:19], a[144:147], v[128:131], v[16:19]\n"
"buffer_load_dwordx4 v251, s[52:55], s66 offen lds\n"
"v_mfma_f32_16x16x32_bf16 v[48:51], a[144:147], v[136:139], v[48:51]\n"
"s_add_u32 m0, s71, 61440\n"
"v_add_f32 v212, v212, v171\n"
"v_cvt_pk_bf16_f32 v169, v170, v171\n"
"v_mfma_f32_16x16x32_bf16 v[80:83], a[144:147], v[144:147], v[80:83]\n"
"v_exp_f32 v173, v173\n"
"v_mfma_f32_16x16x32_bf16 v[112:115], a[144:147], v[152:155], v[112:115]\n"
"buffer_load_dwordx4 v252, s[52:55], s66 offen lds\n"
"v_mfma_f32_16x16x32_bf16 v[20:23], a[148:151], v[128:131], v[20:23]\n"
"s_add_u32 m0, s71, 65536\n"
"ds_read_b64_tr_b16 a[144:145], v229 offset:51328\n"
"ds_read_b64_tr_b16 a[146:147], v229 offset:51584\n"
"v_mfma_f32_16x16x32_bf16 v[52:55], a[148:151], v[136:139], v[52:55]\n"
"v_add_f32 v212, v212, v172\n"
"v_mfma_f32_16x16x32_bf16 v[84:87], a[148:151], v[144:147], v[84:87]\n"
"buffer_load_dwordx4 v253, s[52:55], s66 offen lds\n"
"v_mfma_f32_16x16x32_bf16 v[116:119], a[148:151], v[152:155], v[116:119]\n"
"s_add_u32 m0, s71, 69632\n"
"ds_read_b64_tr_b16 a[148:149], v229 offset:51360\n"
"ds_read_b64_tr_b16 a[150:151], v229 offset:51616\n"
"v_mfma_f32_16x16x32_bf16 v[24:27], a[152:155], v[128:131], v[24:27]\n"
"v_exp_f32 v174, v174\n"
"v_mfma_f32_16x16x32_bf16 v[56:59], a[152:155], v[136:139], v[56:59]\n"
"buffer_load_dwordx4 v254, s[52:55], s66 offen lds\n"
"v_mfma_f32_16x16x32_bf16 v[88:91], a[152:155], v[144:147], v[88:91]\n"
"s_add_u32 m0, s71, 73728\n"
"v_add_f32 v212, v212, v173\n"
"v_cvt_pk_bf16_f32 v170, v172, v173\n"
"v_mfma_f32_16x16x32_bf16 v[120:123], a[152:155], v[152:155], v[120:123]\n"
"s_mov_b32 s66, 294912\n"
"ds_read_b64_tr_b16 a[152:153], v229 offset:51392\n"
"ds_read_b64_tr_b16 a[154:155], v229 offset:51648\n"
"v_mfma_f32_16x16x32_bf16 v[28:31], a[156:159], v[128:131], v[28:31]\n"
"buffer_load_dwordx4 v246, s[52:55], s66 offen lds\n"
"v_mfma_f32_16x16x32_bf16 v[60:63], a[156:159], v[136:139], v[60:63]\n"
"s_add_u32 m0, s71, 77824\n"
"v_exp_f32 v175, v175\n"
"v_mfma_f32_16x16x32_bf16 v[92:95], a[156:159], v[144:147], v[92:95]\n"
"v_add_f32 v212, v212, v174\n"
"v_mfma_f32_16x16x32_bf16 v[124:127], a[156:159], v[152:155], v[124:127]\n"
"buffer_load_dwordx4 v247, s[52:55], s66 offen lds\n"
"s_nop 0\n"
"s_add_u32 m0, s71, 81920\n"
"ds_read_b64_tr_b16 a[156:157], v229 offset:51424\n"
"ds_read_b64_tr_b16 a[158:159], v229 offset:51680\n"
"s_nop 0\n"
"s_waitcnt lgkmcnt(0)\n"
"s_barrier\n"
"v_mfma_f32_16x16x32_bf16 v[0:3], a[128:131], v[160:163], v[0:3]\n"
"buffer_load_dwordx4 v248, s[52:55], s66 offen lds\n"
"v_mfma_f32_16x16x32_bf16 v[4:7], a[132:135], v[160:163], v[4:7]\n"
"s_add_u32 m0, s71, 86016\n"
"v_add_f32 v212, v212, v175\n"
"v_cvt_pk_bf16_f32 v171, v174, v175\n"
"v_mfma_f32_16x16x32_bf16 v[8:11], a[136:139], v[160:163], v[8:11]\n"
"v_exp_f32 v176, v176\n"
"v_mfma_f32_16x16x32_bf16 v[12:15], a[140:143], v[160:163], v[12:15]\n"
"buffer_load_dwordx4 v249, s[52:55], s66 offen lds\n"
"v_mfma_f32_16x16x32_bf16 v[16:19], a[144:147], v[160:163], v[16:19]\n"
"v_exp_f32 v177, v177\n"
"v_mfma_f32_16x16x32_bf16 v[20:23], a[148:151], v[160:163], v[20:23]\n"
"v_exp_f32 v178, v178\n"
"v_mfma_f32_16x16x32_bf16 v[24:27], a[152:155], v[160:163], v[24:27]\n"
"v_cvt_pk_bf16_f32 v176, v176, v177\n"
"v_mfma_f32_16x16x32_bf16 v[28:31], a[156:159], v[160:163], v[28:31]\n"
"v_exp_f32 v179, v179\n"
"v_mfma_f32_16x16x32_bf16 v[32:35], a[128:131], v[168:171], v[32:35]\n"
"v_exp_f32 v180, v180\n"
"v_mfma_f32_16x16x32_bf16 v[36:39], a[132:135], v[168:171], v[36:39]\n"
"v_cvt_pk_bf16_f32 v177, v178, v179\n"
"v_mfma_f32_16x16x32_bf16 v[40:43], a[136:139], v[168:171], v[40:43]\n"
"v_exp_f32 v181, v181\n"
"v_mfma_f32_16x16x32_bf16 v[44:47], a[140:143], v[168:171], v[44:47]\n"
"v_exp_f32 v182, v182\n"
"v_mfma_f32_16x16x32_bf16 v[48:51], a[144:147], v[168:171], v[48:51]\n"
"v_cvt_pk_bf16_f32 v178, v180, v181\n"
"v_mfma_f32_16x16x32_bf16 v[52:55], a[148:151], v[168:171], v[52:55]\n"
"v_exp_f32 v183, v183\n"
"v_mfma_f32_16x16x32_bf16 v[56:59], a[152:155], v[168:171], v[56:59]\n"
"v_cvt_pk_bf16_f32 v179, v182, v183\n"
"v_mfma_f32_16x16x32_bf16 v[60:63], a[156:159], v[168:171], v[60:63]\n"
"v_exp_f32 v184, v184\n"
"v_mfma_f32_16x16x32_bf16 v[216:219], a[160:163], v[176:179], v[216:219]\n"
"v_exp_f32 v185, v185\n"
"v_mfma_f32_16x16x32_bf16 v[64:67], a[128:131], v[176:179], v[64:67]\n"
"v_exp_f32 v186, v186\n"
"v_mfma_f32_16x16x32_bf16 v[68:71], a[132:135], v[176:179], v[68:71]\n"
"v_cvt_pk_bf16_f32 v184, v184, v185\n"
"v_mfma_f32_16x16x32_bf16 v[72:75], a[136:139], v[176:179], v[72:75]\n"
"v_exp_f32 v187, v187\n"
"v_mfma_f32_16x16x32_bf16 v[76:79], a[140:143], v[176:179], v[76:79]\n"
"v_exp_f32 v188, v188\n"
"v_mfma_f32_16x16x32_bf16 v[80:83], a[144:147], v[176:179], v[80:83]\n"
"v_cvt_pk_bf16_f32 v185, v186, v187\n"
"v_mfma_f32_16x16x32_bf16 v[84:87], a[148:151], v[176:179], v[84:87]\n"
"v_exp_f32 v189, v189\n"
"v_mfma_f32_16x16x32_bf16 v[88:91], a[152:155], v[176:179], v[88:91]\n"
"v_exp_f32 v190, v190\n"
"v_mfma_f32_16x16x32_bf16 v[92:95], a[156:159], v[176:179], v[92:95]\n"
"v_cvt_pk_bf16_f32 v186, v188, v189\n"
"L_tp0_3_1:\n"
"s_cbranch_execz L_tb0_3\n"
"v_exp_f32 v191, v191\n"
"s_nop 0\n"
"v_cvt_pk_bf16_f32 v187, v190, v191\n"
"s_nop 0\n"
"v_rcp_f32 v192, v208\n"
"s_nop 0\n"
"v_mfma_f32_16x16x32_bf16 v[220:223], a[160:163], v[184:187], v[220:223]\n"
"v_mul_f32 v0, v0, v192\n"
"v_mul_f32 v1, v1, v192\n"
"v_mfma_f32_16x16x32_bf16 v[96:99], a[128:131], v[184:187], v[96:99]\n"
"v_mul_f32 v2, v2, v192\n"
"v_mul_f32 v3, v3, v192\n"
"v_mfma_f32_16x16x32_bf16 v[100:103], a[132:135], v[184:187], v[100:103]\n"
"v_mul_f32 v4, v4, v192\n"
"v_mul_f32 v5, v5, v192\n"
"v_mfma_f32_16x16x32_bf16 v[104:107], a[136:139], v[184:187], v[104:107]\n"
"v_mul_f32 v6, v6, v192\n"
"v_mul_f32 v7, v7, v192\n"
"v_mfma_f32_16x16x32_bf16 v[108:111], a[140:143], v[184:187], v[108:111]\n"
"v_cvt_pk_bf16_f32 v0, v0, v1\n"
"v_cvt_pk_bf16_f32 v1, v2, v3\n"
"v_mfma_f32_16x16x32_bf16 v[112:115], a[144:147], v[184:187], v[112:115]\n"
"v_cvt_pk_bf16_f32 v2, v4, v5\n"
"v_cvt_pk_bf16_f32 v3, v6, v7\n"
"v_mfma_f32_16x16x32_bf16 v[116:119], a[148:151], v[184:187], v[116:119]\n"
"buffer_store_dwordx4 v[0:3], v230, s[60:63], s88 offen offset:0 sc1\n"
"v_mfma_f32_16x16x32_bf16 v[120:123], a[152:155], v[184:187], v[120:123]\n"
"v_mul_f32 v8, v8, v192\n"
"v_mul_f32 v9, v9, v192\n"
"v_mfma_f32_16x16x32_bf16 v[124:127], a[156:159], v[184:187], v[124:127]\n"
"v_mul_f32 v10, v10, v192\n"
"v_mul_f32 v11, v11, v192\n"
"v_mul_f32 v12, v12, v192\n"
"v_mul_f32 v13, v13, v192\n"
"v_mul_f32 v14, v14, v192\n"
"v_mul_f32 v15, v15, v192\n"
"v_cvt_pk_bf16_f32 v8, v8, v9\n"
"v_cvt_pk_bf16_f32 v9, v10, v11\n"
"v_cvt_pk_bf16_f32 v10, v12, v13\n"
"v_cvt_pk_bf16_f32 v11, v14, v15\n"
"buffer_store_dwordx4 v[8:11], v230, s[60:63], s88 offen offset:1024 sc1\n"
"v_mul_f32 v16, v16, v192\n"
"v_mul_f32 v17, v17, v192\n"
"v_mul_f32 v18, v18, v192\n"
"v_mul_f32 v19, v19, v192\n"
"v_mul_f32 v20, v20, v192\n"
"v_mul_f32 v21, v21, v192\n"
"v_mul_f32 v22, v22, v192\n"
"v_mul_f32 v23, v23, v192\n"
"v_cvt_pk_bf16_f32 v16, v16, v17\n"
"v_cvt_pk_bf16_f32 v17, v18, v19\n"
"v_cvt_pk_bf16_f32 v18, v20, v21\n"
"v_cvt_pk_bf16_f32 v19, v22, v23\n"
"buffer_store_dwordx4 v[16:19], v230, s[60:63], s88 offen offset:2048 sc1\n"
"v_mul_f32 v24, v24, v192\n"
"v_mul_f32 v25, v25, v192\n"
"v_mul_f32 v26, v26, v192\n"
"v_mul_f32 v27, v27, v192\n"
"v_mul_f32 v28, v28, v192\n"
"v_mul_f32 v29, v29, v192\n"
"v_mul_f32 v30, v30, v192\n"
"v_mul_f32 v31, v31, v192\n"
"v_cvt_pk_bf16_f32 v24, v24, v25\n"
"v_cvt_pk_bf16_f32 v25, v26, v27\n"
"v_cvt_pk_bf16_f32 v26, v28, v29\n"
"v_cvt_pk_bf16_f32 v27, v30, v31\n"
"buffer_store_dwordx4 v[24:27], v230, s[60:63], s88 offen offset:3072 sc1\n"
"s_nop 3\n"
"v_mov_b32 v213, v212\n"
"s_nop 1\n"
"v_permlane16_swap_b32 v213, v212\n"
"s_nop 1\n"
"v_add_f32 v212, v212, v213\n"
"v_mov_b32 v213, v212\n"
"s_nop 1\n"
"v_permlane32_swap_b32 v213, v212\n"
"s_nop 1\n"
"v_add_f32 v212, v212, v213\n"
"v_rcp_f32 v196, v212\n"
"s_nop 0\n"
"v_pk_mul_f32 v[32:33], v[32:33], v[196:197] op_sel_hi:[1,0]\n"
"v_pk_mul_f32 v[34:35], v[34:35], v[196:197] op_sel_hi:[1,0]\n"
"v_pk_mul_f32 v[36:37], v[36:37], v[196:197] op_sel_hi:[1,0]\n"
"v_pk_mul_f32 v[38:39], v[38:39], v[196:197] op_sel_hi:[1,0]\n"
"v_cvt_pk_bf16_f32 v32, v32, v33\n"
"v_cvt_pk_bf16_f32 v33, v34, v35\n"
"v_cvt_pk_bf16_f32 v34, v36, v37\n"
"v_cvt_pk_bf16_f32 v35, v38, v39\n"
"buffer_store_dwordx4 v[32:35], v230, s[60:63], s89 offen offset:0 sc1\n"
"v_pk_mul_f32 v[40:41], v[40:41], v[196:197] op_sel_hi:[1,0]\n"
"v_pk_mul_f32 v[42:43], v[42:43], v[196:197] op_sel_hi:[1,0]\n"
"v_pk_mul_f32 v[44:45], v[44:45], v[196:197] op_sel_hi:[1,0]\n"
"v_pk_mul_f32 v[46:47], v[46:47], v[196:197] op_sel_hi:[1,0]\n"
"v_cvt_pk_bf16_f32 v40, v40, v41\n"
"v_cvt_pk_bf16_f32 v41, v42, v43\n"
"v_cvt_pk_bf16_f32 v42, v44, v45\n"
"v_cvt_pk_bf16_f32 v43, v46, v47\n"
"buffer_store_dwordx4 v[40:43], v230, s[60:63], s89 offen offset:1024 sc1\n"
"v_pk_mul_f32 v[48:49], v[48:49], v[196:197] op_sel_hi:[1,0]\n"
"v_pk_mul_f32 v[50:51], v[50:51], v[196:197] op_sel_hi:[1,0]\n"
"v_pk_mul_f32 v[52:53], v[52:53], v[196:197] op_sel_hi:[1,0]\n"
"v_pk_mul_f32 v[54:55], v[54:55], v[196:197] op_sel_hi:[1,0]\n"
"v_cvt_pk_bf16_f32 v48, v48, v49\n"
"v_cvt_pk_bf16_f32 v49, v50, v51\n"
"v_cvt_pk_bf16_f32 v50, v52, v53\n"
"v_cvt_pk_bf16_f32 v51, v54, v55\n"
"buffer_store_dwordx4 v[48:51], v230, s[60:63], s89 offen offset:2048 sc1\n"
"v_pk_mul_f32 v[56:57], v[56:57], v[196:197] op_sel_hi:[1,0]\n"
"v_pk_mul_f32 v[58:59], v[58:59], v[196:197] op_sel_hi:[1,0]\n"
"v_pk_mul_f32 v[60:61], v[60:61], v[196:197] op_sel_hi:[1,0]\n"
"v_pk_mul_f32 v[62:63], v[62:63], v[196:197] op_sel_hi:[1,0]\n"
"v_cvt_pk_bf16_f32 v56, v56, v57\n"
"v_cvt_pk_bf16_f32 v57, v58, v59\n"
"v_cvt_pk_bf16_f32 v58, v60, v61\n"
"v_cvt_pk_bf16_f32 v59, v62, v63\n"
"buffer_store_dwordx4 v[56:59], v230, s[60:63], s89 offen offset:3072 sc1\n"
"v_rcp_f32 v200, v216\n"
"s_nop 0\n"
"v_pk_mul_f32 v[64:65], v[64:65], v[200:201] op_sel_hi:[1,0]\n"
"v_pk_mul_f32 v[66:67], v[66:67], v[200:201] op_sel_hi:[1,0]\n"
"v_pk_mul_f32 v[68:69], v[68:69], v[200:201] op_sel_hi:[1,0]\n"
"v_pk_mul_f32 v[70:71], v[70:71], v[200:201] op_sel_hi:[1,0]\n"
"v_cvt_pk_bf16_f32 v64, v64, v65\n"
"v_cvt_pk_bf16_f32 v65, v66, v67\n"
"v_cvt_pk_bf16_f32 v66, v68, v69\n"
"v_cvt_pk_bf16_f32 v67, v70, v71\n"
"buffer_store_dwordx4 v[64:67], v230, s[60:63], s90 offen offset:0 sc1\n"
"v_pk_mul_f32 v[72:73], v[72:73], v[200:201] op_sel_hi:[1,0]\n"
"v_pk_mul_f32 v[74:75], v[74:75], v[200:201] op_sel_hi:[1,0]\n"
"v_pk_mul_f32 v[76:77], v[76:77], v[200:201] op_sel_hi:[1,0]\n"
"v_pk_mul_f32 v[78:79], v[78:79], v[200:201] op_sel_hi:[1,0]\n"
"v_cvt_pk_bf16_f32 v72, v72, v73\n"
"v_cvt_pk_bf16_f32 v73, v74, v75\n"
"v_cvt_pk_bf16_f32 v74, v76, v77\n"
"v_cvt_pk_bf16_f32 v75, v78, v79\n"
"buffer_store_dwordx4 v[72:75], v230, s[60:63], s90 offen offset:1024 sc1\n"
"v_pk_mul_f32 v[80:81], v[80:81], v[200:201] op_sel_hi:[1,0]\n"
"v_pk_mul_f32 v[82:83], v[82:83], v[200:201] op_sel_hi:[1,0]\n"
"v_pk_mul_f32 v[84:85], v[84:85], v[200:201] op_sel_hi:[1,0]\n"
"v_pk_mul_f32 v[86:87], v[86:87], v[200:201] op_sel_hi:[1,0]\n"
"v_cvt_pk_bf16_f32 v80, v80, v81\n"
"v_cvt_pk_bf16_f32 v81, v82, v83\n"
"v_cvt_pk_bf16_f32 v82, v84, v85\n"
"v_cvt_pk_bf16_f32 v83, v86, v87\n"
"buffer_store_dwordx4 v[80:83], v230, s[60:63], s90 offen offset:2048 sc1\n"
"v_pk_mul_f32 v[88:89], v[88:89], v[200:201] op_sel_hi:[1,0]\n"
"v_pk_mul_f32 v[90:91], v[90:91], v[200:201] op_sel_hi:[1,0]\n"
"v_pk_mul_f32 v[92:93], v[92:93], v[200:201] op_sel_hi:[1,0]\n"
"v_pk_mul_f32 v[94:95], v[94:95], v[200:201] op_sel_hi:[1,0]\n"
"v_cvt_pk_bf16_f32 v88, v88, v89\n"
"v_cvt_pk_bf16_f32 v89, v90, v91\n"
"v_cvt_pk_bf16_f32 v90, v92, v93\n"
"v_cvt_pk_bf16_f32 v91, v94, v95\n"
"buffer_store_dwordx4 v[88:91], v230, s[60:63], s90 offen offset:3072 sc1\n"
"s_nop 3\n"
"v_rcp_f32 v204, v220\n"
"s_nop 0\n"
"v_pk_mul_f32 v[96:97], v[96:97], v[204:205] op_sel_hi:[1,0]\n"
"v_pk_mul_f32 v[98:99], v[98:99], v[204:205] op_sel_hi:[1,0]\n"
"v_pk_mul_f32 v[100:101], v[100:101], v[204:205] op_sel_hi:[1,0]\n"
"v_pk_mul_f32 v[102:103], v[102:103], v[204:205] op_sel_hi:[1,0]\n"
"v_cvt_pk_bf16_f32 v96, v96, v97\n"
"v_cvt_pk_bf16_f32 v97, v98, v99\n"
"v_cvt_pk_bf16_f32 v98, v100, v101\n"
"v_cvt_pk_bf16_f32 v99, v102, v103\n"
"buffer_store_dwordx4 v[96:99], v230, s[60:63], s92 offen offset:0 sc1\n"
"v_pk_mul_f32 v[104:105], v[104:105], v[204:205] op_sel_hi:[1,0]\n"
"v_pk_mul_f32 v[106:107], v[106:107], v[204:205] op_sel_hi:[1,0]\n"
"v_pk_mul_f32 v[108:109], v[108:109], v[204:205] op_sel_hi:[1,0]\n"
"v_pk_mul_f32 v[110:111], v[110:111], v[204:205] op_sel_hi:[1,0]\n"
"v_cvt_pk_bf16_f32 v104, v104, v105\n"
"v_cvt_pk_bf16_f32 v105, v106, v107\n"
"v_cvt_pk_bf16_f32 v106, v108, v109\n"
"v_cvt_pk_bf16_f32 v107, v110, v111\n"
"buffer_store_dwordx4 v[104:107], v230, s[60:63], s92 offen offset:1024 sc1\n"
"v_pk_mul_f32 v[112:113], v[112:113], v[204:205] op_sel_hi:[1,0]\n"
"v_pk_mul_f32 v[114:115], v[114:115], v[204:205] op_sel_hi:[1,0]\n"
"v_pk_mul_f32 v[116:117], v[116:117], v[204:205] op_sel_hi:[1,0]\n"
"v_pk_mul_f32 v[118:119], v[118:119], v[204:205] op_sel_hi:[1,0]\n"
"v_cvt_pk_bf16_f32 v112, v112, v113\n"
"v_cvt_pk_bf16_f32 v113, v114, v115\n"
"v_cvt_pk_bf16_f32 v114, v116, v117\n"
"v_cvt_pk_bf16_f32 v115, v118, v119\n"
"buffer_store_dwordx4 v[112:115], v230, s[60:63], s92 offen offset:2048 sc1\n"
"v_pk_mul_f32 v[120:121], v[120:121], v[204:205] op_sel_hi:[1,0]\n"
"v_pk_mul_f32 v[122:123], v[122:123], v[204:205] op_sel_hi:[1,0]\n"
"v_pk_mul_f32 v[124:125], v[124:125], v[204:205] op_sel_hi:[1,0]\n"
"v_pk_mul_f32 v[126:127], v[126:127], v[204:205] op_sel_hi:[1,0]\n"
"v_cvt_pk_bf16_f32 v120, v120, v121\n"
"v_cvt_pk_bf16_f32 v121, v122, v123\n"
"v_cvt_pk_bf16_f32 v122, v124, v125\n"
"v_cvt_pk_bf16_f32 v123, v126, v127\n"
"buffer_store_dwordx4 v[120:123], v230, s[60:63], s92 offen offset:3072 sc1\n"
"v_add_f32 v237, v208, v212\n"
"v_add_f32 v238, v216, v220\n"
"v_add_f32 v237, v237, v238\n"
"v_cmp_nge_f32 vcc, s81, v237\n"
"s_nop 1\n"
"s_cbranch_vccnz L_at_slow\n"
"L_at_back:\n"
"v_mbcnt_lo_u32_b32 v199, -1, 0\n"
"v_mbcnt_hi_u32_b32 v199, -1, v199\n"
"v_lshlrev_b32 v186, 4, v199\n"
"v_lshlrev_b32 v187, 3, v199\n"
"s_lshr_b32 s75, s68, 1\n"
"s_lshl_b32 s72, s68, 10\n"
"s_add_u32 s71, s69, s72\n"
"v_accvgpr_read_b32 v177, a173\n"
"v_accvgpr_read_b32 v178, a174\n"
"v_accvgpr_read_b32 v179, a175\n"
"v_accvgpr_read_b32 v180, a176\n"
"v_accvgpr_read_b32 v181, a177\n"
"v_accvgpr_read_b32 v182, a178\n"
"v_accvgpr_read_b32 v183, a179\n"
"v_accvgpr_read_b32 v184, a180\n"
"v_accvgpr_read_b32 v185, a181\n"
"v_accvgpr_read_b32 v168, a182\n"
"v_accvgpr_read_b32 v169, a183\n"
"v_accvgpr_read_b32 v170, a184\n"
"v_accvgpr_read_b32 v171, a185\n"
"v_accvgpr_read_b32 v172, a186\n"
"v_accvgpr_read_b32 v173, a187\n"
"v_accvgpr_read_b32 v174, a188\n"
"v_accvgpr_read_b32 v175, a189\n"
"v_accvgpr_read_b32 v176, a190\n"
"s_mov_b64 s[48:49], %5\n"
"s_mul_i32 s99, %15, 288\n"
"s_add_u32 s48, s48, s99\n"
"s_addc_u32 s49, s49, 0\n"
"s_and_b32 s49, s49, 0xffff\n"
"s_mov_b32 s50, 5308416\n"
"s_mov_b32 s51, 0x20000\n"
"s_mov_b64 s[44:45], %6\n"
"s_mul_i32 s99, %11, 1327104\n"
"s_mul_i32 s98, %13, 288\n"
"s_add_u32 s99, s99, s98\n"
"s_add_u32 s44, s44, s99\n"
"s_addc_u32 s45, s45, 0\n"
"s_and_b32 s45, s45, 0xffff\n"
"s_mov_b32 s46, 1327104\n"
"s_mov_b32 s47, 0x20000\n"
"s_mov_b64 s[56:57], %8\n"
"s_and_b32 s57, s57, 0xffff\n"
"s_mov_b32 s58, 2359296\n"
"s_mov_b32 s59, 0x20000\n"
"s_and_b32 s65, %9, 1\n"
"s_lshr_b32 s75, %9, 1\n"
"s_and_b32 s70, %15, 1\n"
"s_mul_i32 s86, %12, 1048576\n"
"s_mul_i32 s98, s65, 524288\n"
"s_add_u32 s86, s86, s98\n"
"s_mul_i32 s98, %11, 32768\n"
"s_add_u32 s86, s86, s98\n"
"s_add_u32 s86, s86, 8388608\n"
"s_mul_i32 s86, %12, 1048576\n"
"s_mul_i32 s98, %9, 262144\n"
"s_add_u32 s86, s86, s98\n"
"s_mul_i32 s98, %11, 32768\n"
"s_add_u32 s86, s86, s98\n"
"s_add_u32 s86, s86, 8388608\n"
"s_mul_i32 s87, %14, 294912\n"
"s_mul_i32 s98, %9, 73728\n"
"s_add_u32 s87, s87, s98\n"
"s_add_u32 s87, s87, 50331648\n"
"s_mul_i32 s88, %12, 1179648\n"
"s_mul_i32 s98, s65, 589824\n"
"s_add_u32 s88, s88, s98\n"
"s_mul_i32 s98, %11, 36864\n"
"s_add_u32 s88, s88, s98\n"
"s_add_u32 s88, s88, 67108864\n"
"s_mul_i32 s89, %12, 589824\n"
"s_mul_i32 s98, s65, 294912\n"
"s_add_u32 s89, s89, s98\n"
"s_mul_i32 s98, s75, 147456\n"
"s_add_u32 s89, s89, s98\n"
"s_mul_i32 s98, %11, 9216\n"
"s_add_u32 s89, s89, s98\n"
"s_mul_i32 s98, %13, 2304\n"
"s_add_u32 s89, s89, s98\n"
"s_mul_i32 s88, %12, 1179648\n"
"s_mul_i32 s98, %9, 294912\n"
"s_add_u32 s88, s88, s98\n"
"s_mul_i32 s98, %11, 36864\n"
"s_add_u32 s88, s88, s98\n"
"s_add_u32 s88, s88, 67108864\n"
"s_mul_i32 s89, %12, 589824\n"
"s_mul_i32 s98, %9, 147456\n"
"s_add_u32 s89, s89, s98\n"
"s_mul_i32 s98, %11, 9216\n"
"s_add_u32 s89, s89, s98\n"
"s_mul_i32 s98, %13, 2304\n"
"s_add_u32 s89, s89, s98\n"
"s_mul_i32 s90, %12, 589824\n"
"s_mul_i32 s98, %11, 147456\n"
"s_add_u32 s90, s90, s98\n"
"s_mul_i32 s98, %13, 36864\n"
"s_add_u32 s90, s90, s98\n"
"s_mul_i32 s91, %12, 147456\n"
"s_mul_i32 s98, %11, 36864\n"
"s_add_u32 s91, s91, s98\n"
"s_mul_i32 s98, %13, 64\n"
"s_add_u32 s91, s91, s98\n"
"s_add_u32 s91, s91, 50331648\n"
"s_mul_i32 s72, %15, 9\n"
"s_lshr_b32 s72, s72, 1\n"
"s_mul_i32 s92, %14, 2359296\n"
"s_mul_i32 s98, %9, 589824\n"
"s_add_u32 s92, s92, s98\n"
"s_mul_i32 s98, s72, 1024\n"
"s_add_u32 s92, s92, s98\n"
"s_add_u32 s92, s92, 67108864\n"
"s_mov_b32 s94, %12\n"
"s_lshr_b32 s72, %15, 3\n"
"s_mul_i32 s95, %14, 4\n"
"s_mul_i32 s98, s72, 1\n"
"s_add_u32 s95, s95, s98\n"
"s_lshr_b32 s72, %12, 1\n"
"s_mul_i32 s96, s72, 4\n"
"s_mul_i32 s98, %11, 1\n"
"s_add_u32 s96, s96, s98\n"
"s_lshr_b32 s72, %15, 3\n"
"s_mul_i32 s95, %14, 16\n"
"s_mul_i32 s98, %9, 4\n"
"s_add_u32 s95, s95, s98\n"
"s_mul_i32 s98, s72, 1\n"
"s_add_u32 s95, s95, s98\n"
"s_mul_i32 s96, %12, 8\n"
"s_mul_i32 s98, s75, 4\n"
"s_add_u32 s96, s96, s98\n"
"s_mul_i32 s98, %11, 1\n"
"s_add_u32 s96, s96, s98\n"
"s_cmp_eq_u32 s68, 3\n"
"s_cbranch_scc0 L_ts1_0\n"
"s_mov_b64 exec, 0\n"
"s_branch L_tc1_0\n"
"L_tb1_0:\n"
"s_mov_b64 exec, -1\n"
"L_ts1_0:\n"
"s_cmp_eq_u32 s68, 2\n"
"s_cbranch_scc0 L_ts1_1\n"
"s_mov_b64 exec, 0\n"
"s_branch L_tc1_1\n"
"L_tb1_1:\n"
"s_mov_b64 exec, -1\n"
"L_ts1_1:\n"
"s_cmp_eq_u32 s68, 1\n"
"s_cbranch_scc0 L_ts1_2\n"
"s_mov_b64 exec, 0\n"
"s_branch L_tc1_2\n"
"L_tb1_2:\n"
"s_mov_b64 exec, -1\n"
"L_ts1_2:\n"
"s_cmp_eq_u32 s68, 0\n"
"s_cbranch_scc0 L_ts1_3\n"
"s_mov_b64 exec, 0\n"
"s_branch L_tc1_3\n"
"L_tb1_3:\n"
"s_mov_b64 exec, -1\n"
"L_ts1_3:\n"
"s_lshl_b32 s72, s95, 8\n"
"s_add_u32 s72, s72, 327680\n"
"v_mov_b32 v200, 1\n"
"s_waitcnt vmcnt(0)\n"
"s_mov_b64 exec, 1\n"
"buffer_atomic_add v200, off, s[60:63], s72\n"
"s_mov_b64 exec, -1\n"
"s_barrier\n"
"v_accvgpr_read_b32 v112, a12\n"
"v_accvgpr_read_b32 v113, a13\n"
"v_accvgpr_read_b32 v114, a14\n"
"v_accvgpr_read_b32 v115, a15\n"
"v_accvgpr_read_b32 v116, a16\n"
"v_accvgpr_read_b32 v117, a17\n"
"v_accvgpr_read_b32 v118, a18\n"
"v_accvgpr_read_b32 v119, a19\n"
"v_accvgpr_read_b32 v120, a20\n"
"s_add_u32 m0, s71, 90112\n"
"s_nop 0\n"
"buffer_load_dwordx4 v250, s[52:55], s66 offen lds\n"
"v_accvgpr_read_b32 v121, a21\n"
"v_accvgpr_read_b32 v122, a22\n"
"v_accvgpr_read_b32 v123, a23\n"
"v_accvgpr_read_b32 v124, a24\n"
"v_accvgpr_read_b32 v125, a25\n"
"v_accvgpr_read_b32 v126, a26\n"
"v_accvgpr_read_b32 v127, a27\n"
"v_accvgpr_read_b32 v128, a28\n"
"s_add_u32 m0, s71, 94208\n"
"s_nop 0\n"
"buffer_load_dwordx4 v251, s[52:55], s66 offen lds\n"
"v_accvgpr_read_b32 v129, a29\n"
"v_accvgpr_read_b32 v130, a30\n"
"v_accvgpr_read_b32 v131, a31\n"
"v_accvgpr_read_b32 v132, a32\n"
"v_accvgpr_read_b32 v133, a33\n"
"v_accvgpr_read_b32 v134, a34\n"
"v_accvgpr_read_b32 v135, a35\n"
"v_accvgpr_read_b32 v136, a36\n"
"s_add_u32 m0, s71, 98304\n"
"s_nop 0\n"
"buffer_load_dwordx4 v252, s[52:55], s66 offen lds\n"
"v_accvgpr_read_b32 v137, a37\n"
"v_accvgpr_read_b32 v138, a38\n"
"v_accvgpr_read_b32 v139, a39\n"
"v_accvgpr_read_b32 v140, a40\n"
"v_accvgpr_read_b32 v141, a41\n"
"v_accvgpr_read_b32 v142, a42\n"
"v_accvgpr_read_b32 v143, a43\n"
"v_accvgpr_read_b32 v144, a44\n"
"s_add_u32 m0, s71, 102400\n"
"s_nop 0\n"
"buffer_load_dwordx4 v253, s[52:55], s66 offen lds\n"
"v_accvgpr_read_b32 v145, a45\n"
"v_accvgpr_read_b32 v146, a46\n"
"v_accvgpr_read_b32 v147, a47\n"
"v_accvgpr_read_b32 v148, a48\n"
"v_accvgpr_read_b32 v149, a49\n"
"v_accvgpr_read_b32 v150, a50\n"
"v_accvgpr_read_b32 v151, a51\n"
"v_accvgpr_read_b32 v152, a52\n"
"s_add_u32 m0, s71, 106496\n"
"s_nop 0\n"
"buffer_load_dwordx4 v254, s[52:55], s66 offen lds\n"
"v_accvgpr_read_b32 v153, a53\n"
"v_accvgpr_read_b32 v154, a54\n"
"v_accvgpr_read_b32 v155, a55\n"
"v_accvgpr_read_b32 v156, a56\n"
"v_accvgpr_read_b32 v157, a57\n"
"v_accvgpr_read_b32 v158, a58\n"
"v_accvgpr_read_b32 v159, a59\n"
"s_add_u32 s72, s69, 145408\n"
"s_mov_b32 s98, 0x0c04000c\n"
"s_mov_b32 s99, 0x0402010c\n"
"v_perm_b32 v115, v113, v112, s98\n"
"s_add_u32 m0, s71, 110592\n"
"s_mov_b32 s66, 442368\n"
"s_nop 0\n"
"buffer_load_dwordx4 v246, s[52:55], s66 offen lds\n"
"v_perm_b32 v115, v114, v115, s99\n"
"v_perm_b32 v119, v117, v116, s98\n"
"v_perm_b32 v119, v118, v119, s99\n"
"v_perm_b32 v123, v121, v120, s98\n"
"v_perm_b32 v123, v122, v123, s99\n"
"v_perm_b32 v127, v125, v124, s98\n"
"v_perm_b32 v127, v126, v127, s99\n"
"v_pk_add_f32 v[112:113], v[112:113], v[116:117]\n"
"s_add_u32 m0, s71, 114688\n"
"s_nop 0\n"
"buffer_load_dwordx4 v247, s[52:55], s66 offen lds\n"
"v_pk_add_f32 v[120:121], v[120:121], v[124:125]\n"
"v_pk_add_f32 v[112:113], v[112:113], v[120:121]\n"
"v_cvt_pk_bf16_f32 v116, v112, v113\n"
"v_lshlrev_b32 v112, 16, v116\n"
"v_and_b32 v113, 0xffff0000, v116\n"
"v_pk_add_f32 v[114:115], v[114:115], v[118:119]\n"
"v_pk_add_f32 v[122:123], v[122:123], v[126:127]\n"
"v_pk_add_f32 v[114:115], v[114:115], v[122:123]\n"
"s_add_u32 m0, s71, 118784\n"
"s_nop 0\n"
"buffer_load_dwordx4 v248, s[52:55], s66 offen lds\n"
"v_cvt_pk_bf16_f32 v116, v114, v115\n"
"v_lshlrev_b32 v114, 16, v116\n"
"v_and_b32 v115, 0xffff0000, v116\n"
"v_add_u32 v231, s72, v231\n"
"ds_write_b128 v231, v[112:115]\n"
"s_mov_b32 s98, 0x0c04000c\n"
"s_mov_b32 s99, 0x0402010c\n"
"v_perm_b32 v131, v129, v128, s98\n"
"s_add_u32 m0, s71, 122880\n"
"s_nop 0\n"
"buffer_load_dwordx4 v249, s[52:55], s66 offen lds\n"
"v_perm_b32 v131, v130, v131, s99\n"
"v_perm_b32 v135, v133, v132, s98\n"
"v_perm_b32 v135, v134, v135, s99\n"
"v_perm_b32 v139, v137, v136, s98\n"
"v_perm_b32 v139, v138, v139, s99\n"
"v_perm_b32 v143, v141, v140, s98\n"
"v_perm_b32 v143, v142, v143, s99\n"
"v_pk_add_f32 v[128:129], v[128:129], v[132:133]\n"
"s_add_u32 m0, s71, 126976\n"
"s_nop 0\n"
"buffer_load_dwordx4 v250, s[52:55], s66 offen lds\n"
"v_pk_add_f32 v[136:137], v[136:137], v[140:141]\n"
"v_pk_add_f32 v[128:129], v[128:129], v[136:137]\n"
"v_cvt_pk_bf16_f32 v132, v128, v129\n"
"v_lshlrev_b32 v128, 16, v132\n"
"v_and_b32 v129, 0xffff0000, v132\n"
"v_pk_add_f32 v[130:131], v[130:131], v[134:135]\n"
"v_pk_add_f32 v[138:139], v[138:139], v[142:143]\n"
"v_pk_add_f32 v[130:131], v[130:131], v[138:139]\n"
"s_add_u32 m0, s71, 131072\n"
"s_nop 0\n"
"buffer_load_dwordx4 v251, s[52:55], s66 offen lds\n"
"v_cvt_pk_bf16_f32 v132, v130, v131\n"
"v_lshlrev_b32 v130, 16, v132\n"
"v_and_b32 v131, 0xffff0000, v132\n"
"v_add_u32 v232, s72, v232\n"
"ds_write_b128 v232, v[128:131]\n"
"s_mov_b32 s98, 0x0c04000c\n"
"s_mov_b32 s99, 0x0402010c\n"
"v_perm_b32 v147, v145, v144, s98\n"
"s_add_u32 m0, s71, 135168\n"
"s_nop 0\n"
"buffer_load_dwordx4 v252, s[52:55], s66 offen lds\n"
"v_perm_b32 v147, v146, v147, s99\n"
"v_perm_b32 v151, v149, v148, s98\n"
"v_perm_b32 v151, v150, v151, s99\n"
"v_perm_b32 v155, v153, v152, s98\n"
"v_perm_b32 v155, v154, v155, s99\n"
"v_perm_b32 v159, v157, v156, s98\n"
"v_perm_b32 v159, v158, v159, s99\n"
"v_pk_add_f32 v[144:145], v[144:145], v[148:149]\n"
"s_add_u32 m0, s71, 139264\n"
"s_nop 0\n"
"buffer_load_dwordx4 v253, s[52:55], s66 offen lds\n"
"v_pk_add_f32 v[152:153], v[152:153], v[156:157]\n"
"v_pk_add_f32 v[144:145], v[144:145], v[152:153]\n"
"v_cvt_pk_bf16_f32 v148, v144, v145\n"
"v_lshlrev_b32 v144, 16, v148\n"
"v_and_b32 v145, 0xffff0000, v148\n"
"v_pk_add_f32 v[146:147], v[146:147], v[150:151]\n"
"v_pk_add_f32 v[154:155], v[154:155], v[158:159]\n"
"v_pk_add_f32 v[146:147], v[146:147], v[154:155]\n"
"s_add_u32 m0, s71, 143360\n"
"s_nop 0\n"
"buffer_load_dwordx4 v254, s[52:55], s66 offen lds\n"
"v_cvt_pk_bf16_f32 v148, v146, v147\n"
"v_lshlrev_b32 v146, 16, v148\n"
"v_and_b32 v147, 0xffff0000, v148\n"
"v_add_u32 v234, s72, v234\n"
"ds_write_b128 v234, v[144:147]\n"
"s_waitcnt lgkmcnt(0)\n"
"v_lshrrev_b32 v203, 2, v199\n"
"v_mul_u32_u24 v203, 288, v203\n"
"v_and_b32 v204, 3, v199\n"
"v_lshl_add_u32 v203, v204, 3, v203\n"
"v_add_u32 v203, s69, v203\n"
"v_mov_b32 v188, v203\n"
"v_add_u32 v191, 256, v203\n"
"s_add_u32 s82, s86, 0\n"
"s_add_u32 s83, s86, 131072\n"
"v_add_u32 v189, 55296, v188\n"
"v_add_u32 v192, 55296, v191\n"
"v_add_u32 v190, 110592, v188\n"
"v_add_u32 v193, 110592, v191\n"
"s_mul_i32 s72, s68, 18432\n"
"s_add_u32 s72, s72, s69\n"
"v_add_u32 v194, s72, v186\n"
"s_xor_b32 s73, s68, 2\n"
"s_mul_i32 s72, s73, 18432\n"
"s_add_u32 s72, s72, s69\n"
"v_add_u32 v195, s72, v186\n"
"v_bfe_u32 v200, v199, 2, 2\n"
"v_mul_u32_u24 v196, 36864, v200\n"
"v_lshrrev_b32 v200, 4, v199\n"
"v_lshl_add_u32 v196, v200, 6, v196\n"
"v_and_b32 v200, 3, v199\n"
"v_lshl_add_u32 v196, v200, 4, v196\n"
"s_waitcnt vmcnt(44)\n"
"s_barrier\n"
"ds_read_b64_tr_b16 v[72:73], v188 offset:0\n"
"ds_read_b64_tr_b16 v[74:75], v188 offset:4608\n"
"ds_read_b64_tr_b16 v[76:77], v188 offset:9216\n"
"ds_read_b64_tr_b16 v[78:79], v188 offset:13824\n"
"ds_read_b64_tr_b16 v[80:81], v188 offset:32\n"
"ds_read_b64_tr_b16 v[82:83], v188 offset:4640\n"
"ds_read_b64_tr_b16 v[84:85], v188 offset:9248\n"
"ds_read_b64_tr_b16 v[86:87], v188 offset:13856\n"
"s_lshl_b32 s72, s96, 8\n"
"s_add_u32 s72, s72, 327680\n"
"s_mov_b32 s97, 20000\n"
"L_po_poll8:\n"
"buffer_load_dword v208, off, s[60:63], s72 sc1\n"
"s_waitcnt vmcnt(0)\n"
"v_readfirstlane_b32 s73, v208\n"
"s_nop 1\n"
"s_cmp_ge_u32 s73, 8\n"
"s_cbranch_scc1 L_po_polld9\n"
"s_sleep 2\n"
"s_sub_u32 s97, s97, 1\n"
"s_cbranch_scc0 L_po_poll8\n"
"L_po_polld9:\n"
"s_add_u32 s78, s82, 0\n"
"buffer_load_dwordx4 a[60:63], v186, s[60:63], s78 offen offset:0 sc1\n"
"s_add_u32 s79, s83, 0\n"
"buffer_load_dwordx4 a[64:67], v186, s[60:63], s79 offen offset:0 sc1\n"
"buffer_load_dwordx4 a[68:71], v186, s[60:63], s78 offen offset:1024 sc1\n"
"buffer_load_dwordx4 a[72:75], v186, s[60:63], s79 offen offset:1024 sc1\n"
"s_waitcnt vmcnt(0) lgkmcnt(4)\n"
"v_mfma_f32_16x16x32_bf16 v[0:3], v[72:75], a[60:63], 0\n"
"buffer_load_dwordx4 a[76:79], v186, s[60:63], s78 offen offset:2048 sc1\n"
"v_mfma_f32_16x16x32_bf16 v[0:3], v[76:79], a[68:71], v[0:3]\n"
"s_nop 0\n"
"ds_read_b64_tr_b16 v[88:89], v188 offset:64\n"
"ds_read_b64_tr_b16 v[90:91], v188 offset:4672\n"
"v_mfma_f32_16x16x32_bf16 v[36:39], v[72:75], a[64:67], 0\n"
"s_nop 0\n"
"ds_read_b64_tr_b16 v[92:93], v188 offset:9280\n"
"ds_read_b64_tr_b16 v[94:95], v188 offset:13888\n"
"v_mfma_f32_16x16x32_bf16 v[36:39], v[76:79], a[72:75], v[36:39]\n"
"s_waitcnt lgkmcnt(4)\n"
"v_mfma_f32_16x16x32_bf16 v[4:7], v[80:83], a[60:63], 0\n"
"buffer_load_dwordx4 a[80:83], v186, s[60:63], s79 offen offset:2048 sc1\n"
"v_mfma_f32_16x16x32_bf16 v[4:7], v[84:87], a[68:71], v[4:7]\n"
"s_nop 0\n"
"ds_read_b64_tr_b16 v[96:97], v188 offset:96\n"
"ds_read_b64_tr_b16 v[98:99], v188 offset:4704\n"
"v_mfma_f32_16x16x32_bf16 v[40:43], v[80:83], a[64:67], 0\n"
"s_nop 0\n"
"ds_read_b64_tr_b16 v[100:101], v188 offset:9312\n"
"ds_read_b64_tr_b16 v[102:103], v188 offset:13920\n"
"v_mfma_f32_16x16x32_bf16 v[40:43], v[84:87], a[72:75], v[40:43]\n"
"s_waitcnt lgkmcnt(4)\n"
"v_mfma_f32_16x16x32_bf16 v[8:11], v[88:91], a[60:63], 0\n"
"buffer_load_dwordx4 a[84:87], v186, s[60:63], s78 offen offset:3072 sc1\n"
"v_mfma_f32_16x16x32_bf16 v[8:11], v[92:95], a[68:71], v[8:11]\n"
"s_nop 0\n"
"ds_read_b64_tr_b16 v[72:73], v188 offset:128\n"
"ds_read_b64_tr_b16 v[74:75], v188 offset:4736\n"
"v_mfma_f32_16x16x32_bf16 v[44:47], v[88:91], a[64:67], 0\n"
"L_tc1_0:\n"
"s_cbranch_execz L_tb1_0\n"
"ds_read_b64_tr_b16 v[76:77], v188 offset:9344\n"
"ds_read_b64_tr_b16 v[78:79], v188 offset:13952\n"
"v_mfma_f32_16x16x32_bf16 v[44:47], v[92:95], a[72:75], v[44:47]\n"
"s_waitcnt lgkmcnt(4)\n"
"v_mfma_f32_16x16x32_bf16 v[12:15], v[96:99], a[60:63], 0\n"
"buffer_load_dwordx4 a[88:91], v186, s[60:63], s79 offen offset:3072 sc1\n"
"v_mfma_f32_16x16x32_bf16 v[12:15], v[100:103], a[68:71], v[12:15]\n"
"s_add_u32 s78, s82, 4096\n"
"ds_read_b64_tr_b16 v[80:81], v188 offset:160\n"
"ds_read_b64_tr_b16 v[82:83], v188 offset:4768\n"
"v_mfma_f32_16x16x32_bf16 v[48:51], v[96:99], a[64:67], 0\n"
"s_nop 0\n"
"ds_read_b64_tr_b16 v[84:85], v188 offset:9376\n"
"ds_read_b64_tr_b16 v[86:87], v188 offset:13984\n"
"v_mfma_f32_16x16x32_bf16 v[48:51], v[100:103], a[72:75], v[48:51]\n"
"s_waitcnt lgkmcnt(4)\n"
"v_mfma_f32_16x16x32_bf16 v[16:19], v[72:75], a[60:63], 0\n"
"buffer_load_dwordx4 a[92:95], v186, s[60:63], s78 offen offset:0 sc1\n"
"v_mfma_f32_16x16x32_bf16 v[16:19], v[76:79], a[68:71], v[16:19]\n"
"s_add_u32 s79, s83, 4096\n"
"ds_read_b64_tr_b16 v[88:89], v188 offset:192\n"
"ds_read_b64_tr_b16 v[90:91], v188 offset:4800\n"
"v_mfma_f32_16x16x32_bf16 v[52:55], v[72:75], a[64:67], 0\n"
"s_nop 0\n"
"ds_read_b64_tr_b16 v[92:93], v188 offset:9408\n"
"ds_read_b64_tr_b16 v[94:95], v188 offset:14016\n"
"v_mfma_f32_16x16x32_bf16 v[52:55], v[76:79], a[72:75], v[52:55]\n"
"s_waitcnt lgkmcnt(4)\n"
"v_mfma_f32_16x16x32_bf16 v[20:23], v[80:83], a[60:63], 0\n"
"s_nop 0\n"
"ds_read_b64_tr_b16 v[96:97], v188 offset:224\n"
"ds_read_b64_tr_b16 v[98:99], v188 offset:4832\n"
"v_mfma_f32_16x16x32_bf16 v[20:23], v[84:87], a[68:71], v[20:23]\n"
"buffer_load_dwordx4 a[96:99], v186, s[60:63], s79 offen offset:0 sc1\n"
"v_mfma_f32_16x16x32_bf16 v[56:59], v[80:83], a[64:67], 0\n"
"s_nop 0\n"
"ds_read_b64_tr_b16 v[100:101], v188 offset:9440\n"
"ds_read_b64_tr_b16 v[102:103], v188 offset:14048\n"
"v_mfma_f32_16x16x32_bf16 v[56:59], v[84:87], a[72:75], v[56:59]\n"
"s_waitcnt lgkmcnt(4)\n"
"v_mfma_f32_16x16x32_bf16 v[24:27], v[88:91], a[60:63], 0\n"
"s_nop 0\n"
"ds_read_b64_tr_b16 v[72:73], v191 offset:0\n"
"ds_read_b64_tr_b16 v[74:75], v191 offset:4608\n"
"v_mfma_f32_16x16x32_bf16 v[24:27], v[92:95], a[68:71], v[24:27]\n"
"buffer_load_dwordx4 a[100:103], v186, s[60:63], s78 offen offset:1024 sc1\n"
"v_mfma_f32_16x16x32_bf16 v[60:63], v[88:91], a[64:67], 0\n"
"s_nop 0\n"
"ds_read_b64_tr_b16 v[76:77], v191 offset:9216\n"
"ds_read_b64_tr_b16 v[78:79], v191 offset:13824\n"
"v_mfma_f32_16x16x32_bf16 v[60:63], v[92:95], a[72:75], v[60:63]\n"
"s_waitcnt lgkmcnt(4)\n"
"v_mfma_f32_16x16x32_bf16 v[28:31], v[96:99], a[60:63], 0\n"
"s_nop 0\n"
"ds_read_b64_tr_b16 v[80:81], v188 offset:18432\n"
"ds_read_b64_tr_b16 v[82:83], v188 offset:23040\n"
"v_mfma_f32_16x16x32_bf16 v[28:31], v[100:103], a[68:71], v[28:31]\n"
"s_nop 0\n"
"ds_read_b64_tr_b16 v[84:85], v188 offset:27648\n"
"ds_read_b64_tr_b16 v[86:87], v188 offset:32256\n"
"v_mfma_f32_16x16x32_bf16 v[64:67], v[96:99], a[64:67], 0\n"
"buffer_load_dwordx4 a[104:107], v186, s[60:63], s79 offen offset:1024 sc1\n"
"v_mfma_f32_16x16x32_bf16 v[64:67], v[100:103], a[72:75], v[64:67]\n"
"s_waitcnt lgkmcnt(4)\n"
"v_mfma_f32_16x16x32_bf16 v[32:35], v[72:75], a[60:63], 0\n"
"s_nop 0\n"
"ds_read_b64_tr_b16 v[88:89], v188 offset:18464\n"
"ds_read_b64_tr_b16 v[90:91], v188 offset:23072\n"
"v_mfma_f32_16x16x32_bf16 v[32:35], v[76:79], a[68:71], v[32:35]\n"
"s_nop 0\n"
"ds_read_b64_tr_b16 v[92:93], v188 offset:27680\n"
"ds_read_b64_tr_b16 v[94:95], v188 offset:32288\n"
"v_mfma_f32_16x16x32_bf16 v[68:71], v[72:75], a[64:67], 0\n"
"buffer_load_dwordx4 a[108:111], v186, s[60:63], s78 offen offset:2048 sc1\n"
"v_mfma_f32_16x16x32_bf16 v[68:71], v[76:79], a[72:75], v[68:71]\n"
"s_waitcnt vmcnt(5) lgkmcnt(4)\n"
"v_mfma_f32_16x16x32_bf16 v[0:3], v[80:83], a[76:79], v[0:3]\n"
"s_nop 0\n"
"ds_read_b64_tr_b16 v[96:97], v188 offset:18496\n"
"ds_read_b64_tr_b16 v[98:99], v188 offset:23104\n"
"v_mfma_f32_16x16x32_bf16 v[0:3], v[84:87], a[84:87], v[0:3]\n"
"s_nop 0\n"
"ds_read_b64_tr_b16 v[100:101], v188 offset:27712\n"
"ds_read_b64_tr_b16 v[102:103], v188 offset:32320\n"
"v_mfma_f32_16x16x32_bf16 v[36:39], v[80:83], a[80:83], v[36:39]\n"
"v_mfma_f32_16x16x32_bf16 v[36:39], v[84:87], a[88:91], v[36:39]\n"
"s_waitcnt lgkmcnt(4)\n"
"v_mfma_f32_16x16x32_bf16 v[4:7], v[88:91], a[76:79], v[4:7]\n"
"buffer_load_dwordx4 a[112:115], v186, s[60:63], s79 offen offset:2048 sc1\n"
"v_mfma_f32_16x16x32_bf16 v[4:7], v[92:95], a[84:87], v[4:7]\n"
"s_nop 0\n"
"ds_read_b64_tr_b16 v[72:73], v188 offset:18528\n"
"ds_read_b64_tr_b16 v[74:75], v188 offset:23136\n"
"v_mfma_f32_16x16x32_bf16 v[40:43], v[88:91], a[80:83], v[40:43]\n"
"s_nop 0\n"
"ds_read_b64_tr_b16 v[76:77], v188 offset:27744\n"
"ds_read_b64_tr_b16 v[78:79], v188 offset:32352\n"
"v_mfma_f32_16x16x32_bf16 v[40:43], v[92:95], a[88:91], v[40:43]\n"
"s_waitcnt lgkmcnt(4)\n"
"v_mfma_f32_16x16x32_bf16 v[8:11], v[96:99], a[76:79], v[8:11]\n"
"buffer_load_dwordx4 a[116:119], v186, s[60:63], s78 offen offset:3072 sc1\n"
"v_mfma_f32_16x16x32_bf16 v[8:11], v[100:103], a[84:87], v[8:11]\n"
"s_nop 0\n"
"ds_read_b64_tr_b16 v[80:81], v188 offset:18560\n"
"ds_read_b64_tr_b16 v[82:83], v188 offset:23168\n"
"v_mfma_f32_16x16x32_bf16 v[44:47], v[96:99], a[80:83], v[44:47]\n"
"s_nop 0\n"
"ds_read_b64_tr_b16 v[84:85], v188 offset:27776\n"
"ds_read_b64_tr_b16 v[86:87], v188 offset:32384\n"
"v_mfma_f32_16x16x32_bf16 v[44:47], v[100:103], a[88:91], v[44:47]\n"
"s_waitcnt lgkmcnt(4)\n"
"v_mfma_f32_16x16x32_bf16 v[12:15], v[72:75], a[76:79], v[12:15]\n"
"buffer_load_dwordx4 a[120:123], v186, s[60:63], s79 offen offset:3072 sc1\n"
"v_mfma_f32_16x16x32_bf16 v[12:15], v[76:79], a[84:87], v[12:15]\n"
"s_add_u32 s78, s82, 8192\n"
"ds_read_b64_tr_b16 v[88:89], v188 offset:18592\n"
"ds_read_b64_tr_b16 v[90:91], v188 offset:23200\n"
"v_mfma_f32_16x16x32_bf16 v[48:51], v[72:75], a[80:83], v[48:51]\n"
"s_nop 0\n"
"ds_read_b64_tr_b16 v[92:93], v188 offset:27808\n"
"ds_read_b64_tr_b16 v[94:95], v188 offset:32416\n"
"v_mfma_f32_16x16x32_bf16 v[48:51], v[76:79], a[88:91], v[48:51]\n"
"s_waitcnt lgkmcnt(4)\n"
"v_mfma_f32_16x16x32_bf16 v[16:19], v[80:83], a[76:79], v[16:19]\n"
"buffer_load_dwordx4 a[124:127], v186, s[60:63], s78 offen offset:0 sc1\n"
"v_mfma_f32_16x16x32_bf16 v[16:19], v[84:87], a[84:87], v[16:19]\n"
"s_add_u32 s79, s83, 8192\n"
"ds_read_b64_tr_b16 v[96:97], v188 offset:18624\n"
"ds_read_b64_tr_b16 v[98:99], v188 offset:23232\n"
"v_mfma_f32_16x16x32_bf16 v[52:55], v[80:83], a[80:83], v[52:55]\n"
"s_nop 0\n"
"ds_read_b64_tr_b16 v[100:101], v188 offset:27840\n"
"ds_read_b64_tr_b16 v[102:103], v188 offset:32448\n"
"v_mfma_f32_16x16x32_bf16 v[52:55], v[84:87], a[88:91], v[52:55]\n"
"s_waitcnt lgkmcnt(4)\n"
"v_mfma_f32_16x16x32_bf16 v[20:23], v[88:91], a[76:79], v[20:23]\n"
"s_nop 0\n"
"ds_read_b64_tr_b16 v[72:73], v188 offset:18656\n"
"ds_read_b64_tr_b16 v[74:75], v188 offset:23264\n"
"v_mfma_f32_16x16x32_bf16 v[20:23], v[92:95], a[84:87], v[20:23]\n"
"buffer_load_dwordx4 a[128:131], v186, s[60:63], s79 offen offset:0 sc1\n"
"v_mfma_f32_16x16x32_bf16 v[56:59], v[88:91], a[80:83], v[56:59]\n"
"s_nop 0\n"
"ds_read_b64_tr_b16 v[76:77], v188 offset:27872\n"
"ds_read_b64_tr_b16 v[78:79], v188 offset:32480\n"
"v_mfma_f32_16x16x32_bf16 v[56:59], v[92:95], a[88:91], v[56:59]\n"
"s_waitcnt lgkmcnt(4)\n"
"v_mfma_f32_16x16x32_bf16 v[24:27], v[96:99], a[76:79], v[24:27]\n"
"s_nop 0\n"
"ds_read_b64_tr_b16 v[80:81], v191 offset:18432\n"
"ds_read_b64_tr_b16 v[82:83], v191 offset:23040\n"
"v_mfma_f32_16x16x32_bf16 v[24:27], v[100:103], a[84:87], v[24:27]\n"
"buffer_load_dwordx4 a[132:135], v186, s[60:63], s78 offen offset:1024 sc1\n"
"v_mfma_f32_16x16x32_bf16 v[60:63], v[96:99], a[80:83], v[60:63]\n"
"s_nop 0\n"
"ds_read_b64_tr_b16 v[84:85], v191 offset:27648\n"
"ds_read_b64_tr_b16 v[86:87], v191 offset:32256\n"
"v_mfma_f32_16x16x32_bf16 v[60:63], v[100:103], a[88:91], v[60:63]\n"
"s_waitcnt vmcnt(52) lgkmcnt(4)\n"
"s_barrier\n"
"v_mfma_f32_16x16x32_bf16 v[28:31], v[72:75], a[76:79], v[28:31]\n"
"s_nop 0\n"
"ds_read_b64_tr_b16 v[88:89], v188 offset:36864\n"
"ds_read_b64_tr_b16 v[90:91], v188 offset:41472\n"
"v_mfma_f32_16x16x32_bf16 v[28:31], v[76:79], a[84:87], v[28:31]\n"
"s_nop 0\n"
"ds_read_b64_tr_b16 v[92:93], v188 offset:46080\n"
"ds_read_b64_tr_b16 v[94:95], v188 offset:50688\n"
"v_mfma_f32_16x16x32_bf16 v[64:67], v[72:75], a[80:83], v[64:67]\n"
"buffer_load_dwordx4 a[136:139], v186, s[60:63], s79 offen offset:1024 sc1\n"
"v_mfma_f32_16x16x32_bf16 v[64:67], v[76:79], a[88:91], v[64:67]\n"
"s_waitcnt vmcnt(48) lgkmcnt(4)\n"
"s_barrier\n"
"v_mfma_f32_16x16x32_bf16 v[32:35], v[80:83], a[76:79], v[32:35]\n"
"s_nop 0\n"
"ds_read_b64_tr_b16 v[96:97], v188 offset:36896\n"
"ds_read_b64_tr_b16 v[98:99], v188 offset:41504\n"
"v_mfma_f32_16x16x32_bf16 v[32:35], v[84:87], a[84:87], v[32:35]\n"
"s_nop 0\n"
"ds_read_b64_tr_b16 v[100:101], v188 offset:46112\n"
"ds_read_b64_tr_b16 v[102:103], v188 offset:50720\n"
"v_mfma_f32_16x16x32_bf16 v[68:71], v[80:83], a[80:83], v[68:71]\n"
"buffer_load_dwordx4 a[140:143], v186, s[60:63], s78 offen offset:2048 sc1\n"
"v_mfma_f32_16x16x32_bf16 v[68:71], v[84:87], a[88:91], v[68:71]\n"
"s_waitcnt vmcnt(9) lgkmcnt(4)\n"
"v_mfma_f32_16x16x32_bf16 v[0:3], v[88:91], a[92:95], v[0:3]\n"
"s_nop 0\n"
"ds_read_b64_tr_b16 v[72:73], v188 offset:36928\n"
"ds_read_b64_tr_b16 v[74:75], v188 offset:41536\n"
"v_mfma_f32_16x16x32_bf16 v[0:3], v[92:95], a[100:103], v[0:3]\n"
"s_nop 0\n"
"ds_read_b64_tr_b16 v[76:77], v188 offset:46144\n"
"ds_read_b64_tr_b16 v[78:79], v188 offset:50752\n"
"v_mfma_f32_16x16x32_bf16 v[36:39], v[88:91], a[96:99], v[36:39]\n"
"v_mfma_f32_16x16x32_bf16 v[36:39], v[92:95], a[104:107], v[36:39]\n"
"s_waitcnt lgkmcnt(4)\n"
"v_mfma_f32_16x16x32_bf16 v[4:7], v[96:99], a[92:95], v[4:7]\n"
"buffer_load_dwordx4 a[144:147], v186, s[60:63], s79 offen offset:2048 sc1\n"
"v_mfma_f32_16x16x32_bf16 v[4:7], v[100:103], a[100:103], v[4:7]\n"
"s_nop 0\n"
"ds_read_b64_tr_b16 v[80:81], v188 offset:36960\n"
"ds_read_b64_tr_b16 v[82:83], v188 offset:41568\n"
"v_mfma_f32_16x16x32_bf16 v[40:43], v[96:99], a[96:99], v[40:43]\n"
"s_nop 0\n"
"ds_read_b64_tr_b16 v[84:85], v188 offset:46176\n"
"ds_read_b64_tr_b16 v[86:87], v188 offset:50784\n"
"v_mfma_f32_16x16x32_bf16 v[40:43], v[100:103], a[104:107], v[40:43]\n"
"s_waitcnt lgkmcnt(4)\n"
"v_mfma_f32_16x16x32_bf16 v[8:11], v[72:75], a[92:95], v[8:11]\n"
"buffer_load_dwordx4 a[148:151], v186, s[60:63], s78 offen offset:3072 sc1\n"
"v_mfma_f32_16x16x32_bf16 v[8:11], v[76:79], a[100:103], v[8:11]\n"
"s_nop 0\n"
"ds_read_b64_tr_b16 v[88:89], v188 offset:36992\n"
"ds_read_b64_tr_b16 v[90:91], v188 offset:41600\n"
"v_mfma_f32_16x16x32_bf16 v[44:47], v[72:75], a[96:99], v[44:47]\n"
"s_nop 0\n"
"ds_read_b64_tr_b16 v[92:93], v188 offset:46208\n"
"ds_read_b64_tr_b16 v[94:95], v188 offset:50816\n"
"v_mfma_f32_16x16x32_bf16 v[44:47], v[76:79], a[104:107], v[44:47]\n"
"s_waitcnt lgkmcnt(4)\n"
"v_mfma_f32_16x16x32_bf16 v[12:15], v[80:83], a[92:95], v[12:15]\n"
"buffer_load_dwordx4 a[152:155], v186, s[60:63], s79 offen offset:3072 sc1\n"
"v_mfma_f32_16x16x32_bf16 v[12:15], v[84:87], a[100:103], v[12:15]\n"
"s_add_u32 s78, s82, 12288\n"
"ds_read_b64_tr_b16 v[96:97], v188 offset:37024\n"
"ds_read_b64_tr_b16 v[98:99], v188 offset:41632\n"
"v_mfma_f32_16x16x32_bf16 v[48:51], v[80:83], a[96:99], v[48:51]\n"
"s_nop 0\n"
"ds_read_b64_tr_b16 v[100:101], v188 offset:46240\n"
"ds_read_b64_tr_b16 v[102:103], v188 offset:50848\n"
"v_mfma_f32_16x16x32_bf16 v[48:51], v[84:87], a[104:107], v[48:51]\n"
"s_waitcnt lgkmcnt(4)\n"
"v_mfma_f32_16x16x32_bf16 v[16:19], v[88:91], a[92:95], v[16:19]\n"
"buffer_load_dwordx4 a[156:159], v186, s[60:63], s78 offen offset:0 sc1\n"
"v_mfma_f32_16x16x32_bf16 v[16:19], v[92:95], a[100:103], v[16:19]\n"
"s_add_u32 s79, s83, 12288\n"
"ds_read_b64_tr_b16 v[72:73], v188 offset:37056\n"
"ds_read_b64_tr_b16 v[74:75], v188 offset:41664\n"
"v_mfma_f32_16x16x32_bf16 v[52:55], v[88:91], a[96:99], v[52:55]\n"
"s_nop 0\n"
"ds_read_b64_tr_b16 v[76:77], v188 offset:46272\n"
"ds_read_b64_tr_b16 v[78:79], v188 offset:50880\n"
"v_mfma_f32_16x16x32_bf16 v[52:55], v[92:95], a[104:107], v[52:55]\n"
"s_waitcnt lgkmcnt(4)\n"
"v_mfma_f32_16x16x32_bf16 v[20:23], v[96:99], a[92:95], v[20:23]\n"
"s_nop 0\n"
"ds_read_b64_tr_b16 v[80:81], v188 offset:37088\n"
"ds_read_b64_tr_b16 v[82:83], v188 offset:41696\n"
"v_mfma_f32_16x16x32_bf16 v[20:23], v[100:103], a[100:103], v[20:23]\n"
"buffer_load_dwordx4 a[160:163], v186, s[60:63], s79 offen offset:0 sc1\n"
"v_mfma_f32_16x16x32_bf16 v[56:59], v[96:99], a[96:99], v[56:59]\n"
"s_nop 0\n"
"ds_read_b64_tr_b16 v[84:85], v188 offset:46304\n"
"ds_read_b64_tr_b16 v[86:87], v188 offset:50912\n"
"v_mfma_f32_16x16x32_bf16 v[56:59], v[100:103], a[104:107], v[56:59]\n"
"s_waitcnt lgkmcnt(4)\n"
"v_mfma_f32_16x16x32_bf16 v[24:27], v[72:75], a[92:95], v[24:27]\n"
"s_nop 0\n"
"ds_read_b64_tr_b16 v[88:89], v191 offset:36864\n"
"ds_read_b64_tr_b16 v[90:91], v191 offset:41472\n"
"v_mfma_f32_16x16x32_bf16 v[24:27], v[76:79], a[100:103], v[24:27]\n"
"buffer_load_dwordx4 a[164:167], v186, s[60:63], s78 offen offset:1024 sc1\n"
"v_mfma_f32_16x16x32_bf16 v[60:63], v[72:75], a[96:99], v[60:63]\n"
"s_nop 0\n"
"ds_read_b64_tr_b16 v[92:93], v191 offset:46080\n"
"ds_read_b64_tr_b16 v[94:95], v191 offset:50688\n"
"v_mfma_f32_16x16x32_bf16 v[60:63], v[76:79], a[104:107], v[60:63]\n"
"s_waitcnt lgkmcnt(4)\n"
"v_mfma_f32_16x16x32_bf16 v[28:31], v[80:83], a[92:95], v[28:31]\n"
"s_nop 0\n"
"ds_read_b64_tr_b16 v[96:97], v189 offset:0\n"
"ds_read_b64_tr_b16 v[98:99], v189 offset:4608\n"
"v_mfma_f32_16x16x32_bf16 v[28:31], v[84:87], a[100:103], v[28:31]\n"
"s_nop 0\n"
"ds_read_b64_tr_b16 v[100:101], v189 offset:9216\n"
"ds_read_b64_tr_b16 v[102:103], v189 offset:13824\n"
"v_mfma_f32_16x16x32_bf16 v[64:67], v[80:83], a[96:99], v[64:67]\n"
"buffer_load_dwordx4 a[168:171], v186, s[60:63], s79 offen offset:1024 sc1\n"
"v_mfma_f32_16x16x32_bf16 v[64:67], v[84:87], a[104:107], v[64:67]\n"
"s_waitcnt lgkmcnt(4)\n"
"v_mfma_f32_16x16x32_bf16 v[32:35], v[88:91], a[92:95], v[32:35]\n"
"s_add_u32 m0, s71, 0\n"
"ds_read_b64_tr_b16 v[72:73], v189 offset:32\n"
"ds_read_b64_tr_b16 v[74:75], v189 offset:4640\n"
"v_mfma_f32_16x16x32_bf16 v[32:35], v[92:95], a[100:103], v[32:35]\n"
"s_mov_b32 s66, 589824\n"
"ds_read_b64_tr_b16 v[76:77], v189 offset:9248\n"
"ds_read_b64_tr_b16 v[78:79], v189 offset:13856\n"
"v_mfma_f32_16x16x32_bf16 v[68:71], v[88:91], a[96:99], v[68:71]\n"
"buffer_load_dwordx4 v246, s[52:55], s66 offen lds\n"
"v_mfma_f32_16x16x32_bf16 v[68:71], v[92:95], a[104:107], v[68:71]\n"
"s_waitcnt vmcnt(13) lgkmcnt(4)\n"
"v_mfma_f32_16x16x32_bf16 v[0:3], v[96:99], a[108:111], v[0:3]\n"
"s_add_u32 m0, s71, 4096\n"
"ds_read_b64_tr_b16 v[80:81], v189 offset:64\n"
"ds_read_b64_tr_b16 v[82:83], v189 offset:4672\n"
"v_mfma_f32_16x16x32_bf16 v[0:3], v[100:103], a[116:119], v[0:3]\n"
"s_nop 0\n"
"ds_read_b64_tr_b16 v[84:85], v189 offset:9280\n"
"ds_read_b64_tr_b16 v[86:87], v189 offset:13888\n"
"v_mfma_f32_16x16x32_bf16 v[36:39], v[96:99], a[112:115], v[36:39]\n"
"v_mfma_f32_16x16x32_bf16 v[36:39], v[100:103], a[120:123], v[36:39]\n"
"s_waitcnt lgkmcnt(4)\n"
"v_mfma_f32_16x16x32_bf16 v[4:7], v[72:75], a[108:111], v[4:7]\n"
"buffer_load_dwordx4 v247, s[52:55], s66 offen lds\n"
"v_mfma_f32_16x16x32_bf16 v[4:7], v[76:79], a[116:119], v[4:7]\n"
"s_nop 0\n"
"ds_read_b64_tr_b16 v[88:89], v189 offset:96\n"
"ds_read_b64_tr_b16 v[90:91], v189 offset:4704\n"
"v_mfma_f32_16x16x32_bf16 v[40:43], v[72:75], a[112:115], v[40:43]\n"
"s_nop 0\n"
"ds_read_b64_tr_b16 v[92:93], v189 offset:9312\n"
"ds_read_b64_tr_b16 v[94:95], v189 offset:13920\n"
"v_mfma_f32_16x16x32_bf16 v[40:43], v[76:79], a[120:123], v[40:43]\n"
"s_waitcnt lgkmcnt(4)\n"
"v_mfma_f32_16x16x32_bf16 v[8:11], v[80:83], a[108:111], v[8:11]\n"
"buffer_load_dwordx4 a[172:175], v186, s[60:63], s78 offen offset:2048 sc1\n"
"v_mfma_f32_16x16x32_bf16 v[8:11], v[84:87], a[116:119], v[8:11]\n"
"s_nop 0\n"
"ds_read_b64_tr_b16 v[96:97], v189 offset:128\n"
"ds_read_b64_tr_b16 v[98:99], v189 offset:4736\n"
"v_mfma_f32_16x16x32_bf16 v[44:47], v[80:83], a[112:115], v[44:47]\n"
"s_nop 0\n"
"ds_read_b64_tr_b16 v[100:101], v189 offset:9344\n"
"ds_read_b64_tr_b16 v[102:103], v189 offset:13952\n"
"v_mfma_f32_16x16x32_bf16 v[44:47], v[84:87], a[120:123], v[44:47]\n"
"s_waitcnt lgkmcnt(4)\n"
"v_mfma_f32_16x16x32_bf16 v[12:15], v[88:91], a[108:111], v[12:15]\n"
"buffer_load_dwordx4 a[176:179], v186, s[60:63], s79 offen offset:2048 sc1\n"
"v_mfma_f32_16x16x32_bf16 v[12:15], v[92:95], a[116:119], v[12:15]\n"
"s_nop 0\n"
"ds_read_b64_tr_b16 v[72:73], v189 offset:160\n"
"ds_read_b64_tr_b16 v[74:75], v189 offset:4768\n"
"v_mfma_f32_16x16x32_bf16 v[48:51], v[88:91], a[112:115], v[48:51]\n"
"s_nop 0\n"
"ds_read_b64_tr_b16 v[76:77], v189 offset:9376\n"
"ds_read_b64_tr_b16 v[78:79], v189 offset:13984\n"
"v_mfma_f32_16x16x32_bf16 v[48:51], v[92:95], a[120:123], v[48:51]\n"
"s_waitcnt lgkmcnt(4)\n"
"v_mfma_f32_16x16x32_bf16 v[16:19], v[96:99], a[108:111], v[16:19]\n"
"buffer_load_dwordx4 a[180:183], v186, s[60:63], s78 offen offset:3072 sc1\n"
"v_mfma_f32_16x16x32_bf16 v[16:19], v[100:103], a[116:119], v[16:19]\n"
"s_nop 0\n"
"ds_read_b64_tr_b16 v[80:81], v189 offset:192\n"
"ds_read_b64_tr_b16 v[82:83], v189 offset:4800\n"
"v_mfma_f32_16x16x32_bf16 v[52:55], v[96:99], a[112:115], v[52:55]\n"
"s_nop 0\n"
"ds_read_b64_tr_b16 v[84:85], v189 offset:9408\n"
"ds_read_b64_tr_b16 v[86:87], v189 offset:14016\n"
"v_mfma_f32_16x16x32_bf16 v[52:55], v[100:103], a[120:123], v[52:55]\n"
"s_waitcnt lgkmcnt(4)\n"
"v_mfma_f32_16x16x32_bf16 v[20:23], v[72:75], a[108:111], v[20:23]\n"
"s_nop 0\n"
"ds_read_b64_tr_b16 v[88:89], v189 offset:224\n"
"ds_read_b64_tr_b16 v[90:91], v189 offset:4832\n"
"v_mfma_f32_16x16x32_bf16 v[20:23], v[76:79], a[116:119], v[20:23]\n"
"buffer_load_dwordx4 a[184:187], v186, s[60:63], s79 offen offset:3072 sc1\n"
"v_mfma_f32_16x16x32_bf16 v[56:59], v[72:75], a[112:115], v[56:59]\n"
"s_add_u32 m0, s71, 8192\n"
"ds_read_b64_tr_b16 v[92:93], v189 offset:9440\n"
"ds_read_b64_tr_b16 v[94:95], v189 offset:14048\n"
"v_mfma_f32_16x16x32_bf16 v[56:59], v[76:79], a[120:123], v[56:59]\n"
"s_waitcnt lgkmcnt(4)\n"
"v_mfma_f32_16x16x32_bf16 v[24:27], v[80:83], a[108:111], v[24:27]\n"
"s_nop 0\n"
"ds_read_b64_tr_b16 v[96:97], v192 offset:0\n"
"ds_read_b64_tr_b16 v[98:99], v192 offset:4608\n"
"v_mfma_f32_16x16x32_bf16 v[24:27], v[84:87], a[116:119], v[24:27]\n"
"buffer_load_dwordx4 v248, s[52:55], s66 offen lds\n"
"v_mfma_f32_16x16x32_bf16 v[60:63], v[80:83], a[112:115], v[60:63]\n"
"s_add_u32 m0, s71, 12288\n"
"ds_read_b64_tr_b16 v[100:101], v192 offset:9216\n"
"ds_read_b64_tr_b16 v[102:103], v192 offset:13824\n"
"v_mfma_f32_16x16x32_bf16 v[60:63], v[84:87], a[120:123], v[60:63]\n"
"s_waitcnt vmcnt(45) lgkmcnt(4)\n"
"s_barrier\n"
"v_mfma_f32_16x16x32_bf16 v[28:31], v[88:91], a[108:111], v[28:31]\n"
"s_nop 0\n"
"ds_read_b64_tr_b16 v[72:73], v189 offset:18432\n"
"ds_read_b64_tr_b16 v[74:75], v189 offset:23040\n"
"v_mfma_f32_16x16x32_bf16 v[28:31], v[92:95], a[116:119], v[28:31]\n"
"s_nop 0\n"
"ds_read_b64_tr_b16 v[76:77], v189 offset:27648\n"
"ds_read_b64_tr_b16 v[78:79], v189 offset:32256\n"
"v_mfma_f32_16x16x32_bf16 v[64:67], v[88:91], a[112:115], v[64:67]\n"
"buffer_load_dwordx4 v249, s[52:55], s66 offen lds\n"
"v_mfma_f32_16x16x32_bf16 v[64:67], v[92:95], a[120:123], v[64:67]\n"
"s_waitcnt lgkmcnt(4)\n"
"s_barrier\n"
"v_mfma_f32_16x16x32_bf16 v[32:35], v[96:99], a[108:111], v[32:35]\n"
"s_add_u32 m0, s71, 16384\n"
"ds_read_b64_tr_b16 v[80:81], v189 offset:18464\n"
"ds_read_b64_tr_b16 v[82:83], v189 offset:23072\n"
"v_mfma_f32_16x16x32_bf16 v[32:35], v[100:103], a[116:119], v[32:35]\n"
"s_nop 0\n"
"ds_read_b64_tr_b16 v[84:85], v189 offset:27680\n"
"ds_read_b64_tr_b16 v[86:87], v189 offset:32288\n"
"v_mfma_f32_16x16x32_bf16 v[68:71], v[96:99], a[112:115], v[68:71]\n"
"buffer_load_dwordx4 v250, s[52:55], s66 offen lds\n"
"v_mfma_f32_16x16x32_bf16 v[68:71], v[100:103], a[120:123], v[68:71]\n"
"s_waitcnt vmcnt(17) lgkmcnt(4)\n"
"v_mfma_f32_16x16x32_bf16 v[0:3], v[72:75], a[124:127], v[0:3]\n"
"s_add_u32 m0, s71, 20480\n"
"ds_read_b64_tr_b16 v[88:89], v189 offset:18496\n"
"ds_read_b64_tr_b16 v[90:91], v189 offset:23104\n"
"v_mfma_f32_16x16x32_bf16 v[0:3], v[76:79], a[132:135], v[0:3]\n"
"s_nop 0\n"
"ds_read_b64_tr_b16 v[92:93], v189 offset:27712\n"
"ds_read_b64_tr_b16 v[94:95], v189 offset:32320\n"
"v_mfma_f32_16x16x32_bf16 v[36:39], v[72:75], a[128:131], v[36:39]\n"
"v_mfma_f32_16x16x32_bf16 v[36:39], v[76:79], a[136:139], v[36:39]\n"
"s_waitcnt lgkmcnt(4)\n"
"v_mfma_f32_16x16x32_bf16 v[4:7], v[80:83], a[124:127], v[4:7]\n"
"buffer_load_dwordx4 v251, s[52:55], s66 offen lds\n"
"v_mfma_f32_16x16x32_bf16 v[4:7], v[84:87], a[132:135], v[4:7]\n"
"s_add_u32 m0, s71, 24576\n"
"ds_read_b64_tr_b16 v[96:97], v189 offset:18528\n"
"ds_read_b64_tr_b16 v[98:99], v189 offset:23136\n"
"v_mfma_f32_16x16x32_bf16 v[40:43], v[80:83], a[128:131], v[40:43]\n"
"s_nop 0\n"
"ds_read_b64_tr_b16 v[100:101], v189 offset:27744\n"
"ds_read_b64_tr_b16 v[102:103], v189 offset:32352\n"
"v_mfma_f32_16x16x32_bf16 v[40:43], v[84:87], a[136:139], v[40:43]\n"
"s_waitcnt lgkmcnt(4)\n"
"v_mfma_f32_16x16x32_bf16 v[8:11], v[88:91], a[124:127], v[8:11]\n"
"buffer_load_dwordx4 v252, s[52:55], s66 offen lds\n"
"v_mfma_f32_16x16x32_bf16 v[8:11], v[92:95], a[132:135], v[8:11]\n"
"s_add_u32 m0, s71, 28672\n"
"ds_read_b64_tr_b16 v[72:73], v189 offset:18560\n"
"ds_read_b64_tr_b16 v[74:75], v189 offset:23168\n"
"v_mfma_f32_16x16x32_bf16 v[44:47], v[88:91], a[128:131], v[44:47]\n"
"s_nop 0\n"
"ds_read_b64_tr_b16 v[76:77], v189 offset:27776\n"
"ds_read_b64_tr_b16 v[78:79], v189 offset:32384\n"
"v_mfma_f32_16x16x32_bf16 v[44:47], v[92:95], a[136:139], v[44:47]\n"
"s_waitcnt lgkmcnt(4)\n"
"v_mfma_f32_16x16x32_bf16 v[12:15], v[96:99], a[124:127], v[12:15]\n"
"buffer_load_dwordx4 v253, s[52:55], s66 offen lds\n"
"v_mfma_f32_16x16x32_bf16 v[12:15], v[100:103], a[132:135], v[12:15]\n"
"s_add_u32 m0, s71, 32768\n"
"ds_read_b64_tr_b16 v[80:81], v189 offset:18592\n"
"ds_read_b64_tr_b16 v[82:83], v189 offset:23200\n"
"v_mfma_f32_16x16x32_bf16 v[48:51], v[96:99], a[128:131], v[48:51]\n"
"s_nop 0\n"
"ds_read_b64_tr_b16 v[84:85], v189 offset:27808\n"
"ds_read_b64_tr_b16 v[86:87], v189 offset:32416\n"
"v_mfma_f32_16x16x32_bf16 v[48:51], v[100:103], a[136:139], v[48:51]\n"
"s_waitcnt lgkmcnt(4)\n"
"v_mfma_f32_16x16x32_bf16 v[16:19], v[72:75], a[124:127], v[16:19]\n"
"buffer_load_dwordx4 v254, s[52:55], s66 offen lds\n"
"v_mfma_f32_16x16x32_bf16 v[16:19], v[76:79], a[132:135], v[16:19]\n"
"s_add_u32 s78, s82, 16384\n"
"ds_read_b64_tr_b16 v[88:89], v189 offset:18624\n"
"ds_read_b64_tr_b16 v[90:91], v189 offset:23232\n"
"v_mfma_f32_16x16x32_bf16 v[52:55], v[72:75], a[128:131], v[52:55]\n"
"s_nop 0\n"
"ds_read_b64_tr_b16 v[92:93], v189 offset:27840\n"
"ds_read_b64_tr_b16 v[94:95], v189 offset:32448\n"
"v_mfma_f32_16x16x32_bf16 v[52:55], v[76:79], a[136:139], v[52:55]\n"
"s_waitcnt lgkmcnt(4)\n"
"v_mfma_f32_16x16x32_bf16 v[20:23], v[80:83], a[124:127], v[20:23]\n"
"s_nop 0\n"
"ds_read_b64_tr_b16 v[96:97], v189 offset:18656\n"
"ds_read_b64_tr_b16 v[98:99], v189 offset:23264\n"
"v_mfma_f32_16x16x32_bf16 v[20:23], v[84:87], a[132:135], v[20:23]\n"
"buffer_load_dwordx4 a[188:191], v186, s[60:63], s78 offen offset:0 sc1\n"
"v_mfma_f32_16x16x32_bf16 v[56:59], v[80:83], a[128:131], v[56:59]\n"
"s_add_u32 s79, s83, 16384\n"
"ds_read_b64_tr_b16 v[100:101], v189 offset:27872\n"
"ds_read_b64_tr_b16 v[102:103], v189 offset:32480\n"
"v_mfma_f32_16x16x32_bf16 v[56:59], v[84:87], a[136:139], v[56:59]\n"
"s_waitcnt lgkmcnt(4)\n"
"v_mfma_f32_16x16x32_bf16 v[24:27], v[88:91], a[124:127], v[24:27]\n"
"s_nop 0\n"
"ds_read_b64_tr_b16 v[72:73], v192 offset:18432\n"
"ds_read_b64_tr_b16 v[74:75], v192 offset:23040\n"
"v_mfma_f32_16x16x32_bf16 v[24:27], v[92:95], a[132:135], v[24:27]\n"
"buffer_load_dwordx4 a[192:195], v186, s[60:63], s79 offen offset:0 sc1\n"
"v_mfma_f32_16x16x32_bf16 v[60:63], v[88:91], a[128:131], v[60:63]\n"
"s_nop 0\n"
"ds_read_b64_tr_b16 v[76:77], v192 offset:27648\n"
"ds_read_b64_tr_b16 v[78:79], v192 offset:32256\n"
"v_mfma_f32_16x16x32_bf16 v[60:63], v[92:95], a[136:139], v[60:63]\n"
"s_waitcnt lgkmcnt(4)\n"
"v_mfma_f32_16x16x32_bf16 v[28:31], v[96:99], a[124:127], v[28:31]\n"
"s_nop 0\n"
"ds_read_b64_tr_b16 v[80:81], v189 offset:36864\n"
"ds_read_b64_tr_b16 v[82:83], v189 offset:41472\n"
"v_mfma_f32_16x16x32_bf16 v[28:31], v[100:103], a[132:135], v[28:31]\n"
"s_nop 0\n"
"ds_read_b64_tr_b16 v[84:85], v189 offset:46080\n"
"ds_read_b64_tr_b16 v[86:87], v189 offset:50688\n"
"v_mfma_f32_16x16x32_bf16 v[64:67], v[96:99], a[128:131], v[64:67]\n"
"buffer_load_dwordx4 a[196:199], v186, s[60:63], s78 offen offset:1024 sc1\n"
"v_mfma_f32_16x16x32_bf16 v[64:67], v[100:103], a[136:139], v[64:67]\n"
"s_waitcnt lgkmcnt(4)\n"
"v_mfma_f32_16x16x32_bf16 v[32:35], v[72:75], a[124:127], v[32:35]\n"
"s_nop 0\n"
"ds_read_b64_tr_b16 v[88:89], v189 offset:36896\n"
"ds_read_b64_tr_b16 v[90:91], v189 offset:41504\n"
"v_mfma_f32_16x16x32_bf16 v[32:35], v[76:79], a[132:135], v[32:35]\n"
"s_nop 0\n"
"ds_read_b64_tr_b16 v[92:93], v189 offset:46112\n"
"ds_read_b64_tr_b16 v[94:95], v189 offset:50720\n"
"v_mfma_f32_16x16x32_bf16 v[68:71], v[72:75], a[128:131], v[68:71]\n"
"buffer_load_dwordx4 a[200:203], v186, s[60:63], s79 offen offset:1024 sc1\n"
"v_mfma_f32_16x16x32_bf16 v[68:71], v[76:79], a[136:139], v[68:71]\n"
"s_waitcnt vmcnt(21) lgkmcnt(4)\n"
"v_mfma_f32_16x16x32_bf16 v[0:3], v[80:83], a[140:143], v[0:3]\n"
"s_add_u32 m0, s71, 36864\n"
"ds_read_b64_tr_b16 v[96:97], v189 offset:36928\n"
"ds_read_b64_tr_b16 v[98:99], v189 offset:41536\n"
"v_mfma_f32_16x16x32_bf16 v[0:3], v[84:87], a[148:151], v[0:3]\n"
"s_mov_b32 s66, 737280\n"
"ds_read_b64_tr_b16 v[100:101], v189 offset:46144\n"
"ds_read_b64_tr_b16 v[102:103], v189 offset:50752\n"
"v_mfma_f32_16x16x32_bf16 v[36:39], v[80:83], a[144:147], v[36:39]\n"
"v_mfma_f32_16x16x32_bf16 v[36:39], v[84:87], a[152:155], v[36:39]\n"
"s_waitcnt lgkmcnt(4)\n"
"v_mfma_f32_16x16x32_bf16 v[4:7], v[88:91], a[140:143], v[4:7]\n"
"buffer_load_dwordx4 v246, s[52:55], s66 offen lds\n"
"v_mfma_f32_16x16x32_bf16 v[4:7], v[92:95], a[148:151], v[4:7]\n"
"s_add_u32 m0, s71, 40960\n"
"ds_read_b64_tr_b16 v[72:73], v189 offset:36960\n"
"ds_read_b64_tr_b16 v[74:75], v189 offset:41568\n"
"v_mfma_f32_16x16x32_bf16 v[40:43], v[88:91], a[144:147], v[40:43]\n"
"s_nop 0\n"
"ds_read_b64_tr_b16 v[76:77], v189 offset:46176\n"
"ds_read_b64_tr_b16 v[78:79], v189 offset:50784\n"
"v_mfma_f32_16x16x32_bf16 v[40:43], v[92:95], a[152:155], v[40:43]\n"
"s_waitcnt lgkmcnt(4)\n"
"v_mfma_f32_16x16x32_bf16 v[8:11], v[96:99], a[140:143], v[8:11]\n"
"buffer_load_dwordx4 v247, s[52:55], s66 offen lds\n"
"v_mfma_f32_16x16x32_bf16 v[8:11], v[100:103], a[148:151], v[8:11]\n"
"s_nop 0\n"
"ds_read_b64_tr_b16 v[80:81], v189 offset:36992\n"
"ds_read_b64_tr_b16 v[82:83], v189 offset:41600\n"
"v_mfma_f32_16x16x32_bf16 v[44:47], v[96:99], a[144:147], v[44:47]\n"
"s_nop 0\n"
"ds_read_b64_tr_b16 v[84:85], v189 offset:46208\n"
"ds_read_b64_tr_b16 v[86:87], v189 offset:50816\n"
"v_mfma_f32_16x16x32_bf16 v[44:47], v[100:103], a[152:155], v[44:47]\n"
"s_waitcnt lgkmcnt(4)\n"
"v_mfma_f32_16x16x32_bf16 v[12:15], v[72:75], a[140:143], v[12:15]\n"
"buffer_load_dwordx4 a[204:207], v186, s[60:63], s78 offen offset:2048 sc1\n"
"v_mfma_f32_16x16x32_bf16 v[12:15], v[76:79], a[148:151], v[12:15]\n"
"s_nop 0\n"
"ds_read_b64_tr_b16 v[88:89], v189 offset:37024\n"
"ds_read_b64_tr_b16 v[90:91], v189 offset:41632\n"
"v_mfma_f32_16x16x32_bf16 v[48:51], v[72:75], a[144:147], v[48:51]\n"
"s_nop 0\n"
"ds_read_b64_tr_b16 v[92:93], v189 offset:46240\n"
"ds_read_b64_tr_b16 v[94:95], v189 offset:50848\n"
"v_mfma_f32_16x16x32_bf16 v[48:51], v[76:79], a[152:155], v[48:51]\n"
"s_waitcnt lgkmcnt(4)\n"
"v_mfma_f32_16x16x32_bf16 v[16:19], v[80:83], a[140:143], v[16:19]\n"
"buffer_load_dwordx4 a[208:211], v186, s[60:63], s79 offen offset:2048 sc1\n"
"v_mfma_f32_16x16x32_bf16 v[16:19], v[84:87], a[148:151], v[16:19]\n"
"s_nop 0\n"
"ds_read_b64_tr_b16 v[96:97], v189 offset:37056\n"
"ds_read_b64_tr_b16 v[98:99], v189 offset:41664\n"
"v_mfma_f32_16x16x32_bf16 v[52:55], v[80:83], a[144:147], v[52:55]\n"
"s_nop 0\n"
"ds_read_b64_tr_b16 v[100:101], v189 offset:46272\n"
"ds_read_b64_tr_b16 v[102:103], v189 offset:50880\n"
"v_mfma_f32_16x16x32_bf16 v[52:55], v[84:87], a[152:155], v[52:55]\n"
"s_waitcnt lgkmcnt(4)\n"
"v_mfma_f32_16x16x32_bf16 v[20:23], v[88:91], a[140:143], v[20:23]\n"
"s_nop 0\n"
"ds_read_b64_tr_b16 v[72:73], v189 offset:37088\n"
"ds_read_b64_tr_b16 v[74:75], v189 offset:41696\n"
"v_mfma_f32_16x16x32_bf16 v[20:23], v[92:95], a[148:151], v[20:23]\n"
"buffer_load_dwordx4 a[212:215], v186, s[60:63], s78 offen offset:3072 sc1\n"
"v_mfma_f32_16x16x32_bf16 v[56:59], v[88:91], a[144:147], v[56:59]\n"
"L_tc1_1:\n"
"s_cbranch_execz L_tb1_1\n"
"ds_read_b64_tr_b16 v[76:77], v189 offset:46304\n"
"ds_read_b64_tr_b16 v[78:79], v189 offset:50912\n"
"v_mfma_f32_16x16x32_bf16 v[56:59], v[92:95], a[152:155], v[56:59]\n"
"s_waitcnt lgkmcnt(4)\n"
"v_mfma_f32_16x16x32_bf16 v[24:27], v[96:99], a[140:143], v[24:27]\n"
"s_nop 0\n"
"ds_read_b64_tr_b16 v[80:81], v192 offset:36864\n"
"ds_read_b64_tr_b16 v[82:83], v192 offset:41472\n"
"v_mfma_f32_16x16x32_bf16 v[24:27], v[100:103], a[148:151], v[24:27]\n"
"buffer_load_dwordx4 a[216:219], v186, s[60:63], s79 offen offset:3072 sc1\n"
"v_mfma_f32_16x16x32_bf16 v[60:63], v[96:99], a[144:147], v[60:63]\n"
"s_add_u32 m0, s71, 45056\n"
"ds_read_b64_tr_b16 v[84:85], v192 offset:46080\n"
"ds_read_b64_tr_b16 v[86:87], v192 offset:50688\n"
"v_mfma_f32_16x16x32_bf16 v[60:63], v[100:103], a[152:155], v[60:63]\n"
"s_waitcnt vmcnt(52) lgkmcnt(4)\n"
"s_barrier\n"
"v_mfma_f32_16x16x32_bf16 v[28:31], v[72:75], a[140:143], v[28:31]\n"
"s_nop 0\n"
"ds_read_b64_tr_b16 v[88:89], v190 offset:0\n"
"ds_read_b64_tr_b16 v[90:91], v190 offset:4608\n"
"v_mfma_f32_16x16x32_bf16 v[28:31], v[76:79], a[148:151], v[28:31]\n"
"s_nop 0\n"
"ds_read_b64_tr_b16 v[92:93], v190 offset:9216\n"
"ds_read_b64_tr_b16 v[94:95], v190 offset:13824\n"
"v_mfma_f32_16x16x32_bf16 v[64:67], v[72:75], a[144:147], v[64:67]\n"
"buffer_load_dwordx4 v248, s[52:55], s66 offen lds\n"
"v_mfma_f32_16x16x32_bf16 v[64:67], v[76:79], a[152:155], v[64:67]\n"
"s_waitcnt vmcnt(48) lgkmcnt(4)\n"
"s_barrier\n"
"v_mfma_f32_16x16x32_bf16 v[32:35], v[80:83], a[140:143], v[32:35]\n"
"s_add_u32 m0, s71, 49152\n"
"ds_read_b64_tr_b16 v[96:97], v190 offset:32\n"
"ds_read_b64_tr_b16 v[98:99], v190 offset:4640\n"
"v_mfma_f32_16x16x32_bf16 v[32:35], v[84:87], a[148:151], v[32:35]\n"
"s_nop 0\n"
"ds_read_b64_tr_b16 v[100:101], v190 offset:9248\n"
"ds_read_b64_tr_b16 v[102:103], v190 offset:13856\n"
"v_mfma_f32_16x16x32_bf16 v[68:71], v[80:83], a[144:147], v[68:71]\n"
"buffer_load_dwordx4 v249, s[52:55], s66 offen lds\n"
"v_mfma_f32_16x16x32_bf16 v[68:71], v[84:87], a[152:155], v[68:71]\n"
"s_waitcnt vmcnt(25) lgkmcnt(4)\n"
"v_mfma_f32_16x16x32_bf16 v[0:3], v[88:91], a[156:159], v[0:3]\n"
"s_add_u32 m0, s71, 53248\n"
"ds_read_b64_tr_b16 v[72:73], v190 offset:64\n"
"ds_read_b64_tr_b16 v[74:75], v190 offset:4672\n"
"v_mfma_f32_16x16x32_bf16 v[0:3], v[92:95], a[164:167], v[0:3]\n"
"s_nop 0\n"
"ds_read_b64_tr_b16 v[76:77], v190 offset:9280\n"
"ds_read_b64_tr_b16 v[78:79], v190 offset:13888\n"
"v_mfma_f32_16x16x32_bf16 v[36:39], v[88:91], a[160:163], v[36:39]\n"
"v_mfma_f32_16x16x32_bf16 v[36:39], v[92:95], a[168:171], v[36:39]\n"
"s_waitcnt lgkmcnt(4)\n"
"v_mfma_f32_16x16x32_bf16 v[4:7], v[96:99], a[156:159], v[4:7]\n"
"buffer_load_dwordx4 v250, s[52:55], s66 offen lds\n"
"v_mfma_f32_16x16x32_bf16 v[4:7], v[100:103], a[164:167], v[4:7]\n"
"s_add_u32 m0, s71, 57344\n"
"ds_read_b64_tr_b16 v[80:81], v190 offset:96\n"
"ds_read_b64_tr_b16 v[82:83], v190 offset:4704\n"
"v_mfma_f32_16x16x32_bf16 v[40:43], v[96:99], a[160:163], v[40:43]\n"
"s_nop 0\n"
"ds_read_b64_tr_b16 v[84:85], v190 offset:9312\n"
"ds_read_b64_tr_b16 v[86:87], v190 offset:13920\n"
"v_mfma_f32_16x16x32_bf16 v[40:43], v[100:103], a[168:171], v[40:43]\n"
"s_waitcnt lgkmcnt(4)\n"
"v_mfma_f32_16x16x32_bf16 v[8:11], v[72:75], a[156:159], v[8:11]\n"
"buffer_load_dwordx4 v251, s[52:55], s66 offen lds\n"
"v_mfma_f32_16x16x32_bf16 v[8:11], v[76:79], a[164:167], v[8:11]\n"
"s_add_u32 m0, s71, 61440\n"
"ds_read_b64_tr_b16 v[88:89], v190 offset:128\n"
"ds_read_b64_tr_b16 v[90:91], v190 offset:4736\n"
"v_mfma_f32_16x16x32_bf16 v[44:47], v[72:75], a[160:163], v[44:47]\n"
"s_nop 0\n"
"ds_read_b64_tr_b16 v[92:93], v190 offset:9344\n"
"ds_read_b64_tr_b16 v[94:95], v190 offset:13952\n"
"v_mfma_f32_16x16x32_bf16 v[44:47], v[76:79], a[168:171], v[44:47]\n"
"s_waitcnt lgkmcnt(4)\n"
"v_mfma_f32_16x16x32_bf16 v[12:15], v[80:83], a[156:159], v[12:15]\n"
"buffer_load_dwordx4 v252, s[52:55], s66 offen lds\n"
"v_mfma_f32_16x16x32_bf16 v[12:15], v[84:87], a[164:167], v[12:15]\n"
"s_add_u32 m0, s71, 65536\n"
"ds_read_b64_tr_b16 v[96:97], v190 offset:160\n"
"ds_read_b64_tr_b16 v[98:99], v190 offset:4768\n"
"v_mfma_f32_16x16x32_bf16 v[48:51], v[80:83], a[160:163], v[48:51]\n"
"s_nop 0\n"
"ds_read_b64_tr_b16 v[100:101], v190 offset:9376\n"
"ds_read_b64_tr_b16 v[102:103], v190 offset:13984\n"
"v_mfma_f32_16x16x32_bf16 v[48:51], v[84:87], a[168:171], v[48:51]\n"
"s_waitcnt lgkmcnt(4)\n"
"v_mfma_f32_16x16x32_bf16 v[16:19], v[88:91], a[156:159], v[16:19]\n"
"buffer_load_dwordx4 v253, s[52:55], s66 offen lds\n"
"v_mfma_f32_16x16x32_bf16 v[16:19], v[92:95], a[164:167], v[16:19]\n"
"s_add_u32 m0, s71, 69632\n"
"ds_read_b64_tr_b16 v[72:73], v190 offset:192\n"
"ds_read_b64_tr_b16 v[74:75], v190 offset:4800\n"
"v_mfma_f32_16x16x32_bf16 v[52:55], v[88:91], a[160:163], v[52:55]\n"
"s_nop 0\n"
"ds_read_b64_tr_b16 v[76:77], v190 offset:9408\n"
"ds_read_b64_tr_b16 v[78:79], v190 offset:14016\n"
"v_mfma_f32_16x16x32_bf16 v[52:55], v[92:95], a[168:171], v[52:55]\n"
"s_waitcnt lgkmcnt(4)\n"
"v_mfma_f32_16x16x32_bf16 v[20:23], v[96:99], a[156:159], v[20:23]\n"
"s_nop 0\n"
"ds_read_b64_tr_b16 v[80:81], v190 offset:224\n"
"ds_read_b64_tr_b16 v[82:83], v190 offset:4832\n"
"v_mfma_f32_16x16x32_bf16 v[20:23], v[100:103], a[164:167], v[20:23]\n"
"buffer_load_dwordx4 v254, s[52:55], s66 offen lds\n"
"v_mfma_f32_16x16x32_bf16 v[56:59], v[96:99], a[160:163], v[56:59]\n"
"s_add_u32 s78, s82, 20480\n"
"ds_read_b64_tr_b16 v[84:85], v190 offset:9440\n"
"ds_read_b64_tr_b16 v[86:87], v190 offset:14048\n"
"v_mfma_f32_16x16x32_bf16 v[56:59], v[100:103], a[168:171], v[56:59]\n"
"s_waitcnt lgkmcnt(4)\n"
"v_mfma_f32_16x16x32_bf16 v[24:27], v[72:75], a[156:159], v[24:27]\n"
"s_nop 0\n"
"ds_read_b64_tr_b16 v[88:89], v193 offset:0\n"
"ds_read_b64_tr_b16 v[90:91], v193 offset:4608\n"
"v_mfma_f32_16x16x32_bf16 v[24:27], v[76:79], a[164:167], v[24:27]\n"
"buffer_load_dwordx4 a[220:223], v186, s[60:63], s78 offen offset:0 sc1\n"
"v_mfma_f32_16x16x32_bf16 v[60:63], v[72:75], a[160:163], v[60:63]\n"
"s_add_u32 s79, s83, 20480\n"
"ds_read_b64_tr_b16 v[92:93], v193 offset:9216\n"
"ds_read_b64_tr_b16 v[94:95], v193 offset:13824\n"
"v_mfma_f32_16x16x32_bf16 v[60:63], v[76:79], a[168:171], v[60:63]\n"
"s_waitcnt lgkmcnt(4)\n"
"v_mfma_f32_16x16x32_bf16 v[28:31], v[80:83], a[156:159], v[28:31]\n"
"s_nop 0\n"
"ds_read_b64_tr_b16 v[96:97], v190 offset:18432\n"
"ds_read_b64_tr_b16 v[98:99], v190 offset:23040\n"
"v_mfma_f32_16x16x32_bf16 v[28:31], v[84:87], a[164:167], v[28:31]\n"
"s_nop 0\n"
"ds_read_b64_tr_b16 v[100:101], v190 offset:27648\n"
"ds_read_b64_tr_b16 v[102:103], v190 offset:32256\n"
"v_mfma_f32_16x16x32_bf16 v[64:67], v[80:83], a[160:163], v[64:67]\n"
"buffer_load_dwordx4 a[224:227], v186, s[60:63], s79 offen offset:0 sc1\n"
"v_mfma_f32_16x16x32_bf16 v[64:67], v[84:87], a[168:171], v[64:67]\n"
"s_waitcnt lgkmcnt(4)\n"
"v_mfma_f32_16x16x32_bf16 v[32:35], v[88:91], a[156:159], v[32:35]\n"
"s_nop 0\n"
"ds_read_b64_tr_b16 v[72:73], v190 offset:18464\n"
"ds_read_b64_tr_b16 v[74:75], v190 offset:23072\n"
"v_mfma_f32_16x16x32_bf16 v[32:35], v[92:95], a[164:167], v[32:35]\n"
"s_nop 0\n"
"ds_read_b64_tr_b16 v[76:77], v190 offset:27680\n"
"ds_read_b64_tr_b16 v[78:79], v190 offset:32288\n"
"v_mfma_f32_16x16x32_bf16 v[68:71], v[88:91], a[160:163], v[68:71]\n"
"buffer_load_dwordx4 a[228:231], v186, s[60:63], s78 offen offset:1024 sc1\n"
"v_mfma_f32_16x16x32_bf16 v[68:71], v[92:95], a[168:171], v[68:71]\n"
"s_waitcnt vmcnt(27) lgkmcnt(4)\n"
"v_mfma_f32_16x16x32_bf16 v[0:3], v[96:99], a[172:175], v[0:3]\n"
"s_nop 0\n"
"ds_read_b64_tr_b16 v[80:81], v190 offset:18496\n"
"ds_read_b64_tr_b16 v[82:83], v190 offset:23104\n"
"v_mfma_f32_16x16x32_bf16 v[0:3], v[100:103], a[180:183], v[0:3]\n"
"s_nop 0\n"
"ds_read_b64_tr_b16 v[84:85], v190 offset:27712\n"
"ds_read_b64_tr_b16 v[86:87], v190 offset:32320\n"
"v_mfma_f32_16x16x32_bf16 v[36:39], v[96:99], a[176:179], v[36:39]\n"
"v_mfma_f32_16x16x32_bf16 v[36:39], v[100:103], a[184:187], v[36:39]\n"
"s_waitcnt lgkmcnt(4)\n"
"v_mfma_f32_16x16x32_bf16 v[4:7], v[72:75], a[172:175], v[4:7]\n"
"buffer_load_dwordx4 a[232:235], v186, s[60:63], s79 offen offset:1024 sc1\n"
"v_mfma_f32_16x16x32_bf16 v[4:7], v[76:79], a[180:183], v[4:7]\n"
"s_add_u32 m0, s71, 73728\n"
"ds_read_b64_tr_b16 v[88:89], v190 offset:18528\n"
"ds_read_b64_tr_b16 v[90:91], v190 offset:23136\n"
"v_mfma_f32_16x16x32_bf16 v[40:43], v[72:75], a[176:179], v[40:43]\n"
"s_mov_b32 s66, 884736\n"
"ds_read_b64_tr_b16 v[92:93], v190 offset:27744\n"
"ds_read_b64_tr_b16 v[94:95], v190 offset:32352\n"
"v_mfma_f32_16x16x32_bf16 v[40:43], v[76:79], a[184:187], v[40:43]\n"
"s_waitcnt lgkmcnt(4)\n"
"v_mfma_f32_16x16x32_bf16 v[8:11], v[80:83], a[172:175], v[8:11]\n"
"buffer_load_dwordx4 v246, s[52:55], s66 offen lds\n"
"v_mfma_f32_16x16x32_bf16 v[8:11], v[84:87], a[180:183], v[8:11]\n"
"s_add_u32 m0, s71, 77824\n"
"ds_read_b64_tr_b16 v[96:97], v190 offset:18560\n"
"ds_read_b64_tr_b16 v[98:99], v190 offset:23168\n"
"v_mfma_f32_16x16x32_bf16 v[44:47], v[80:83], a[176:179], v[44:47]\n"
"s_nop 0\n"
"ds_read_b64_tr_b16 v[100:101], v190 offset:27776\n"
"ds_read_b64_tr_b16 v[102:103], v190 offset:32384\n"
"v_mfma_f32_16x16x32_bf16 v[44:47], v[84:87], a[184:187], v[44:47]\n"
"s_waitcnt lgkmcnt(4)\n"
"v_mfma_f32_16x16x32_bf16 v[12:15], v[88:91], a[172:175], v[12:15]\n"
"buffer_load_dwordx4 v247, s[52:55], s66 offen lds\n"
"v_mfma_f32_16x16x32_bf16 v[12:15], v[92:95], a[180:183], v[12:15]\n"
"s_nop 0\n"
"ds_read_b64_tr_b16 v[72:73], v190 offset:18592\n"
"ds_read_b64_tr_b16 v[74:75], v190 offset:23200\n"
"v_mfma_f32_16x16x32_bf16 v[48:51], v[88:91], a[176:179], v[48:51]\n"
"s_nop 0\n"
"ds_read_b64_tr_b16 v[76:77], v190 offset:27808\n"
"ds_read_b64_tr_b16 v[78:79], v190 offset:32416\n"
"v_mfma_f32_16x16x32_bf16 v[48:51], v[92:95], a[184:187], v[48:51]\n"
"s_waitcnt lgkmcnt(4)\n"
"v_mfma_f32_16x16x32_bf16 v[16:19], v[96:99], a[172:175], v[16:19]\n"
"buffer_load_dwordx4 a[236:239], v186, s[60:63], s78 offen offset:2048 sc1\n"
"v_mfma_f32_16x16x32_bf16 v[16:19], v[100:103], a[180:183], v[16:19]\n"
"s_nop 0\n"
"ds_read_b64_tr_b16 v[80:81], v190 offset:18624\n"
"ds_read_b64_tr_b16 v[82:83], v190 offset:23232\n"
"v_mfma_f32_16x16x32_bf16 v[52:55], v[96:99], a[176:179], v[52:55]\n"
"s_nop 0\n"
"ds_read_b64_tr_b16 v[84:85], v190 offset:27840\n"
"ds_read_b64_tr_b16 v[86:87], v190 offset:32448\n"
"v_mfma_f32_16x16x32_bf16 v[52:55], v[100:103], a[184:187], v[52:55]\n"
"s_waitcnt lgkmcnt(4)\n"
"v_mfma_f32_16x16x32_bf16 v[20:23], v[72:75], a[172:175], v[20:23]\n"
"s_nop 0\n"
"ds_read_b64_tr_b16 v[88:89], v190 offset:18656\n"
"ds_read_b64_tr_b16 v[90:91], v190 offset:23264\n"
"v_mfma_f32_16x16x32_bf16 v[20:23], v[76:79], a[180:183], v[20:23]\n"
"buffer_load_dwordx4 a[240:243], v186, s[60:63], s79 offen offset:2048 sc1\n"
"v_mfma_f32_16x16x32_bf16 v[56:59], v[72:75], a[176:179], v[56:59]\n"
"s_nop 0\n"
"ds_read_b64_tr_b16 v[92:93], v190 offset:27872\n"
"ds_read_b64_tr_b16 v[94:95], v190 offset:32480\n"
"v_mfma_f32_16x16x32_bf16 v[56:59], v[76:79], a[184:187], v[56:59]\n"
"s_waitcnt lgkmcnt(4)\n"
"v_mfma_f32_16x16x32_bf16 v[24:27], v[80:83], a[172:175], v[24:27]\n"
"s_nop 0\n"
"ds_read_b64_tr_b16 v[96:97], v193 offset:18432\n"
"ds_read_b64_tr_b16 v[98:99], v193 offset:23040\n"
"v_mfma_f32_16x16x32_bf16 v[24:27], v[84:87], a[180:183], v[24:27]\n"
"buffer_load_dwordx4 a[244:247], v186, s[60:63], s78 offen offset:3072 sc1\n"
"v_mfma_f32_16x16x32_bf16 v[60:63], v[80:83], a[176:179], v[60:63]\n"
"s_nop 0\n"
"ds_read_b64_tr_b16 v[100:101], v193 offset:27648\n"
"ds_read_b64_tr_b16 v[102:103], v193 offset:32256\n"
"v_mfma_f32_16x16x32_bf16 v[60:63], v[84:87], a[184:187], v[60:63]\n"
"s_waitcnt vmcnt(26) lgkmcnt(4)\n"
"s_barrier\n"
"v_mfma_f32_16x16x32_bf16 v[28:31], v[88:91], a[172:175], v[28:31]\n"
"s_nop 0\n"
"ds_read_b64_tr_b16 v[72:73], v188 offset:0\n"
"ds_read_b64_tr_b16 v[74:75], v188 offset:4608\n"
"v_mfma_f32_16x16x32_bf16 v[28:31], v[92:95], a[180:183], v[28:31]\n"
"s_nop 0\n"
"ds_read_b64_tr_b16 v[76:77], v188 offset:9216\n"
"ds_read_b64_tr_b16 v[78:79], v188 offset:13824\n"
"v_mfma_f32_16x16x32_bf16 v[64:67], v[88:91], a[176:179], v[64:67]\n"
"buffer_load_dwordx4 a[248:251], v186, s[60:63], s79 offen offset:3072 sc1\n"
"v_mfma_f32_16x16x32_bf16 v[64:67], v[92:95], a[184:187], v[64:67]\n"
"s_waitcnt lgkmcnt(4)\n"
"s_barrier\n"
"v_mfma_f32_16x16x32_bf16 v[32:35], v[96:99], a[172:175], v[32:35]\n"
"s_add_u32 m0, s71, 81920\n"
"ds_read_b64_tr_b16 v[80:81], v188 offset:32\n"
"ds_read_b64_tr_b16 v[82:83], v188 offset:4640\n"
"v_mfma_f32_16x16x32_bf16 v[32:35], v[100:103], a[180:183], v[32:35]\n"
"s_nop 0\n"
"ds_read_b64_tr_b16 v[84:85], v188 offset:9248\n"
"ds_read_b64_tr_b16 v[86:87], v188 offset:13856\n"
"v_mfma_f32_16x16x32_bf16 v[68:71], v[96:99], a[176:179], v[68:71]\n"
"buffer_load_dwordx4 v248, s[52:55], s66 offen lds\n"
"v_mfma_f32_16x16x32_bf16 v[68:71], v[100:103], a[184:187], v[68:71]\n"
"s_waitcnt vmcnt(24) lgkmcnt(4)\n"
"v_mfma_f32_16x16x32_bf16 v[0:3], v[72:75], a[188:191], v[0:3]\n"
"s_add_u32 m0, s71, 86016\n"
"ds_read_b64_tr_b16 v[88:89], v188 offset:64\n"
"ds_read_b64_tr_b16 v[90:91], v188 offset:4672\n"
"v_mfma_f32_16x16x32_bf16 v[0:3], v[76:79], a[196:199], v[0:3]\n"
"s_nop 0\n"
"ds_read_b64_tr_b16 v[92:93], v188 offset:9280\n"
"ds_read_b64_tr_b16 v[94:95], v188 offset:13888\n"
"v_mfma_f32_16x16x32_bf16 v[36:39], v[72:75], a[192:195], v[36:39]\n"
"v_mfma_f32_16x16x32_bf16 v[36:39], v[76:79], a[200:203], v[36:39]\n"
"s_waitcnt lgkmcnt(4)\n"
"v_mfma_f32_16x16x32_bf16 v[4:7], v[80:83], a[188:191], v[4:7]\n"
"buffer_load_dwordx4 v249, s[52:55], s66 offen lds\n"
"v_mfma_f32_16x16x32_bf16 v[4:7], v[84:87], a[196:199], v[4:7]\n"
"s_add_u32 m0, s71, 90112\n"
"ds_read_b64_tr_b16 v[96:97], v188 offset:96\n"
"ds_read_b64_tr_b16 v[98:99], v188 offset:4704\n"
"v_mfma_f32_16x16x32_bf16 v[40:43], v[80:83], a[192:195], v[40:43]\n"
"s_nop 0\n"
"ds_read_b64_tr_b16 v[100:101], v188 offset:9312\n"
"ds_read_b64_tr_b16 v[102:103], v188 offset:13920\n"
"v_mfma_f32_16x16x32_bf16 v[40:43], v[84:87], a[200:203], v[40:43]\n"
"s_waitcnt lgkmcnt(4)\n"
"v_mfma_f32_16x16x32_bf16 v[8:11], v[88:91], a[188:191], v[8:11]\n"
"buffer_load_dwordx4 v250, s[52:55], s66 offen lds\n"
"v_mfma_f32_16x16x32_bf16 v[8:11], v[92:95], a[196:199], v[8:11]\n"
"s_add_u32 m0, s71, 94208\n"
"ds_read_b64_tr_b16 v[72:73], v188 offset:128\n"
"ds_read_b64_tr_b16 v[74:75], v188 offset:4736\n"
"v_mfma_f32_16x16x32_bf16 v[44:47], v[88:91], a[192:195], v[44:47]\n"
"s_nop 0\n"
"ds_read_b64_tr_b16 v[76:77], v188 offset:9344\n"
"ds_read_b64_tr_b16 v[78:79], v188 offset:13952\n"
"v_mfma_f32_16x16x32_bf16 v[44:47], v[92:95], a[200:203], v[44:47]\n"
"s_waitcnt lgkmcnt(4)\n"
"v_mfma_f32_16x16x32_bf16 v[12:15], v[96:99], a[188:191], v[12:15]\n"
"buffer_load_dwordx4 v251, s[52:55], s66 offen lds\n"
"v_mfma_f32_16x16x32_bf16 v[12:15], v[100:103], a[196:199], v[12:15]\n"
"s_add_u32 m0, s71, 98304\n"
"ds_read_b64_tr_b16 v[80:81], v188 offset:160\n"
"ds_read_b64_tr_b16 v[82:83], v188 offset:4768\n"
"v_mfma_f32_16x16x32_bf16 v[48:51], v[96:99], a[192:195], v[48:51]\n"
"s_nop 0\n"
"ds_read_b64_tr_b16 v[84:85], v188 offset:9376\n"
"ds_read_b64_tr_b16 v[86:87], v188 offset:13984\n"
"v_mfma_f32_16x16x32_bf16 v[48:51], v[100:103], a[200:203], v[48:51]\n"
"s_waitcnt lgkmcnt(4)\n"
"v_mfma_f32_16x16x32_bf16 v[16:19], v[72:75], a[188:191], v[16:19]\n"
"buffer_load_dwordx4 v252, s[52:55], s66 offen lds\n"
"v_mfma_f32_16x16x32_bf16 v[16:19], v[76:79], a[196:199], v[16:19]\n"
"s_add_u32 m0, s71, 102400\n"
"ds_read_b64_tr_b16 v[88:89], v188 offset:192\n"
"ds_read_b64_tr_b16 v[90:91], v188 offset:4800\n"
"v_mfma_f32_16x16x32_bf16 v[52:55], v[72:75], a[192:195], v[52:55]\n"
"s_nop 0\n"
"ds_read_b64_tr_b16 v[92:93], v188 offset:9408\n"
"ds_read_b64_tr_b16 v[94:95], v188 offset:14016\n"
"v_mfma_f32_16x16x32_bf16 v[52:55], v[76:79], a[200:203], v[52:55]\n"
"s_waitcnt lgkmcnt(4)\n"
"v_mfma_f32_16x16x32_bf16 v[20:23], v[80:83], a[188:191], v[20:23]\n"
"s_nop 0\n"
"ds_read_b64_tr_b16 v[96:97], v188 offset:224\n"
"ds_read_b64_tr_b16 v[98:99], v188 offset:4832\n"
"v_mfma_f32_16x16x32_bf16 v[20:23], v[84:87], a[196:199], v[20:23]\n"
"buffer_load_dwordx4 v253, s[52:55], s66 offen lds\n"
"v_mfma_f32_16x16x32_bf16 v[56:59], v[80:83], a[192:195], v[56:59]\n"
"s_add_u32 m0, s71, 106496\n"
"ds_read_b64_tr_b16 v[100:101], v188 offset:9440\n"
"ds_read_b64_tr_b16 v[102:103], v188 offset:14048\n"
"v_mfma_f32_16x16x32_bf16 v[56:59], v[84:87], a[200:203], v[56:59]\n"
"s_waitcnt lgkmcnt(4)\n"
"v_mfma_f32_16x16x32_bf16 v[24:27], v[88:91], a[188:191], v[24:27]\n"
"s_nop 0\n"
"ds_read_b64_tr_b16 v[72:73], v191 offset:0\n"
"ds_read_b64_tr_b16 v[74:75], v191 offset:4608\n"
"v_mfma_f32_16x16x32_bf16 v[24:27], v[92:95], a[196:199], v[24:27]\n"
"buffer_load_dwordx4 v254, s[52:55], s66 offen lds\n"
"v_mfma_f32_16x16x32_bf16 v[60:63], v[88:91], a[192:195], v[60:63]\n"
"s_add_u32 m0, s71, 110592\n"
"ds_read_b64_tr_b16 v[76:77], v191 offset:9216\n"
"ds_read_b64_tr_b16 v[78:79], v191 offset:13824\n"
"v_mfma_f32_16x16x32_bf16 v[60:63], v[92:95], a[200:203], v[60:63]\n"
"s_waitcnt lgkmcnt(4)\n"
"v_mfma_f32_16x16x32_bf16 v[28:31], v[96:99], a[188:191], v[28:31]\n"
"s_mov_b32 s66, 1032192\n"
"ds_read_b64_tr_b16 v[80:81], v188 offset:18432\n"
"ds_read_b64_tr_b16 v[82:83], v188 offset:23040\n"
"v_mfma_f32_16x16x32_bf16 v[28:31], v[100:103], a[196:199], v[28:31]\n"
"s_nop 0\n"
"ds_read_b64_tr_b16 v[84:85], v188 offset:27648\n"
"ds_read_b64_tr_b16 v[86:87], v188 offset:32256\n"
"v_mfma_f32_16x16x32_bf16 v[64:67], v[96:99], a[192:195], v[64:67]\n"
"buffer_load_dwordx4 v246, s[52:55], s66 offen lds\n"
"v_mfma_f32_16x16x32_bf16 v[64:67], v[100:103], a[200:203], v[64:67]\n"
"s_waitcnt lgkmcnt(4)\n"
"v_mfma_f32_16x16x32_bf16 v[32:35], v[72:75], a[188:191], v[32:35]\n"
"s_add_u32 s78, s82, 24576\n"
"ds_read_b64_tr_b16 v[88:89], v188 offset:18464\n"
"ds_read_b64_tr_b16 v[90:91], v188 offset:23072\n"
"v_mfma_f32_16x16x32_bf16 v[32:35], v[76:79], a[196:199], v[32:35]\n"
"s_nop 0\n"
"ds_read_b64_tr_b16 v[92:93], v188 offset:27680\n"
"ds_read_b64_tr_b16 v[94:95], v188 offset:32288\n"
"v_mfma_f32_16x16x32_bf16 v[68:71], v[72:75], a[192:195], v[68:71]\n"
"buffer_load_dwordx4 a[60:63], v186, s[60:63], s78 offen offset:0 sc1\n"
"v_mfma_f32_16x16x32_bf16 v[68:71], v[76:79], a[200:203], v[68:71]\n"
"s_waitcnt vmcnt(26) lgkmcnt(4)\n"
"v_mfma_f32_16x16x32_bf16 v[0:3], v[80:83], a[204:207], v[0:3]\n"
"s_add_u32 s79, s83, 24576\n"
"ds_read_b64_tr_b16 v[96:97], v188 offset:18496\n"
"ds_read_b64_tr_b16 v[98:99], v188 offset:23104\n"
"v_mfma_f32_16x16x32_bf16 v[0:3], v[84:87], a[212:215], v[0:3]\n"
"s_nop 0\n"
"ds_read_b64_tr_b16 v[100:101], v188 offset:27712\n"
"ds_read_b64_tr_b16 v[102:103], v188 offset:32320\n"
"v_mfma_f32_16x16x32_bf16 v[36:39], v[80:83], a[208:211], v[36:39]\n"
"v_mfma_f32_16x16x32_bf16 v[36:39], v[84:87], a[216:219], v[36:39]\n"
"s_waitcnt lgkmcnt(4)\n"
"v_mfma_f32_16x16x32_bf16 v[4:7], v[88:91], a[204:207], v[4:7]\n"
"buffer_load_dwordx4 a[64:67], v186, s[60:63], s79 offen offset:0 sc1\n"
"v_mfma_f32_16x16x32_bf16 v[4:7], v[92:95], a[212:215], v[4:7]\n"
"s_nop 0\n"
"ds_read_b64_tr_b16 v[72:73], v188 offset:18528\n"
"ds_read_b64_tr_b16 v[74:75], v188 offset:23136\n"
"v_mfma_f32_16x16x32_bf16 v[40:43], v[88:91], a[208:211], v[40:43]\n"
"s_nop 0\n"
"ds_read_b64_tr_b16 v[76:77], v188 offset:27744\n"
"ds_read_b64_tr_b16 v[78:79], v188 offset:32352\n"
"v_mfma_f32_16x16x32_bf16 v[40:43], v[92:95], a[216:219], v[40:43]\n"
"s_waitcnt lgkmcnt(4)\n"
"v_mfma_f32_16x16x32_bf16 v[8:11], v[96:99], a[204:207], v[8:11]\n"
"buffer_load_dwordx4 a[68:71], v186, s[60:63], s78 offen offset:1024 sc1\n"
"v_mfma_f32_16x16x32_bf16 v[8:11], v[100:103], a[212:215], v[8:11]\n"
"s_nop 0\n"
"ds_read_b64_tr_b16 v[80:81], v188 offset:18560\n"
"ds_read_b64_tr_b16 v[82:83], v188 offset:23168\n"
"v_mfma_f32_16x16x32_bf16 v[44:47], v[96:99], a[208:211], v[44:47]\n"
"s_nop 0\n"
"ds_read_b64_tr_b16 v[84:85], v188 offset:27776\n"
"ds_read_b64_tr_b16 v[86:87], v188 offset:32384\n"
"v_mfma_f32_16x16x32_bf16 v[44:47], v[100:103], a[216:219], v[44:47]\n"
"s_waitcnt lgkmcnt(4)\n"
"v_mfma_f32_16x16x32_bf16 v[12:15], v[72:75], a[204:207], v[12:15]\n"
"buffer_load_dwordx4 a[72:75], v186, s[60:63], s79 offen offset:1024 sc1\n"
"v_mfma_f32_16x16x32_bf16 v[12:15], v[76:79], a[212:215], v[12:15]\n"
"s_add_u32 m0, s71, 114688\n"
"ds_read_b64_tr_b16 v[88:89], v188 offset:18592\n"
"ds_read_b64_tr_b16 v[90:91], v188 offset:23200\n"
"v_mfma_f32_16x16x32_bf16 v[48:51], v[72:75], a[208:211], v[48:51]\n"
"s_nop 0\n"
"ds_read_b64_tr_b16 v[92:93], v188 offset:27808\n"
"ds_read_b64_tr_b16 v[94:95], v188 offset:32416\n"
"v_mfma_f32_16x16x32_bf16 v[48:51], v[76:79], a[216:219], v[48:51]\n"
"s_waitcnt lgkmcnt(4)\n"
"v_mfma_f32_16x16x32_bf16 v[16:19], v[80:83], a[204:207], v[16:19]\n"
"buffer_load_dwordx4 v247, s[52:55], s66 offen lds\n"
"v_mfma_f32_16x16x32_bf16 v[16:19], v[84:87], a[212:215], v[16:19]\n"
"s_add_u32 m0, s71, 118784\n"
"ds_read_b64_tr_b16 v[96:97], v188 offset:18624\n"
"ds_read_b64_tr_b16 v[98:99], v188 offset:23232\n"
"v_mfma_f32_16x16x32_bf16 v[52:55], v[80:83], a[208:211], v[52:55]\n"
"s_nop 0\n"
"ds_read_b64_tr_b16 v[100:101], v188 offset:27840\n"
"ds_read_b64_tr_b16 v[102:103], v188 offset:32448\n"
"v_mfma_f32_16x16x32_bf16 v[52:55], v[84:87], a[216:219], v[52:55]\n"
"s_waitcnt lgkmcnt(4)\n"
"v_mfma_f32_16x16x32_bf16 v[20:23], v[88:91], a[204:207], v[20:23]\n"
"s_nop 0\n"
"ds_read_b64_tr_b16 v[72:73], v188 offset:18656\n"
"ds_read_b64_tr_b16 v[74:75], v188 offset:23264\n"
"v_mfma_f32_16x16x32_bf16 v[20:23], v[92:95], a[212:215], v[20:23]\n"
"buffer_load_dwordx4 v248, s[52:55], s66 offen lds\n"
"v_mfma_f32_16x16x32_bf16 v[56:59], v[88:91], a[208:211], v[56:59]\n"
"s_add_u32 m0, s71, 122880\n"
"ds_read_b64_tr_b16 v[76:77], v188 offset:27872\n"
"ds_read_b64_tr_b16 v[78:79], v188 offset:32480\n"
"v_mfma_f32_16x16x32_bf16 v[56:59], v[92:95], a[216:219], v[56:59]\n"
"s_waitcnt lgkmcnt(4)\n"
"v_mfma_f32_16x16x32_bf16 v[24:27], v[96:99], a[204:207], v[24:27]\n"
"s_nop 0\n"
"ds_read_b64_tr_b16 v[80:81], v191 offset:18432\n"
"ds_read_b64_tr_b16 v[82:83], v191 offset:23040\n"
"v_mfma_f32_16x16x32_bf16 v[24:27], v[100:103], a[212:215], v[24:27]\n"
"buffer_load_dwordx4 v249, s[52:55], s66 offen lds\n"
"v_mfma_f32_16x16x32_bf16 v[60:63], v[96:99], a[208:211], v[60:63]\n"
"s_add_u32 m0, s71, 126976\n"
"ds_read_b64_tr_b16 v[84:85], v191 offset:27648\n"
"ds_read_b64_tr_b16 v[86:87], v191 offset:32256\n"
"v_mfma_f32_16x16x32_bf16 v[60:63], v[100:103], a[216:219], v[60:63]\n"
"s_waitcnt vmcnt(25) lgkmcnt(4)\n"
"s_barrier\n"
"v_mfma_f32_16x16x32_bf16 v[28:31], v[72:75], a[204:207], v[28:31]\n"
"s_nop 0\n"
"ds_read_b64_tr_b16 v[88:89], v188 offset:36864\n"
"ds_read_b64_tr_b16 v[90:91], v188 offset:41472\n"
"v_mfma_f32_16x16x32_bf16 v[28:31], v[76:79], a[212:215], v[28:31]\n"
"s_nop 0\n"
"ds_read_b64_tr_b16 v[92:93], v188 offset:46080\n"
"ds_read_b64_tr_b16 v[94:95], v188 offset:50688\n"
"v_mfma_f32_16x16x32_bf16 v[64:67], v[72:75], a[208:211], v[64:67]\n"
"buffer_load_dwordx4 v250, s[52:55], s66 offen lds\n"
"v_mfma_f32_16x16x32_bf16 v[64:67], v[76:79], a[216:219], v[64:67]\n"
"s_waitcnt lgkmcnt(4)\n"
"s_barrier\n"
"v_mfma_f32_16x16x32_bf16 v[32:35], v[80:83], a[204:207], v[32:35]\n"
"s_add_u32 m0, s71, 131072\n"
"ds_read_b64_tr_b16 v[96:97], v188 offset:36896\n"
"ds_read_b64_tr_b16 v[98:99], v188 offset:41504\n"
"v_mfma_f32_16x16x32_bf16 v[32:35], v[84:87], a[212:215], v[32:35]\n"
"s_nop 0\n"
"ds_read_b64_tr_b16 v[100:101], v188 offset:46112\n"
"ds_read_b64_tr_b16 v[102:103], v188 offset:50720\n"
"v_mfma_f32_16x16x32_bf16 v[68:71], v[80:83], a[208:211], v[68:71]\n"
"buffer_load_dwordx4 v251, s[52:55], s66 offen lds\n"
"v_mfma_f32_16x16x32_bf16 v[68:71], v[84:87], a[216:219], v[68:71]\n"
"s_waitcnt vmcnt(23) lgkmcnt(4)\n"
"v_mfma_f32_16x16x32_bf16 v[0:3], v[88:91], a[220:223], v[0:3]\n"
"s_add_u32 m0, s71, 135168\n"
"ds_read_b64_tr_b16 v[72:73], v188 offset:36928\n"
"ds_read_b64_tr_b16 v[74:75], v188 offset:41536\n"
"v_mfma_f32_16x16x32_bf16 v[0:3], v[92:95], a[228:231], v[0:3]\n"
"s_nop 0\n"
"ds_read_b64_tr_b16 v[76:77], v188 offset:46144\n"
"ds_read_b64_tr_b16 v[78:79], v188 offset:50752\n"
"v_mfma_f32_16x16x32_bf16 v[36:39], v[88:91], a[224:227], v[36:39]\n"
"v_mfma_f32_16x16x32_bf16 v[36:39], v[92:95], a[232:235], v[36:39]\n"
"s_waitcnt lgkmcnt(4)\n"
"v_mfma_f32_16x16x32_bf16 v[4:7], v[96:99], a[220:223], v[4:7]\n"
"buffer_load_dwordx4 v252, s[52:55], s66 offen lds\n"
"v_mfma_f32_16x16x32_bf16 v[4:7], v[100:103], a[228:231], v[4:7]\n"
"s_add_u32 m0, s71, 139264\n"
"ds_read_b64_tr_b16 v[80:81], v188 offset:36960\n"
"ds_read_b64_tr_b16 v[82:83], v188 offset:41568\n"
"v_mfma_f32_16x16x32_bf16 v[40:43], v[96:99], a[224:227], v[40:43]\n"
"s_nop 0\n"
"ds_read_b64_tr_b16 v[84:85], v188 offset:46176\n"
"ds_read_b64_tr_b16 v[86:87], v188 offset:50784\n"
"v_mfma_f32_16x16x32_bf16 v[40:43], v[100:103], a[232:235], v[40:43]\n"
"s_waitcnt lgkmcnt(4)\n"
"v_mfma_f32_16x16x32_bf16 v[8:11], v[72:75], a[220:223], v[8:11]\n"
"buffer_load_dwordx4 v253, s[52:55], s66 offen lds\n"
"v_mfma_f32_16x16x32_bf16 v[8:11], v[76:79], a[228:231], v[8:11]\n"
"s_add_u32 m0, s71, 143360\n"
"ds_read_b64_tr_b16 v[88:89], v188 offset:36992\n"
"ds_read_b64_tr_b16 v[90:91], v188 offset:41600\n"
"v_mfma_f32_16x16x32_bf16 v[44:47], v[72:75], a[224:227], v[44:47]\n"
"s_nop 0\n"
"ds_read_b64_tr_b16 v[92:93], v188 offset:46208\n"
"ds_read_b64_tr_b16 v[94:95], v188 offset:50816\n"
"v_mfma_f32_16x16x32_bf16 v[44:47], v[76:79], a[232:235], v[44:47]\n"
"s_waitcnt lgkmcnt(4)\n"
"v_mfma_f32_16x16x32_bf16 v[12:15], v[80:83], a[220:223], v[12:15]\n"
"buffer_load_dwordx4 v254, s[52:55], s66 offen lds\n"
"v_mfma_f32_16x16x32_bf16 v[12:15], v[84:87], a[228:231], v[12:15]\n"
"s_nop 0\n"
"ds_read_b64_tr_b16 v[96:97], v188 offset:37024\n"
"ds_read_b64_tr_b16 v[98:99], v188 offset:41632\n"
"v_mfma_f32_16x16x32_bf16 v[48:51], v[80:83], a[224:227], v[48:51]\n"
"s_nop 0\n"
"ds_read_b64_tr_b16 v[100:101], v188 offset:46240\n"
"ds_read_b64_tr_b16 v[102:103], v188 offset:50848\n"
"v_mfma_f32_16x16x32_bf16 v[48:51], v[84:87], a[232:235], v[48:51]\n"
"s_waitcnt lgkmcnt(4)\n"
"v_mfma_f32_16x16x32_bf16 v[16:19], v[88:91], a[220:223], v[16:19]\n"
"buffer_load_dwordx4 a[76:79], v186, s[60:63], s78 offen offset:2048 sc1\n"
"v_mfma_f32_16x16x32_bf16 v[16:19], v[92:95], a[228:231], v[16:19]\n"
"s_nop 0\n"
"ds_read_b64_tr_b16 v[72:73], v188 offset:37056\n"
"ds_read_b64_tr_b16 v[74:75], v188 offset:41664\n"
"v_mfma_f32_16x16x32_bf16 v[52:55], v[88:91], a[224:227], v[52:55]\n"
"s_nop 0\n"
"ds_read_b64_tr_b16 v[76:77], v188 offset:46272\n"
"ds_read_b64_tr_b16 v[78:79], v188 offset:50880\n"
"v_mfma_f32_16x16x32_bf16 v[52:55], v[92:95], a[232:235], v[52:55]\n"
"s_waitcnt lgkmcnt(4)\n"
"v_mfma_f32_16x16x32_bf16 v[20:23], v[96:99], a[220:223], v[20:23]\n"
"s_nop 0\n"
"ds_read_b64_tr_b16 v[80:81], v188 offset:37088\n"
"ds_read_b64_tr_b16 v[82:83], v188 offset:41696\n"
"v_mfma_f32_16x16x32_bf16 v[20:23], v[100:103], a[228:231], v[20:23]\n"
"buffer_load_dwordx4 a[80:83], v186, s[60:63], s79 offen offset:2048 sc1\n"
"v_mfma_f32_16x16x32_bf16 v[56:59], v[96:99], a[224:227], v[56:59]\n"
"s_nop 0\n"
"ds_read_b64_tr_b16 v[84:85], v188 offset:46304\n"
"ds_read_b64_tr_b16 v[86:87], v188 offset:50912\n"
"v_mfma_f32_16x16x32_bf16 v[56:59], v[100:103], a[232:235], v[56:59]\n"
"s_waitcnt lgkmcnt(4)\n"
"v_mfma_f32_16x16x32_bf16 v[24:27], v[72:75], a[220:223], v[24:27]\n"
"s_nop 0\n"
"ds_read_b64_tr_b16 v[88:89], v191 offset:36864\n"
"ds_read_b64_tr_b16 v[90:91], v191 offset:41472\n"
"v_mfma_f32_16x16x32_bf16 v[24:27], v[76:79], a[228:231], v[24:27]\n"
"buffer_load_dwordx4 a[84:87], v186, s[60:63], s78 offen offset:3072 sc1\n"
"v_mfma_f32_16x16x32_bf16 v[60:63], v[72:75], a[224:227], v[60:63]\n"
"s_nop 0\n"
"ds_read_b64_tr_b16 v[92:93], v191 offset:46080\n"
"ds_read_b64_tr_b16 v[94:95], v191 offset:50688\n"
"v_mfma_f32_16x16x32_bf16 v[60:63], v[76:79], a[232:235], v[60:63]\n"
"s_waitcnt lgkmcnt(4)\n"
"v_mfma_f32_16x16x32_bf16 v[28:31], v[80:83], a[220:223], v[28:31]\n"
"s_nop 0\n"
"ds_read_b64_tr_b16 v[96:97], v189 offset:0\n"
"ds_read_b64_tr_b16 v[98:99], v189 offset:4608\n"
"v_mfma_f32_16x16x32_bf16 v[28:31], v[84:87], a[228:231], v[28:31]\n"
"s_nop 0\n"
"ds_read_b64_tr_b16 v[100:101], v189 offset:9216\n"
"ds_read_b64_tr_b16 v[102:103], v189 offset:13824\n"
"v_mfma_f32_16x16x32_bf16 v[64:67], v[80:83], a[224:227], v[64:67]\n"
"buffer_load_dwordx4 a[88:91], v186, s[60:63], s79 offen offset:3072 sc1\n"
"v_mfma_f32_16x16x32_bf16 v[64:67], v[84:87], a[232:235], v[64:67]\n"
"s_waitcnt lgkmcnt(4)\n"
"v_mfma_f32_16x16x32_bf16 v[32:35], v[88:91], a[220:223], v[32:35]\n"
"s_add_u32 s78, s82, 28672\n"
"ds_read_b64_tr_b16 v[72:73], v189 offset:32\n"
"ds_read_b64_tr_b16 v[74:75], v189 offset:4640\n"
"v_mfma_f32_16x16x32_bf16 v[32:35], v[92:95], a[228:231], v[32:35]\n"
"L_tc1_2:\n"
"s_cbranch_execz L_tb1_2\n"
"ds_read_b64_tr_b16 v[76:77], v189 offset:9248\n"
"ds_read_b64_tr_b16 v[78:79], v189 offset:13856\n"
"v_mfma_f32_16x16x32_bf16 v[68:71], v[88:91], a[224:227], v[68:71]\n"
"buffer_load_dwordx4 a[92:95], v186, s[60:63], s78 offen offset:0 sc1\n"
"v_mfma_f32_16x16x32_bf16 v[68:71], v[92:95], a[232:235], v[68:71]\n"
"s_waitcnt vmcnt(25) lgkmcnt(4)\n"
"v_mfma_f32_16x16x32_bf16 v[0:3], v[96:99], a[236:239], v[0:3]\n"
"s_add_u32 s79, s83, 28672\n"
"ds_read_b64_tr_b16 v[80:81], v189 offset:64\n"
"ds_read_b64_tr_b16 v[82:83], v189 offset:4672\n"
"v_mfma_f32_16x16x32_bf16 v[0:3], v[100:103], a[244:247], v[0:3]\n"
"s_nop 0\n"
"ds_read_b64_tr_b16 v[84:85], v189 offset:9280\n"
"ds_read_b64_tr_b16 v[86:87], v189 offset:13888\n"
"v_mfma_f32_16x16x32_bf16 v[36:39], v[96:99], a[240:243], v[36:39]\n"
"v_mfma_f32_16x16x32_bf16 v[36:39], v[100:103], a[248:251], v[36:39]\n"
"s_waitcnt lgkmcnt(4)\n"
"v_mfma_f32_16x16x32_bf16 v[4:7], v[72:75], a[236:239], v[4:7]\n"
"buffer_load_dwordx4 a[96:99], v186, s[60:63], s79 offen offset:0 sc1\n"
"v_mfma_f32_16x16x32_bf16 v[4:7], v[76:79], a[244:247], v[4:7]\n"
"s_nop 0\n"
"ds_read_b64_tr_b16 v[88:89], v189 offset:96\n"
"ds_read_b64_tr_b16 v[90:91], v189 offset:4704\n"
"v_mfma_f32_16x16x32_bf16 v[40:43], v[72:75], a[240:243], v[40:43]\n"
"s_nop 0\n"
"ds_read_b64_tr_b16 v[92:93], v189 offset:9312\n"
"ds_read_b64_tr_b16 v[94:95], v189 offset:13920\n"
"v_mfma_f32_16x16x32_bf16 v[40:43], v[76:79], a[248:251], v[40:43]\n"
"s_waitcnt lgkmcnt(4)\n"
"v_mfma_f32_16x16x32_bf16 v[8:11], v[80:83], a[236:239], v[8:11]\n"
"buffer_load_dwordx4 a[100:103], v186, s[60:63], s78 offen offset:1024 sc1\n"
"v_mfma_f32_16x16x32_bf16 v[8:11], v[84:87], a[244:247], v[8:11]\n"
"s_nop 0\n"
"ds_read_b64_tr_b16 v[96:97], v189 offset:128\n"
"ds_read_b64_tr_b16 v[98:99], v189 offset:4736\n"
"v_mfma_f32_16x16x32_bf16 v[44:47], v[80:83], a[240:243], v[44:47]\n"
"s_nop 0\n"
"ds_read_b64_tr_b16 v[100:101], v189 offset:9344\n"
"ds_read_b64_tr_b16 v[102:103], v189 offset:13952\n"
"v_mfma_f32_16x16x32_bf16 v[44:47], v[84:87], a[248:251], v[44:47]\n"
"s_waitcnt lgkmcnt(4)\n"
"v_mfma_f32_16x16x32_bf16 v[12:15], v[88:91], a[236:239], v[12:15]\n"
"buffer_load_dwordx4 a[104:107], v186, s[60:63], s79 offen offset:1024 sc1\n"
"v_mfma_f32_16x16x32_bf16 v[12:15], v[92:95], a[244:247], v[12:15]\n"
"s_nop 0\n"
"ds_read_b64_tr_b16 v[72:73], v189 offset:160\n"
"ds_read_b64_tr_b16 v[74:75], v189 offset:4768\n"
"v_mfma_f32_16x16x32_bf16 v[48:51], v[88:91], a[240:243], v[48:51]\n"
"s_nop 0\n"
"ds_read_b64_tr_b16 v[76:77], v189 offset:9376\n"
"ds_read_b64_tr_b16 v[78:79], v189 offset:13984\n"
"v_mfma_f32_16x16x32_bf16 v[48:51], v[92:95], a[248:251], v[48:51]\n"
"s_waitcnt lgkmcnt(4)\n"
"v_mfma_f32_16x16x32_bf16 v[16:19], v[96:99], a[236:239], v[16:19]\n"
"buffer_load_dwordx4 a[108:111], v186, s[60:63], s78 offen offset:2048 sc1\n"
"v_mfma_f32_16x16x32_bf16 v[16:19], v[100:103], a[244:247], v[16:19]\n"
"s_nop 0\n"
"ds_read_b64_tr_b16 v[80:81], v189 offset:192\n"
"ds_read_b64_tr_b16 v[82:83], v189 offset:4800\n"
"v_mfma_f32_16x16x32_bf16 v[52:55], v[96:99], a[240:243], v[52:55]\n"
"s_nop 0\n"
"ds_read_b64_tr_b16 v[84:85], v189 offset:9408\n"
"ds_read_b64_tr_b16 v[86:87], v189 offset:14016\n"
"v_mfma_f32_16x16x32_bf16 v[52:55], v[100:103], a[248:251], v[52:55]\n"
"s_waitcnt lgkmcnt(4)\n"
"v_mfma_f32_16x16x32_bf16 v[20:23], v[72:75], a[236:239], v[20:23]\n"
"s_nop 0\n"
"ds_read_b64_tr_b16 v[88:89], v189 offset:224\n"
"ds_read_b64_tr_b16 v[90:91], v189 offset:4832\n"
"v_mfma_f32_16x16x32_bf16 v[20:23], v[76:79], a[244:247], v[20:23]\n"
"buffer_load_dwordx4 a[112:115], v186, s[60:63], s79 offen offset:2048 sc1\n"
"v_mfma_f32_16x16x32_bf16 v[56:59], v[72:75], a[240:243], v[56:59]\n"
"s_nop 0\n"
"ds_read_b64_tr_b16 v[92:93], v189 offset:9440\n"
"ds_read_b64_tr_b16 v[94:95], v189 offset:14048\n"
"v_mfma_f32_16x16x32_bf16 v[56:59], v[76:79], a[248:251], v[56:59]\n"
"s_waitcnt lgkmcnt(4)\n"
"v_mfma_f32_16x16x32_bf16 v[24:27], v[80:83], a[236:239], v[24:27]\n"
"s_nop 0\n"
"ds_read_b64_tr_b16 v[96:97], v192 offset:0\n"
"ds_read_b64_tr_b16 v[98:99], v192 offset:4608\n"
"v_mfma_f32_16x16x32_bf16 v[24:27], v[84:87], a[244:247], v[24:27]\n"
"buffer_load_dwordx4 a[116:119], v186, s[60:63], s78 offen offset:3072 sc1\n"
"v_mfma_f32_16x16x32_bf16 v[60:63], v[80:83], a[240:243], v[60:63]\n"
"s_nop 0\n"
"ds_read_b64_tr_b16 v[100:101], v192 offset:9216\n"
"ds_read_b64_tr_b16 v[102:103], v192 offset:13824\n"
"v_mfma_f32_16x16x32_bf16 v[60:63], v[84:87], a[248:251], v[60:63]\n"
"s_waitcnt vmcnt(24) lgkmcnt(4)\n"
"s_barrier\n"
"v_mfma_f32_16x16x32_bf16 v[28:31], v[88:91], a[236:239], v[28:31]\n"
"s_nop 0\n"
"ds_read_b64_tr_b16 v[72:73], v189 offset:18432\n"
"ds_read_b64_tr_b16 v[74:75], v189 offset:23040\n"
"v_mfma_f32_16x16x32_bf16 v[28:31], v[92:95], a[244:247], v[28:31]\n"
"s_nop 0\n"
"ds_read_b64_tr_b16 v[76:77], v189 offset:27648\n"
"ds_read_b64_tr_b16 v[78:79], v189 offset:32256\n"
"v_mfma_f32_16x16x32_bf16 v[64:67], v[88:91], a[240:243], v[64:67]\n"
"buffer_load_dwordx4 a[120:123], v186, s[60:63], s79 offen offset:3072 sc1\n"
"v_mfma_f32_16x16x32_bf16 v[64:67], v[92:95], a[248:251], v[64:67]\n"
"s_waitcnt lgkmcnt(4)\n"
"v_mfma_f32_16x16x32_bf16 v[32:35], v[96:99], a[236:239], v[32:35]\n"
"s_add_u32 m0, s71, 0\n"
"ds_read_b64_tr_b16 v[80:81], v189 offset:18464\n"
"ds_read_b64_tr_b16 v[82:83], v189 offset:23072\n"
"v_mfma_f32_16x16x32_bf16 v[32:35], v[100:103], a[244:247], v[32:35]\n"
"s_mov_b32 s77, 0\n"
"ds_read_b64_tr_b16 v[84:85], v189 offset:27680\n"
"ds_read_b64_tr_b16 v[86:87], v189 offset:32288\n"
"v_mfma_f32_16x16x32_bf16 v[68:71], v[96:99], a[240:243], v[68:71]\n"
"buffer_load_dwordx4 v177, s[48:51], s77 offen lds\n"
"v_mfma_f32_16x16x32_bf16 v[68:71], v[100:103], a[248:251], v[68:71]\n"
"s_waitcnt vmcnt(21) lgkmcnt(4)\n"
"v_mfma_f32_16x16x32_bf16 v[0:3], v[72:75], a[60:63], v[0:3]\n"
"s_add_u32 m0, s71, 4096\n"
"ds_read_b64_tr_b16 v[88:89], v189 offset:18496\n"
"ds_read_b64_tr_b16 v[90:91], v189 offset:23104\n"
"v_mfma_f32_16x16x32_bf16 v[0:3], v[76:79], a[68:71], v[0:3]\n"
"s_nop 0\n"
"ds_read_b64_tr_b16 v[92:93], v189 offset:27712\n"
"ds_read_b64_tr_b16 v[94:95], v189 offset:32320\n"
"v_mfma_f32_16x16x32_bf16 v[36:39], v[72:75], a[64:67], v[36:39]\n"
"v_mfma_f32_16x16x32_bf16 v[36:39], v[76:79], a[72:75], v[36:39]\n"
"s_waitcnt lgkmcnt(4)\n"
"v_mfma_f32_16x16x32_bf16 v[4:7], v[80:83], a[60:63], v[4:7]\n"
"buffer_load_dwordx4 v178, s[48:51], s77 offen lds\n"
"v_mfma_f32_16x16x32_bf16 v[4:7], v[84:87], a[68:71], v[4:7]\n"
"s_add_u32 m0, s71, 8192\n"
"ds_read_b64_tr_b16 v[96:97], v189 offset:18528\n"
"ds_read_b64_tr_b16 v[98:99], v189 offset:23136\n"
"v_mfma_f32_16x16x32_bf16 v[40:43], v[80:83], a[64:67], v[40:43]\n"
"s_nop 0\n"
"ds_read_b64_tr_b16 v[100:101], v189 offset:27744\n"
"ds_read_b64_tr_b16 v[102:103], v189 offset:32352\n"
"v_mfma_f32_16x16x32_bf16 v[40:43], v[84:87], a[72:75], v[40:43]\n"
"s_waitcnt lgkmcnt(4)\n"
"v_mfma_f32_16x16x32_bf16 v[8:11], v[88:91], a[60:63], v[8:11]\n"
"buffer_load_dwordx4 v179, s[48:51], s77 offen lds\n"
"v_mfma_f32_16x16x32_bf16 v[8:11], v[92:95], a[68:71], v[8:11]\n"
"s_add_u32 m0, s71, 12288\n"
"ds_read_b64_tr_b16 v[72:73], v189 offset:18560\n"
"ds_read_b64_tr_b16 v[74:75], v189 offset:23168\n"
"v_mfma_f32_16x16x32_bf16 v[44:47], v[88:91], a[64:67], v[44:47]\n"
"s_nop 0\n"
"ds_read_b64_tr_b16 v[76:77], v189 offset:27776\n"
"ds_read_b64_tr_b16 v[78:79], v189 offset:32384\n"
"v_mfma_f32_16x16x32_bf16 v[44:47], v[92:95], a[72:75], v[44:47]\n"
"s_waitcnt lgkmcnt(4)\n"
"v_mfma_f32_16x16x32_bf16 v[12:15], v[96:99], a[60:63], v[12:15]\n"
"buffer_load_dwordx4 v180, s[48:51], s77 offen lds\n"
"v_mfma_f32_16x16x32_bf16 v[12:15], v[100:103], a[68:71], v[12:15]\n"
"s_add_u32 m0, s71, 16384\n"
"ds_read_b64_tr_b16 v[80:81], v189 offset:18592\n"
"ds_read_b64_tr_b16 v[82:83], v189 offset:23200\n"
"v_mfma_f32_16x16x32_bf16 v[48:51], v[96:99], a[64:67], v[48:51]\n"
"s_nop 0\n"
"ds_read_b64_tr_b16 v[84:85], v189 offset:27808\n"
"ds_read_b64_tr_b16 v[86:87], v189 offset:32416\n"
"v_mfma_f32_16x16x32_bf16 v[48:51], v[100:103], a[72:75], v[48:51]\n"
"s_waitcnt lgkmcnt(4)\n"
"v_mfma_f32_16x16x32_bf16 v[16:19], v[72:75], a[60:63], v[16:19]\n"
"buffer_load_dwordx4 v181, s[48:51], s77 offen lds\n"
"v_mfma_f32_16x16x32_bf16 v[16:19], v[76:79], a[68:71], v[16:19]\n"
"s_add_u32 m0, s71, 20480\n"
"ds_read_b64_tr_b16 v[88:89], v189 offset:18624\n"
"ds_read_b64_tr_b16 v[90:91], v189 offset:23232\n"
"v_mfma_f32_16x16x32_bf16 v[52:55], v[72:75], a[64:67], v[52:55]\n"
"s_nop 0\n"
"ds_read_b64_tr_b16 v[92:93], v189 offset:27840\n"
"ds_read_b64_tr_b16 v[94:95], v189 offset:32448\n"
"v_mfma_f32_16x16x32_bf16 v[52:55], v[76:79], a[72:75], v[52:55]\n"
"s_waitcnt lgkmcnt(4)\n"
"v_mfma_f32_16x16x32_bf16 v[20:23], v[80:83], a[60:63], v[20:23]\n"
"s_nop 0\n"
"ds_read_b64_tr_b16 v[96:97], v189 offset:18656\n"
"ds_read_b64_tr_b16 v[98:99], v189 offset:23264\n"
"v_mfma_f32_16x16x32_bf16 v[20:23], v[84:87], a[68:71], v[20:23]\n"
"buffer_load_dwordx4 v182, s[48:51], s77 offen lds\n"
"v_mfma_f32_16x16x32_bf16 v[56:59], v[80:83], a[64:67], v[56:59]\n"
"s_add_u32 m0, s71, 24576\n"
"ds_read_b64_tr_b16 v[100:101], v189 offset:27872\n"
"ds_read_b64_tr_b16 v[102:103], v189 offset:32480\n"
"v_mfma_f32_16x16x32_bf16 v[56:59], v[84:87], a[72:75], v[56:59]\n"
"s_waitcnt lgkmcnt(4)\n"
"v_mfma_f32_16x16x32_bf16 v[24:27], v[88:91], a[60:63], v[24:27]\n"
"s_nop 0\n"
"ds_read_b64_tr_b16 v[72:73], v192 offset:18432\n"
"ds_read_b64_tr_b16 v[74:75], v192 offset:23040\n"
"v_mfma_f32_16x16x32_bf16 v[24:27], v[92:95], a[68:71], v[24:27]\n"
"buffer_load_dwordx4 v183, s[48:51], s77 offen lds\n"
"v_mfma_f32_16x16x32_bf16 v[60:63], v[88:91], a[64:67], v[60:63]\n"
"s_add_u32 m0, s71, 28672\n"
"ds_read_b64_tr_b16 v[76:77], v192 offset:27648\n"
"ds_read_b64_tr_b16 v[78:79], v192 offset:32256\n"
"v_mfma_f32_16x16x32_bf16 v[60:63], v[92:95], a[72:75], v[60:63]\n"
"s_waitcnt lgkmcnt(4)\n"
"v_mfma_f32_16x16x32_bf16 v[28:31], v[96:99], a[60:63], v[28:31]\n"
"s_nop 0\n"
"ds_read_b64_tr_b16 v[80:81], v189 offset:36864\n"
"ds_read_b64_tr_b16 v[82:83], v189 offset:41472\n"
"v_mfma_f32_16x16x32_bf16 v[28:31], v[100:103], a[68:71], v[28:31]\n"
"s_nop 0\n"
"ds_read_b64_tr_b16 v[84:85], v189 offset:46080\n"
"ds_read_b64_tr_b16 v[86:87], v189 offset:50688\n"
"v_mfma_f32_16x16x32_bf16 v[64:67], v[96:99], a[64:67], v[64:67]\n"
"buffer_load_dwordx4 v184, s[48:51], s77 offen lds\n"
"v_mfma_f32_16x16x32_bf16 v[64:67], v[100:103], a[72:75], v[64:67]\n"
"s_waitcnt vmcnt(20) lgkmcnt(4)\n"
"s_barrier\n"
"v_mfma_f32_16x16x32_bf16 v[32:35], v[72:75], a[60:63], v[32:35]\n"
"s_add_u32 m0, s71, 32768\n"
"ds_read_b64_tr_b16 v[88:89], v190 offset:0\n"
"ds_read_b64_tr_b16 v[90:91], v190 offset:4608\n"
"v_mfma_f32_16x16x32_bf16 v[32:35], v[76:79], a[68:71], v[32:35]\n"
"s_nop 0\n"
"ds_read_b64_tr_b16 v[92:93], v190 offset:9216\n"
"ds_read_b64_tr_b16 v[94:95], v190 offset:13824\n"
"v_mfma_f32_16x16x32_bf16 v[68:71], v[72:75], a[64:67], v[68:71]\n"
"buffer_load_dwordx4 v185, s[48:51], s77 offen lds\n"
"v_mfma_f32_16x16x32_bf16 v[68:71], v[76:79], a[72:75], v[68:71]\n"
"s_waitcnt vmcnt(17) lgkmcnt(4)\n"
"v_mfma_f32_16x16x32_bf16 v[0:3], v[80:83], a[76:79], v[0:3]\n"
"s_nop 0\n"
"ds_read_b64_tr_b16 v[96:97], v190 offset:18432\n"
"ds_read_b64_tr_b16 v[98:99], v190 offset:23040\n"
"v_mfma_f32_16x16x32_bf16 v[0:3], v[84:87], a[84:87], v[0:3]\n"
"s_nop 0\n"
"ds_read_b64_tr_b16 v[100:101], v190 offset:27648\n"
"ds_read_b64_tr_b16 v[102:103], v190 offset:32256\n"
"v_mfma_f32_16x16x32_bf16 v[36:39], v[80:83], a[80:83], v[36:39]\n"
"v_mfma_f32_16x16x32_bf16 v[36:39], v[84:87], a[88:91], v[36:39]\n"
"s_waitcnt vmcnt(13) lgkmcnt(4)\n"
"v_mfma_f32_16x16x32_bf16 v[0:3], v[88:91], a[92:95], v[0:3]\n"
"s_nop 0\n"
"ds_read_b64_tr_b16 v[72:73], v189 offset:36896\n"
"ds_read_b64_tr_b16 v[74:75], v189 offset:41504\n"
"v_mfma_f32_16x16x32_bf16 v[0:3], v[92:95], a[100:103], v[0:3]\n"
"s_nop 0\n"
"ds_read_b64_tr_b16 v[76:77], v189 offset:46112\n"
"ds_read_b64_tr_b16 v[78:79], v189 offset:50720\n"
"v_mfma_f32_16x16x32_bf16 v[36:39], v[88:91], a[96:99], v[36:39]\n"
"v_mfma_f32_16x16x32_bf16 v[36:39], v[92:95], a[104:107], v[36:39]\n"
"s_waitcnt vmcnt(9) lgkmcnt(4)\n"
"v_mfma_f32_16x16x32_bf16 v[0:3], v[96:99], a[108:111], v[0:3]\n"
"s_nop 0\n"
"ds_read_b64_tr_b16 v[80:81], v190 offset:32\n"
"ds_read_b64_tr_b16 v[82:83], v190 offset:4640\n"
"v_mfma_f32_16x16x32_bf16 v[0:3], v[100:103], a[116:119], v[0:3]\n"
"s_nop 0\n"
"ds_read_b64_tr_b16 v[84:85], v190 offset:9248\n"
"ds_read_b64_tr_b16 v[86:87], v190 offset:13856\n"
"v_mfma_f32_16x16x32_bf16 v[36:39], v[96:99], a[112:115], v[36:39]\n"
"v_mfma_f32_16x16x32_bf16 v[36:39], v[100:103], a[120:123], v[36:39]\n"
"s_waitcnt lgkmcnt(4)\n"
"v_mfma_f32_16x16x32_bf16 v[4:7], v[72:75], a[76:79], v[4:7]\n"
"s_nop 0\n"
"ds_read_b64_tr_b16 v[88:89], v190 offset:18464\n"
"ds_read_b64_tr_b16 v[90:91], v190 offset:23072\n"
"v_mfma_f32_16x16x32_bf16 v[4:7], v[76:79], a[84:87], v[4:7]\n"
"s_nop 0\n"
"ds_read_b64_tr_b16 v[92:93], v190 offset:27680\n"
"ds_read_b64_tr_b16 v[94:95], v190 offset:32288\n"
"v_mfma_f32_16x16x32_bf16 v[40:43], v[72:75], a[80:83], v[40:43]\n"
"v_mfma_f32_16x16x32_bf16 v[40:43], v[76:79], a[88:91], v[40:43]\n"
"s_waitcnt lgkmcnt(4)\n"
"v_mfma_f32_16x16x32_bf16 v[4:7], v[80:83], a[92:95], v[4:7]\n"
"s_nop 0\n"
"ds_read_b64_tr_b16 v[96:97], v189 offset:36928\n"
"ds_read_b64_tr_b16 v[98:99], v189 offset:41536\n"
"v_mfma_f32_16x16x32_bf16 v[4:7], v[84:87], a[100:103], v[4:7]\n"
"s_nop 0\n"
"ds_read_b64_tr_b16 v[100:101], v189 offset:46144\n"
"ds_read_b64_tr_b16 v[102:103], v189 offset:50752\n"
"v_mfma_f32_16x16x32_bf16 v[40:43], v[80:83], a[96:99], v[40:43]\n"
"v_cvt_pk_bf16_f32 v0, v0, v1\n"
"v_cvt_pk_bf16_f32 v1, v2, v3\n"
"v_mfma_f32_16x16x32_bf16 v[40:43], v[84:87], a[104:107], v[40:43]\n"
"s_waitcnt lgkmcnt(4)\n"
"v_cvt_pk_bf16_f32 v2, v36, v37\n"
"v_cvt_pk_bf16_f32 v3, v38, v39\n"
"v_mfma_f32_16x16x32_bf16 v[4:7], v[88:91], a[108:111], v[4:7]\n"
"s_add_u32 s67, s89, 33554432\n"
"ds_read_b64_tr_b16 v[72:73], v190 offset:64\n"
"ds_read_b64_tr_b16 v[74:75], v190 offset:4672\n"
"v_mfma_f32_16x16x32_bf16 v[4:7], v[92:95], a[116:119], v[4:7]\n"
"buffer_store_dwordx4 v[0:3], v196, s[60:63], s67 offen offset:0 sc1\n"
"v_mfma_f32_16x16x32_bf16 v[40:43], v[88:91], a[112:115], v[40:43]\n"
"s_nop 0\n"
"ds_read_b64_tr_b16 v[76:77], v190 offset:9280\n"
"ds_read_b64_tr_b16 v[78:79], v190 offset:13888\n"
"v_mfma_f32_16x16x32_bf16 v[40:43], v[92:95], a[120:123], v[40:43]\n"
"s_waitcnt lgkmcnt(4)\n"
"v_mfma_f32_16x16x32_bf16 v[8:11], v[96:99], a[76:79], v[8:11]\n"
"s_nop 0\n"
"ds_read_b64_tr_b16 v[80:81], v190 offset:18496\n"
"ds_read_b64_tr_b16 v[82:83], v190 offset:23104\n"
"v_mfma_f32_16x16x32_bf16 v[8:11], v[100:103], a[84:87], v[8:11]\n"
"s_nop 0\n"
"ds_read_b64_tr_b16 v[84:85], v190 offset:27712\n"
"ds_read_b64_tr_b16 v[86:87], v190 offset:32320\n"
"v_mfma_f32_16x16x32_bf16 v[44:47], v[96:99], a[80:83], v[44:47]\n"
"v_mfma_f32_16x16x32_bf16 v[44:47], v[100:103], a[88:91], v[44:47]\n"
"s_waitcnt lgkmcnt(4)\n"
"v_mfma_f32_16x16x32_bf16 v[8:11], v[72:75], a[92:95], v[8:11]\n"
"s_nop 0\n"
"ds_read_b64_tr_b16 v[88:89], v189 offset:36960\n"
"ds_read_b64_tr_b16 v[90:91], v189 offset:41568\n"
"v_mfma_f32_16x16x32_bf16 v[8:11], v[76:79], a[100:103], v[8:11]\n"
"s_nop 0\n"
"ds_read_b64_tr_b16 v[92:93], v189 offset:46176\n"
"ds_read_b64_tr_b16 v[94:95], v189 offset:50784\n"
"v_mfma_f32_16x16x32_bf16 v[44:47], v[72:75], a[96:99], v[44:47]\n"
"v_cvt_pk_bf16_f32 v4, v4, v5\n"
"v_cvt_pk_bf16_f32 v5, v6, v7\n"
"v_mfma_f32_16x16x32_bf16 v[44:47], v[76:79], a[104:107], v[44:47]\n"
"s_waitcnt lgkmcnt(4)\n"
"v_cvt_pk_bf16_f32 v6, v40, v41\n"
"v_cvt_pk_bf16_f32 v7, v42, v43\n"
"v_mfma_f32_16x16x32_bf16 v[8:11], v[80:83], a[108:111], v[8:11]\n"
"buffer_store_dwordx4 v[4:7], v196, s[60:63], s67 offen offset:256 sc1\n"
"v_mfma_f32_16x16x32_bf16 v[8:11], v[84:87], a[116:119], v[8:11]\n"
"s_nop 0\n"
"ds_read_b64_tr_b16 v[96:97], v190 offset:96\n"
"ds_read_b64_tr_b16 v[98:99], v190 offset:4704\n"
"v_mfma_f32_16x16x32_bf16 v[44:47], v[80:83], a[112:115], v[44:47]\n"
"s_nop 0\n"
"ds_read_b64_tr_b16 v[100:101], v190 offset:9312\n"
"ds_read_b64_tr_b16 v[102:103], v190 offset:13920\n"
"v_mfma_f32_16x16x32_bf16 v[44:47], v[84:87], a[120:123], v[44:47]\n"
"s_waitcnt lgkmcnt(4)\n"
"v_mfma_f32_16x16x32_bf16 v[12:15], v[88:91], a[76:79], v[12:15]\n"
"s_nop 0\n"
"ds_read_b64_tr_b16 v[72:73], v190 offset:18528\n"
"ds_read_b64_tr_b16 v[74:75], v190 offset:23136\n"
"v_mfma_f32_16x16x32_bf16 v[12:15], v[92:95], a[84:87], v[12:15]\n"
"s_nop 0\n"
"ds_read_b64_tr_b16 v[76:77], v190 offset:27744\n"
"ds_read_b64_tr_b16 v[78:79], v190 offset:32352\n"
"v_mfma_f32_16x16x32_bf16 v[48:51], v[88:91], a[80:83], v[48:51]\n"
"v_mfma_f32_16x16x32_bf16 v[48:51], v[92:95], a[88:91], v[48:51]\n"
"s_waitcnt lgkmcnt(4)\n"
"v_mfma_f32_16x16x32_bf16 v[12:15], v[96:99], a[92:95], v[12:15]\n"
"s_nop 0\n"
"ds_read_b64_tr_b16 v[80:81], v189 offset:36992\n"
"ds_read_b64_tr_b16 v[82:83], v189 offset:41600\n"
"v_mfma_f32_16x16x32_bf16 v[12:15], v[100:103], a[100:103], v[12:15]\n"
"s_nop 0\n"
"ds_read_b64_tr_b16 v[84:85], v189 offset:46208\n"
"ds_read_b64_tr_b16 v[86:87], v189 offset:50816\n"
"v_mfma_f32_16x16x32_bf16 v[48:51], v[96:99], a[96:99], v[48:51]\n"
"v_cvt_pk_bf16_f32 v8, v8, v9\n"
"v_cvt_pk_bf16_f32 v9, v10, v11\n"
"v_mfma_f32_16x16x32_bf16 v[48:51], v[100:103], a[104:107], v[48:51]\n"
"s_waitcnt lgkmcnt(4)\n"
"v_cvt_pk_bf16_f32 v10, v44, v45\n"
"v_cvt_pk_bf16_f32 v11, v46, v47\n"
"v_mfma_f32_16x16x32_bf16 v[12:15], v[72:75], a[108:111], v[12:15]\n"
"buffer_store_dwordx4 v[8:11], v196, s[60:63], s67 offen offset:512 sc1\n"
"v_mfma_f32_16x16x32_bf16 v[12:15], v[76:79], a[116:119], v[12:15]\n"
"s_nop 0\n"
"ds_read_b64_tr_b16 v[88:89], v190 offset:128\n"
"ds_read_b64_tr_b16 v[90:91], v190 offset:4736\n"
"v_mfma_f32_16x16x32_bf16 v[48:51], v[72:75], a[112:115], v[48:51]\n"
"s_nop 0\n"
"ds_read_b64_tr_b16 v[92:93], v190 offset:9344\n"
"ds_read_b64_tr_b16 v[94:95], v190 offset:13952\n"
"v_mfma_f32_16x16x32_bf16 v[48:51], v[76:79], a[120:123], v[48:51]\n"
"s_waitcnt lgkmcnt(4)\n"
"v_mfma_f32_16x16x32_bf16 v[16:19], v[80:83], a[76:79], v[16:19]\n"
"s_nop 0\n"
"ds_read_b64_tr_b16 v[96:97], v190 offset:18560\n"
"ds_read_b64_tr_b16 v[98:99], v190 offset:23168\n"
"v_mfma_f32_16x16x32_bf16 v[16:19], v[84:87], a[84:87], v[16:19]\n"
"s_nop 0\n"
"ds_read_b64_tr_b16 v[100:101], v190 offset:27776\n"
"ds_read_b64_tr_b16 v[102:103], v190 offset:32384\n"
"v_mfma_f32_16x16x32_bf16 v[52:55], v[80:83], a[80:83], v[52:55]\n"
"v_mfma_f32_16x16x32_bf16 v[52:55], v[84:87], a[88:91], v[52:55]\n"
"s_waitcnt lgkmcnt(4)\n"
"v_mfma_f32_16x16x32_bf16 v[16:19], v[88:91], a[92:95], v[16:19]\n"
"s_nop 0\n"
"ds_read_b64_tr_b16 v[72:73], v189 offset:37024\n"
"ds_read_b64_tr_b16 v[74:75], v189 offset:41632\n"
"v_mfma_f32_16x16x32_bf16 v[16:19], v[92:95], a[100:103], v[16:19]\n"
"s_nop 0\n"
"ds_read_b64_tr_b16 v[76:77], v189 offset:46240\n"
"ds_read_b64_tr_b16 v[78:79], v189 offset:50848\n"
"v_mfma_f32_16x16x32_bf16 v[52:55], v[88:91], a[96:99], v[52:55]\n"
"v_cvt_pk_bf16_f32 v12, v12, v13\n"
"v_cvt_pk_bf16_f32 v13, v14, v15\n"
"v_mfma_f32_16x16x32_bf16 v[52:55], v[92:95], a[104:107], v[52:55]\n"
"s_waitcnt lgkmcnt(4)\n"
"v_cvt_pk_bf16_f32 v14, v48, v49\n"
"v_cvt_pk_bf16_f32 v15, v50, v51\n"
"v_mfma_f32_16x16x32_bf16 v[16:19], v[96:99], a[108:111], v[16:19]\n"
"buffer_store_dwordx4 v[12:15], v196, s[60:63], s67 offen offset:768 sc1\n"
"v_mfma_f32_16x16x32_bf16 v[16:19], v[100:103], a[116:119], v[16:19]\n"
"s_nop 0\n"
"ds_read_b64_tr_b16 v[80:81], v190 offset:160\n"
"ds_read_b64_tr_b16 v[82:83], v190 offset:4768\n"
"v_mfma_f32_16x16x32_bf16 v[52:55], v[96:99], a[112:115], v[52:55]\n"
"s_nop 0\n"
"ds_read_b64_tr_b16 v[84:85], v190 offset:9376\n"
"ds_read_b64_tr_b16 v[86:87], v190 offset:13984\n"
"v_mfma_f32_16x16x32_bf16 v[52:55], v[100:103], a[120:123], v[52:55]\n"
"s_waitcnt lgkmcnt(4)\n"
"v_mfma_f32_16x16x32_bf16 v[20:23], v[72:75], a[76:79], v[20:23]\n"
"s_nop 0\n"
"ds_read_b64_tr_b16 v[88:89], v190 offset:18592\n"
"ds_read_b64_tr_b16 v[90:91], v190 offset:23200\n"
"v_mfma_f32_16x16x32_bf16 v[20:23], v[76:79], a[84:87], v[20:23]\n"
"s_nop 0\n"
"ds_read_b64_tr_b16 v[92:93], v190 offset:27808\n"
"ds_read_b64_tr_b16 v[94:95], v190 offset:32416\n"
"v_mfma_f32_16x16x32_bf16 v[56:59], v[72:75], a[80:83], v[56:59]\n"
"v_mfma_f32_16x16x32_bf16 v[56:59], v[76:79], a[88:91], v[56:59]\n"
"s_waitcnt lgkmcnt(4)\n"
"v_mfma_f32_16x16x32_bf16 v[20:23], v[80:83], a[92:95], v[20:23]\n"
"s_nop 0\n"
"ds_read_b64_tr_b16 v[96:97], v189 offset:37056\n"
"ds_read_b64_tr_b16 v[98:99], v189 offset:41664\n"
"v_mfma_f32_16x16x32_bf16 v[20:23], v[84:87], a[100:103], v[20:23]\n"
"s_nop 0\n"
"ds_read_b64_tr_b16 v[100:101], v189 offset:46272\n"
"ds_read_b64_tr_b16 v[102:103], v189 offset:50880\n"
"v_mfma_f32_16x16x32_bf16 v[56:59], v[80:83], a[96:99], v[56:59]\n"
"v_cvt_pk_bf16_f32 v16, v16, v17\n"
"v_cvt_pk_bf16_f32 v17, v18, v19\n"
"v_mfma_f32_16x16x32_bf16 v[56:59], v[84:87], a[104:107], v[56:59]\n"
"s_waitcnt lgkmcnt(4)\n"
"v_cvt_pk_bf16_f32 v18, v52, v53\n"
"v_cvt_pk_bf16_f32 v19, v54, v55\n"
"v_mfma_f32_16x16x32_bf16 v[20:23], v[88:91], a[108:111], v[20:23]\n"
"buffer_store_dwordx4 v[16:19], v196, s[60:63], s67 offen offset:1024 sc1\n"
"v_mfma_f32_16x16x32_bf16 v[20:23], v[92:95], a[116:119], v[20:23]\n"
"s_nop 0\n"
"ds_read_b64_tr_b16 v[72:73], v190 offset:192\n"
"ds_read_b64_tr_b16 v[74:75], v190 offset:4800\n"
"v_mfma_f32_16x16x32_bf16 v[56:59], v[88:91], a[112:115], v[56:59]\n"
"s_nop 0\n"
"ds_read_b64_tr_b16 v[76:77], v190 offset:9408\n"
"ds_read_b64_tr_b16 v[78:79], v190 offset:14016\n"
"v_mfma_f32_16x16x32_bf16 v[56:59], v[92:95], a[120:123], v[56:59]\n"
"s_waitcnt lgkmcnt(4)\n"
"v_mfma_f32_16x16x32_bf16 v[24:27], v[96:99], a[76:79], v[24:27]\n"
"s_nop 0\n"
"ds_read_b64_tr_b16 v[80:81], v190 offset:18624\n"
"ds_read_b64_tr_b16 v[82:83], v190 offset:23232\n"
"v_mfma_f32_16x16x32_bf16 v[24:27], v[100:103], a[84:87], v[24:27]\n"
"s_nop 0\n"
"ds_read_b64_tr_b16 v[84:85], v190 offset:27840\n"
"ds_read_b64_tr_b16 v[86:87], v190 offset:32448\n"
"v_mfma_f32_16x16x32_bf16 v[60:63], v[96:99], a[80:83], v[60:63]\n"
"v_mfma_f32_16x16x32_bf16 v[60:63], v[100:103], a[88:91], v[60:63]\n"
"s_waitcnt lgkmcnt(4)\n"
"v_mfma_f32_16x16x32_bf16 v[24:27], v[72:75], a[92:95], v[24:27]\n"
"s_nop 0\n"
"ds_read_b64_tr_b16 v[88:89], v189 offset:37088\n"
"ds_read_b64_tr_b16 v[90:91], v189 offset:41696\n"
"v_mfma_f32_16x16x32_bf16 v[24:27], v[76:79], a[100:103], v[24:27]\n"
"s_nop 0\n"
"ds_read_b64_tr_b16 v[92:93], v189 offset:46304\n"
"ds_read_b64_tr_b16 v[94:95], v189 offset:50912\n"
"v_mfma_f32_16x16x32_bf16 v[60:63], v[72:75], a[96:99], v[60:63]\n"
"v_cvt_pk_bf16_f32 v20, v20, v21\n"
"v_cvt_pk_bf16_f32 v21, v22, v23\n"
"v_mfma_f32_16x16x32_bf16 v[60:63], v[76:79], a[104:107], v[60:63]\n"
"s_waitcnt lgkmcnt(4)\n"
"v_cvt_pk_bf16_f32 v22, v56, v57\n"
"v_cvt_pk_bf16_f32 v23, v58, v59\n"
"v_mfma_f32_16x16x32_bf16 v[24:27], v[80:83], a[108:111], v[24:27]\n"
"buffer_store_dwordx4 v[20:23], v196, s[60:63], s67 offen offset:1280 sc1\n"
"v_mfma_f32_16x16x32_bf16 v[24:27], v[84:87], a[116:119], v[24:27]\n"
"s_nop 0\n"
"ds_read_b64_tr_b16 v[96:97], v190 offset:224\n"
"ds_read_b64_tr_b16 v[98:99], v190 offset:4832\n"
"v_mfma_f32_16x16x32_bf16 v[60:63], v[80:83], a[112:115], v[60:63]\n"
"s_nop 0\n"
"ds_read_b64_tr_b16 v[100:101], v190 offset:9440\n"
"ds_read_b64_tr_b16 v[102:103], v190 offset:14048\n"
"v_mfma_f32_16x16x32_bf16 v[60:63], v[84:87], a[120:123], v[60:63]\n"
"s_waitcnt lgkmcnt(4)\n"
"v_mfma_f32_16x16x32_bf16 v[28:31], v[88:91], a[76:79], v[28:31]\n"
"s_nop 0\n"
"ds_read_b64_tr_b16 v[72:73], v190 offset:18656\n"
"ds_read_b64_tr_b16 v[74:75], v190 offset:23264\n"
"v_mfma_f32_16x16x32_bf16 v[28:31], v[92:95], a[84:87], v[28:31]\n"
"s_nop 0\n"
"ds_read_b64_tr_b16 v[76:77], v190 offset:27872\n"
"ds_read_b64_tr_b16 v[78:79], v190 offset:32480\n"
"v_mfma_f32_16x16x32_bf16 v[64:67], v[88:91], a[80:83], v[64:67]\n"
"v_mfma_f32_16x16x32_bf16 v[64:67], v[92:95], a[88:91], v[64:67]\n"
"s_waitcnt lgkmcnt(4)\n"
"v_mfma_f32_16x16x32_bf16 v[28:31], v[96:99], a[92:95], v[28:31]\n"
"s_nop 0\n"
"ds_read_b64_tr_b16 v[80:81], v192 offset:36864\n"
"ds_read_b64_tr_b16 v[82:83], v192 offset:41472\n"
"v_mfma_f32_16x16x32_bf16 v[28:31], v[100:103], a[100:103], v[28:31]\n"
"s_nop 0\n"
"ds_read_b64_tr_b16 v[84:85], v192 offset:46080\n"
"ds_read_b64_tr_b16 v[86:87], v192 offset:50688\n"
"v_mfma_f32_16x16x32_bf16 v[64:67], v[96:99], a[96:99], v[64:67]\n"
"v_cvt_pk_bf16_f32 v24, v24, v25\n"
"v_cvt_pk_bf16_f32 v25, v26, v27\n"
"v_mfma_f32_16x16x32_bf16 v[64:67], v[100:103], a[104:107], v[64:67]\n"
"s_waitcnt lgkmcnt(4)\n"
"v_cvt_pk_bf16_f32 v26, v60, v61\n"
"v_cvt_pk_bf16_f32 v27, v62, v63\n"
"v_mfma_f32_16x16x32_bf16 v[28:31], v[72:75], a[108:111], v[28:31]\n"
"buffer_store_dwordx4 v[24:27], v196, s[60:63], s67 offen offset:1536 sc1\n"
"v_mfma_f32_16x16x32_bf16 v[28:31], v[76:79], a[116:119], v[28:31]\n"
"s_nop 0\n"
"ds_read_b64_tr_b16 v[88:89], v193 offset:0\n"
"ds_read_b64_tr_b16 v[90:91], v193 offset:4608\n"
"v_mfma_f32_16x16x32_bf16 v[64:67], v[72:75], a[112:115], v[64:67]\n"
"s_nop 0\n"
"ds_read_b64_tr_b16 v[92:93], v193 offset:9216\n"
"ds_read_b64_tr_b16 v[94:95], v193 offset:13824\n"
"v_mfma_f32_16x16x32_bf16 v[64:67], v[76:79], a[120:123], v[64:67]\n"
"s_waitcnt lgkmcnt(4)\n"
"v_mfma_f32_16x16x32_bf16 v[32:35], v[80:83], a[76:79], v[32:35]\n"
"s_nop 0\n"
"ds_read_b64_tr_b16 v[96:97], v193 offset:18432\n"
"ds_read_b64_tr_b16 v[98:99], v193 offset:23040\n"
"v_mfma_f32_16x16x32_bf16 v[32:35], v[84:87], a[84:87], v[32:35]\n"
"s_nop 0\n"
"ds_read_b64_tr_b16 v[100:101], v193 offset:27648\n"
"ds_read_b64_tr_b16 v[102:103], v193 offset:32256\n"
"v_mfma_f32_16x16x32_bf16 v[68:71], v[80:83], a[80:83], v[68:71]\n"
"v_mfma_f32_16x16x32_bf16 v[68:71], v[84:87], a[88:91], v[68:71]\n"
"s_waitcnt lgkmcnt(4)\n"
"v_mfma_f32_16x16x32_bf16 v[32:35], v[88:91], a[92:95], v[32:35]\n"
"v_cvt_pk_bf16_f32 v28, v28, v29\n"
"v_cvt_pk_bf16_f32 v29, v30, v31\n"
"v_mfma_f32_16x16x32_bf16 v[32:35], v[92:95], a[100:103], v[32:35]\n"
"v_cvt_pk_bf16_f32 v30, v64, v65\n"
"v_cvt_pk_bf16_f32 v31, v66, v67\n"
"v_mfma_f32_16x16x32_bf16 v[68:71], v[88:91], a[96:99], v[68:71]\n"
"buffer_store_dwordx4 v[28:31], v196, s[60:63], s67 offen offset:1792 sc1\n"
"v_mfma_f32_16x16x32_bf16 v[68:71], v[92:95], a[104:107], v[68:71]\n"
"s_waitcnt lgkmcnt(0)\n"
"v_mfma_f32_16x16x32_bf16 v[32:35], v[96:99], a[108:111], v[32:35]\n"
"v_mfma_f32_16x16x32_bf16 v[32:35], v[100:103], a[116:119], v[32:35]\n"
"v_mfma_f32_16x16x32_bf16 v[68:71], v[96:99], a[112:115], v[68:71]\n"
"v_mfma_f32_16x16x32_bf16 v[68:71], v[100:103], a[120:123], v[68:71]\n"
"s_nop 7\n"
"v_cvt_pk_bf16_f32 v32, v32, v33\n"
"v_cvt_pk_bf16_f32 v33, v34, v35\n"
"v_cvt_pk_bf16_f32 v34, v68, v69\n"
"v_cvt_pk_bf16_f32 v35, v70, v71\n"
"buffer_store_dwordx4 v[32:35], v196, s[60:63], s67 offen offset:2048 sc1\n"
"s_add_u32 m0, s71, 36864\n"
"s_mov_b32 s77, 1179648\n"
"s_nop 0\n"
"buffer_load_dwordx4 v177, s[48:51], s77 offen lds\n"
"s_add_u32 m0, s71, 40960\n"
"s_nop 0\n"
"buffer_load_dwordx4 v178, s[48:51], s77 offen lds\n"
"s_add_u32 m0, s71, 45056\n"
"s_nop 0\n"
"buffer_load_dwordx4 v179, s[48:51], s77 offen lds\n"
"s_add_u32 m0, s71, 49152\n"
"s_nop 0\n"
"buffer_load_dwordx4 v180, s[48:51], s77 offen lds\n"
"s_add_u32 m0, s71, 53248\n"
"s_nop 0\n"
"buffer_load_dwordx4 v181, s[48:51], s77 offen lds\n"
"s_add_u32 m0, s71, 57344\n"
"s_nop 0\n"
"buffer_load_dwordx4 v182, s[48:51], s77 offen lds\n"
"s_add_u32 m0, s71, 61440\n"
"L_tc1_3:\n"
"s_cbranch_execz L_tb1_3\n"
"buffer_load_dwordx4 v183, s[48:51], s77 offen lds\n"
"s_add_u32 m0, s71, 65536\n"
"s_nop 0\n"
"buffer_load_dwordx4 v184, s[48:51], s77 offen lds\n"
"s_add_u32 m0, s71, 69632\n"
"s_nop 0\n"
"buffer_load_dwordx4 v185, s[48:51], s77 offen lds\n"
"s_lshl2_add_u32 s74, s94, s68\n"
"s_cmp_eq_u32 s68, 3\n"
"s_cbranch_scc0 L_ts2_0\n"
"s_mov_b64 exec, 0\n"
"s_branch L_tc2_0\n"
"L_tb2_0:\n"
"s_mov_b64 exec, -1\n"
"L_ts2_0:\n"
"s_cmp_eq_u32 s68, 2\n"
"s_cbranch_scc0 L_ts2_1\n"
"s_mov_b64 exec, 0\n"
"s_branch L_tc2_1\n"
"L_tb2_1:\n"
"s_mov_b64 exec, -1\n"
"L_ts2_1:\n"
"s_cmp_eq_u32 s68, 1\n"
"s_cbranch_scc0 L_ts2_2\n"
"s_mov_b64 exec, 0\n"
"s_branch L_tc2_2\n"
"L_tb2_2:\n"
"s_mov_b64 exec, -1\n"
"L_ts2_2:\n"
"s_cmp_eq_u32 s68, 0\n"
"s_cbranch_scc0 L_ts2_3\n"
"s_mov_b64 exec, 0\n"
"s_branch L_tc2_3\n"
"L_tb2_3:\n"
"s_mov_b64 exec, -1\n"
"L_ts2_3:\n"
"s_lshl_b32 s72, s74, 8\n"
"s_add_u32 s72, s72, 393216\n"
"v_mov_b32 v200, 1\n"
"s_waitcnt vmcnt(9)\n"
"s_mov_b64 exec, 1\n"
"buffer_atomic_add v200, off, s[60:63], s72\n"
"s_mov_b64 exec, -1\n"
"s_barrier\n"
"v_lshrrev_b32 v203, 2, v199\n"
"v_mul_u32_u24 v203, 288, v203\n"
"v_and_b32 v204, 3, v199\n"
"v_lshl_add_u32 v203, v204, 3, v203\n"
"v_add_u32 v203, s69, v203\n"
"s_lshl_b32 s72, s70, 5\n"
"v_add_u32 v188, s72, v203\n"
"s_lshl_b32 s72, s70, 8\n"
"s_sub_u32 s72, 256, s72\n"
"v_add_u32 v191, s72, v203\n"
"s_add_u32 s82, s87, 0\n"
"s_add_u32 s83, s87, 18432\n"
"s_add_u32 s84, s87, 36864\n"
"s_add_u32 s85, s87, 55296\n"
"v_add_u32 v189, 55296, v188\n"
"v_add_u32 v192, 55296, v191\n"
"v_add_u32 v190, 110592, v188\n"
"v_add_u32 v193, 110592, v191\n"
"v_accvgpr_read_b32 v48, a0\n"
"v_accvgpr_read_b32 v49, a1\n"
"v_accvgpr_read_b32 v50, a2\n"
"v_accvgpr_read_b32 v51, a3\n"
"v_accvgpr_read_b32 v52, a4\n"
"v_accvgpr_read_b32 v53, a5\n"
"v_accvgpr_read_b32 v54, a6\n"
"v_accvgpr_read_b32 v55, a7\n"
"v_accvgpr_read_b32 v56, a8\n"
"v_accvgpr_read_b32 v57, a9\n"
"v_accvgpr_read_b32 v58, a10\n"
"v_accvgpr_read_b32 v59, a11\n"
"s_add_u32 s74, s69, 150016\n"
"v_add_u32 v203, s74, v243\n"
"ds_read_b128 v[60:63], v203 offset:0\n"
"ds_read_b128 v[64:67], v203 offset:1024\n"
"ds_read_b128 v[68:71], v203 offset:2048\n"
"ds_read_b128 v[84:87], v203 offset:2304\n"
"ds_read_b128 v[88:91], v203 offset:3328\n"
"ds_read_b128 v[92:95], v203 offset:4352\n"
"ds_read_b128 v[72:75], v203 offset:4608\n"
"ds_read_b128 v[76:79], v203 offset:5632\n"
"ds_read_b128 v[80:83], v203 offset:6656\n"
"s_waitcnt lgkmcnt(0)\n"
"s_mov_b32 s72, 0x3fb8aa3b\n"
"v_add_f32 v72, 1.0, v72\n"
"v_add_f32 v73, 1.0, v73\n"
"v_add_f32 v74, 1.0, v74\n"
"v_add_f32 v75, 1.0, v75\n"
"v_mul_f32 v72, s72, v72\n"
"v_mul_f32 v73, s72, v73\n"
"v_mul_f32 v74, s72, v74\n"
"v_mul_f32 v75, s72, v75\n"
"v_mul_f32 v84, s72, v84\n"
"v_mul_f32 v85, s72, v85\n"
"v_mul_f32 v86, s72, v86\n"
"v_mul_f32 v87, s72, v87\n"
"v_add_f32 v76, 1.0, v76\n"
"v_add_f32 v77, 1.0, v77\n"
"v_add_f32 v78, 1.0, v78\n"
"v_add_f32 v79, 1.0, v79\n"
"v_mul_f32 v76, s72, v76\n"
"v_mul_f32 v77, s72, v77\n"
"v_mul_f32 v78, s72, v78\n"
"v_mul_f32 v79, s72, v79\n"
"v_mul_f32 v88, s72, v88\n"
"v_mul_f32 v89, s72, v89\n"
"v_mul_f32 v90, s72, v90\n"
"v_mul_f32 v91, s72, v91\n"
"v_add_f32 v80, 1.0, v80\n"
"v_add_f32 v81, 1.0, v81\n"
"v_add_f32 v82, 1.0, v82\n"
"v_add_f32 v83, 1.0, v83\n"
"v_mul_f32 v80, s72, v80\n"
"v_mul_f32 v81, s72, v81\n"
"v_mul_f32 v82, s72, v82\n"
"v_mul_f32 v83, s72, v83\n"
"v_mul_f32 v92, s72, v92\n"
"v_mul_f32 v93, s72, v93\n"
"v_mul_f32 v94, s72, v94\n"
"v_mul_f32 v95, s72, v95\n"
"v_mov_b32 v142, 0x3f80\n"
"v_mov_b32 v143, 0x3f800000\n"
"v_mov_b32 v112, 0\n"
"v_mov_b32 v113, 0\n"
"v_mov_b32 v114, 0\n"
"v_mov_b32 v115, 0\n"
"v_mov_b32 v116, 0\n"
"v_mov_b32 v117, 0\n"
"v_mov_b32 v118, 0\n"
"v_mov_b32 v119, 0\n"
"v_and_b32 v141, 3, v199\n"
"v_lshlrev_b32 v141, 4, v141\n"
"s_add_u32 s74, s69, 147456\n"
"v_add_u32 v141, s74, v141\n"
"s_lshl_b32 s74, s68, 2\n"
"v_add_u32 v140, s74, v141\n"
"v_lshrrev_b32 v60, 16, v60\n"
"v_lshrrev_b32 v62, 16, v62\n"
"v_lshrrev_b32 v64, 16, v64\n"
"v_lshrrev_b32 v66, 16, v66\n"
"v_lshrrev_b32 v68, 16, v68\n"
"v_lshrrev_b32 v70, 16, v70\n"
"v_lshlrev_b32 v96, 16, v48\n"
"v_and_b32 v97, 0xffff0000, v48\n"
"v_lshlrev_b32 v98, 16, v49\n"
"v_and_b32 v99, 0xffff0000, v49\n"
"v_lshlrev_b32 v100, 16, v50\n"
"v_and_b32 v101, 0xffff0000, v50\n"
"v_lshlrev_b32 v102, 16, v51\n"
"v_and_b32 v103, 0xffff0000, v51\n"
"v_lshlrev_b32 v104, 16, v52\n"
"v_and_b32 v105, 0xffff0000, v52\n"
"v_lshlrev_b32 v106, 16, v53\n"
"v_and_b32 v107, 0xffff0000, v53\n"
"v_lshlrev_b32 v108, 16, v54\n"
"v_and_b32 v109, 0xffff0000, v54\n"
"v_lshlrev_b32 v110, 16, v55\n"
"v_and_b32 v111, 0xffff0000, v55\n"
"s_cmp_eq_u32 s68, 0\n"
"s_cbranch_scc0 L_po_w010\n"
"v_lshlrev_b32 v112, 16, v56\n"
"v_and_b32 v113, 0xffff0000, v56\n"
"v_lshlrev_b32 v114, 16, v57\n"
"v_and_b32 v115, 0xffff0000, v57\n"
"v_lshlrev_b32 v116, 16, v58\n"
"v_and_b32 v117, 0xffff0000, v58\n"
"v_lshlrev_b32 v118, 16, v59\n"
"v_and_b32 v119, 0xffff0000, v59\n"
"L_po_w010:\n"
"s_mov_b32 s75, 0x3c000000\n"
"s_cmp_eq_u32 s68, 0\n"
"s_cselect_b32 s75, 0x3baaaaab, s75\n"
"s_lshl2_add_u32 s74, s94, %11\n"
"s_cmp_eq_u32 s68, 0\n"
"s_cbranch_scc0 L_po_w011\n"
"s_lshl_b32 s72, s74, 8\n"
"s_add_u32 s72, s72, 393216\n"
"s_mov_b32 s97, 20000\n"
"L_po_poll12:\n"
"buffer_load_dword v208, off, s[60:63], s72 sc1\n"
"s_waitcnt vmcnt(0)\n"
"v_readfirstlane_b32 s73, v208\n"
"s_nop 1\n"
"s_cmp_ge_u32 s73, 16\n"
"s_cbranch_scc1 L_po_polld13\n"
"s_sleep 2\n"
"s_sub_u32 s97, s97, 1\n"
"s_cbranch_scc0 L_po_poll12\n"
"L_po_polld13:\n"
"L_po_w011:\n"
"s_barrier\n"
"s_lshl_b32 s72, s68, 10\n"
"s_add_u32 s72, s72, s90\n"
"s_add_u32 s72, s72, 33554432\n"
"s_add_u32 s73, s72, 0\n"
"buffer_load_dwordx4 v[0:3], v186, s[60:63], s73 offen sc1\n"
"s_add_u32 s73, s72, 9216\n"
"buffer_load_dwordx4 v[4:7], v186, s[60:63], s73 offen sc1\n"
"s_add_u32 s73, s72, 18432\n"
"buffer_load_dwordx4 v[8:11], v186, s[60:63], s73 offen sc1\n"
"s_add_u32 s73, s72, 27648\n"
"buffer_load_dwordx4 v[12:15], v186, s[60:63], s73 offen sc1\n"
"s_add_u32 s73, s72, 4096\n"
"buffer_load_dwordx4 v[16:19], v186, s[60:63], s73 offen sc1\n"
"s_add_u32 s73, s72, 13312\n"
"buffer_load_dwordx4 v[20:23], v186, s[60:63], s73 offen sc1\n"
"s_add_u32 s73, s72, 22528\n"
"buffer_load_dwordx4 v[24:27], v186, s[60:63], s73 offen sc1\n"
"s_add_u32 s73, s72, 31744\n"
"buffer_load_dwordx4 v[28:31], v186, s[60:63], s73 offen sc1\n"
"s_cmp_eq_u32 s68, 0\n"
"s_cbranch_scc0 L_po_w014\n"
"s_add_u32 s73, s72, 8192\n"
"buffer_load_dwordx4 v[32:35], v186, s[60:63], s73 offen sc1\n"
"s_add_u32 s73, s72, 17408\n"
"buffer_load_dwordx4 v[36:39], v186, s[60:63], s73 offen sc1\n"
"s_add_u32 s73, s72, 26624\n"
"buffer_load_dwordx4 v[40:43], v186, s[60:63], s73 offen sc1\n"
"s_add_u32 s73, s72, 35840\n"
"buffer_load_dwordx4 v[44:47], v186, s[60:63], s73 offen sc1\n"
"L_po_w014:\n"
"s_waitcnt vmcnt(4)\n"
"v_dot2_f32_bf16 v96, v0, v60, v96\n"
"v_dot2_f32_bf16 v97, v0, v61, v97\n"
"v_dot2_f32_bf16 v98, v1, v62, v98\n"
"v_dot2_f32_bf16 v99, v1, v63, v99\n"
"v_dot2_f32_bf16 v100, v2, v60, v100\n"
"v_dot2_f32_bf16 v101, v2, v61, v101\n"
"v_dot2_f32_bf16 v102, v3, v62, v102\n"
"v_dot2_f32_bf16 v103, v3, v63, v103\n"
"v_dot2_f32_bf16 v96, v4, v60, v96\n"
"v_dot2_f32_bf16 v97, v4, v61, v97\n"
"v_dot2_f32_bf16 v98, v5, v62, v98\n"
"v_dot2_f32_bf16 v99, v5, v63, v99\n"
"v_dot2_f32_bf16 v100, v6, v60, v100\n"
"v_dot2_f32_bf16 v101, v6, v61, v101\n"
"v_dot2_f32_bf16 v102, v7, v62, v102\n"
"v_dot2_f32_bf16 v103, v7, v63, v103\n"
"v_dot2_f32_bf16 v96, v8, v60, v96\n"
"v_dot2_f32_bf16 v97, v8, v61, v97\n"
"v_dot2_f32_bf16 v98, v9, v62, v98\n"
"v_dot2_f32_bf16 v99, v9, v63, v99\n"
"v_dot2_f32_bf16 v100, v10, v60, v100\n"
"v_dot2_f32_bf16 v101, v10, v61, v101\n"
"v_dot2_f32_bf16 v102, v11, v62, v102\n"
"v_dot2_f32_bf16 v103, v11, v63, v103\n"
"v_dot2_f32_bf16 v96, v12, v60, v96\n"
"v_dot2_f32_bf16 v97, v12, v61, v97\n"
"v_dot2_f32_bf16 v98, v13, v62, v98\n"
"v_dot2_f32_bf16 v99, v13, v63, v99\n"
"v_dot2_f32_bf16 v100, v14, v60, v100\n"
"v_dot2_f32_bf16 v101, v14, v61, v101\n"
"v_dot2_f32_bf16 v102, v15, v62, v102\n"
"v_dot2_f32_bf16 v103, v15, v63, v103\n"
"s_nop 2\n"
"v_cvt_pk_bf16_f32 v48, v96, v97\n"
"v_cvt_pk_bf16_f32 v49, v98, v99\n"
"v_cvt_pk_bf16_f32 v50, v100, v101\n"
"v_cvt_pk_bf16_f32 v51, v102, v103\n"
"v_lshlrev_b32 v96, 16, v48\n"
"v_and_b32 v97, 0xffff0000, v48\n"
"v_lshlrev_b32 v98, 16, v49\n"
"v_and_b32 v99, 0xffff0000, v49\n"
"v_lshlrev_b32 v100, 16, v50\n"
"v_and_b32 v101, 0xffff0000, v50\n"
"v_lshlrev_b32 v102, 16, v51\n"
"v_and_b32 v103, 0xffff0000, v51\n"
"s_waitcnt vmcnt(0)\n"
"v_dot2_f32_bf16 v104, v16, v64, v104\n"
"v_dot2_f32_bf16 v105, v16, v65, v105\n"
"v_dot2_f32_bf16 v106, v17, v66, v106\n"
"v_dot2_f32_bf16 v107, v17, v67, v107\n"
"v_dot2_f32_bf16 v108, v18, v64, v108\n"
"v_dot2_f32_bf16 v109, v18, v65, v109\n"
"v_dot2_f32_bf16 v110, v19, v66, v110\n"
"v_dot2_f32_bf16 v111, v19, v67, v111\n"
"v_dot2_f32_bf16 v104, v20, v64, v104\n"
"v_dot2_f32_bf16 v105, v20, v65, v105\n"
"v_dot2_f32_bf16 v106, v21, v66, v106\n"
"v_dot2_f32_bf16 v107, v21, v67, v107\n"
"v_dot2_f32_bf16 v108, v22, v64, v108\n"
"v_dot2_f32_bf16 v109, v22, v65, v109\n"
"v_dot2_f32_bf16 v110, v23, v66, v110\n"
"v_dot2_f32_bf16 v111, v23, v67, v111\n"
"v_dot2_f32_bf16 v104, v24, v64, v104\n"
"v_dot2_f32_bf16 v105, v24, v65, v105\n"
"v_dot2_f32_bf16 v106, v25, v66, v106\n"
"v_dot2_f32_bf16 v107, v25, v67, v107\n"
"v_dot2_f32_bf16 v108, v26, v64, v108\n"
"v_dot2_f32_bf16 v109, v26, v65, v109\n"
"v_dot2_f32_bf16 v110, v27, v66, v110\n"
"v_dot2_f32_bf16 v111, v27, v67, v111\n"
"v_dot2_f32_bf16 v104, v28, v64, v104\n"
"v_dot2_f32_bf16 v105, v28, v65, v105\n"
"v_dot2_f32_bf16 v106, v29, v66, v106\n"
"v_dot2_f32_bf16 v107, v29, v67, v107\n"
"v_dot2_f32_bf16 v108, v30, v64, v108\n"
"v_dot2_f32_bf16 v109, v30, v65, v109\n"
"v_dot2_f32_bf16 v110, v31, v66, v110\n"
"v_dot2_f32_bf16 v111, v31, v67, v111\n"
"s_nop 2\n"
"v_cvt_pk_bf16_f32 v52, v104, v105\n"
"v_cvt_pk_bf16_f32 v53, v106, v107\n"
"v_cvt_pk_bf16_f32 v54, v108, v109\n"
"v_cvt_pk_bf16_f32 v55, v110, v111\n"
"v_lshlrev_b32 v104, 16, v52\n"
"v_and_b32 v105, 0xffff0000, v52\n"
"v_lshlrev_b32 v106, 16, v53\n"
"v_and_b32 v107, 0xffff0000, v53\n"
"v_lshlrev_b32 v108, 16, v54\n"
"v_and_b32 v109, 0xffff0000, v54\n"
"v_lshlrev_b32 v110, 16, v55\n"
"v_and_b32 v111, 0xffff0000, v55\n"
"s_cmp_eq_u32 s68, 0\n"
"s_cbranch_scc0 L_po_w015\n"
"s_waitcnt vmcnt(0)\n"
"v_dot2_f32_bf16 v112, v32, v68, v112\n"
"v_dot2_f32_bf16 v113, v32, v69, v113\n"
"v_dot2_f32_bf16 v114, v33, v70, v114\n"
"v_dot2_f32_bf16 v115, v33, v71, v115\n"
"v_dot2_f32_bf16 v116, v34, v68, v116\n"
"v_dot2_f32_bf16 v117, v34, v69, v117\n"
"v_dot2_f32_bf16 v118, v35, v70, v118\n"
"v_dot2_f32_bf16 v119, v35, v71, v119\n"
"v_dot2_f32_bf16 v112, v36, v68, v112\n"
"v_dot2_f32_bf16 v113, v36, v69, v113\n"
"v_dot2_f32_bf16 v114, v37, v70, v114\n"
"v_dot2_f32_bf16 v115, v37, v71, v115\n"
"v_dot2_f32_bf16 v116, v38, v68, v116\n"
"v_dot2_f32_bf16 v117, v38, v69, v117\n"
"v_dot2_f32_bf16 v118, v39, v70, v118\n"
"v_dot2_f32_bf16 v119, v39, v71, v119\n"
"v_dot2_f32_bf16 v112, v40, v68, v112\n"
"v_dot2_f32_bf16 v113, v40, v69, v113\n"
"v_dot2_f32_bf16 v114, v41, v70, v114\n"
"v_dot2_f32_bf16 v115, v41, v71, v115\n"
"v_dot2_f32_bf16 v116, v42, v68, v116\n"
"v_dot2_f32_bf16 v117, v42, v69, v117\n"
"v_dot2_f32_bf16 v118, v43, v70, v118\n"
"v_dot2_f32_bf16 v119, v43, v71, v119\n"
"v_dot2_f32_bf16 v112, v44, v68, v112\n"
"v_dot2_f32_bf16 v113, v44, v69, v113\n"
"v_dot2_f32_bf16 v114, v45, v70, v114\n"
"v_dot2_f32_bf16 v115, v45, v71, v115\n"
"v_dot2_f32_bf16 v116, v46, v68, v116\n"
"v_dot2_f32_bf16 v117, v46, v69, v117\n"
"v_dot2_f32_bf16 v118, v47, v70, v118\n"
"v_dot2_f32_bf16 v119, v47, v71, v119\n"
"s_nop 2\n"
"v_cvt_pk_bf16_f32 v56, v112, v113\n"
"v_cvt_pk_bf16_f32 v57, v114, v115\n"
"v_cvt_pk_bf16_f32 v58, v116, v117\n"
"v_cvt_pk_bf16_f32 v59, v118, v119\n"
"v_lshlrev_b32 v112, 16, v56\n"
"v_and_b32 v113, 0xffff0000, v56\n"
"v_lshlrev_b32 v114, 16, v57\n"
"v_and_b32 v115, 0xffff0000, v57\n"
"v_lshlrev_b32 v116, 16, v58\n"
"v_and_b32 v117, 0xffff0000, v58\n"
"v_lshlrev_b32 v118, 16, v59\n"
"v_and_b32 v119, 0xffff0000, v59\n"
"L_po_w015:\n"
"v_pk_add_f32 v[124:125], v[96:97], v[98:99]\n"
"v_pk_add_f32 v[126:127], v[104:105], v[106:107]\n"
"v_pk_add_f32 v[124:125], v[124:125], v[126:127]\n"
"v_pk_add_f32 v[126:127], v[112:113], v[114:115]\n"
"v_pk_add_f32 v[124:125], v[124:125], v[126:127]\n"
"v_add_f32 v0, v124, v125\n"
"v_pk_add_f32 v[132:133], v[100:101], v[102:103]\n"
"v_pk_add_f32 v[134:135], v[108:109], v[110:111]\n"
"s_add_u32 m0, s71, 73728\n"
"s_mov_b32 s77, 2359296\n"
"v_pk_add_f32 v[132:133], v[132:133], v[134:135]\n"
"buffer_load_dwordx4 v177, s[48:51], s77 offen lds\n"
"v_pk_add_f32 v[134:135], v[116:117], v[118:119]\n"
"v_pk_add_f32 v[132:133], v[132:133], v[134:135]\n"
"v_add_f32 v2, v132, v133\n"
"s_nop 1\n"
"v_add_f32_dpp v0, v0, v0 row_ror:4 row_mask:0xf bank_mask:0xf\n"
"v_add_f32_dpp v2, v2, v2 row_ror:4 row_mask:0xf bank_mask:0xf\n"
"s_nop 1\n"
"v_add_f32_dpp v0, v0, v0 row_ror:8 row_mask:0xf bank_mask:0xf\n"
"v_add_f32_dpp v2, v2, v2 row_ror:8 row_mask:0xf bank_mask:0xf\n"
"v_mov_b32 v128, v0\n"
"v_mov_b32 v129, v2\n"
"s_nop 1\n"
"v_permlane16_swap_b32 v128, v0\n"
"v_permlane16_swap_b32 v129, v2\n"
"s_nop 1\n"
"v_add_f32 v0, v0, v128\n"
"v_add_f32 v2, v2, v129\n"
"v_mov_b32 v128, v0\n"
"v_mov_b32 v129, v2\n"
"s_nop 1\n"
"v_permlane32_swap_b32 v128, v0\n"
"v_permlane32_swap_b32 v129, v2\n"
"s_nop 1\n"
"v_add_f32 v0, v0, v128\n"
"v_add_f32 v2, v2, v129\n"
"s_add_u32 m0, s71, 77824\n"
"v_mul_f32 v4, s75, v0\n"
"buffer_load_dwordx4 v178, s[48:51], s77 offen lds\n"
"v_mul_f32 v6, s75, v2\n"
"v_pk_add_f32 v[96:97], v[96:97], v[4:5] op_sel_hi:[1,0] neg_lo:[0,1] neg_hi:[0,1]\n"
"v_pk_add_f32 v[98:99], v[98:99], v[4:5] op_sel_hi:[1,0] neg_lo:[0,1] neg_hi:[0,1]\n"
"v_pk_mul_f32 v[8:9], v[96:97], v[96:97]\n"
"v_pk_fma_f32 v[8:9], v[98:99], v[98:99], v[8:9]\n"
"v_pk_add_f32 v[100:101], v[100:101], v[6:7] op_sel_hi:[1,0] neg_lo:[0,1] neg_hi:[0,1]\n"
"v_pk_add_f32 v[102:103], v[102:103], v[6:7] op_sel_hi:[1,0] neg_lo:[0,1] neg_hi:[0,1]\n"
"s_add_u32 m0, s71, 81920\n"
"v_pk_mul_f32 v[10:11], v[100:101], v[100:101]\n"
"buffer_load_dwordx4 v179, s[48:51], s77 offen lds\n"
"v_pk_fma_f32 v[10:11], v[102:103], v[102:103], v[10:11]\n"
"v_pk_add_f32 v[104:105], v[104:105], v[4:5] op_sel_hi:[1,0] neg_lo:[0,1] neg_hi:[0,1]\n"
"v_pk_add_f32 v[106:107], v[106:107], v[4:5] op_sel_hi:[1,0] neg_lo:[0,1] neg_hi:[0,1]\n"
"v_pk_fma_f32 v[8:9], v[104:105], v[104:105], v[8:9]\n"
"v_pk_fma_f32 v[8:9], v[106:107], v[106:107], v[8:9]\n"
"v_pk_add_f32 v[108:109], v[108:109], v[6:7] op_sel_hi:[1,0] neg_lo:[0,1] neg_hi:[0,1]\n"
"v_pk_add_f32 v[110:111], v[110:111], v[6:7] op_sel_hi:[1,0] neg_lo:[0,1] neg_hi:[0,1]\n"
"s_add_u32 m0, s71, 86016\n"
"v_pk_fma_f32 v[10:11], v[108:109], v[108:109], v[10:11]\n"
"buffer_load_dwordx4 v180, s[48:51], s77 offen lds\n"
"v_pk_fma_f32 v[10:11], v[110:111], v[110:111], v[10:11]\n"
"s_cmp_eq_u32 s68, 0\n"
"s_cbranch_scc0 L_po_w016\n"
"v_pk_add_f32 v[112:113], v[112:113], v[4:5] op_sel_hi:[1,0] neg_lo:[0,1] neg_hi:[0,1]\n"
"v_pk_add_f32 v[114:115], v[114:115], v[4:5] op_sel_hi:[1,0] neg_lo:[0,1] neg_hi:[0,1]\n"
"v_pk_fma_f32 v[8:9], v[112:113], v[112:113], v[8:9]\n"
"v_pk_fma_f32 v[8:9], v[114:115], v[114:115], v[8:9]\n"
"v_pk_add_f32 v[116:117], v[116:117], v[6:7] op_sel_hi:[1,0] neg_lo:[0,1] neg_hi:[0,1]\n"
"v_pk_add_f32 v[118:119], v[118:119], v[6:7] op_sel_hi:[1,0] neg_lo:[0,1] neg_hi:[0,1]\n"
"v_pk_fma_f32 v[10:11], v[116:117], v[116:117], v[10:11]\n"
"v_pk_fma_f32 v[10:11], v[118:119], v[118:119], v[10:11]\n"
"L_po_w016:\n"
"v_add_f32 v1, v8, v9\n"
"v_add_f32 v3, v10, v11\n"
"s_nop 1\n"
"v_add_f32_dpp v1, v1, v1 row_ror:4 row_mask:0xf bank_mask:0xf\n"
"v_add_f32_dpp v3, v3, v3 row_ror:4 row_mask:0xf bank_mask:0xf\n"
"s_nop 1\n"
"v_add_f32_dpp v1, v1, v1 row_ror:8 row_mask:0xf bank_mask:0xf\n"
"v_add_f32_dpp v3, v3, v3 row_ror:8 row_mask:0xf bank_mask:0xf\n"
"v_mov_b32 v128, v1\n"
"v_mov_b32 v129, v3\n"
"s_nop 1\n"
"v_permlane16_swap_b32 v128, v1\n"
"v_permlane16_swap_b32 v129, v3\n"
"s_nop 1\n"
"v_add_f32 v1, v1, v128\n"
"v_add_f32 v3, v3, v129\n"
"v_mov_b32 v128, v1\n"
"v_mov_b32 v129, v3\n"
"s_nop 1\n"
"v_permlane32_swap_b32 v128, v1\n"
"v_permlane32_swap_b32 v129, v3\n"
"s_nop 1\n"
"v_add_f32 v1, v1, v128\n"
"v_add_f32 v3, v3, v129\n"
"ds_write_b32 v140, v0 offset:0\n"
"ds_write_b32 v140, v1 offset:64\n"
"ds_write_b32 v140, v2 offset:128\n"
"ds_write_b32 v140, v3 offset:192\n"
"s_waitcnt lgkmcnt(0)\n"
"s_barrier\n"
"ds_read_b128 v[32:35], v141 offset:0\n"
"ds_read_b128 v[36:39], v141 offset:64\n"
"ds_read_b128 v[40:43], v141 offset:128\n"
"ds_read_b128 v[44:47], v141 offset:192\n"
"v_mov_b32 v124, 0x3727c5ac\n"
"s_mov_b32 s72, 987991609\n"
"s_waitcnt lgkmcnt(0)\n"
"v_add_f32 v126, v32, v33\n"
"v_add_f32 v127, v34, v35\n"
"v_add_f32 v126, v126, v127\n"
"v_mul_f32 v126, s72, v126\n"
"s_add_u32 m0, s71, 90112\n"
"v_sub_f32 v132, v4, v126\n"
"buffer_load_dwordx4 v181, s[48:51], s77 offen lds\n"
"v_fmac_f32 v32, 3275751424, v126\n"
"v_mul_f32 v32, v32, v32\n"
"v_fmac_f32 v36, 1001040555, v32\n"
"v_fmac_f32 v33, 3271557120, v126\n"
"v_mul_f32 v33, v33, v33\n"
"v_fmac_f32 v37, 1006632960, v33\n"
"s_add_u32 m0, s71, 94208\n"
"v_fmac_f32 v34, 3271557120, v126\n"
"buffer_load_dwordx4 v182, s[48:51], s77 offen lds\n"
"v_mul_f32 v34, v34, v34\n"
"v_fmac_f32 v38, 1006632960, v34\n"
"v_fmac_f32 v35, 3271557120, v126\n"
"v_mul_f32 v35, v35, v35\n"
"v_fmac_f32 v39, 1006632960, v35\n"
"v_add_f32 v36, v36, v37\n"
"v_add_f32 v38, v38, v39\n"
"s_add_u32 m0, s71, 98304\n"
"v_add_f32 v36, v36, v38\n"
"buffer_load_dwordx4 v183, s[48:51], s77 offen lds\n"
"v_fma_f32 v136, v36, s72, v124\n"
"v_add_f32 v126, v40, v41\n"
"v_add_f32 v127, v42, v43\n"
"v_add_f32 v126, v126, v127\n"
"v_mul_f32 v126, s72, v126\n"
"v_sub_f32 v134, v6, v126\n"
"v_fmac_f32 v40, 3275751424, v126\n"
"s_add_u32 m0, s71, 102400\n"
"v_mul_f32 v40, v40, v40\n"
"buffer_load_dwordx4 v184, s[48:51], s77 offen lds\n"
"v_fmac_f32 v44, 1001040555, v40\n"
"v_fmac_f32 v41, 3271557120, v126\n"
"v_mul_f32 v41, v41, v41\n"
"v_fmac_f32 v45, 1006632960, v41\n"
"v_fmac_f32 v42, 3271557120, v126\n"
"v_mul_f32 v42, v42, v42\n"
"v_fmac_f32 v46, 1006632960, v42\n"
"s_add_u32 m0, s71, 106496\n"
"v_fmac_f32 v43, 3271557120, v126\n"
"buffer_load_dwordx4 v185, s[48:51], s77 offen lds\n"
"v_mul_f32 v43, v43, v43\n"
"v_fmac_f32 v47, 1006632960, v43\n"
"v_add_f32 v44, v44, v45\n"
"v_add_f32 v46, v46, v47\n"
"v_add_f32 v44, v44, v46\n"
"v_fma_f32 v138, v44, s72, v124\n"
"v_rsq_f32 v136, v136\n"
"v_rsq_f32 v138, v138\n"
"s_lshl_b32 s74, s68, 11\n"
"s_add_u32 s74, s74, s91\n"
"v_mul_f32 v132, v132, v136\n"
"v_mul_f32 v134, v134, v138\n"
"v_pk_fma_f32 v[96:97], v[96:97], v[136:137], v[132:133] op_sel_hi:[1,0,0]\n"
"v_pk_fma_f32 v[98:99], v[98:99], v[136:137], v[132:133] op_sel_hi:[1,0,0]\n"
"v_pk_fma_f32 v[96:97], v[96:97], v[72:73], v[84:85]\n"
"v_pk_fma_f32 v[98:99], v[98:99], v[74:75], v[86:87]\n"
"v_cvt_pk_bf16_f32 v16, v96, v97\n"
"v_cvt_pk_bf16_f32 v17, v98, v99\n"
"v_pk_fma_f32 v[100:101], v[100:101], v[138:139], v[134:135] op_sel_hi:[1,0,0]\n"
"v_pk_fma_f32 v[102:103], v[102:103], v[138:139], v[134:135] op_sel_hi:[1,0,0]\n"
"v_pk_fma_f32 v[100:101], v[100:101], v[72:73], v[84:85]\n"
"v_pk_fma_f32 v[102:103], v[102:103], v[74:75], v[86:87]\n"
"v_cvt_pk_bf16_f32 v18, v100, v101\n"
"v_cvt_pk_bf16_f32 v19, v102, v103\n"
"s_add_u32 s73, s74, 0\n"
"s_nop 0\n"
"v_permlane16_swap_b32 v16, v18\n"
"v_permlane16_swap_b32 v17, v19\n"
"s_nop 0\n"
"buffer_store_dwordx4 v[16:19], v245, s[60:63], s73 offen sc1\n"
"v_pk_fma_f32 v[104:105], v[104:105], v[136:137], v[132:133] op_sel_hi:[1,0,0]\n"
"v_pk_fma_f32 v[106:107], v[106:107], v[136:137], v[132:133] op_sel_hi:[1,0,0]\n"
"v_pk_fma_f32 v[104:105], v[104:105], v[76:77], v[88:89]\n"
"v_pk_fma_f32 v[106:107], v[106:107], v[78:79], v[90:91]\n"
"v_cvt_pk_bf16_f32 v20, v104, v105\n"
"v_cvt_pk_bf16_f32 v21, v106, v107\n"
"v_pk_fma_f32 v[108:109], v[108:109], v[138:139], v[134:135] op_sel_hi:[1,0,0]\n"
"v_pk_fma_f32 v[110:111], v[110:111], v[138:139], v[134:135] op_sel_hi:[1,0,0]\n"
"v_pk_fma_f32 v[108:109], v[108:109], v[76:77], v[88:89]\n"
"v_pk_fma_f32 v[110:111], v[110:111], v[78:79], v[90:91]\n"
"v_cvt_pk_bf16_f32 v22, v108, v109\n"
"v_cvt_pk_bf16_f32 v23, v110, v111\n"
"s_add_u32 s73, s74, 8192\n"
"s_nop 0\n"
"v_permlane16_swap_b32 v20, v22\n"
"v_permlane16_swap_b32 v21, v23\n"
"s_nop 0\n"
"buffer_store_dwordx4 v[20:23], v245, s[60:63], s73 offen sc1\n"
"s_cmp_eq_u32 s68, 0\n"
"s_cbranch_scc0 L_po_w017\n"
"v_pk_fma_f32 v[112:113], v[112:113], v[136:137], v[132:133] op_sel_hi:[1,0,0]\n"
"v_pk_fma_f32 v[114:115], v[114:115], v[136:137], v[132:133] op_sel_hi:[1,0,0]\n"
"v_pk_fma_f32 v[112:113], v[112:113], v[80:81], v[92:93]\n"
"v_pk_fma_f32 v[114:115], v[114:115], v[82:83], v[94:95]\n"
"v_cvt_pk_bf16_f32 v24, v112, v113\n"
"v_cvt_pk_bf16_f32 v25, v114, v115\n"
"v_pk_fma_f32 v[116:117], v[116:117], v[138:139], v[134:135] op_sel_hi:[1,0,0]\n"
"v_pk_fma_f32 v[118:119], v[118:119], v[138:139], v[134:135] op_sel_hi:[1,0,0]\n"
"v_pk_fma_f32 v[116:117], v[116:117], v[80:81], v[92:93]\n"
"v_pk_fma_f32 v[118:119], v[118:119], v[82:83], v[94:95]\n"
"v_cvt_pk_bf16_f32 v26, v116, v117\n"
"v_cvt_pk_bf16_f32 v27, v118, v119\n"
"s_add_u32 s73, s74, 16384\n"
"s_nop 0\n"
"v_permlane16_swap_b32 v24, v26\n"
"v_permlane16_swap_b32 v25, v27\n"
"s_nop 0\n"
"buffer_store_dwordx4 v[24:27], v245, s[60:63], s73 offen sc1\n"
"L_po_w017:\n"
"v_accvgpr_write_b32 a0, v48\n"
"v_accvgpr_write_b32 a1, v49\n"
"v_accvgpr_write_b32 a2, v50\n"
"v_accvgpr_write_b32 a3, v51\n"
"v_accvgpr_write_b32 a4, v52\n"
"v_accvgpr_write_b32 a5, v53\n"
"v_accvgpr_write_b32 a6, v54\n"
"v_accvgpr_write_b32 a7, v55\n"
"v_accvgpr_write_b32 a8, v56\n"
"v_accvgpr_write_b32 a9, v57\n"
"v_accvgpr_write_b32 a10, v58\n"
"v_accvgpr_write_b32 a11, v59\n"
"s_lshr_b32 s74, %11, 1\n"
"s_lshl1_add_u32 s74, %12, s74\n"
"s_waitcnt vmcnt(0)\n"
"s_waitcnt vmcnt(0)\n"
"s_barrier\n"
"s_cmp_eq_u32 s68, 0\n"
"s_cbranch_scc0 L_po_w018\n"
"s_lshl_b32 s72, s74, 8\n"
"s_add_u32 s72, s72, 90112\n"
"v_mov_b32 v200, 1\n"
"s_mov_b64 exec, 1\n"
"buffer_atomic_add v200, off, s[60:63], s72\n"
"s_mov_b64 exec, -1\n"
"L_po_w018:\n"
"s_add_u32 m0, s71, 110592\n"
"s_mov_b32 s77, 3538944\n"
"s_nop 0\n"
"buffer_load_dwordx4 v177, s[48:51], s77 offen lds\n"
"s_add_u32 m0, s71, 114688\n"
"s_nop 0\n"
"buffer_load_dwordx4 v178, s[48:51], s77 offen lds\n"
"s_add_u32 m0, s71, 118784\n"
"s_nop 0\n"
"buffer_load_dwordx4 v179, s[48:51], s77 offen lds\n"
"s_add_u32 m0, s71, 122880\n"
"s_nop 0\n"
"buffer_load_dwordx4 v180, s[48:51], s77 offen lds\n"
"s_add_u32 m0, s71, 126976\n"
"s_nop 0\n"
"buffer_load_dwordx4 v181, s[48:51], s77 offen lds\n"
"s_add_u32 m0, s71, 131072\n"
"s_nop 0\n"
"buffer_load_dwordx4 v182, s[48:51], s77 offen lds\n"
"s_add_u32 m0, s71, 135168\n"
"s_nop 0\n"
"buffer_load_dwordx4 v183, s[48:51], s77 offen lds\n"
"s_add_u32 m0, s71, 139264\n"
"s_nop 0\n"
"buffer_load_dwordx4 v184, s[48:51], s77 offen lds\n"
"s_add_u32 m0, s71, 143360\n"
"s_nop 0\n"
"buffer_load_dwordx4 v185, s[48:51], s77 offen lds\n"
"s_lshl_b32 s72, s70, 10\n"
"v_add_u32 v197, s72, v186\n"
"s_mul_i32 s72, s70, 3584\n"
"s_sub_u32 s72, 4096, s72\n"
"v_add_u32 v198, s72, v187\n"
"s_add_u32 s86, s92, 147456\n"
"s_add_u32 s87, s92, 294912\n"
"s_add_u32 s91, s92, 442368\n"
"s_lshl2_add_u32 s74, %14, s68\n"
"s_lshl_b32 s65, s95, 8\n"
"s_add_u32 s65, s65, 360448\n"
"s_lshl_b32 s75, s96, 8\n"
"s_add_u32 s75, s75, 360448\n"
"v_lshrrev_b32 v203, 2, v199\n"
"v_mul_u32_u24 v203, 288, v203\n"
"v_and_b32 v204, 3, v199\n"
"v_lshl_add_u32 v203, v204, 3, v203\n"
"v_add_u32 v203, s69, v203\n"
"v_mov_b32 v246, v203\n"
"v_add_u32 v249, 256, v203\n"
"L_tc2_0:\n"
"s_cbranch_execz L_tp2_0_1\n"
"v_add_u32 v247, 55296, v246\n"
"v_add_u32 v250, 55296, v249\n"
"v_add_u32 v248, 110592, v246\n"
"v_add_u32 v251, 110592, v249\n"
"s_waitcnt vmcnt(47)\n"
"s_barrier\n"
"ds_read_b64_tr_b16 v[144:145], v188 offset:0\n"
"ds_read_b64_tr_b16 v[146:147], v188 offset:4608\n"
"ds_read_b64_tr_b16 v[148:149], v188 offset:9216\n"
"ds_read_b64_tr_b16 v[150:151], v188 offset:13824\n"
"ds_read_b64_tr_b16 v[152:153], v188 offset:32\n"
"ds_read_b64_tr_b16 v[154:155], v188 offset:4640\n"
"ds_read_b64_tr_b16 v[156:157], v188 offset:9248\n"
"ds_read_b64_tr_b16 v[158:159], v188 offset:13856\n"
"s_lshl_b32 s72, s74, 8\n"
"s_add_u32 s72, s72, 90112\n"
"s_mov_b32 s97, 20000\n"
"L_po_poll19:\n"
"buffer_load_dword v208, off, s[60:63], s72 sc1\n"
"s_waitcnt vmcnt(0)\n"
"v_readfirstlane_b32 s73, v208\n"
"s_nop 1\n"
"s_cmp_ge_u32 s73, 8\n"
"s_cbranch_scc1 L_po_polld20\n"
"s_sleep 2\n"
"s_sub_u32 s97, s97, 1\n"
"s_cbranch_scc0 L_po_poll19\n"
"L_po_polld20:\n"
"s_add_u32 s78, s82, 0\n"
"buffer_load_dwordx4 a[12:15], v186, s[60:63], s78 offen offset:0 sc1\n"
"s_add_u32 s79, s83, 0\n"
"buffer_load_dwordx4 a[16:19], v186, s[60:63], s79 offen offset:0 sc1\n"
"s_add_u32 s80, s84, 0\n"
"buffer_load_dwordx4 a[20:23], v186, s[60:63], s80 offen offset:0 sc1\n"
"s_add_u32 s81, s85, 0\n"
"buffer_load_dwordx4 a[24:27], v186, s[60:63], s81 offen offset:0 sc1\n"
"buffer_load_dwordx4 a[28:31], v186, s[60:63], s78 offen offset:1024 sc1\n"
"buffer_load_dwordx4 a[32:35], v186, s[60:63], s79 offen offset:1024 sc1\n"
"buffer_load_dwordx4 a[36:39], v186, s[60:63], s80 offen offset:1024 sc1\n"
"buffer_load_dwordx4 a[40:43], v186, s[60:63], s81 offen offset:1024 sc1\n"
"s_waitcnt vmcnt(0) lgkmcnt(4)\n"
"v_mfma_f32_16x16x32_bf16 v[0:3], v[144:147], a[12:15], 0\n"
"buffer_load_dwordx4 a[44:47], v186, s[60:63], s78 offen offset:2048 sc1\n"
"v_mfma_f32_16x16x32_bf16 v[0:3], v[148:151], a[28:31], v[0:3]\n"
"s_nop 0\n"
"ds_read_b64_tr_b16 v[160:161], v188 offset:64\n"
"ds_read_b64_tr_b16 v[162:163], v188 offset:4672\n"
"v_mfma_f32_16x16x32_bf16 v[36:39], v[144:147], a[16:19], 0\n"
"v_mfma_f32_16x16x32_bf16 v[36:39], v[148:151], a[32:35], v[36:39]\n"
"buffer_load_dwordx4 a[48:51], v186, s[60:63], s79 offen offset:2048 sc1\n"
"v_mfma_f32_16x16x32_bf16 v[72:75], v[144:147], a[20:23], 0\n"
"s_nop 0\n"
"ds_read_b64_tr_b16 v[164:165], v188 offset:9280\n"
"ds_read_b64_tr_b16 v[166:167], v188 offset:13888\n"
"v_mfma_f32_16x16x32_bf16 v[72:75], v[148:151], a[36:39], v[72:75]\n"
"v_mfma_f32_16x16x32_bf16 v[108:111], v[144:147], a[24:27], 0\n"
"v_mfma_f32_16x16x32_bf16 v[108:111], v[148:151], a[40:43], v[108:111]\n"
"s_waitcnt lgkmcnt(4)\n"
"v_mfma_f32_16x16x32_bf16 v[4:7], v[152:155], a[12:15], 0\n"
"buffer_load_dwordx4 a[52:55], v186, s[60:63], s80 offen offset:2048 sc1\n"
"v_mfma_f32_16x16x32_bf16 v[4:7], v[156:159], a[28:31], v[4:7]\n"
"s_nop 0\n"
"ds_read_b64_tr_b16 v[144:145], v188 offset:96\n"
"ds_read_b64_tr_b16 v[146:147], v188 offset:4704\n"
"v_mfma_f32_16x16x32_bf16 v[40:43], v[152:155], a[16:19], 0\n"
"v_mfma_f32_16x16x32_bf16 v[40:43], v[156:159], a[32:35], v[40:43]\n"
"s_nop 0\n"
"ds_read_b64_tr_b16 v[148:149], v188 offset:9312\n"
"ds_read_b64_tr_b16 v[150:151], v188 offset:13920\n"
"v_mfma_f32_16x16x32_bf16 v[76:79], v[152:155], a[20:23], 0\n"
"v_mfma_f32_16x16x32_bf16 v[76:79], v[156:159], a[36:39], v[76:79]\n"
"buffer_load_dwordx4 a[56:59], v186, s[60:63], s81 offen offset:2048 sc1\n"
"v_mfma_f32_16x16x32_bf16 v[112:115], v[152:155], a[24:27], 0\n"
"v_mfma_f32_16x16x32_bf16 v[112:115], v[156:159], a[40:43], v[112:115]\n"
"s_waitcnt lgkmcnt(4)\n"
"v_mfma_f32_16x16x32_bf16 v[8:11], v[160:163], a[12:15], 0\n"
"s_nop 0\n"
"ds_read_b64_tr_b16 v[152:153], v188 offset:128\n"
"ds_read_b64_tr_b16 v[154:155], v188 offset:4736\n"
"v_mfma_f32_16x16x32_bf16 v[8:11], v[164:167], a[28:31], v[8:11]\n"
"v_mfma_f32_16x16x32_bf16 v[44:47], v[160:163], a[16:19], 0\n"
"buffer_load_dwordx4 a[60:63], v186, s[60:63], s78 offen offset:3072 sc1\n"
"v_mfma_f32_16x16x32_bf16 v[44:47], v[164:167], a[32:35], v[44:47]\n"
"s_nop 0\n"
"ds_read_b64_tr_b16 v[156:157], v188 offset:9344\n"
"ds_read_b64_tr_b16 v[158:159], v188 offset:13952\n"
"v_mfma_f32_16x16x32_bf16 v[80:83], v[160:163], a[20:23], 0\n"
"v_mfma_f32_16x16x32_bf16 v[80:83], v[164:167], a[36:39], v[80:83]\n"
"v_mfma_f32_16x16x32_bf16 v[116:119], v[160:163], a[24:27], 0\n"
"v_mfma_f32_16x16x32_bf16 v[116:119], v[164:167], a[40:43], v[116:119]\n"
"s_waitcnt lgkmcnt(4)\n"
"v_mfma_f32_16x16x32_bf16 v[12:15], v[144:147], a[12:15], 0\n"
"buffer_load_dwordx4 a[64:67], v186, s[60:63], s79 offen offset:3072 sc1\n"
"v_mfma_f32_16x16x32_bf16 v[12:15], v[148:151], a[28:31], v[12:15]\n"
"s_nop 0\n"
"ds_read_b64_tr_b16 v[160:161], v188 offset:160\n"
"ds_read_b64_tr_b16 v[162:163], v188 offset:4768\n"
"v_mfma_f32_16x16x32_bf16 v[48:51], v[144:147], a[16:19], 0\n"
"v_mfma_f32_16x16x32_bf16 v[48:51], v[148:151], a[32:35], v[48:51]\n"
"s_nop 0\n"
"ds_read_b64_tr_b16 v[164:165], v188 offset:9376\n"
"ds_read_b64_tr_b16 v[166:167], v188 offset:13984\n"
"v_mfma_f32_16x16x32_bf16 v[84:87], v[144:147], a[20:23], 0\n"
"buffer_load_dwordx4 a[68:71], v186, s[60:63], s80 offen offset:3072 sc1\n"
"v_mfma_f32_16x16x32_bf16 v[84:87], v[148:151], a[36:39], v[84:87]\n"
"v_mfma_f32_16x16x32_bf16 v[120:123], v[144:147], a[24:27], 0\n"
"v_mfma_f32_16x16x32_bf16 v[120:123], v[148:151], a[40:43], v[120:123]\n"
"s_waitcnt lgkmcnt(4)\n"
"v_mfma_f32_16x16x32_bf16 v[16:19], v[152:155], a[12:15], 0\n"
"s_nop 0\n"
"ds_read_b64_tr_b16 v[144:145], v188 offset:192\n"
"ds_read_b64_tr_b16 v[146:147], v188 offset:4800\n"
"v_mfma_f32_16x16x32_bf16 v[16:19], v[156:159], a[28:31], v[16:19]\n"
"buffer_load_dwordx4 a[72:75], v186, s[60:63], s81 offen offset:3072 sc1\n"
"v_mfma_f32_16x16x32_bf16 v[52:55], v[152:155], a[16:19], 0\n"
"s_add_u32 s78, s82, 4096\n"
"v_mfma_f32_16x16x32_bf16 v[52:55], v[156:159], a[32:35], v[52:55]\n"
"s_nop 0\n"
"ds_read_b64_tr_b16 v[148:149], v188 offset:9408\n"
"ds_read_b64_tr_b16 v[150:151], v188 offset:14016\n"
"v_mfma_f32_16x16x32_bf16 v[88:91], v[152:155], a[20:23], 0\n"
"v_mfma_f32_16x16x32_bf16 v[88:91], v[156:159], a[36:39], v[88:91]\n"
"v_mfma_f32_16x16x32_bf16 v[124:127], v[152:155], a[24:27], 0\n"
"buffer_load_dwordx4 a[76:79], v186, s[60:63], s78 offen offset:0 sc1\n"
"v_mfma_f32_16x16x32_bf16 v[124:127], v[156:159], a[40:43], v[124:127]\n"
"s_waitcnt lgkmcnt(4)\n"
"v_mfma_f32_16x16x32_bf16 v[20:23], v[160:163], a[12:15], 0\n"
"s_add_u32 s79, s83, 4096\n"
"ds_read_b64_tr_b16 v[152:153], v188 offset:224\n"
"ds_read_b64_tr_b16 v[154:155], v188 offset:4832\n"
"v_mfma_f32_16x16x32_bf16 v[20:23], v[164:167], a[28:31], v[20:23]\n"
"v_mfma_f32_16x16x32_bf16 v[56:59], v[160:163], a[16:19], 0\n"
"v_mfma_f32_16x16x32_bf16 v[56:59], v[164:167], a[32:35], v[56:59]\n"
"buffer_load_dwordx4 a[80:83], v186, s[60:63], s79 offen offset:0 sc1\n"
"v_mfma_f32_16x16x32_bf16 v[92:95], v[160:163], a[20:23], 0\n"
"s_add_u32 s80, s84, 4096\n"
"ds_read_b64_tr_b16 v[156:157], v188 offset:9440\n"
"ds_read_b64_tr_b16 v[158:159], v188 offset:14048\n"
"v_mfma_f32_16x16x32_bf16 v[92:95], v[164:167], a[36:39], v[92:95]\n"
"v_mfma_f32_16x16x32_bf16 v[128:131], v[160:163], a[24:27], 0\n"
"v_mfma_f32_16x16x32_bf16 v[128:131], v[164:167], a[40:43], v[128:131]\n"
"s_waitcnt lgkmcnt(4)\n"
"v_mfma_f32_16x16x32_bf16 v[24:27], v[144:147], a[12:15], 0\n"
"buffer_load_dwordx4 a[84:87], v186, s[60:63], s80 offen offset:0 sc1\n"
"v_mfma_f32_16x16x32_bf16 v[24:27], v[148:151], a[28:31], v[24:27]\n"
"s_add_u32 s81, s85, 4096\n"
"ds_read_b64_tr_b16 v[160:161], v191 offset:0\n"
"ds_read_b64_tr_b16 v[162:163], v191 offset:4608\n"
"v_mfma_f32_16x16x32_bf16 v[60:63], v[144:147], a[16:19], 0\n"
"v_mfma_f32_16x16x32_bf16 v[60:63], v[148:151], a[32:35], v[60:63]\n"
"s_nop 0\n"
"ds_read_b64_tr_b16 v[164:165], v191 offset:9216\n"
"ds_read_b64_tr_b16 v[166:167], v191 offset:13824\n"
"v_mfma_f32_16x16x32_bf16 v[96:99], v[144:147], a[20:23], 0\n"
"v_mfma_f32_16x16x32_bf16 v[96:99], v[148:151], a[36:39], v[96:99]\n"
"buffer_load_dwordx4 a[88:91], v186, s[60:63], s81 offen offset:0 sc1\n"
"v_mfma_f32_16x16x32_bf16 v[132:135], v[144:147], a[24:27], 0\n"
"v_mfma_f32_16x16x32_bf16 v[132:135], v[148:151], a[40:43], v[132:135]\n"
"s_waitcnt lgkmcnt(4)\n"
"v_mfma_f32_16x16x32_bf16 v[28:31], v[152:155], a[12:15], 0\n"
"s_nop 0\n"
"ds_read_b64_tr_b16 v[144:145], v188 offset:18432\n"
"ds_read_b64_tr_b16 v[146:147], v188 offset:23040\n"
"v_mfma_f32_16x16x32_bf16 v[28:31], v[156:159], a[28:31], v[28:31]\n"
"v_mfma_f32_16x16x32_bf16 v[64:67], v[152:155], a[16:19], 0\n"
"buffer_load_dwordx4 a[92:95], v186, s[60:63], s78 offen offset:1024 sc1\n"
"v_mfma_f32_16x16x32_bf16 v[64:67], v[156:159], a[32:35], v[64:67]\n"
"s_nop 0\n"
"ds_read_b64_tr_b16 v[148:149], v188 offset:27648\n"
"ds_read_b64_tr_b16 v[150:151], v188 offset:32256\n"
"v_mfma_f32_16x16x32_bf16 v[100:103], v[152:155], a[20:23], 0\n"
"v_mfma_f32_16x16x32_bf16 v[100:103], v[156:159], a[36:39], v[100:103]\n"
"v_mfma_f32_16x16x32_bf16 v[136:139], v[152:155], a[24:27], 0\n"
"v_mfma_f32_16x16x32_bf16 v[136:139], v[156:159], a[40:43], v[136:139]\n"
"s_waitcnt lgkmcnt(4)\n"
"v_mfma_f32_16x16x32_bf16 v[32:35], v[160:163], a[12:15], 0\n"
"buffer_load_dwordx4 a[96:99], v186, s[60:63], s79 offen offset:1024 sc1\n"
"v_mfma_f32_16x16x32_bf16 v[32:35], v[164:167], a[28:31], v[32:35]\n"
"s_nop 0\n"
"ds_read_b64_tr_b16 v[152:153], v188 offset:18464\n"
"ds_read_b64_tr_b16 v[154:155], v188 offset:23072\n"
"v_mfma_f32_16x16x32_bf16 v[68:71], v[160:163], a[16:19], 0\n"
"v_mfma_f32_16x16x32_bf16 v[68:71], v[164:167], a[32:35], v[68:71]\n"
"s_nop 0\n"
"ds_read_b64_tr_b16 v[156:157], v188 offset:27680\n"
"ds_read_b64_tr_b16 v[158:159], v188 offset:32288\n"
"v_mfma_f32_16x16x32_bf16 v[104:107], v[160:163], a[20:23], 0\n"
"buffer_load_dwordx4 a[100:103], v186, s[60:63], s80 offen offset:1024 sc1\n"
"v_mfma_f32_16x16x32_bf16 v[104:107], v[164:167], a[36:39], v[104:107]\n"
"v_mfma_f32_16x16x32_bf16 v[140:143], v[160:163], a[24:27], 0\n"
"v_mfma_f32_16x16x32_bf16 v[140:143], v[164:167], a[40:43], v[140:143]\n"
"s_waitcnt vmcnt(7) lgkmcnt(4)\n"
"v_mfma_f32_16x16x32_bf16 v[0:3], v[144:147], a[44:47], v[0:3]\n"
"s_nop 0\n"
"ds_read_b64_tr_b16 v[160:161], v188 offset:18496\n"
"ds_read_b64_tr_b16 v[162:163], v188 offset:23104\n"
"v_mfma_f32_16x16x32_bf16 v[0:3], v[148:151], a[60:63], v[0:3]\n"
"buffer_load_dwordx4 a[104:107], v186, s[60:63], s81 offen offset:1024 sc1\n"
"v_mfma_f32_16x16x32_bf16 v[36:39], v[144:147], a[48:51], v[36:39]\n"
"v_mfma_f32_16x16x32_bf16 v[36:39], v[148:151], a[64:67], v[36:39]\n"
"s_nop 0\n"
"ds_read_b64_tr_b16 v[164:165], v188 offset:27712\n"
"ds_read_b64_tr_b16 v[166:167], v188 offset:32320\n"
"v_mfma_f32_16x16x32_bf16 v[72:75], v[144:147], a[52:55], v[72:75]\n"
"v_mfma_f32_16x16x32_bf16 v[72:75], v[148:151], a[68:71], v[72:75]\n"
"v_mfma_f32_16x16x32_bf16 v[108:111], v[144:147], a[56:59], v[108:111]\n"
"buffer_load_dwordx4 a[108:111], v186, s[60:63], s78 offen offset:2048 sc1\n"
"v_mfma_f32_16x16x32_bf16 v[108:111], v[148:151], a[72:75], v[108:111]\n"
"s_waitcnt lgkmcnt(4)\n"
"v_mfma_f32_16x16x32_bf16 v[4:7], v[152:155], a[44:47], v[4:7]\n"
"s_nop 0\n"
"ds_read_b64_tr_b16 v[144:145], v188 offset:18528\n"
"ds_read_b64_tr_b16 v[146:147], v188 offset:23136\n"
"v_mfma_f32_16x16x32_bf16 v[4:7], v[156:159], a[60:63], v[4:7]\n"
"v_mfma_f32_16x16x32_bf16 v[40:43], v[152:155], a[48:51], v[40:43]\n"
"v_mfma_f32_16x16x32_bf16 v[40:43], v[156:159], a[64:67], v[40:43]\n"
"buffer_load_dwordx4 a[112:115], v186, s[60:63], s79 offen offset:2048 sc1\n"
"v_mfma_f32_16x16x32_bf16 v[76:79], v[152:155], a[52:55], v[76:79]\n"
"s_nop 0\n"
"ds_read_b64_tr_b16 v[148:149], v188 offset:27744\n"
"ds_read_b64_tr_b16 v[150:151], v188 offset:32352\n"
"v_mfma_f32_16x16x32_bf16 v[76:79], v[156:159], a[68:71], v[76:79]\n"
"v_mfma_f32_16x16x32_bf16 v[112:115], v[152:155], a[56:59], v[112:115]\n"
"v_mfma_f32_16x16x32_bf16 v[112:115], v[156:159], a[72:75], v[112:115]\n"
"s_waitcnt lgkmcnt(4)\n"
"v_mfma_f32_16x16x32_bf16 v[8:11], v[160:163], a[44:47], v[8:11]\n"
"buffer_load_dwordx4 a[116:119], v186, s[60:63], s80 offen offset:2048 sc1\n"
"v_mfma_f32_16x16x32_bf16 v[8:11], v[164:167], a[60:63], v[8:11]\n"
"s_nop 0\n"
"ds_read_b64_tr_b16 v[152:153], v188 offset:18560\n"
"ds_read_b64_tr_b16 v[154:155], v188 offset:23168\n"
"v_mfma_f32_16x16x32_bf16 v[44:47], v[160:163], a[48:51], v[44:47]\n"
"v_mfma_f32_16x16x32_bf16 v[44:47], v[164:167], a[64:67], v[44:47]\n"
"s_nop 0\n"
"ds_read_b64_tr_b16 v[156:157], v188 offset:27776\n"
"ds_read_b64_tr_b16 v[158:159], v188 offset:32384\n"
"v_mfma_f32_16x16x32_bf16 v[80:83], v[160:163], a[52:55], v[80:83]\n"
"v_mfma_f32_16x16x32_bf16 v[80:83], v[164:167], a[68:71], v[80:83]\n"
"buffer_load_dwordx4 a[120:123], v186, s[60:63], s81 offen offset:2048 sc1\n"
"v_mfma_f32_16x16x32_bf16 v[116:119], v[160:163], a[56:59], v[116:119]\n"
"v_mfma_f32_16x16x32_bf16 v[116:119], v[164:167], a[72:75], v[116:119]\n"
"s_waitcnt lgkmcnt(4)\n"
"v_mfma_f32_16x16x32_bf16 v[12:15], v[144:147], a[44:47], v[12:15]\n"
"s_nop 0\n"
"ds_read_b64_tr_b16 v[160:161], v188 offset:18592\n"
"ds_read_b64_tr_b16 v[162:163], v188 offset:23200\n"
"v_mfma_f32_16x16x32_bf16 v[12:15], v[148:151], a[60:63], v[12:15]\n"
"v_mfma_f32_16x16x32_bf16 v[48:51], v[144:147], a[48:51], v[48:51]\n"
"buffer_load_dwordx4 a[124:127], v186, s[60:63], s78 offen offset:3072 sc1\n"
"v_mfma_f32_16x16x32_bf16 v[48:51], v[148:151], a[64:67], v[48:51]\n"
"s_nop 0\n"
"ds_read_b64_tr_b16 v[164:165], v188 offset:27808\n"
"ds_read_b64_tr_b16 v[166:167], v188 offset:32416\n"
"v_mfma_f32_16x16x32_bf16 v[84:87], v[144:147], a[52:55], v[84:87]\n"
"v_mfma_f32_16x16x32_bf16 v[84:87], v[148:151], a[68:71], v[84:87]\n"
"v_mfma_f32_16x16x32_bf16 v[120:123], v[144:147], a[56:59], v[120:123]\n"
"v_mfma_f32_16x16x32_bf16 v[120:123], v[148:151], a[72:75], v[120:123]\n"
"s_waitcnt lgkmcnt(4)\n"
"v_mfma_f32_16x16x32_bf16 v[16:19], v[152:155], a[44:47], v[16:19]\n"
"buffer_load_dwordx4 a[128:131], v186, s[60:63], s79 offen offset:3072 sc1\n"
"v_mfma_f32_16x16x32_bf16 v[16:19], v[156:159], a[60:63], v[16:19]\n"
"s_nop 0\n"
"ds_read_b64_tr_b16 v[144:145], v188 offset:18624\n"
"ds_read_b64_tr_b16 v[146:147], v188 offset:23232\n"
"v_mfma_f32_16x16x32_bf16 v[52:55], v[152:155], a[48:51], v[52:55]\n"
"v_mfma_f32_16x16x32_bf16 v[52:55], v[156:159], a[64:67], v[52:55]\n"
"s_nop 0\n"
"ds_read_b64_tr_b16 v[148:149], v188 offset:27840\n"
"ds_read_b64_tr_b16 v[150:151], v188 offset:32448\n"
"v_mfma_f32_16x16x32_bf16 v[88:91], v[152:155], a[52:55], v[88:91]\n"
"buffer_load_dwordx4 a[132:135], v186, s[60:63], s80 offen offset:3072 sc1\n"
"v_mfma_f32_16x16x32_bf16 v[88:91], v[156:159], a[68:71], v[88:91]\n"
"v_mfma_f32_16x16x32_bf16 v[124:127], v[152:155], a[56:59], v[124:127]\n"
"v_mfma_f32_16x16x32_bf16 v[124:127], v[156:159], a[72:75], v[124:127]\n"
"s_waitcnt lgkmcnt(4)\n"
"v_mfma_f32_16x16x32_bf16 v[20:23], v[160:163], a[44:47], v[20:23]\n"
"s_nop 0\n"
"ds_read_b64_tr_b16 v[152:153], v188 offset:18656\n"
"ds_read_b64_tr_b16 v[154:155], v188 offset:23264\n"
"v_mfma_f32_16x16x32_bf16 v[20:23], v[164:167], a[60:63], v[20:23]\n"
"buffer_load_dwordx4 a[136:139], v186, s[60:63], s81 offen offset:3072 sc1\n"
"v_mfma_f32_16x16x32_bf16 v[56:59], v[160:163], a[48:51], v[56:59]\n"
"s_add_u32 s78, s82, 8192\n"
"v_mfma_f32_16x16x32_bf16 v[56:59], v[164:167], a[64:67], v[56:59]\n"
"s_nop 0\n"
"ds_read_b64_tr_b16 v[156:157], v188 offset:27872\n"
"ds_read_b64_tr_b16 v[158:159], v188 offset:32480\n"
"v_mfma_f32_16x16x32_bf16 v[92:95], v[160:163], a[52:55], v[92:95]\n"
"v_mfma_f32_16x16x32_bf16 v[92:95], v[164:167], a[68:71], v[92:95]\n"
"v_mfma_f32_16x16x32_bf16 v[128:131], v[160:163], a[56:59], v[128:131]\n"
"buffer_load_dwordx4 a[140:143], v186, s[60:63], s78 offen offset:0 sc1\n"
"v_mfma_f32_16x16x32_bf16 v[128:131], v[164:167], a[72:75], v[128:131]\n"
"s_waitcnt lgkmcnt(4)\n"
"v_mfma_f32_16x16x32_bf16 v[24:27], v[144:147], a[44:47], v[24:27]\n"
"s_add_u32 s79, s83, 8192\n"
"ds_read_b64_tr_b16 v[160:161], v191 offset:18432\n"
"ds_read_b64_tr_b16 v[162:163], v191 offset:23040\n"
"v_mfma_f32_16x16x32_bf16 v[24:27], v[148:151], a[60:63], v[24:27]\n"
"v_mfma_f32_16x16x32_bf16 v[60:63], v[144:147], a[48:51], v[60:63]\n"
"v_mfma_f32_16x16x32_bf16 v[60:63], v[148:151], a[64:67], v[60:63]\n"
"buffer_load_dwordx4 a[144:147], v186, s[60:63], s79 offen offset:0 sc1\n"
"v_mfma_f32_16x16x32_bf16 v[96:99], v[144:147], a[52:55], v[96:99]\n"
"s_add_u32 s80, s84, 8192\n"
"ds_read_b64_tr_b16 v[164:165], v191 offset:27648\n"
"ds_read_b64_tr_b16 v[166:167], v191 offset:32256\n"
"v_mfma_f32_16x16x32_bf16 v[96:99], v[148:151], a[68:71], v[96:99]\n"
"v_mfma_f32_16x16x32_bf16 v[132:135], v[144:147], a[56:59], v[132:135]\n"
"v_mfma_f32_16x16x32_bf16 v[132:135], v[148:151], a[72:75], v[132:135]\n"
"s_waitcnt vmcnt(52) lgkmcnt(4)\n"
"s_barrier\n"
"v_mfma_f32_16x16x32_bf16 v[28:31], v[152:155], a[44:47], v[28:31]\n"
"buffer_load_dwordx4 a[148:151], v186, s[60:63], s80 offen offset:0 sc1\n"
"v_mfma_f32_16x16x32_bf16 v[28:31], v[156:159], a[60:63], v[28:31]\n"
"s_add_u32 s81, s85, 8192\n"
"ds_read_b64_tr_b16 v[144:145], v188 offset:36864\n"
"ds_read_b64_tr_b16 v[146:147], v188 offset:41472\n"
"v_mfma_f32_16x16x32_bf16 v[64:67], v[152:155], a[48:51], v[64:67]\n"
"v_mfma_f32_16x16x32_bf16 v[64:67], v[156:159], a[64:67], v[64:67]\n"
"s_nop 0\n"
"ds_read_b64_tr_b16 v[148:149], v188 offset:46080\n"
"ds_read_b64_tr_b16 v[150:151], v188 offset:50688\n"
"v_mfma_f32_16x16x32_bf16 v[100:103], v[152:155], a[52:55], v[100:103]\n"
"v_mfma_f32_16x16x32_bf16 v[100:103], v[156:159], a[68:71], v[100:103]\n"
"buffer_load_dwordx4 a[152:155], v186, s[60:63], s81 offen offset:0 sc1\n"
"v_mfma_f32_16x16x32_bf16 v[136:139], v[152:155], a[56:59], v[136:139]\n"
"v_mfma_f32_16x16x32_bf16 v[136:139], v[156:159], a[72:75], v[136:139]\n"
"s_waitcnt vmcnt(48) lgkmcnt(4)\n"
"s_barrier\n"
"v_mfma_f32_16x16x32_bf16 v[32:35], v[160:163], a[44:47], v[32:35]\n"
"s_nop 0\n"
"ds_read_b64_tr_b16 v[152:153], v188 offset:36896\n"
"ds_read_b64_tr_b16 v[154:155], v188 offset:41504\n"
"v_mfma_f32_16x16x32_bf16 v[32:35], v[164:167], a[60:63], v[32:35]\n"
"v_mfma_f32_16x16x32_bf16 v[68:71], v[160:163], a[48:51], v[68:71]\n"
"buffer_load_dwordx4 a[156:159], v186, s[60:63], s78 offen offset:1024 sc1\n"
"v_mfma_f32_16x16x32_bf16 v[68:71], v[164:167], a[64:67], v[68:71]\n"
"s_nop 0\n"
"ds_read_b64_tr_b16 v[156:157], v188 offset:46112\n"
"ds_read_b64_tr_b16 v[158:159], v188 offset:50720\n"
"v_mfma_f32_16x16x32_bf16 v[104:107], v[160:163], a[52:55], v[104:107]\n"
"v_mfma_f32_16x16x32_bf16 v[104:107], v[164:167], a[68:71], v[104:107]\n"
"v_mfma_f32_16x16x32_bf16 v[140:143], v[160:163], a[56:59], v[140:143]\n"
"v_mfma_f32_16x16x32_bf16 v[140:143], v[164:167], a[72:75], v[140:143]\n"
"s_waitcnt vmcnt(13) lgkmcnt(4)\n"
"v_mfma_f32_16x16x32_bf16 v[0:3], v[144:147], a[76:79], v[0:3]\n"
"buffer_load_dwordx4 a[160:163], v186, s[60:63], s79 offen offset:1024 sc1\n"
"v_mfma_f32_16x16x32_bf16 v[0:3], v[148:151], a[92:95], v[0:3]\n"
"s_nop 0\n"
"ds_read_b64_tr_b16 v[160:161], v188 offset:36928\n"
"ds_read_b64_tr_b16 v[162:163], v188 offset:41536\n"
"v_mfma_f32_16x16x32_bf16 v[36:39], v[144:147], a[80:83], v[36:39]\n"
"v_mfma_f32_16x16x32_bf16 v[36:39], v[148:151], a[96:99], v[36:39]\n"
"s_nop 0\n"
"ds_read_b64_tr_b16 v[164:165], v188 offset:46144\n"
"ds_read_b64_tr_b16 v[166:167], v188 offset:50752\n"
"v_mfma_f32_16x16x32_bf16 v[72:75], v[144:147], a[84:87], v[72:75]\n"
"buffer_load_dwordx4 a[164:167], v186, s[60:63], s80 offen offset:1024 sc1\n"
"v_mfma_f32_16x16x32_bf16 v[72:75], v[148:151], a[100:103], v[72:75]\n"
"v_mfma_f32_16x16x32_bf16 v[108:111], v[144:147], a[88:91], v[108:111]\n"
"v_mfma_f32_16x16x32_bf16 v[108:111], v[148:151], a[104:107], v[108:111]\n"
"s_waitcnt lgkmcnt(4)\n"
"v_mfma_f32_16x16x32_bf16 v[4:7], v[152:155], a[76:79], v[4:7]\n"
"s_nop 0\n"
"ds_read_b64_tr_b16 v[144:145], v188 offset:36960\n"
"ds_read_b64_tr_b16 v[146:147], v188 offset:41568\n"
"v_mfma_f32_16x16x32_bf16 v[4:7], v[156:159], a[92:95], v[4:7]\n"
"buffer_load_dwordx4 a[168:171], v186, s[60:63], s81 offen offset:1024 sc1\n"
"v_mfma_f32_16x16x32_bf16 v[40:43], v[152:155], a[80:83], v[40:43]\n"
"v_mfma_f32_16x16x32_bf16 v[40:43], v[156:159], a[96:99], v[40:43]\n"
"s_nop 0\n"
"ds_read_b64_tr_b16 v[148:149], v188 offset:46176\n"
"ds_read_b64_tr_b16 v[150:151], v188 offset:50784\n"
"v_mfma_f32_16x16x32_bf16 v[76:79], v[152:155], a[84:87], v[76:79]\n"
"v_mfma_f32_16x16x32_bf16 v[76:79], v[156:159], a[100:103], v[76:79]\n"
"v_mfma_f32_16x16x32_bf16 v[112:115], v[152:155], a[88:91], v[112:115]\n"
"buffer_load_dwordx4 a[172:175], v186, s[60:63], s78 offen offset:2048 sc1\n"
"v_mfma_f32_16x16x32_bf16 v[112:115], v[156:159], a[104:107], v[112:115]\n"
"s_waitcnt lgkmcnt(4)\n"
"v_mfma_f32_16x16x32_bf16 v[8:11], v[160:163], a[76:79], v[8:11]\n"
"s_nop 0\n"
"ds_read_b64_tr_b16 v[152:153], v189 offset:0\n"
"ds_read_b64_tr_b16 v[154:155], v189 offset:4608\n"
"v_mfma_f32_16x16x32_bf16 v[8:11], v[164:167], a[92:95], v[8:11]\n"
"v_mfma_f32_16x16x32_bf16 v[44:47], v[160:163], a[80:83], v[44:47]\n"
"v_mfma_f32_16x16x32_bf16 v[44:47], v[164:167], a[96:99], v[44:47]\n"
"buffer_load_dwordx4 a[176:179], v186, s[60:63], s79 offen offset:2048 sc1\n"
"v_mfma_f32_16x16x32_bf16 v[80:83], v[160:163], a[84:87], v[80:83]\n"
"s_nop 0\n"
"ds_read_b64_tr_b16 v[156:157], v189 offset:9216\n"
"ds_read_b64_tr_b16 v[158:159], v189 offset:13824\n"
"v_mfma_f32_16x16x32_bf16 v[80:83], v[164:167], a[100:103], v[80:83]\n"
"v_mfma_f32_16x16x32_bf16 v[116:119], v[160:163], a[88:91], v[116:119]\n"
"v_mfma_f32_16x16x32_bf16 v[116:119], v[164:167], a[104:107], v[116:119]\n"
"s_waitcnt lgkmcnt(4)\n"
"v_mfma_f32_16x16x32_bf16 v[12:15], v[144:147], a[76:79], v[12:15]\n"
"buffer_load_dwordx4 a[180:183], v186, s[60:63], s80 offen offset:2048 sc1\n"
"v_mfma_f32_16x16x32_bf16 v[12:15], v[148:151], a[92:95], v[12:15]\n"
"s_nop 0\n"
"ds_read_b64_tr_b16 v[160:161], v189 offset:32\n"
"ds_read_b64_tr_b16 v[162:163], v189 offset:4640\n"
"v_mfma_f32_16x16x32_bf16 v[48:51], v[144:147], a[80:83], v[48:51]\n"
"v_mfma_f32_16x16x32_bf16 v[48:51], v[148:151], a[96:99], v[48:51]\n"
"s_nop 0\n"
"ds_read_b64_tr_b16 v[164:165], v189 offset:9248\n"
"ds_read_b64_tr_b16 v[166:167], v189 offset:13856\n"
"v_mfma_f32_16x16x32_bf16 v[84:87], v[144:147], a[84:87], v[84:87]\n"
"v_mfma_f32_16x16x32_bf16 v[84:87], v[148:151], a[100:103], v[84:87]\n"
"buffer_load_dwordx4 a[184:187], v186, s[60:63], s81 offen offset:2048 sc1\n"
"v_mfma_f32_16x16x32_bf16 v[120:123], v[144:147], a[88:91], v[120:123]\n"
"v_mfma_f32_16x16x32_bf16 v[120:123], v[148:151], a[104:107], v[120:123]\n"
"s_waitcnt vmcnt(12) lgkmcnt(4)\n"
"v_mfma_f32_16x16x32_bf16 v[0:3], v[152:155], a[108:111], v[0:3]\n"
"s_nop 0\n"
"ds_read_b64_tr_b16 v[144:145], v189 offset:64\n"
"ds_read_b64_tr_b16 v[146:147], v189 offset:4672\n"
"v_mfma_f32_16x16x32_bf16 v[0:3], v[156:159], a[124:127], v[0:3]\n"
"v_mfma_f32_16x16x32_bf16 v[36:39], v[152:155], a[112:115], v[36:39]\n"
"buffer_load_dwordx4 a[188:191], v186, s[60:63], s78 offen offset:3072 sc1\n"
"v_mfma_f32_16x16x32_bf16 v[36:39], v[156:159], a[128:131], v[36:39]\n"
"s_nop 0\n"
"ds_read_b64_tr_b16 v[148:149], v189 offset:9280\n"
"ds_read_b64_tr_b16 v[150:151], v189 offset:13888\n"
"v_mfma_f32_16x16x32_bf16 v[72:75], v[152:155], a[116:119], v[72:75]\n"
"v_mfma_f32_16x16x32_bf16 v[72:75], v[156:159], a[132:135], v[72:75]\n"
"v_mfma_f32_16x16x32_bf16 v[108:111], v[152:155], a[120:123], v[108:111]\n"
"v_mfma_f32_16x16x32_bf16 v[108:111], v[156:159], a[136:139], v[108:111]\n"
"s_waitcnt lgkmcnt(4)\n"
"v_mfma_f32_16x16x32_bf16 v[4:7], v[160:163], a[108:111], v[4:7]\n"
"buffer_load_dwordx4 a[192:195], v186, s[60:63], s79 offen offset:3072 sc1\n"
"v_mfma_f32_16x16x32_bf16 v[4:7], v[164:167], a[124:127], v[4:7]\n"
"s_nop 0\n"
"ds_read_b64_tr_b16 v[152:153], v189 offset:96\n"
"ds_read_b64_tr_b16 v[154:155], v189 offset:4704\n"
"v_mfma_f32_16x16x32_bf16 v[40:43], v[160:163], a[112:115], v[40:43]\n"
"v_mfma_f32_16x16x32_bf16 v[40:43], v[164:167], a[128:131], v[40:43]\n"
"s_nop 0\n"
"ds_read_b64_tr_b16 v[156:157], v189 offset:9312\n"
"ds_read_b64_tr_b16 v[158:159], v189 offset:13920\n"
"v_mfma_f32_16x16x32_bf16 v[76:79], v[160:163], a[116:119], v[76:79]\n"
"buffer_load_dwordx4 a[196:199], v186, s[60:63], s80 offen offset:3072 sc1\n"
"v_mfma_f32_16x16x32_bf16 v[76:79], v[164:167], a[132:135], v[76:79]\n"
"v_mfma_f32_16x16x32_bf16 v[112:115], v[160:163], a[120:123], v[112:115]\n"
"v_mfma_f32_16x16x32_bf16 v[112:115], v[164:167], a[136:139], v[112:115]\n"
"s_waitcnt lgkmcnt(4)\n"
"v_mfma_f32_16x16x32_bf16 v[8:11], v[144:147], a[108:111], v[8:11]\n"
"s_nop 0\n"
"ds_read_b64_tr_b16 v[160:161], v188 offset:36992\n"
"ds_read_b64_tr_b16 v[162:163], v188 offset:41600\n"
"v_mfma_f32_16x16x32_bf16 v[8:11], v[148:151], a[124:127], v[8:11]\n"
"buffer_load_dwordx4 a[200:203], v186, s[60:63], s81 offen offset:3072 sc1\n"
"v_mfma_f32_16x16x32_bf16 v[44:47], v[144:147], a[112:115], v[44:47]\n"
"s_add_u32 s78, s82, 12288\n"
"v_mfma_f32_16x16x32_bf16 v[44:47], v[148:151], a[128:131], v[44:47]\n"
"s_nop 0\n"
"ds_read_b64_tr_b16 v[164:165], v188 offset:46208\n"
"ds_read_b64_tr_b16 v[166:167], v188 offset:50816\n"
"v_mfma_f32_16x16x32_bf16 v[80:83], v[144:147], a[116:119], v[80:83]\n"
"v_mfma_f32_16x16x32_bf16 v[80:83], v[148:151], a[132:135], v[80:83]\n"
"v_mfma_f32_16x16x32_bf16 v[116:119], v[144:147], a[120:123], v[116:119]\n"
"buffer_load_dwordx4 a[204:207], v186, s[60:63], s78 offen offset:0 sc1\n"
"v_mfma_f32_16x16x32_bf16 v[116:119], v[148:151], a[136:139], v[116:119]\n"
"s_waitcnt lgkmcnt(4)\n"
"v_mfma_f32_16x16x32_bf16 v[12:15], v[152:155], a[108:111], v[12:15]\n"
"s_add_u32 s79, s83, 12288\n"
"ds_read_b64_tr_b16 v[144:145], v188 offset:37024\n"
"ds_read_b64_tr_b16 v[146:147], v188 offset:41632\n"
"v_mfma_f32_16x16x32_bf16 v[12:15], v[156:159], a[124:127], v[12:15]\n"
"v_mfma_f32_16x16x32_bf16 v[48:51], v[152:155], a[112:115], v[48:51]\n"
"v_mfma_f32_16x16x32_bf16 v[48:51], v[156:159], a[128:131], v[48:51]\n"
"buffer_load_dwordx4 a[208:211], v186, s[60:63], s79 offen offset:0 sc1\n"
"v_mfma_f32_16x16x32_bf16 v[84:87], v[152:155], a[116:119], v[84:87]\n"
"s_add_u32 s80, s84, 12288\n"
"ds_read_b64_tr_b16 v[148:149], v188 offset:46240\n"
"ds_read_b64_tr_b16 v[150:151], v188 offset:50848\n"
"v_mfma_f32_16x16x32_bf16 v[84:87], v[156:159], a[132:135], v[84:87]\n"
"v_mfma_f32_16x16x32_bf16 v[120:123], v[152:155], a[120:123], v[120:123]\n"
"v_mfma_f32_16x16x32_bf16 v[120:123], v[156:159], a[136:139], v[120:123]\n"
"s_waitcnt vmcnt(52) lgkmcnt(4)\n"
"s_barrier\n"
"v_mfma_f32_16x16x32_bf16 v[16:19], v[160:163], a[76:79], v[16:19]\n"
"buffer_load_dwordx4 a[212:215], v186, s[60:63], s80 offen offset:0 sc1\n"
"v_mfma_f32_16x16x32_bf16 v[16:19], v[164:167], a[92:95], v[16:19]\n"
"s_add_u32 s81, s85, 12288\n"
"ds_read_b64_tr_b16 v[152:153], v189 offset:18432\n"
"ds_read_b64_tr_b16 v[154:155], v189 offset:23040\n"
"v_mfma_f32_16x16x32_bf16 v[52:55], v[160:163], a[80:83], v[52:55]\n"
"v_mfma_f32_16x16x32_bf16 v[52:55], v[164:167], a[96:99], v[52:55]\n"
"s_nop 0\n"
"ds_read_b64_tr_b16 v[156:157], v189 offset:27648\n"
"ds_read_b64_tr_b16 v[158:159], v189 offset:32256\n"
"v_mfma_f32_16x16x32_bf16 v[88:91], v[160:163], a[84:87], v[88:91]\n"
"v_mfma_f32_16x16x32_bf16 v[88:91], v[164:167], a[100:103], v[88:91]\n"
"buffer_load_dwordx4 a[216:219], v186, s[60:63], s81 offen offset:0 sc1\n"
"v_mfma_f32_16x16x32_bf16 v[124:127], v[160:163], a[88:91], v[124:127]\n"
"v_mfma_f32_16x16x32_bf16 v[124:127], v[164:167], a[104:107], v[124:127]\n"
"s_waitcnt vmcnt(48) lgkmcnt(4)\n"
"v_mfma_f32_16x16x32_bf16 v[20:23], v[144:147], a[76:79], v[20:23]\n"
"s_nop 0\n"
"ds_read_b64_tr_b16 v[160:161], v189 offset:18464\n"
"ds_read_b64_tr_b16 v[162:163], v189 offset:23072\n"
"v_mfma_f32_16x16x32_bf16 v[20:23], v[148:151], a[92:95], v[20:23]\n"
"v_mfma_f32_16x16x32_bf16 v[56:59], v[144:147], a[80:83], v[56:59]\n"
"buffer_load_dwordx4 a[220:223], v186, s[60:63], s78 offen offset:1024 sc1\n"
"v_mfma_f32_16x16x32_bf16 v[56:59], v[148:151], a[96:99], v[56:59]\n"
"s_nop 0\n"
"ds_read_b64_tr_b16 v[164:165], v189 offset:27680\n"
"ds_read_b64_tr_b16 v[166:167], v189 offset:32288\n"
"v_mfma_f32_16x16x32_bf16 v[92:95], v[144:147], a[84:87], v[92:95]\n"
"v_mfma_f32_16x16x32_bf16 v[92:95], v[148:151], a[100:103], v[92:95]\n"
"v_mfma_f32_16x16x32_bf16 v[128:131], v[144:147], a[88:91], v[128:131]\n"
"v_mfma_f32_16x16x32_bf16 v[128:131], v[148:151], a[104:107], v[128:131]\n"
"s_waitcnt vmcnt(13) lgkmcnt(4)\n"
"v_mfma_f32_16x16x32_bf16 v[0:3], v[152:155], a[140:143], v[0:3]\n"
"buffer_load_dwordx4 a[224:227], v186, s[60:63], s79 offen offset:1024 sc1\n"
"v_mfma_f32_16x16x32_bf16 v[0:3], v[156:159], a[156:159], v[0:3]\n"
"s_nop 0\n"
"ds_read_b64_tr_b16 v[144:145], v189 offset:18496\n"
"ds_read_b64_tr_b16 v[146:147], v189 offset:23104\n"
"v_mfma_f32_16x16x32_bf16 v[36:39], v[152:155], a[144:147], v[36:39]\n"
"v_mfma_f32_16x16x32_bf16 v[36:39], v[156:159], a[160:163], v[36:39]\n"
"s_nop 0\n"
"ds_read_b64_tr_b16 v[148:149], v189 offset:27712\n"
"ds_read_b64_tr_b16 v[150:151], v189 offset:32320\n"
"v_mfma_f32_16x16x32_bf16 v[72:75], v[152:155], a[148:151], v[72:75]\n"
"buffer_load_dwordx4 a[228:231], v186, s[60:63], s80 offen offset:1024 sc1\n"
"v_mfma_f32_16x16x32_bf16 v[72:75], v[156:159], a[164:167], v[72:75]\n"
"v_mfma_f32_16x16x32_bf16 v[108:111], v[152:155], a[152:155], v[108:111]\n"
"v_mfma_f32_16x16x32_bf16 v[108:111], v[156:159], a[168:171], v[108:111]\n"
"s_waitcnt lgkmcnt(4)\n"
"v_mfma_f32_16x16x32_bf16 v[4:7], v[160:163], a[140:143], v[4:7]\n"
"s_nop 0\n"
"ds_read_b64_tr_b16 v[152:153], v189 offset:18528\n"
"ds_read_b64_tr_b16 v[154:155], v189 offset:23136\n"
"v_mfma_f32_16x16x32_bf16 v[4:7], v[164:167], a[156:159], v[4:7]\n"
"buffer_load_dwordx4 a[232:235], v186, s[60:63], s81 offen offset:1024 sc1\n"
"v_mfma_f32_16x16x32_bf16 v[40:43], v[160:163], a[144:147], v[40:43]\n"
"s_add_u32 m0, s71, 0\n"
"v_mfma_f32_16x16x32_bf16 v[40:43], v[164:167], a[160:163], v[40:43]\n"
"s_mov_b32 s77, 4718592\n"
"ds_read_b64_tr_b16 v[156:157], v189 offset:27744\n"
"ds_read_b64_tr_b16 v[158:159], v189 offset:32352\n"
"v_mfma_f32_16x16x32_bf16 v[76:79], v[160:163], a[148:151], v[76:79]\n"
"v_mfma_f32_16x16x32_bf16 v[76:79], v[164:167], a[164:167], v[76:79]\n"
"v_mfma_f32_16x16x32_bf16 v[112:115], v[160:163], a[152:155], v[112:115]\n"
"buffer_load_dwordx4 v177, s[48:51], s77 offen lds\n"
"v_mfma_f32_16x16x32_bf16 v[112:115], v[164:167], a[168:171], v[112:115]\n"
"s_waitcnt lgkmcnt(4)\n"
"v_mfma_f32_16x16x32_bf16 v[8:11], v[144:147], a[140:143], v[8:11]\n"
"s_add_u32 m0, s71, 4096\n"
"ds_read_b64_tr_b16 v[160:161], v188 offset:37056\n"
"ds_read_b64_tr_b16 v[162:163], v188 offset:41664\n"
"v_mfma_f32_16x16x32_bf16 v[8:11], v[148:151], a[156:159], v[8:11]\n"
"v_mfma_f32_16x16x32_bf16 v[44:47], v[144:147], a[144:147], v[44:47]\n"
"v_mfma_f32_16x16x32_bf16 v[44:47], v[148:151], a[160:163], v[44:47]\n"
"buffer_load_dwordx4 v178, s[48:51], s77 offen lds\n"
"v_mfma_f32_16x16x32_bf16 v[80:83], v[144:147], a[148:151], v[80:83]\n"
"s_add_u32 m0, s71, 8192\n"
"ds_read_b64_tr_b16 v[164:165], v188 offset:46272\n"
"ds_read_b64_tr_b16 v[166:167], v188 offset:50880\n"
"v_mfma_f32_16x16x32_bf16 v[80:83], v[148:151], a[164:167], v[80:83]\n"
"v_mfma_f32_16x16x32_bf16 v[116:119], v[144:147], a[152:155], v[116:119]\n"
"v_mfma_f32_16x16x32_bf16 v[116:119], v[148:151], a[168:171], v[116:119]\n"
"s_waitcnt lgkmcnt(4)\n"
"v_mfma_f32_16x16x32_bf16 v[12:15], v[152:155], a[140:143], v[12:15]\n"
"buffer_load_dwordx4 v179, s[48:51], s77 offen lds\n"
"v_mfma_f32_16x16x32_bf16 v[12:15], v[156:159], a[156:159], v[12:15]\n"
"s_add_u32 m0, s71, 12288\n"
"ds_read_b64_tr_b16 v[144:145], v189 offset:36864\n"
"ds_read_b64_tr_b16 v[146:147], v189 offset:41472\n"
"v_mfma_f32_16x16x32_bf16 v[48:51], v[152:155], a[144:147], v[48:51]\n"
"v_mfma_f32_16x16x32_bf16 v[48:51], v[156:159], a[160:163], v[48:51]\n"
"L_tc2_1:\n"
"s_cbranch_execz L_tp2_1_1\n"
"ds_read_b64_tr_b16 v[148:149], v189 offset:46080\n"
"ds_read_b64_tr_b16 v[150:151], v189 offset:50688\n"
"v_mfma_f32_16x16x32_bf16 v[84:87], v[152:155], a[148:151], v[84:87]\n"
"v_mfma_f32_16x16x32_bf16 v[84:87], v[156:159], a[164:167], v[84:87]\n"
"buffer_load_dwordx4 v180, s[48:51], s77 offen lds\n"
"v_mfma_f32_16x16x32_bf16 v[120:123], v[152:155], a[152:155], v[120:123]\n"
"s_add_u32 m0, s71, 16384\n"
"v_mfma_f32_16x16x32_bf16 v[120:123], v[156:159], a[168:171], v[120:123]\n"
"s_waitcnt lgkmcnt(4)\n"
"v_mfma_f32_16x16x32_bf16 v[24:27], v[160:163], a[76:79], v[24:27]\n"
"s_nop 0\n"
"ds_read_b64_tr_b16 v[152:153], v189 offset:36896\n"
"ds_read_b64_tr_b16 v[154:155], v189 offset:41504\n"
"v_mfma_f32_16x16x32_bf16 v[24:27], v[164:167], a[92:95], v[24:27]\n"
"v_mfma_f32_16x16x32_bf16 v[60:63], v[160:163], a[80:83], v[60:63]\n"
"buffer_load_dwordx4 v181, s[48:51], s77 offen lds\n"
"v_mfma_f32_16x16x32_bf16 v[60:63], v[164:167], a[96:99], v[60:63]\n"
"s_add_u32 m0, s71, 20480\n"
"ds_read_b64_tr_b16 v[156:157], v189 offset:46112\n"
"ds_read_b64_tr_b16 v[158:159], v189 offset:50720\n"
"v_mfma_f32_16x16x32_bf16 v[96:99], v[160:163], a[84:87], v[96:99]\n"
"v_mfma_f32_16x16x32_bf16 v[96:99], v[164:167], a[100:103], v[96:99]\n"
"v_mfma_f32_16x16x32_bf16 v[132:135], v[160:163], a[88:91], v[132:135]\n"
"v_mfma_f32_16x16x32_bf16 v[132:135], v[164:167], a[104:107], v[132:135]\n"
"s_waitcnt vmcnt(13) lgkmcnt(4)\n"
"v_mfma_f32_16x16x32_bf16 v[0:3], v[144:147], a[172:175], v[0:3]\n"
"buffer_load_dwordx4 v182, s[48:51], s77 offen lds\n"
"v_mfma_f32_16x16x32_bf16 v[0:3], v[148:151], a[188:191], v[0:3]\n"
"s_nop 0\n"
"ds_read_b64_tr_b16 v[160:161], v189 offset:36928\n"
"ds_read_b64_tr_b16 v[162:163], v189 offset:41536\n"
"v_mfma_f32_16x16x32_bf16 v[36:39], v[144:147], a[176:179], v[36:39]\n"
"v_mfma_f32_16x16x32_bf16 v[36:39], v[148:151], a[192:195], v[36:39]\n"
"s_nop 0\n"
"ds_read_b64_tr_b16 v[164:165], v189 offset:46144\n"
"ds_read_b64_tr_b16 v[166:167], v189 offset:50752\n"
"v_mfma_f32_16x16x32_bf16 v[72:75], v[144:147], a[180:183], v[72:75]\n"
"buffer_load_dwordx4 a[12:15], v186, s[60:63], s78 offen offset:2048 sc1\n"
"v_mfma_f32_16x16x32_bf16 v[72:75], v[148:151], a[196:199], v[72:75]\n"
"v_mfma_f32_16x16x32_bf16 v[108:111], v[144:147], a[184:187], v[108:111]\n"
"v_mfma_f32_16x16x32_bf16 v[108:111], v[148:151], a[200:203], v[108:111]\n"
"s_waitcnt lgkmcnt(4)\n"
"v_mfma_f32_16x16x32_bf16 v[4:7], v[152:155], a[172:175], v[4:7]\n"
"s_nop 0\n"
"ds_read_b64_tr_b16 v[144:145], v189 offset:36960\n"
"ds_read_b64_tr_b16 v[146:147], v189 offset:41568\n"
"v_mfma_f32_16x16x32_bf16 v[4:7], v[156:159], a[188:191], v[4:7]\n"
"buffer_load_dwordx4 a[16:19], v186, s[60:63], s79 offen offset:2048 sc1\n"
"v_mfma_f32_16x16x32_bf16 v[40:43], v[152:155], a[176:179], v[40:43]\n"
"v_mfma_f32_16x16x32_bf16 v[40:43], v[156:159], a[192:195], v[40:43]\n"
"s_nop 0\n"
"ds_read_b64_tr_b16 v[148:149], v189 offset:46176\n"
"ds_read_b64_tr_b16 v[150:151], v189 offset:50784\n"
"v_mfma_f32_16x16x32_bf16 v[76:79], v[152:155], a[180:183], v[76:79]\n"
"v_mfma_f32_16x16x32_bf16 v[76:79], v[156:159], a[196:199], v[76:79]\n"
"v_mfma_f32_16x16x32_bf16 v[112:115], v[152:155], a[184:187], v[112:115]\n"
"buffer_load_dwordx4 a[20:23], v186, s[60:63], s80 offen offset:2048 sc1\n"
"v_mfma_f32_16x16x32_bf16 v[112:115], v[156:159], a[200:203], v[112:115]\n"
"s_waitcnt lgkmcnt(4)\n"
"v_mfma_f32_16x16x32_bf16 v[8:11], v[160:163], a[172:175], v[8:11]\n"
"s_nop 0\n"
"ds_read_b64_tr_b16 v[152:153], v188 offset:37088\n"
"ds_read_b64_tr_b16 v[154:155], v188 offset:41696\n"
"v_mfma_f32_16x16x32_bf16 v[8:11], v[164:167], a[188:191], v[8:11]\n"
"v_mfma_f32_16x16x32_bf16 v[44:47], v[160:163], a[176:179], v[44:47]\n"
"v_mfma_f32_16x16x32_bf16 v[44:47], v[164:167], a[192:195], v[44:47]\n"
"buffer_load_dwordx4 a[24:27], v186, s[60:63], s81 offen offset:2048 sc1\n"
"v_mfma_f32_16x16x32_bf16 v[80:83], v[160:163], a[180:183], v[80:83]\n"
"s_nop 0\n"
"ds_read_b64_tr_b16 v[156:157], v188 offset:46304\n"
"ds_read_b64_tr_b16 v[158:159], v188 offset:50912\n"
"v_mfma_f32_16x16x32_bf16 v[80:83], v[164:167], a[196:199], v[80:83]\n"
"v_mfma_f32_16x16x32_bf16 v[116:119], v[160:163], a[184:187], v[116:119]\n"
"v_mfma_f32_16x16x32_bf16 v[116:119], v[164:167], a[200:203], v[116:119]\n"
"s_waitcnt vmcnt(52) lgkmcnt(4)\n"
"s_barrier\n"
"v_mfma_f32_16x16x32_bf16 v[12:15], v[144:147], a[172:175], v[12:15]\n"
"buffer_load_dwordx4 a[28:31], v186, s[60:63], s78 offen offset:3072 sc1\n"
"v_mfma_f32_16x16x32_bf16 v[12:15], v[148:151], a[188:191], v[12:15]\n"
"s_nop 0\n"
"ds_read_b64_tr_b16 v[160:161], v190 offset:0\n"
"ds_read_b64_tr_b16 v[162:163], v190 offset:4608\n"
"v_mfma_f32_16x16x32_bf16 v[48:51], v[144:147], a[176:179], v[48:51]\n"
"v_mfma_f32_16x16x32_bf16 v[48:51], v[148:151], a[192:195], v[48:51]\n"
"s_nop 0\n"
"ds_read_b64_tr_b16 v[164:165], v190 offset:9216\n"
"ds_read_b64_tr_b16 v[166:167], v190 offset:13824\n"
"v_mfma_f32_16x16x32_bf16 v[84:87], v[144:147], a[180:183], v[84:87]\n"
"v_mfma_f32_16x16x32_bf16 v[84:87], v[148:151], a[196:199], v[84:87]\n"
"buffer_load_dwordx4 a[32:35], v186, s[60:63], s79 offen offset:3072 sc1\n"
"v_mfma_f32_16x16x32_bf16 v[120:123], v[144:147], a[184:187], v[120:123]\n"
"v_mfma_f32_16x16x32_bf16 v[120:123], v[148:151], a[200:203], v[120:123]\n"
"s_waitcnt vmcnt(48) lgkmcnt(4)\n"
"v_mfma_f32_16x16x32_bf16 v[28:31], v[152:155], a[76:79], v[28:31]\n"
"s_nop 0\n"
"ds_read_b64_tr_b16 v[144:145], v190 offset:32\n"
"ds_read_b64_tr_b16 v[146:147], v190 offset:4640\n"
"v_mfma_f32_16x16x32_bf16 v[28:31], v[156:159], a[92:95], v[28:31]\n"
"v_mfma_f32_16x16x32_bf16 v[64:67], v[152:155], a[80:83], v[64:67]\n"
"buffer_load_dwordx4 a[36:39], v186, s[60:63], s80 offen offset:3072 sc1\n"
"v_mfma_f32_16x16x32_bf16 v[64:67], v[156:159], a[96:99], v[64:67]\n"
"s_nop 0\n"
"ds_read_b64_tr_b16 v[148:149], v190 offset:9248\n"
"ds_read_b64_tr_b16 v[150:151], v190 offset:13856\n"
"v_mfma_f32_16x16x32_bf16 v[100:103], v[152:155], a[84:87], v[100:103]\n"
"v_mfma_f32_16x16x32_bf16 v[100:103], v[156:159], a[100:103], v[100:103]\n"
"v_mfma_f32_16x16x32_bf16 v[136:139], v[152:155], a[88:91], v[136:139]\n"
"v_mfma_f32_16x16x32_bf16 v[136:139], v[156:159], a[104:107], v[136:139]\n"
"s_waitcnt vmcnt(13) lgkmcnt(4)\n"
"v_mfma_f32_16x16x32_bf16 v[0:3], v[160:163], a[204:207], v[0:3]\n"
"buffer_load_dwordx4 a[40:43], v186, s[60:63], s81 offen offset:3072 sc1\n"
"v_mfma_f32_16x16x32_bf16 v[0:3], v[164:167], a[220:223], v[0:3]\n"
"s_add_u32 m0, s71, 24576\n"
"ds_read_b64_tr_b16 v[152:153], v190 offset:64\n"
"ds_read_b64_tr_b16 v[154:155], v190 offset:4672\n"
"v_mfma_f32_16x16x32_bf16 v[36:39], v[160:163], a[208:211], v[36:39]\n"
"v_mfma_f32_16x16x32_bf16 v[36:39], v[164:167], a[224:227], v[36:39]\n"
"s_nop 0\n"
"ds_read_b64_tr_b16 v[156:157], v190 offset:9280\n"
"ds_read_b64_tr_b16 v[158:159], v190 offset:13888\n"
"v_mfma_f32_16x16x32_bf16 v[72:75], v[160:163], a[212:215], v[72:75]\n"
"buffer_load_dwordx4 v183, s[48:51], s77 offen lds\n"
"v_mfma_f32_16x16x32_bf16 v[72:75], v[164:167], a[228:231], v[72:75]\n"
"s_add_u32 m0, s71, 28672\n"
"v_mfma_f32_16x16x32_bf16 v[108:111], v[160:163], a[216:219], v[108:111]\n"
"v_mfma_f32_16x16x32_bf16 v[108:111], v[164:167], a[232:235], v[108:111]\n"
"s_waitcnt lgkmcnt(4)\n"
"v_mfma_f32_16x16x32_bf16 v[4:7], v[144:147], a[204:207], v[4:7]\n"
"s_nop 0\n"
"ds_read_b64_tr_b16 v[160:161], v190 offset:96\n"
"ds_read_b64_tr_b16 v[162:163], v190 offset:4704\n"
"v_mfma_f32_16x16x32_bf16 v[4:7], v[148:151], a[220:223], v[4:7]\n"
"buffer_load_dwordx4 v184, s[48:51], s77 offen lds\n"
"v_mfma_f32_16x16x32_bf16 v[40:43], v[144:147], a[208:211], v[40:43]\n"
"s_add_u32 m0, s71, 32768\n"
"v_mfma_f32_16x16x32_bf16 v[40:43], v[148:151], a[224:227], v[40:43]\n"
"s_nop 0\n"
"ds_read_b64_tr_b16 v[164:165], v190 offset:9312\n"
"ds_read_b64_tr_b16 v[166:167], v190 offset:13920\n"
"v_mfma_f32_16x16x32_bf16 v[76:79], v[144:147], a[212:215], v[76:79]\n"
"v_mfma_f32_16x16x32_bf16 v[76:79], v[148:151], a[228:231], v[76:79]\n"
"v_mfma_f32_16x16x32_bf16 v[112:115], v[144:147], a[216:219], v[112:115]\n"
"buffer_load_dwordx4 v185, s[48:51], s77 offen lds\n"
"v_mfma_f32_16x16x32_bf16 v[112:115], v[148:151], a[232:235], v[112:115]\n"
"s_waitcnt lgkmcnt(4)\n"
"v_mfma_f32_16x16x32_bf16 v[8:11], v[152:155], a[204:207], v[8:11]\n"
"s_add_u32 s78, s82, 16384\n"
"ds_read_b64_tr_b16 v[144:145], v191 offset:36864\n"
"ds_read_b64_tr_b16 v[146:147], v191 offset:41472\n"
"v_mfma_f32_16x16x32_bf16 v[8:11], v[156:159], a[220:223], v[8:11]\n"
"v_mfma_f32_16x16x32_bf16 v[44:47], v[152:155], a[208:211], v[44:47]\n"
"v_mfma_f32_16x16x32_bf16 v[44:47], v[156:159], a[224:227], v[44:47]\n"
"buffer_load_dwordx4 a[44:47], v186, s[60:63], s78 offen offset:0 sc1\n"
"v_mfma_f32_16x16x32_bf16 v[80:83], v[152:155], a[212:215], v[80:83]\n"
"s_add_u32 s79, s83, 16384\n"
"ds_read_b64_tr_b16 v[148:149], v191 offset:46080\n"
"ds_read_b64_tr_b16 v[150:151], v191 offset:50688\n"
"v_mfma_f32_16x16x32_bf16 v[80:83], v[156:159], a[228:231], v[80:83]\n"
"v_mfma_f32_16x16x32_bf16 v[116:119], v[152:155], a[216:219], v[116:119]\n"
"v_mfma_f32_16x16x32_bf16 v[116:119], v[156:159], a[232:235], v[116:119]\n"
"s_waitcnt lgkmcnt(4)\n"
"v_mfma_f32_16x16x32_bf16 v[12:15], v[160:163], a[204:207], v[12:15]\n"
"buffer_load_dwordx4 a[48:51], v186, s[60:63], s79 offen offset:0 sc1\n"
"v_mfma_f32_16x16x32_bf16 v[12:15], v[164:167], a[220:223], v[12:15]\n"
"s_add_u32 s80, s84, 16384\n"
"ds_read_b64_tr_b16 v[152:153], v189 offset:128\n"
"ds_read_b64_tr_b16 v[154:155], v189 offset:4736\n"
"v_mfma_f32_16x16x32_bf16 v[48:51], v[160:163], a[208:211], v[48:51]\n"
"v_mfma_f32_16x16x32_bf16 v[48:51], v[164:167], a[224:227], v[48:51]\n"
"s_nop 0\n"
"ds_read_b64_tr_b16 v[156:157], v189 offset:9344\n"
"ds_read_b64_tr_b16 v[158:159], v189 offset:13952\n"
"v_mfma_f32_16x16x32_bf16 v[84:87], v[160:163], a[212:215], v[84:87]\n"
"v_mfma_f32_16x16x32_bf16 v[84:87], v[164:167], a[228:231], v[84:87]\n"
"buffer_load_dwordx4 a[52:55], v186, s[60:63], s80 offen offset:0 sc1\n"
"v_mfma_f32_16x16x32_bf16 v[120:123], v[160:163], a[216:219], v[120:123]\n"
"s_add_u32 s81, s85, 16384\n"
"v_mfma_f32_16x16x32_bf16 v[120:123], v[164:167], a[232:235], v[120:123]\n"
"s_waitcnt lgkmcnt(4)\n"
"v_mfma_f32_16x16x32_bf16 v[32:35], v[144:147], a[76:79], v[32:35]\n"
"s_nop 0\n"
"ds_read_b64_tr_b16 v[160:161], v190 offset:18432\n"
"ds_read_b64_tr_b16 v[162:163], v190 offset:23040\n"
"v_mfma_f32_16x16x32_bf16 v[32:35], v[148:151], a[92:95], v[32:35]\n"
"v_mfma_f32_16x16x32_bf16 v[68:71], v[144:147], a[80:83], v[68:71]\n"
"buffer_load_dwordx4 a[56:59], v186, s[60:63], s81 offen offset:0 sc1\n"
"v_mfma_f32_16x16x32_bf16 v[68:71], v[148:151], a[96:99], v[68:71]\n"
"s_nop 0\n"
"ds_read_b64_tr_b16 v[164:165], v190 offset:27648\n"
"ds_read_b64_tr_b16 v[166:167], v190 offset:32256\n"
"v_mfma_f32_16x16x32_bf16 v[104:107], v[144:147], a[84:87], v[104:107]\n"
"v_mfma_f32_16x16x32_bf16 v[104:107], v[148:151], a[100:103], v[104:107]\n"
"v_mfma_f32_16x16x32_bf16 v[140:143], v[144:147], a[88:91], v[140:143]\n"
"v_mfma_f32_16x16x32_bf16 v[140:143], v[148:151], a[104:107], v[140:143]\n"
"s_waitcnt lgkmcnt(4)\n"
"v_mfma_f32_16x16x32_bf16 v[16:19], v[152:155], a[108:111], v[16:19]\n"
"buffer_load_dwordx4 a[60:63], v186, s[60:63], s78 offen offset:1024 sc1\n"
"v_mfma_f32_16x16x32_bf16 v[16:19], v[156:159], a[124:127], v[16:19]\n"
"s_nop 0\n"
"ds_read_b64_tr_b16 v[144:145], v190 offset:18464\n"
"ds_read_b64_tr_b16 v[146:147], v190 offset:23072\n"
"v_mfma_f32_16x16x32_bf16 v[52:55], v[152:155], a[112:115], v[52:55]\n"
"v_mfma_f32_16x16x32_bf16 v[52:55], v[156:159], a[128:131], v[52:55]\n"
"s_nop 0\n"
"ds_read_b64_tr_b16 v[148:149], v190 offset:27680\n"
"ds_read_b64_tr_b16 v[150:151], v190 offset:32288\n"
"v_mfma_f32_16x16x32_bf16 v[88:91], v[152:155], a[116:119], v[88:91]\n"
"buffer_load_dwordx4 a[64:67], v186, s[60:63], s79 offen offset:1024 sc1\n"
"v_mfma_f32_16x16x32_bf16 v[88:91], v[156:159], a[132:135], v[88:91]\n"
"v_mfma_f32_16x16x32_bf16 v[124:127], v[152:155], a[120:123], v[124:127]\n"
"v_mfma_f32_16x16x32_bf16 v[124:127], v[156:159], a[136:139], v[124:127]\n"
"s_waitcnt vmcnt(9) lgkmcnt(4)\n"
"v_mfma_f32_16x16x32_bf16 v[0:3], v[160:163], a[12:15], v[0:3]\n"
"s_nop 0\n"
"ds_read_b64_tr_b16 v[152:153], v190 offset:18496\n"
"ds_read_b64_tr_b16 v[154:155], v190 offset:23104\n"
"v_mfma_f32_16x16x32_bf16 v[0:3], v[164:167], a[28:31], v[0:3]\n"
"buffer_load_dwordx4 a[68:71], v186, s[60:63], s80 offen offset:1024 sc1\n"
"v_mfma_f32_16x16x32_bf16 v[36:39], v[160:163], a[16:19], v[36:39]\n"
"v_mfma_f32_16x16x32_bf16 v[36:39], v[164:167], a[32:35], v[36:39]\n"
"s_nop 0\n"
"ds_read_b64_tr_b16 v[156:157], v190 offset:27712\n"
"ds_read_b64_tr_b16 v[158:159], v190 offset:32320\n"
"v_mfma_f32_16x16x32_bf16 v[72:75], v[160:163], a[20:23], v[72:75]\n"
"v_mfma_f32_16x16x32_bf16 v[72:75], v[164:167], a[36:39], v[72:75]\n"
"v_mfma_f32_16x16x32_bf16 v[108:111], v[160:163], a[24:27], v[108:111]\n"
"buffer_load_dwordx4 a[72:75], v186, s[60:63], s81 offen offset:1024 sc1\n"
"v_mfma_f32_16x16x32_bf16 v[108:111], v[164:167], a[40:43], v[108:111]\n"
"s_waitcnt lgkmcnt(4)\n"
"v_mfma_f32_16x16x32_bf16 v[4:7], v[144:147], a[12:15], v[4:7]\n"
"s_nop 0\n"
"ds_read_b64_tr_b16 v[160:161], v190 offset:18528\n"
"ds_read_b64_tr_b16 v[162:163], v190 offset:23136\n"
"v_mfma_f32_16x16x32_bf16 v[4:7], v[148:151], a[28:31], v[4:7]\n"
"v_mfma_f32_16x16x32_bf16 v[40:43], v[144:147], a[16:19], v[40:43]\n"
"v_mfma_f32_16x16x32_bf16 v[40:43], v[148:151], a[32:35], v[40:43]\n"
"s_nop 0\n"
"ds_read_b64_tr_b16 v[164:165], v190 offset:27744\n"
"ds_read_b64_tr_b16 v[166:167], v190 offset:32352\n"
"v_mfma_f32_16x16x32_bf16 v[76:79], v[144:147], a[20:23], v[76:79]\n"
"v_mfma_f32_16x16x32_bf16 v[76:79], v[148:151], a[36:39], v[76:79]\n"
"v_mfma_f32_16x16x32_bf16 v[112:115], v[144:147], a[24:27], v[112:115]\n"
"v_mfma_f32_16x16x32_bf16 v[112:115], v[148:151], a[40:43], v[112:115]\n"
"s_waitcnt lgkmcnt(4)\n"
"v_mfma_f32_16x16x32_bf16 v[8:11], v[152:155], a[12:15], v[8:11]\n"
"s_nop 0\n"
"ds_read_b64_tr_b16 v[144:145], v189 offset:160\n"
"ds_read_b64_tr_b16 v[146:147], v189 offset:4768\n"
"v_mfma_f32_16x16x32_bf16 v[8:11], v[156:159], a[28:31], v[8:11]\n"
"v_mfma_f32_16x16x32_bf16 v[44:47], v[152:155], a[16:19], v[44:47]\n"
"v_mfma_f32_16x16x32_bf16 v[44:47], v[156:159], a[32:35], v[44:47]\n"
"s_nop 0\n"
"ds_read_b64_tr_b16 v[148:149], v189 offset:9376\n"
"ds_read_b64_tr_b16 v[150:151], v189 offset:13984\n"
"v_mfma_f32_16x16x32_bf16 v[80:83], v[152:155], a[20:23], v[80:83]\n"
"v_mfma_f32_16x16x32_bf16 v[80:83], v[156:159], a[36:39], v[80:83]\n"
"v_mfma_f32_16x16x32_bf16 v[116:119], v[152:155], a[24:27], v[116:119]\n"
"v_mfma_f32_16x16x32_bf16 v[116:119], v[156:159], a[40:43], v[116:119]\n"
"s_waitcnt vmcnt(8) lgkmcnt(4)\n"
"s_barrier\n"
"v_mfma_f32_16x16x32_bf16 v[12:15], v[160:163], a[12:15], v[12:15]\n"
"s_nop 0\n"
"ds_read_b64_tr_b16 v[152:153], v188 offset:0\n"
"ds_read_b64_tr_b16 v[154:155], v188 offset:4608\n"
"v_mfma_f32_16x16x32_bf16 v[12:15], v[164:167], a[28:31], v[12:15]\n"
"v_mfma_f32_16x16x32_bf16 v[48:51], v[160:163], a[16:19], v[48:51]\n"
"v_mfma_f32_16x16x32_bf16 v[48:51], v[164:167], a[32:35], v[48:51]\n"
"s_nop 0\n"
"ds_read_b64_tr_b16 v[156:157], v188 offset:9216\n"
"ds_read_b64_tr_b16 v[158:159], v188 offset:13824\n"
"v_mfma_f32_16x16x32_bf16 v[84:87], v[160:163], a[20:23], v[84:87]\n"
"v_mfma_f32_16x16x32_bf16 v[84:87], v[164:167], a[36:39], v[84:87]\n"
"v_mfma_f32_16x16x32_bf16 v[120:123], v[160:163], a[24:27], v[120:123]\n"
"v_mfma_f32_16x16x32_bf16 v[120:123], v[164:167], a[40:43], v[120:123]\n"
"s_waitcnt lgkmcnt(4)\n"
"v_mfma_f32_16x16x32_bf16 v[20:23], v[144:147], a[108:111], v[20:23]\n"
"s_nop 0\n"
"ds_read_b64_tr_b16 v[160:161], v188 offset:32\n"
"ds_read_b64_tr_b16 v[162:163], v188 offset:4640\n"
"v_mfma_f32_16x16x32_bf16 v[20:23], v[148:151], a[124:127], v[20:23]\n"
"v_mfma_f32_16x16x32_bf16 v[56:59], v[144:147], a[112:115], v[56:59]\n"
"v_mfma_f32_16x16x32_bf16 v[56:59], v[148:151], a[128:131], v[56:59]\n"
"s_nop 0\n"
"ds_read_b64_tr_b16 v[164:165], v188 offset:9248\n"
"ds_read_b64_tr_b16 v[166:167], v188 offset:13856\n"
"v_mfma_f32_16x16x32_bf16 v[92:95], v[144:147], a[116:119], v[92:95]\n"
"v_mfma_f32_16x16x32_bf16 v[92:95], v[148:151], a[132:135], v[92:95]\n"
"v_mfma_f32_16x16x32_bf16 v[128:131], v[144:147], a[120:123], v[128:131]\n"
"v_mfma_f32_16x16x32_bf16 v[128:131], v[148:151], a[136:139], v[128:131]\n"
"s_waitcnt vmcnt(0) lgkmcnt(4)\n"
"v_mfma_f32_16x16x32_bf16 v[0:3], v[152:155], a[44:47], v[0:3]\n"
"s_nop 0\n"
"ds_read_b64_tr_b16 v[144:145], v188 offset:64\n"
"ds_read_b64_tr_b16 v[146:147], v188 offset:4672\n"
"v_mfma_f32_16x16x32_bf16 v[0:3], v[156:159], a[60:63], v[0:3]\n"
"v_mfma_f32_16x16x32_bf16 v[36:39], v[152:155], a[48:51], v[36:39]\n"
"v_mfma_f32_16x16x32_bf16 v[36:39], v[156:159], a[64:67], v[36:39]\n"
"s_nop 0\n"
"ds_read_b64_tr_b16 v[148:149], v188 offset:9280\n"
"ds_read_b64_tr_b16 v[150:151], v188 offset:13888\n"
"v_mfma_f32_16x16x32_bf16 v[72:75], v[152:155], a[52:55], v[72:75]\n"
"v_mfma_f32_16x16x32_bf16 v[72:75], v[156:159], a[68:71], v[72:75]\n"
"v_mfma_f32_16x16x32_bf16 v[108:111], v[152:155], a[56:59], v[108:111]\n"
"v_mfma_f32_16x16x32_bf16 v[108:111], v[156:159], a[72:75], v[108:111]\n"
"s_waitcnt lgkmcnt(4)\n"
"v_mfma_f32_16x16x32_bf16 v[4:7], v[160:163], a[44:47], v[4:7]\n"
"s_nop 0\n"
"ds_read_b64_tr_b16 v[152:153], v188 offset:96\n"
"ds_read_b64_tr_b16 v[154:155], v188 offset:4704\n"
"v_mfma_f32_16x16x32_bf16 v[4:7], v[164:167], a[60:63], v[4:7]\n"
"v_mfma_f32_16x16x32_bf16 v[40:43], v[160:163], a[48:51], v[40:43]\n"
"v_mfma_f32_16x16x32_bf16 v[40:43], v[164:167], a[64:67], v[40:43]\n"
"s_nop 0\n"
"ds_read_b64_tr_b16 v[156:157], v188 offset:9312\n"
"ds_read_b64_tr_b16 v[158:159], v188 offset:13920\n"
"v_mfma_f32_16x16x32_bf16 v[76:79], v[160:163], a[52:55], v[76:79]\n"
"v_mfma_f32_16x16x32_bf16 v[76:79], v[164:167], a[68:71], v[76:79]\n"
"v_mfma_f32_16x16x32_bf16 v[112:115], v[160:163], a[56:59], v[112:115]\n"
"v_mfma_f32_16x16x32_bf16 v[112:115], v[164:167], a[72:75], v[112:115]\n"
"s_waitcnt lgkmcnt(4)\n"
"v_mfma_f32_16x16x32_bf16 v[8:11], v[144:147], a[44:47], v[8:11]\n"
"s_nop 0\n"
"ds_read_b64_tr_b16 v[160:161], v189 offset:192\n"
"ds_read_b64_tr_b16 v[162:163], v189 offset:4800\n"
"v_exp_f32 v200, -v0\n"
"v_exp_f32 v204, -v4\n"
"v_exp_f32 v201, -v1\n"
"v_mfma_f32_16x16x32_bf16 v[8:11], v[148:151], a[60:63], v[8:11]\n"
"v_exp_f32 v205, -v5\n"
"v_exp_f32 v202, -v2\n"
"v_exp_f32 v206, -v6\n"
"v_exp_f32 v203, -v3\n"
"v_mfma_f32_16x16x32_bf16 v[44:47], v[144:147], a[48:51], v[44:47]\n"
"v_exp_f32 v207, -v7\n"
"v_add_f32 v200, 1.0, v200\n"
"v_add_f32 v204, 1.0, v204\n"
"v_add_f32 v201, 1.0, v201\n"
"v_add_f32 v205, 1.0, v205\n"
"v_add_f32 v202, 1.0, v202\n"
"v_add_f32 v206, 1.0, v206\n"
"v_mfma_f32_16x16x32_bf16 v[44:47], v[148:151], a[64:67], v[44:47]\n"
"s_nop 0\n"
"ds_read_b64_tr_b16 v[164:165], v189 offset:9408\n"
"ds_read_b64_tr_b16 v[166:167], v189 offset:14016\n"
"v_add_f32 v203, 1.0, v203\n"
"v_add_f32 v207, 1.0, v207\n"
"v_rcp_f32 v200, v200\n"
"v_rcp_f32 v204, v204\n"
"v_mfma_f32_16x16x32_bf16 v[80:83], v[144:147], a[52:55], v[80:83]\n"
"v_rcp_f32 v201, v201\n"
"v_rcp_f32 v205, v205\n"
"v_rcp_f32 v202, v202\n"
"v_rcp_f32 v206, v206\n"
"v_mfma_f32_16x16x32_bf16 v[80:83], v[148:151], a[68:71], v[80:83]\n"
"v_rcp_f32 v203, v203\n"
"v_rcp_f32 v207, v207\n"
"v_mul_f32 v0, v0, v200\n"
"v_mul_f32 v4, v4, v204\n"
"v_mul_f32 v1, v1, v201\n"
"v_mul_f32 v5, v5, v205\n"
"v_mfma_f32_16x16x32_bf16 v[116:119], v[144:147], a[56:59], v[116:119]\n"
"v_mul_f32 v2, v2, v202\n"
"v_mul_f32 v6, v6, v206\n"
"v_mul_f32 v3, v3, v203\n"
"v_mul_f32 v7, v7, v207\n"
"v_cvt_pk_bf16_f32 v0, v0, v1\n"
"v_cvt_pk_bf16_f32 v1, v2, v3\n"
"v_cvt_pk_bf16_f32 v2, v4, v5\n"
"v_cvt_pk_bf16_f32 v3, v6, v7\n"
"v_mfma_f32_16x16x32_bf16 v[116:119], v[148:151], a[72:75], v[116:119]\n"
"s_waitcnt lgkmcnt(4)\n"
"v_exp_f32 v200, -v36\n"
"v_exp_f32 v204, -v40\n"
"v_exp_f32 v201, -v37\n"
"v_exp_f32 v205, -v41\n"
"v_mfma_f32_16x16x32_bf16 v[12:15], v[152:155], a[44:47], v[12:15]\n"
"buffer_store_dwordx4 v[0:3], v197, s[60:63], s92 offen offset:0 sc1\n"
"v_exp_f32 v202, -v38\n"
"v_exp_f32 v206, -v42\n"
"v_exp_f32 v203, -v39\n"
"v_mfma_f32_16x16x32_bf16 v[12:15], v[156:159], a[60:63], v[12:15]\n"
"s_nop 0\n"
"ds_read_b64_tr_b16 v[144:145], v189 offset:224\n"
"ds_read_b64_tr_b16 v[146:147], v189 offset:4832\n"
"v_exp_f32 v207, -v43\n"
"v_add_f32 v200, 1.0, v200\n"
"v_add_f32 v204, 1.0, v204\n"
"v_add_f32 v201, 1.0, v201\n"
"v_add_f32 v205, 1.0, v205\n"
"v_mfma_f32_16x16x32_bf16 v[48:51], v[152:155], a[48:51], v[48:51]\n"
"v_add_f32 v202, 1.0, v202\n"
"v_add_f32 v206, 1.0, v206\n"
"v_add_f32 v203, 1.0, v203\n"
"v_add_f32 v207, 1.0, v207\n"
"v_rcp_f32 v200, v200\n"
"v_rcp_f32 v204, v204\n"
"v_mfma_f32_16x16x32_bf16 v[48:51], v[156:159], a[64:67], v[48:51]\n"
"s_nop 0\n"
"ds_read_b64_tr_b16 v[148:149], v189 offset:9440\n"
"ds_read_b64_tr_b16 v[150:151], v189 offset:14048\n"
"v_rcp_f32 v201, v201\n"
"v_rcp_f32 v205, v205\n"
"v_rcp_f32 v202, v202\n"
"v_mfma_f32_16x16x32_bf16 v[84:87], v[152:155], a[52:55], v[84:87]\n"
"v_rcp_f32 v206, v206\n"
"v_rcp_f32 v203, v203\n"
"v_rcp_f32 v207, v207\n"
"v_mul_f32 v36, v36, v200\n"
"v_mul_f32 v40, v40, v204\n"
"v_mfma_f32_16x16x32_bf16 v[84:87], v[156:159], a[68:71], v[84:87]\n"
"v_mul_f32 v37, v37, v201\n"
"v_mul_f32 v41, v41, v205\n"
"v_mul_f32 v38, v38, v202\n"
"v_mul_f32 v42, v42, v206\n"
"v_mul_f32 v39, v39, v203\n"
"v_mul_f32 v43, v43, v207\n"
"v_cvt_pk_bf16_f32 v36, v36, v37\n"
"v_cvt_pk_bf16_f32 v37, v38, v39\n"
"v_mfma_f32_16x16x32_bf16 v[120:123], v[152:155], a[56:59], v[120:123]\n"
"v_cvt_pk_bf16_f32 v38, v40, v41\n"
"v_cvt_pk_bf16_f32 v39, v42, v43\n"
"v_exp_f32 v200, -v72\n"
"v_exp_f32 v204, -v76\n"
"v_exp_f32 v201, -v73\n"
"v_mfma_f32_16x16x32_bf16 v[120:123], v[156:159], a[72:75], v[120:123]\n"
"s_waitcnt lgkmcnt(4)\n"
"v_exp_f32 v205, -v77\n"
"v_exp_f32 v202, -v74\n"
"v_exp_f32 v206, -v78\n"
"v_exp_f32 v203, -v75\n"
"v_mfma_f32_16x16x32_bf16 v[24:27], v[160:163], a[108:111], v[24:27]\n"
"buffer_store_dwordx4 v[36:39], v197, s[60:63], s86 offen offset:0 sc1\n"
"v_exp_f32 v207, -v79\n"
"v_add_f32 v200, 1.0, v200\n"
"v_add_f32 v204, 1.0, v204\n"
"v_add_f32 v201, 1.0, v201\n"
"v_add_f32 v205, 1.0, v205\n"
"v_mfma_f32_16x16x32_bf16 v[24:27], v[164:167], a[124:127], v[24:27]\n"
"s_nop 0\n"
"ds_read_b64_tr_b16 v[152:153], v192 offset:0\n"
"ds_read_b64_tr_b16 v[154:155], v192 offset:4608\n"
"v_add_f32 v202, 1.0, v202\n"
"v_add_f32 v206, 1.0, v206\n"
"v_add_f32 v203, 1.0, v203\n"
"v_add_f32 v207, 1.0, v207\n"
"v_rcp_f32 v200, v200\n"
"v_mfma_f32_16x16x32_bf16 v[60:63], v[160:163], a[112:115], v[60:63]\n"
"v_rcp_f32 v204, v204\n"
"v_rcp_f32 v201, v201\n"
"v_rcp_f32 v205, v205\n"
"v_rcp_f32 v202, v202\n"
"v_mfma_f32_16x16x32_bf16 v[60:63], v[164:167], a[128:131], v[60:63]\n"
"s_nop 0\n"
"ds_read_b64_tr_b16 v[156:157], v192 offset:9216\n"
"ds_read_b64_tr_b16 v[158:159], v192 offset:13824\n"
"v_rcp_f32 v206, v206\n"
"v_rcp_f32 v203, v203\n"
"v_rcp_f32 v207, v207\n"
"v_mfma_f32_16x16x32_bf16 v[96:99], v[160:163], a[116:119], v[96:99]\n"
"v_mul_f32 v72, v72, v200\n"
"v_mul_f32 v76, v76, v204\n"
"v_mul_f32 v73, v73, v201\n"
"v_mul_f32 v77, v77, v205\n"
"v_mul_f32 v74, v74, v202\n"
"v_mul_f32 v78, v78, v206\n"
"v_mul_f32 v75, v75, v203\n"
"v_mul_f32 v79, v79, v207\n"
"v_mfma_f32_16x16x32_bf16 v[96:99], v[164:167], a[132:135], v[96:99]\n"
"v_cvt_pk_bf16_f32 v72, v72, v73\n"
"v_cvt_pk_bf16_f32 v73, v74, v75\n"
"v_cvt_pk_bf16_f32 v74, v76, v77\n"
"v_cvt_pk_bf16_f32 v75, v78, v79\n"
"v_exp_f32 v200, -v108\n"
"v_exp_f32 v204, -v112\n"
"v_mfma_f32_16x16x32_bf16 v[132:135], v[160:163], a[120:123], v[132:135]\n"
"buffer_store_dwordx4 v[72:75], v197, s[60:63], s87 offen offset:0 sc1\n"
"v_exp_f32 v201, -v109\n"
"v_exp_f32 v205, -v113\n"
"v_exp_f32 v202, -v110\n"
"v_mfma_f32_16x16x32_bf16 v[132:135], v[164:167], a[136:139], v[132:135]\n"
"s_waitcnt lgkmcnt(4)\n"
"v_exp_f32 v206, -v114\n"
"v_exp_f32 v203, -v111\n"
"v_exp_f32 v207, -v115\n"
"v_add_f32 v200, 1.0, v200\n"
"v_add_f32 v204, 1.0, v204\n"
"v_mfma_f32_16x16x32_bf16 v[28:31], v[144:147], a[108:111], v[28:31]\n"
"s_nop 0\n"
"ds_read_b64_tr_b16 v[160:161], v189 offset:18560\n"
"ds_read_b64_tr_b16 v[162:163], v189 offset:23168\n"
"v_add_f32 v201, 1.0, v201\n"
"v_add_f32 v205, 1.0, v205\n"
"v_add_f32 v202, 1.0, v202\n"
"v_add_f32 v206, 1.0, v206\n"
"v_add_f32 v203, 1.0, v203\n"
"v_add_f32 v207, 1.0, v207\n"
"v_mfma_f32_16x16x32_bf16 v[28:31], v[148:151], a[124:127], v[28:31]\n"
"v_rcp_f32 v200, v200\n"
"v_rcp_f32 v204, v204\n"
"v_rcp_f32 v201, v201\n"
"v_rcp_f32 v205, v205\n"
"v_mfma_f32_16x16x32_bf16 v[64:67], v[144:147], a[112:115], v[64:67]\n"
"v_rcp_f32 v202, v202\n"
"v_rcp_f32 v206, v206\n"
"v_rcp_f32 v203, v203\n"
"v_rcp_f32 v207, v207\n"
"v_mfma_f32_16x16x32_bf16 v[64:67], v[148:151], a[128:131], v[64:67]\n"
"s_nop 0\n"
"ds_read_b64_tr_b16 v[164:165], v189 offset:27776\n"
"ds_read_b64_tr_b16 v[166:167], v189 offset:32384\n"
"v_mul_f32 v108, v108, v200\n"
"v_mul_f32 v112, v112, v204\n"
"v_mul_f32 v109, v109, v201\n"
"v_mul_f32 v113, v113, v205\n"
"v_mul_f32 v110, v110, v202\n"
"v_mul_f32 v114, v114, v206\n"
"v_mfma_f32_16x16x32_bf16 v[100:103], v[144:147], a[116:119], v[100:103]\n"
"v_mul_f32 v111, v111, v203\n"
"v_mul_f32 v115, v115, v207\n"
"v_cvt_pk_bf16_f32 v108, v108, v109\n"
"v_cvt_pk_bf16_f32 v109, v110, v111\n"
"v_cvt_pk_bf16_f32 v110, v112, v113\n"
"v_cvt_pk_bf16_f32 v111, v114, v115\n"
"v_exp_f32 v200, -v8\n"
"v_mfma_f32_16x16x32_bf16 v[100:103], v[148:151], a[132:135], v[100:103]\n"
"buffer_store_dwordx4 v[108:111], v197, s[60:63], s91 offen offset:0 sc1\n"
"v_exp_f32 v204, -v12\n"
"v_exp_f32 v201, -v9\n"
"v_exp_f32 v205, -v13\n"
"v_mfma_f32_16x16x32_bf16 v[136:139], v[144:147], a[120:123], v[136:139]\n"
"v_exp_f32 v202, -v10\n"
"v_exp_f32 v206, -v14\n"
"v_exp_f32 v203, -v11\n"
"v_exp_f32 v207, -v15\n"
"v_mfma_f32_16x16x32_bf16 v[136:139], v[148:151], a[136:139], v[136:139]\n"
"s_waitcnt lgkmcnt(4)\n"
"s_barrier\n"
"v_mfma_f32_16x16x32_bf16 v[32:35], v[152:155], a[108:111], v[32:35]\n"
"s_add_u32 m0, s71, 36864\n"
"ds_read_b64_tr_b16 v[144:145], v189 offset:18592\n"
"ds_read_b64_tr_b16 v[146:147], v189 offset:23200\n"
"v_add_f32 v200, 1.0, v200\n"
"v_add_f32 v204, 1.0, v204\n"
"v_add_f32 v201, 1.0, v201\n"
"v_add_f32 v205, 1.0, v205\n"
"v_add_f32 v202, 1.0, v202\n"
"v_add_f32 v206, 1.0, v206\n"
"v_mfma_f32_16x16x32_bf16 v[32:35], v[156:159], a[124:127], v[32:35]\n"
"s_mov_b32 s76, 0\n"
"v_add_f32 v203, 1.0, v203\n"
"v_add_f32 v207, 1.0, v207\n"
"v_rcp_f32 v200, v200\n"
"v_rcp_f32 v204, v204\n"
"v_rcp_f32 v201, v201\n"
"v_mfma_f32_16x16x32_bf16 v[68:71], v[152:155], a[112:115], v[68:71]\n"
"buffer_load_dwordx4 v168, s[44:47], s76 offen lds\n"
"v_rcp_f32 v205, v205\n"
"v_rcp_f32 v202, v202\n"
"v_rcp_f32 v206, v206\n"
"v_mfma_f32_16x16x32_bf16 v[68:71], v[156:159], a[128:131], v[68:71]\n"
"s_add_u32 m0, s71, 40960\n"
"ds_read_b64_tr_b16 v[148:149], v189 offset:27808\n"
"ds_read_b64_tr_b16 v[150:151], v189 offset:32416\n"
"v_rcp_f32 v203, v203\n"
"v_rcp_f32 v207, v207\n"
"v_mul_f32 v8, v8, v200\n"
"v_mul_f32 v12, v12, v204\n"
"v_mfma_f32_16x16x32_bf16 v[104:107], v[152:155], a[116:119], v[104:107]\n"
"buffer_load_dwordx4 v169, s[44:47], s76 offen lds\n"
"v_mul_f32 v9, v9, v201\n"
"v_mul_f32 v13, v13, v205\n"
"v_mul_f32 v10, v10, v202\n"
"v_mul_f32 v14, v14, v206\n"
"v_mul_f32 v11, v11, v203\n"
"v_mul_f32 v15, v15, v207\n"
"v_mfma_f32_16x16x32_bf16 v[104:107], v[156:159], a[132:135], v[104:107]\n"
"s_add_u32 m0, s71, 45056\n"
"v_cvt_pk_bf16_f32 v8, v8, v9\n"
"v_cvt_pk_bf16_f32 v9, v10, v11\n"
"v_cvt_pk_bf16_f32 v10, v12, v13\n"
"v_cvt_pk_bf16_f32 v11, v14, v15\n"
"v_exp_f32 v200, -v44\n"
"v_exp_f32 v204, -v48\n"
"v_mfma_f32_16x16x32_bf16 v[140:143], v[152:155], a[120:123], v[140:143]\n"
"buffer_load_dwordx4 v170, s[44:47], s76 offen lds\n"
"v_exp_f32 v201, -v45\n"
"v_exp_f32 v205, -v49\n"
"v_exp_f32 v202, -v46\n"
"v_mfma_f32_16x16x32_bf16 v[140:143], v[156:159], a[136:139], v[140:143]\n"
"s_waitcnt lgkmcnt(4)\n"
"v_exp_f32 v206, -v50\n"
"v_exp_f32 v203, -v47\n"
"v_exp_f32 v207, -v51\n"
"v_add_f32 v200, 1.0, v200\n"
"v_add_f32 v204, 1.0, v204\n"
"v_mfma_f32_16x16x32_bf16 v[16:19], v[160:163], a[140:143], v[16:19]\n"
"buffer_store_dwordx4 v[8:11], v197, s[60:63], s92 offen offset:1024 sc1\n"
"v_add_f32 v201, 1.0, v201\n"
"v_add_f32 v205, 1.0, v205\n"
"v_add_f32 v202, 1.0, v202\n"
"v_add_f32 v206, 1.0, v206\n"
"v_add_f32 v203, 1.0, v203\n"
"v_add_f32 v207, 1.0, v207\n"
"v_mfma_f32_16x16x32_bf16 v[16:19], v[164:167], a[156:159], v[16:19]\n"
"L_tc2_2:\n"
"s_cbranch_execz L_tp2_2_1\n"
"s_add_u32 m0, s71, 49152\n"
"ds_read_b64_tr_b16 v[152:153], v189 offset:18624\n"
"ds_read_b64_tr_b16 v[154:155], v189 offset:23232\n"
"v_rcp_f32 v200, v200\n"
"v_rcp_f32 v204, v204\n"
"v_rcp_f32 v201, v201\n"
"v_mfma_f32_16x16x32_bf16 v[52:55], v[160:163], a[144:147], v[52:55]\n"
"buffer_load_dwordx4 v171, s[44:47], s76 offen lds\n"
"v_rcp_f32 v205, v205\n"
"v_rcp_f32 v202, v202\n"
"v_rcp_f32 v206, v206\n"
"v_mfma_f32_16x16x32_bf16 v[52:55], v[164:167], a[160:163], v[52:55]\n"
"s_add_u32 m0, s71, 53248\n"
"ds_read_b64_tr_b16 v[156:157], v189 offset:27840\n"
"ds_read_b64_tr_b16 v[158:159], v189 offset:32448\n"
"v_rcp_f32 v203, v203\n"
"v_rcp_f32 v207, v207\n"
"v_mul_f32 v44, v44, v200\n"
"v_mul_f32 v48, v48, v204\n"
"v_mfma_f32_16x16x32_bf16 v[88:91], v[160:163], a[148:151], v[88:91]\n"
"buffer_load_dwordx4 v172, s[44:47], s76 offen lds\n"
"v_mul_f32 v45, v45, v201\n"
"v_mul_f32 v49, v49, v205\n"
"v_mul_f32 v46, v46, v202\n"
"v_mul_f32 v50, v50, v206\n"
"v_mul_f32 v47, v47, v203\n"
"v_mul_f32 v51, v51, v207\n"
"v_mfma_f32_16x16x32_bf16 v[88:91], v[164:167], a[164:167], v[88:91]\n"
"s_add_u32 m0, s71, 57344\n"
"v_cvt_pk_bf16_f32 v44, v44, v45\n"
"v_cvt_pk_bf16_f32 v45, v46, v47\n"
"v_cvt_pk_bf16_f32 v46, v48, v49\n"
"v_cvt_pk_bf16_f32 v47, v50, v51\n"
"v_exp_f32 v200, -v80\n"
"v_exp_f32 v204, -v84\n"
"v_mfma_f32_16x16x32_bf16 v[124:127], v[160:163], a[152:155], v[124:127]\n"
"buffer_load_dwordx4 v173, s[44:47], s76 offen lds\n"
"v_exp_f32 v201, -v81\n"
"v_exp_f32 v205, -v85\n"
"v_exp_f32 v202, -v82\n"
"v_mfma_f32_16x16x32_bf16 v[124:127], v[164:167], a[168:171], v[124:127]\n"
"s_waitcnt lgkmcnt(4)\n"
"v_exp_f32 v206, -v86\n"
"v_exp_f32 v203, -v83\n"
"v_exp_f32 v207, -v87\n"
"v_add_f32 v200, 1.0, v200\n"
"v_add_f32 v204, 1.0, v204\n"
"v_mfma_f32_16x16x32_bf16 v[20:23], v[144:147], a[140:143], v[20:23]\n"
"buffer_store_dwordx4 v[44:47], v197, s[60:63], s86 offen offset:1024 sc1\n"
"v_add_f32 v201, 1.0, v201\n"
"v_add_f32 v205, 1.0, v205\n"
"v_add_f32 v202, 1.0, v202\n"
"v_add_f32 v206, 1.0, v206\n"
"v_add_f32 v203, 1.0, v203\n"
"v_add_f32 v207, 1.0, v207\n"
"v_mfma_f32_16x16x32_bf16 v[20:23], v[148:151], a[156:159], v[20:23]\n"
"s_add_u32 m0, s71, 61440\n"
"ds_read_b64_tr_b16 v[160:161], v189 offset:18656\n"
"ds_read_b64_tr_b16 v[162:163], v189 offset:23264\n"
"v_rcp_f32 v200, v200\n"
"v_rcp_f32 v204, v204\n"
"v_rcp_f32 v201, v201\n"
"v_mfma_f32_16x16x32_bf16 v[56:59], v[144:147], a[144:147], v[56:59]\n"
"buffer_load_dwordx4 v174, s[44:47], s76 offen lds\n"
"v_rcp_f32 v205, v205\n"
"v_rcp_f32 v202, v202\n"
"v_rcp_f32 v206, v206\n"
"v_mfma_f32_16x16x32_bf16 v[56:59], v[148:151], a[160:163], v[56:59]\n"
"s_add_u32 m0, s71, 65536\n"
"ds_read_b64_tr_b16 v[164:165], v189 offset:27872\n"
"ds_read_b64_tr_b16 v[166:167], v189 offset:32480\n"
"v_rcp_f32 v203, v203\n"
"v_rcp_f32 v207, v207\n"
"v_mul_f32 v80, v80, v200\n"
"v_mul_f32 v84, v84, v204\n"
"v_mfma_f32_16x16x32_bf16 v[92:95], v[144:147], a[148:151], v[92:95]\n"
"buffer_load_dwordx4 v175, s[44:47], s76 offen lds\n"
"v_mul_f32 v81, v81, v201\n"
"v_mul_f32 v85, v85, v205\n"
"v_mul_f32 v82, v82, v202\n"
"v_mul_f32 v86, v86, v206\n"
"v_mul_f32 v83, v83, v203\n"
"v_mul_f32 v87, v87, v207\n"
"v_mfma_f32_16x16x32_bf16 v[92:95], v[148:151], a[164:167], v[92:95]\n"
"s_add_u32 m0, s71, 69632\n"
"v_cvt_pk_bf16_f32 v80, v80, v81\n"
"v_cvt_pk_bf16_f32 v81, v82, v83\n"
"v_cvt_pk_bf16_f32 v82, v84, v85\n"
"v_cvt_pk_bf16_f32 v83, v86, v87\n"
"v_exp_f32 v200, -v116\n"
"v_exp_f32 v204, -v120\n"
"v_mfma_f32_16x16x32_bf16 v[128:131], v[144:147], a[152:155], v[128:131]\n"
"buffer_load_dwordx4 v176, s[44:47], s76 offen lds\n"
"v_exp_f32 v201, -v117\n"
"v_exp_f32 v205, -v121\n"
"v_exp_f32 v202, -v118\n"
"v_mfma_f32_16x16x32_bf16 v[128:131], v[148:151], a[168:171], v[128:131]\n"
"s_waitcnt lgkmcnt(4)\n"
"v_exp_f32 v206, -v122\n"
"v_exp_f32 v203, -v119\n"
"v_exp_f32 v207, -v123\n"
"v_add_f32 v200, 1.0, v200\n"
"v_add_f32 v204, 1.0, v204\n"
"v_mfma_f32_16x16x32_bf16 v[24:27], v[152:155], a[140:143], v[24:27]\n"
"buffer_store_dwordx4 v[80:83], v197, s[60:63], s87 offen offset:1024 sc1\n"
"v_add_f32 v201, 1.0, v201\n"
"v_add_f32 v205, 1.0, v205\n"
"v_add_f32 v202, 1.0, v202\n"
"v_add_f32 v206, 1.0, v206\n"
"v_add_f32 v203, 1.0, v203\n"
"v_add_f32 v207, 1.0, v207\n"
"v_mfma_f32_16x16x32_bf16 v[24:27], v[156:159], a[156:159], v[24:27]\n"
"s_nop 0\n"
"ds_read_b64_tr_b16 v[144:145], v192 offset:18432\n"
"ds_read_b64_tr_b16 v[146:147], v192 offset:23040\n"
"v_rcp_f32 v200, v200\n"
"v_rcp_f32 v204, v204\n"
"v_rcp_f32 v201, v201\n"
"v_mfma_f32_16x16x32_bf16 v[60:63], v[152:155], a[144:147], v[60:63]\n"
"v_rcp_f32 v205, v205\n"
"v_rcp_f32 v202, v202\n"
"v_rcp_f32 v206, v206\n"
"v_rcp_f32 v203, v203\n"
"v_mfma_f32_16x16x32_bf16 v[60:63], v[156:159], a[160:163], v[60:63]\n"
"s_nop 0\n"
"ds_read_b64_tr_b16 v[148:149], v192 offset:27648\n"
"ds_read_b64_tr_b16 v[150:151], v192 offset:32256\n"
"v_rcp_f32 v207, v207\n"
"v_mul_f32 v116, v116, v200\n"
"v_mul_f32 v120, v120, v204\n"
"v_mul_f32 v117, v117, v201\n"
"v_mul_f32 v121, v121, v205\n"
"v_mfma_f32_16x16x32_bf16 v[96:99], v[152:155], a[148:151], v[96:99]\n"
"v_mul_f32 v118, v118, v202\n"
"v_mul_f32 v122, v122, v206\n"
"v_mfma_f32_16x16x32_bf16 v[96:99], v[156:159], a[164:167], v[96:99]\n"
"v_mul_f32 v119, v119, v203\n"
"v_mul_f32 v123, v123, v207\n"
"v_mfma_f32_16x16x32_bf16 v[132:135], v[152:155], a[152:155], v[132:135]\n"
"v_cvt_pk_bf16_f32 v116, v116, v117\n"
"v_cvt_pk_bf16_f32 v117, v118, v119\n"
"v_mfma_f32_16x16x32_bf16 v[132:135], v[156:159], a[168:171], v[132:135]\n"
"s_waitcnt lgkmcnt(4)\n"
"v_cvt_pk_bf16_f32 v118, v120, v121\n"
"v_cvt_pk_bf16_f32 v119, v122, v123\n"
"v_mfma_f32_16x16x32_bf16 v[28:31], v[160:163], a[140:143], v[28:31]\n"
"buffer_store_dwordx4 v[116:119], v197, s[60:63], s91 offen offset:1024 sc1\n"
"v_mfma_f32_16x16x32_bf16 v[28:31], v[164:167], a[156:159], v[28:31]\n"
"s_nop 0\n"
"ds_read_b64_tr_b16 v[152:153], v189 offset:36992\n"
"ds_read_b64_tr_b16 v[154:155], v189 offset:41600\n"
"v_mfma_f32_16x16x32_bf16 v[64:67], v[160:163], a[144:147], v[64:67]\n"
"v_mfma_f32_16x16x32_bf16 v[64:67], v[164:167], a[160:163], v[64:67]\n"
"s_nop 0\n"
"ds_read_b64_tr_b16 v[156:157], v189 offset:46208\n"
"ds_read_b64_tr_b16 v[158:159], v189 offset:50816\n"
"v_mfma_f32_16x16x32_bf16 v[100:103], v[160:163], a[148:151], v[100:103]\n"
"v_mfma_f32_16x16x32_bf16 v[100:103], v[164:167], a[164:167], v[100:103]\n"
"v_mfma_f32_16x16x32_bf16 v[136:139], v[160:163], a[152:155], v[136:139]\n"
"v_mfma_f32_16x16x32_bf16 v[136:139], v[164:167], a[168:171], v[136:139]\n"
"s_waitcnt lgkmcnt(4)\n"
"v_mfma_f32_16x16x32_bf16 v[32:35], v[144:147], a[140:143], v[32:35]\n"
"s_nop 0\n"
"ds_read_b64_tr_b16 v[160:161], v189 offset:37024\n"
"ds_read_b64_tr_b16 v[162:163], v189 offset:41632\n"
"v_mfma_f32_16x16x32_bf16 v[32:35], v[148:151], a[156:159], v[32:35]\n"
"v_mfma_f32_16x16x32_bf16 v[68:71], v[144:147], a[144:147], v[68:71]\n"
"v_mfma_f32_16x16x32_bf16 v[68:71], v[148:151], a[160:163], v[68:71]\n"
"s_nop 0\n"
"ds_read_b64_tr_b16 v[164:165], v189 offset:46240\n"
"ds_read_b64_tr_b16 v[166:167], v189 offset:50848\n"
"v_mfma_f32_16x16x32_bf16 v[104:107], v[144:147], a[148:151], v[104:107]\n"
"v_mfma_f32_16x16x32_bf16 v[104:107], v[148:151], a[164:167], v[104:107]\n"
"v_mfma_f32_16x16x32_bf16 v[140:143], v[144:147], a[152:155], v[140:143]\n"
"v_mfma_f32_16x16x32_bf16 v[140:143], v[148:151], a[168:171], v[140:143]\n"
"s_waitcnt lgkmcnt(4)\n"
"v_mfma_f32_16x16x32_bf16 v[16:19], v[152:155], a[172:175], v[16:19]\n"
"s_nop 0\n"
"ds_read_b64_tr_b16 v[144:145], v189 offset:37056\n"
"ds_read_b64_tr_b16 v[146:147], v189 offset:41664\n"
"v_mfma_f32_16x16x32_bf16 v[16:19], v[156:159], a[188:191], v[16:19]\n"
"v_mfma_f32_16x16x32_bf16 v[52:55], v[152:155], a[176:179], v[52:55]\n"
"v_mfma_f32_16x16x32_bf16 v[52:55], v[156:159], a[192:195], v[52:55]\n"
"s_nop 0\n"
"ds_read_b64_tr_b16 v[148:149], v189 offset:46272\n"
"ds_read_b64_tr_b16 v[150:151], v189 offset:50880\n"
"v_mfma_f32_16x16x32_bf16 v[88:91], v[152:155], a[180:183], v[88:91]\n"
"v_mfma_f32_16x16x32_bf16 v[88:91], v[156:159], a[196:199], v[88:91]\n"
"v_mfma_f32_16x16x32_bf16 v[124:127], v[152:155], a[184:187], v[124:127]\n"
"v_mfma_f32_16x16x32_bf16 v[124:127], v[156:159], a[200:203], v[124:127]\n"
"s_waitcnt lgkmcnt(4)\n"
"v_mfma_f32_16x16x32_bf16 v[20:23], v[160:163], a[172:175], v[20:23]\n"
"s_nop 0\n"
"ds_read_b64_tr_b16 v[152:153], v189 offset:37088\n"
"ds_read_b64_tr_b16 v[154:155], v189 offset:41696\n"
"v_mfma_f32_16x16x32_bf16 v[20:23], v[164:167], a[188:191], v[20:23]\n"
"v_mfma_f32_16x16x32_bf16 v[56:59], v[160:163], a[176:179], v[56:59]\n"
"v_mfma_f32_16x16x32_bf16 v[56:59], v[164:167], a[192:195], v[56:59]\n"
"s_nop 0\n"
"ds_read_b64_tr_b16 v[156:157], v189 offset:46304\n"
"ds_read_b64_tr_b16 v[158:159], v189 offset:50912\n"
"v_mfma_f32_16x16x32_bf16 v[92:95], v[160:163], a[180:183], v[92:95]\n"
"v_mfma_f32_16x16x32_bf16 v[92:95], v[164:167], a[196:199], v[92:95]\n"
"v_mfma_f32_16x16x32_bf16 v[128:131], v[160:163], a[184:187], v[128:131]\n"
"v_mfma_f32_16x16x32_bf16 v[128:131], v[164:167], a[200:203], v[128:131]\n"
"s_waitcnt lgkmcnt(4)\n"
"v_mfma_f32_16x16x32_bf16 v[24:27], v[144:147], a[172:175], v[24:27]\n"
"s_nop 0\n"
"ds_read_b64_tr_b16 v[160:161], v192 offset:36864\n"
"ds_read_b64_tr_b16 v[162:163], v192 offset:41472\n"
"v_mfma_f32_16x16x32_bf16 v[24:27], v[148:151], a[188:191], v[24:27]\n"
"v_mfma_f32_16x16x32_bf16 v[60:63], v[144:147], a[176:179], v[60:63]\n"
"v_mfma_f32_16x16x32_bf16 v[60:63], v[148:151], a[192:195], v[60:63]\n"
"s_nop 0\n"
"ds_read_b64_tr_b16 v[164:165], v192 offset:46080\n"
"ds_read_b64_tr_b16 v[166:167], v192 offset:50688\n"
"v_mfma_f32_16x16x32_bf16 v[96:99], v[144:147], a[180:183], v[96:99]\n"
"v_mfma_f32_16x16x32_bf16 v[96:99], v[148:151], a[196:199], v[96:99]\n"
"v_mfma_f32_16x16x32_bf16 v[132:135], v[144:147], a[184:187], v[132:135]\n"
"v_mfma_f32_16x16x32_bf16 v[132:135], v[148:151], a[200:203], v[132:135]\n"
"s_waitcnt lgkmcnt(4)\n"
"v_mfma_f32_16x16x32_bf16 v[28:31], v[152:155], a[172:175], v[28:31]\n"
"s_nop 0\n"
"ds_read_b64_tr_b16 v[144:145], v190 offset:128\n"
"ds_read_b64_tr_b16 v[146:147], v190 offset:4736\n"
"v_mfma_f32_16x16x32_bf16 v[28:31], v[156:159], a[188:191], v[28:31]\n"
"v_mfma_f32_16x16x32_bf16 v[64:67], v[152:155], a[176:179], v[64:67]\n"
"v_mfma_f32_16x16x32_bf16 v[64:67], v[156:159], a[192:195], v[64:67]\n"
"s_nop 0\n"
"ds_read_b64_tr_b16 v[148:149], v190 offset:9344\n"
"ds_read_b64_tr_b16 v[150:151], v190 offset:13952\n"
"v_mfma_f32_16x16x32_bf16 v[100:103], v[152:155], a[180:183], v[100:103]\n"
"v_mfma_f32_16x16x32_bf16 v[100:103], v[156:159], a[196:199], v[100:103]\n"
"v_mfma_f32_16x16x32_bf16 v[136:139], v[152:155], a[184:187], v[136:139]\n"
"v_mfma_f32_16x16x32_bf16 v[136:139], v[156:159], a[200:203], v[136:139]\n"
"s_waitcnt lgkmcnt(4)\n"
"s_barrier\n"
"v_mov_b32 v209, 1\n"
"s_waitcnt vmcnt(0)\n"
"s_mov_b64 exec, 1\n"
"buffer_atomic_add v209, off, s[60:63], s65\n"
"s_mov_b64 exec, -1\n"
"v_mfma_f32_16x16x32_bf16 v[32:35], v[160:163], a[172:175], v[32:35]\n"
"s_add_u32 m0, s71, 73728\n"
"ds_read_b64_tr_b16 v[152:153], v190 offset:160\n"
"ds_read_b64_tr_b16 v[154:155], v190 offset:4768\n"
"v_mfma_f32_16x16x32_bf16 v[32:35], v[164:167], a[188:191], v[32:35]\n"
"s_mov_b32 s76, 331776\n"
"v_mfma_f32_16x16x32_bf16 v[68:71], v[160:163], a[176:179], v[68:71]\n"
"buffer_load_dwordx4 v168, s[44:47], s76 offen lds\n"
"v_mfma_f32_16x16x32_bf16 v[68:71], v[164:167], a[192:195], v[68:71]\n"
"s_add_u32 m0, s71, 77824\n"
"ds_read_b64_tr_b16 v[156:157], v190 offset:9376\n"
"ds_read_b64_tr_b16 v[158:159], v190 offset:13984\n"
"v_mfma_f32_16x16x32_bf16 v[104:107], v[160:163], a[180:183], v[104:107]\n"
"v_mfma_f32_16x16x32_bf16 v[104:107], v[164:167], a[196:199], v[104:107]\n"
"buffer_load_dwordx4 v169, s[44:47], s76 offen lds\n"
"v_mfma_f32_16x16x32_bf16 v[140:143], v[160:163], a[184:187], v[140:143]\n"
"s_add_u32 m0, s71, 81920\n"
"v_mfma_f32_16x16x32_bf16 v[140:143], v[164:167], a[200:203], v[140:143]\n"
"s_waitcnt lgkmcnt(4)\n"
"v_mfma_f32_16x16x32_bf16 v[16:19], v[144:147], a[204:207], v[16:19]\n"
"s_nop 0\n"
"ds_read_b64_tr_b16 v[160:161], v190 offset:192\n"
"ds_read_b64_tr_b16 v[162:163], v190 offset:4800\n"
"v_mfma_f32_16x16x32_bf16 v[16:19], v[148:151], a[220:223], v[16:19]\n"
"v_mfma_f32_16x16x32_bf16 v[52:55], v[144:147], a[208:211], v[52:55]\n"
"buffer_load_dwordx4 v170, s[44:47], s76 offen lds\n"
"v_mfma_f32_16x16x32_bf16 v[52:55], v[148:151], a[224:227], v[52:55]\n"
"s_add_u32 m0, s71, 86016\n"
"ds_read_b64_tr_b16 v[164:165], v190 offset:9408\n"
"ds_read_b64_tr_b16 v[166:167], v190 offset:14016\n"
"v_mfma_f32_16x16x32_bf16 v[88:91], v[144:147], a[212:215], v[88:91]\n"
"v_mfma_f32_16x16x32_bf16 v[88:91], v[148:151], a[228:231], v[88:91]\n"
"v_mfma_f32_16x16x32_bf16 v[124:127], v[144:147], a[216:219], v[124:127]\n"
"v_mfma_f32_16x16x32_bf16 v[124:127], v[148:151], a[232:235], v[124:127]\n"
"s_waitcnt lgkmcnt(4)\n"
"v_mfma_f32_16x16x32_bf16 v[20:23], v[152:155], a[204:207], v[20:23]\n"
"buffer_load_dwordx4 v171, s[44:47], s76 offen lds\n"
"v_mfma_f32_16x16x32_bf16 v[20:23], v[156:159], a[220:223], v[20:23]\n"
"s_add_u32 m0, s71, 90112\n"
"ds_read_b64_tr_b16 v[144:145], v190 offset:224\n"
"ds_read_b64_tr_b16 v[146:147], v190 offset:4832\n"
"v_mfma_f32_16x16x32_bf16 v[56:59], v[152:155], a[208:211], v[56:59]\n"
"v_mfma_f32_16x16x32_bf16 v[56:59], v[156:159], a[224:227], v[56:59]\n"
"s_nop 0\n"
"ds_read_b64_tr_b16 v[148:149], v190 offset:9440\n"
"ds_read_b64_tr_b16 v[150:151], v190 offset:14048\n"
"v_mfma_f32_16x16x32_bf16 v[92:95], v[152:155], a[212:215], v[92:95]\n"
"buffer_load_dwordx4 v172, s[44:47], s76 offen lds\n"
"v_mfma_f32_16x16x32_bf16 v[92:95], v[156:159], a[228:231], v[92:95]\n"
"s_add_u32 m0, s71, 94208\n"
"v_mfma_f32_16x16x32_bf16 v[128:131], v[152:155], a[216:219], v[128:131]\n"
"v_mfma_f32_16x16x32_bf16 v[128:131], v[156:159], a[232:235], v[128:131]\n"
"s_waitcnt lgkmcnt(4)\n"
"v_mfma_f32_16x16x32_bf16 v[24:27], v[160:163], a[204:207], v[24:27]\n"
"s_nop 0\n"
"ds_read_b64_tr_b16 v[152:153], v193 offset:0\n"
"ds_read_b64_tr_b16 v[154:155], v193 offset:4608\n"
"v_mfma_f32_16x16x32_bf16 v[24:27], v[164:167], a[220:223], v[24:27]\n"
"buffer_load_dwordx4 v173, s[44:47], s76 offen lds\n"
"v_mfma_f32_16x16x32_bf16 v[60:63], v[160:163], a[208:211], v[60:63]\n"
"s_add_u32 m0, s71, 98304\n"
"v_mfma_f32_16x16x32_bf16 v[60:63], v[164:167], a[224:227], v[60:63]\n"
"s_nop 0\n"
"ds_read_b64_tr_b16 v[156:157], v193 offset:9216\n"
"ds_read_b64_tr_b16 v[158:159], v193 offset:13824\n"
"v_mfma_f32_16x16x32_bf16 v[96:99], v[160:163], a[212:215], v[96:99]\n"
"v_mfma_f32_16x16x32_bf16 v[96:99], v[164:167], a[228:231], v[96:99]\n"
"v_mfma_f32_16x16x32_bf16 v[132:135], v[160:163], a[216:219], v[132:135]\n"
"buffer_load_dwordx4 v174, s[44:47], s76 offen lds\n"
"v_mfma_f32_16x16x32_bf16 v[132:135], v[164:167], a[232:235], v[132:135]\n"
"s_waitcnt lgkmcnt(4)\n"
"v_mfma_f32_16x16x32_bf16 v[28:31], v[144:147], a[204:207], v[28:31]\n"
"s_add_u32 m0, s71, 102400\n"
"ds_read_b64_tr_b16 v[160:161], v190 offset:18560\n"
"ds_read_b64_tr_b16 v[162:163], v190 offset:23168\n"
"v_mfma_f32_16x16x32_bf16 v[28:31], v[148:151], a[220:223], v[28:31]\n"
"v_mfma_f32_16x16x32_bf16 v[64:67], v[144:147], a[208:211], v[64:67]\n"
"v_mfma_f32_16x16x32_bf16 v[64:67], v[148:151], a[224:227], v[64:67]\n"
"buffer_load_dwordx4 v175, s[44:47], s76 offen lds\n"
"v_mfma_f32_16x16x32_bf16 v[100:103], v[144:147], a[212:215], v[100:103]\n"
"s_add_u32 m0, s71, 106496\n"
"ds_read_b64_tr_b16 v[164:165], v190 offset:27776\n"
"ds_read_b64_tr_b16 v[166:167], v190 offset:32384\n"
"v_mfma_f32_16x16x32_bf16 v[100:103], v[148:151], a[228:231], v[100:103]\n"
"v_mfma_f32_16x16x32_bf16 v[136:139], v[144:147], a[216:219], v[136:139]\n"
"v_mfma_f32_16x16x32_bf16 v[136:139], v[148:151], a[232:235], v[136:139]\n"
"s_waitcnt lgkmcnt(4)\n"
"v_mfma_f32_16x16x32_bf16 v[32:35], v[152:155], a[204:207], v[32:35]\n"
"buffer_load_dwordx4 v176, s[44:47], s76 offen lds\n"
"v_mfma_f32_16x16x32_bf16 v[32:35], v[156:159], a[220:223], v[32:35]\n"
"s_nop 0\n"
"ds_read_b64_tr_b16 v[144:145], v190 offset:18592\n"
"ds_read_b64_tr_b16 v[146:147], v190 offset:23200\n"
"v_mfma_f32_16x16x32_bf16 v[68:71], v[152:155], a[208:211], v[68:71]\n"
"v_mfma_f32_16x16x32_bf16 v[68:71], v[156:159], a[224:227], v[68:71]\n"
"s_nop 0\n"
"ds_read_b64_tr_b16 v[148:149], v190 offset:27808\n"
"ds_read_b64_tr_b16 v[150:151], v190 offset:32416\n"
"v_mfma_f32_16x16x32_bf16 v[104:107], v[152:155], a[212:215], v[104:107]\n"
"v_mfma_f32_16x16x32_bf16 v[104:107], v[156:159], a[228:231], v[104:107]\n"
"v_mfma_f32_16x16x32_bf16 v[140:143], v[152:155], a[216:219], v[140:143]\n"
"v_mfma_f32_16x16x32_bf16 v[140:143], v[156:159], a[232:235], v[140:143]\n"
"s_waitcnt lgkmcnt(4)\n"
"v_mfma_f32_16x16x32_bf16 v[16:19], v[160:163], a[12:15], v[16:19]\n"
"s_nop 0\n"
"ds_read_b64_tr_b16 v[152:153], v190 offset:18624\n"
"ds_read_b64_tr_b16 v[154:155], v190 offset:23232\n"
"v_mfma_f32_16x16x32_bf16 v[16:19], v[164:167], a[28:31], v[16:19]\n"
"v_mfma_f32_16x16x32_bf16 v[52:55], v[160:163], a[16:19], v[52:55]\n"
"v_mfma_f32_16x16x32_bf16 v[52:55], v[164:167], a[32:35], v[52:55]\n"
"s_nop 0\n"
"ds_read_b64_tr_b16 v[156:157], v190 offset:27840\n"
"ds_read_b64_tr_b16 v[158:159], v190 offset:32448\n"
"v_mfma_f32_16x16x32_bf16 v[88:91], v[160:163], a[20:23], v[88:91]\n"
"v_mfma_f32_16x16x32_bf16 v[88:91], v[164:167], a[36:39], v[88:91]\n"
"v_mfma_f32_16x16x32_bf16 v[124:127], v[160:163], a[24:27], v[124:127]\n"
"v_mfma_f32_16x16x32_bf16 v[124:127], v[164:167], a[40:43], v[124:127]\n"
"s_waitcnt lgkmcnt(4)\n"
"v_mfma_f32_16x16x32_bf16 v[20:23], v[144:147], a[12:15], v[20:23]\n"
"s_nop 0\n"
"ds_read_b64_tr_b16 v[160:161], v190 offset:18656\n"
"ds_read_b64_tr_b16 v[162:163], v190 offset:23264\n"
"v_mfma_f32_16x16x32_bf16 v[20:23], v[148:151], a[28:31], v[20:23]\n"
"v_mfma_f32_16x16x32_bf16 v[56:59], v[144:147], a[16:19], v[56:59]\n"
"v_mfma_f32_16x16x32_bf16 v[56:59], v[148:151], a[32:35], v[56:59]\n"
"s_nop 0\n"
"ds_read_b64_tr_b16 v[164:165], v190 offset:27872\n"
"ds_read_b64_tr_b16 v[166:167], v190 offset:32480\n"
"v_mfma_f32_16x16x32_bf16 v[92:95], v[144:147], a[20:23], v[92:95]\n"
"v_mfma_f32_16x16x32_bf16 v[92:95], v[148:151], a[36:39], v[92:95]\n"
"v_mfma_f32_16x16x32_bf16 v[128:131], v[144:147], a[24:27], v[128:131]\n"
"v_mfma_f32_16x16x32_bf16 v[128:131], v[148:151], a[40:43], v[128:131]\n"
"s_waitcnt lgkmcnt(4)\n"
"buffer_load_dword v208, off, s[60:63], s75 sc1\n"
"v_mfma_f32_16x16x32_bf16 v[24:27], v[152:155], a[12:15], v[24:27]\n"
"s_nop 0\n"
"ds_read_b64_tr_b16 v[144:145], v193 offset:18432\n"
"ds_read_b64_tr_b16 v[146:147], v193 offset:23040\n"
"v_mfma_f32_16x16x32_bf16 v[24:27], v[156:159], a[28:31], v[24:27]\n"
"v_mfma_f32_16x16x32_bf16 v[60:63], v[152:155], a[16:19], v[60:63]\n"
"v_mfma_f32_16x16x32_bf16 v[60:63], v[156:159], a[32:35], v[60:63]\n"
"s_nop 0\n"
"ds_read_b64_tr_b16 v[148:149], v193 offset:27648\n"
"ds_read_b64_tr_b16 v[150:151], v193 offset:32256\n"
"v_mfma_f32_16x16x32_bf16 v[96:99], v[152:155], a[20:23], v[96:99]\n"
"v_mfma_f32_16x16x32_bf16 v[96:99], v[156:159], a[36:39], v[96:99]\n"
"v_mfma_f32_16x16x32_bf16 v[132:135], v[152:155], a[24:27], v[132:135]\n"
"v_mfma_f32_16x16x32_bf16 v[132:135], v[156:159], a[40:43], v[132:135]\n"
"s_waitcnt lgkmcnt(4)\n"
"v_mfma_f32_16x16x32_bf16 v[28:31], v[160:163], a[12:15], v[28:31]\n"
"s_nop 0\n"
"ds_read_b64_tr_b16 v[152:153], v188 offset:128\n"
"ds_read_b64_tr_b16 v[154:155], v188 offset:4736\n"
"v_mfma_f32_16x16x32_bf16 v[28:31], v[164:167], a[28:31], v[28:31]\n"
"v_mfma_f32_16x16x32_bf16 v[64:67], v[160:163], a[16:19], v[64:67]\n"
"v_mfma_f32_16x16x32_bf16 v[64:67], v[164:167], a[32:35], v[64:67]\n"
"s_nop 0\n"
"ds_read_b64_tr_b16 v[156:157], v188 offset:9344\n"
"ds_read_b64_tr_b16 v[158:159], v188 offset:13952\n"
"v_mfma_f32_16x16x32_bf16 v[100:103], v[160:163], a[20:23], v[100:103]\n"
"v_mfma_f32_16x16x32_bf16 v[100:103], v[164:167], a[36:39], v[100:103]\n"
"v_mfma_f32_16x16x32_bf16 v[136:139], v[160:163], a[24:27], v[136:139]\n"
"v_mfma_f32_16x16x32_bf16 v[136:139], v[164:167], a[40:43], v[136:139]\n"
"s_waitcnt lgkmcnt(4)\n"
"s_barrier\n"
"v_mfma_f32_16x16x32_bf16 v[32:35], v[144:147], a[12:15], v[32:35]\n"
"s_add_u32 m0, s71, 110592\n"
"ds_read_b64_tr_b16 v[160:161], v188 offset:160\n"
"ds_read_b64_tr_b16 v[162:163], v188 offset:4768\n"
"v_mfma_f32_16x16x32_bf16 v[32:35], v[148:151], a[28:31], v[32:35]\n"
"s_mov_b32 s76, 663552\n"
"v_mfma_f32_16x16x32_bf16 v[68:71], v[144:147], a[16:19], v[68:71]\n"
"buffer_load_dwordx4 v168, s[44:47], s76 offen lds\n"
"v_mfma_f32_16x16x32_bf16 v[68:71], v[148:151], a[32:35], v[68:71]\n"
"s_add_u32 m0, s71, 114688\n"
"ds_read_b64_tr_b16 v[164:165], v188 offset:9376\n"
"ds_read_b64_tr_b16 v[166:167], v188 offset:13984\n"
"v_mfma_f32_16x16x32_bf16 v[104:107], v[144:147], a[20:23], v[104:107]\n"
"v_mfma_f32_16x16x32_bf16 v[104:107], v[148:151], a[36:39], v[104:107]\n"
"buffer_load_dwordx4 v169, s[44:47], s76 offen lds\n"
"v_mfma_f32_16x16x32_bf16 v[140:143], v[144:147], a[24:27], v[140:143]\n"
"s_add_u32 m0, s71, 118784\n"
"v_mfma_f32_16x16x32_bf16 v[140:143], v[148:151], a[40:43], v[140:143]\n"
"s_waitcnt lgkmcnt(4)\n"
"v_mfma_f32_16x16x32_bf16 v[16:19], v[152:155], a[44:47], v[16:19]\n"
"s_nop 0\n"
"ds_read_b64_tr_b16 v[144:145], v188 offset:192\n"
"ds_read_b64_tr_b16 v[146:147], v188 offset:4800\n"
"v_mfma_f32_16x16x32_bf16 v[16:19], v[156:159], a[60:63], v[16:19]\n"
"v_mfma_f32_16x16x32_bf16 v[52:55], v[152:155], a[48:51], v[52:55]\n"
"buffer_load_dwordx4 v170, s[44:47], s76 offen lds\n"
"v_mfma_f32_16x16x32_bf16 v[52:55], v[156:159], a[64:67], v[52:55]\n"
"s_add_u32 m0, s71, 122880\n"
"ds_read_b64_tr_b16 v[148:149], v188 offset:9408\n"
"ds_read_b64_tr_b16 v[150:151], v188 offset:14016\n"
"v_mfma_f32_16x16x32_bf16 v[88:91], v[152:155], a[52:55], v[88:91]\n"
"v_mfma_f32_16x16x32_bf16 v[88:91], v[156:159], a[68:71], v[88:91]\n"
"v_mfma_f32_16x16x32_bf16 v[124:127], v[152:155], a[56:59], v[124:127]\n"
"v_mfma_f32_16x16x32_bf16 v[124:127], v[156:159], a[72:75], v[124:127]\n"
"s_waitcnt lgkmcnt(4)\n"
"s_waitcnt vmcnt(3)\n"
"v_readfirstlane_b32 s74, v208\n"
"s_nop 1\n"
"s_bfe_u32 s74, s74, 524288\n"
"s_cmp_ge_u32 s74, 8\n"
"s_cbranch_scc1 L_po_abok21\n"
"s_mov_b32 s97, 20000\n"
"L_po_poll22:\n"
"buffer_load_dword v208, off, s[60:63], s75 sc1\n"
"s_waitcnt vmcnt(0)\n"
"v_readfirstlane_b32 s74, v208\n"
"s_nop 1\n"
"s_bfe_u32 s74, s74, 524288\n"
"s_cmp_ge_u32 s74, 8\n"
"s_cbranch_scc1 L_po_polld23\n"
"s_sleep 2\n"
"s_sub_u32 s97, s97, 1\n"
"s_cbranch_scc0 L_po_poll22\n"
"L_po_polld23:\n"
"L_po_abok21:\n"
"s_add_u32 s82, s88, 0\n"
"s_add_u32 s83, s88, 147456\n"
"s_add_u32 s78, s82, 0\n"
"buffer_load_dwordx4 a[76:79], v186, s[60:63], s78 offen offset:0 sc1\n"
"s_add_u32 s79, s83, 0\n"
"buffer_load_dwordx4 a[80:83], v186, s[60:63], s79 offen offset:0 sc1\n"
"buffer_load_dwordx4 a[84:87], v186, s[60:63], s78 offen offset:1024 sc1\n"
"buffer_load_dwordx4 a[88:91], v186, s[60:63], s79 offen offset:1024 sc1\n"
"s_add_u32 s78, s82, 5120\n"
"buffer_load_dwordx4 a[92:95], v186, s[60:63], s78 offen offset:0 sc1\n"
"s_add_u32 s79, s83, 5120\n"
"buffer_load_dwordx4 a[96:99], v186, s[60:63], s79 offen offset:0 sc1\n"
"buffer_load_dwordx4 a[100:103], v186, s[60:63], s78 offen offset:1024 sc1\n"
"buffer_load_dwordx4 a[104:107], v186, s[60:63], s79 offen offset:1024 sc1\n"
"v_mfma_f32_16x16x32_bf16 v[20:23], v[160:163], a[44:47], v[20:23]\n"
"buffer_load_dwordx4 v171, s[44:47], s76 offen lds\n"
"v_mfma_f32_16x16x32_bf16 v[20:23], v[164:167], a[60:63], v[20:23]\n"
"s_add_u32 m0, s71, 126976\n"
"ds_read_b64_tr_b16 v[152:153], v188 offset:224\n"
"ds_read_b64_tr_b16 v[154:155], v188 offset:4832\n"
"v_mfma_f32_16x16x32_bf16 v[56:59], v[160:163], a[48:51], v[56:59]\n"
"v_mfma_f32_16x16x32_bf16 v[56:59], v[164:167], a[64:67], v[56:59]\n"
"s_nop 0\n"
"ds_read_b64_tr_b16 v[156:157], v188 offset:9440\n"
"ds_read_b64_tr_b16 v[158:159], v188 offset:14048\n"
"v_mfma_f32_16x16x32_bf16 v[92:95], v[160:163], a[52:55], v[92:95]\n"
"buffer_load_dwordx4 v172, s[44:47], s76 offen lds\n"
"v_mfma_f32_16x16x32_bf16 v[92:95], v[164:167], a[68:71], v[92:95]\n"
"s_add_u32 m0, s71, 131072\n"
"v_mfma_f32_16x16x32_bf16 v[128:131], v[160:163], a[56:59], v[128:131]\n"
"v_mfma_f32_16x16x32_bf16 v[128:131], v[164:167], a[72:75], v[128:131]\n"
"s_waitcnt lgkmcnt(4)\n"
"v_mfma_f32_16x16x32_bf16 v[24:27], v[144:147], a[44:47], v[24:27]\n"
"s_nop 0\n"
"ds_read_b64_tr_b16 v[160:161], v191 offset:0\n"
"ds_read_b64_tr_b16 v[162:163], v191 offset:4608\n"
"v_exp_f32 v200, -v16\n"
"v_exp_f32 v204, -v20\n"
"v_exp_f32 v201, -v17\n"
"v_mfma_f32_16x16x32_bf16 v[24:27], v[148:151], a[60:63], v[24:27]\n"
"buffer_load_dwordx4 v173, s[44:47], s76 offen lds\n"
"v_exp_f32 v205, -v21\n"
"v_exp_f32 v202, -v18\n"
"v_exp_f32 v206, -v22\n"
"v_mfma_f32_16x16x32_bf16 v[60:63], v[144:147], a[48:51], v[60:63]\n"
"s_add_u32 m0, s71, 135168\n"
"v_exp_f32 v203, -v19\n"
"v_exp_f32 v207, -v23\n"
"v_add_f32 v200, 1.0, v200\n"
"v_add_f32 v204, 1.0, v204\n"
"v_add_f32 v201, 1.0, v201\n"
"v_add_f32 v205, 1.0, v205\n"
"v_mfma_f32_16x16x32_bf16 v[60:63], v[148:151], a[64:67], v[60:63]\n"
"buffer_load_dwordx4 v174, s[44:47], s76 offen lds\n"
"v_add_f32 v202, 1.0, v202\n"
"v_add_f32 v206, 1.0, v206\n"
"v_add_f32 v203, 1.0, v203\n"
"v_add_f32 v207, 1.0, v207\n"
"v_rcp_f32 v200, v200\n"
"v_mfma_f32_16x16x32_bf16 v[96:99], v[144:147], a[52:55], v[96:99]\n"
"s_add_u32 m0, s71, 139264\n"
"ds_read_b64_tr_b16 v[164:165], v191 offset:9216\n"
"ds_read_b64_tr_b16 v[166:167], v191 offset:13824\n"
"v_rcp_f32 v204, v204\n"
"v_rcp_f32 v201, v201\n"
"v_rcp_f32 v205, v205\n"
"v_mfma_f32_16x16x32_bf16 v[96:99], v[148:151], a[68:71], v[96:99]\n"
"buffer_load_dwordx4 v175, s[44:47], s76 offen lds\n"
"v_rcp_f32 v202, v202\n"
"v_rcp_f32 v206, v206\n"
"v_rcp_f32 v203, v203\n"
"v_mfma_f32_16x16x32_bf16 v[132:135], v[144:147], a[56:59], v[132:135]\n"
"s_add_u32 m0, s71, 143360\n"
"v_rcp_f32 v207, v207\n"
"v_mul_f32 v16, v16, v200\n"
"v_mul_f32 v20, v20, v204\n"
"v_mul_f32 v17, v17, v201\n"
"v_mul_f32 v21, v21, v205\n"
"v_mul_f32 v18, v18, v202\n"
"v_mul_f32 v22, v22, v206\n"
"v_mfma_f32_16x16x32_bf16 v[132:135], v[148:151], a[72:75], v[132:135]\n"
"s_waitcnt lgkmcnt(4)\n"
"v_mul_f32 v19, v19, v203\n"
"v_mul_f32 v23, v23, v207\n"
"v_cvt_pk_bf16_f32 v16, v16, v17\n"
"v_cvt_pk_bf16_f32 v17, v18, v19\n"
"v_cvt_pk_bf16_f32 v18, v20, v21\n"
"v_cvt_pk_bf16_f32 v19, v22, v23\n"
"v_exp_f32 v200, -v52\n"
"v_mfma_f32_16x16x32_bf16 v[28:31], v[152:155], a[44:47], v[28:31]\n"
"buffer_load_dwordx4 v176, s[44:47], s76 offen lds\n"
"v_exp_f32 v204, -v56\n"
"v_exp_f32 v201, -v53\n"
"v_exp_f32 v205, -v57\n"
"v_mfma_f32_16x16x32_bf16 v[28:31], v[156:159], a[60:63], v[28:31]\n"
"v_exp_f32 v202, -v54\n"
"v_exp_f32 v206, -v58\n"
"v_exp_f32 v203, -v55\n"
"v_exp_f32 v207, -v59\n"
"v_mfma_f32_16x16x32_bf16 v[64:67], v[152:155], a[48:51], v[64:67]\n"
"buffer_store_dwordx4 v[16:19], v197, s[60:63], s92 offen offset:2048 sc1\n"
"v_add_f32 v200, 1.0, v200\n"
"v_add_f32 v204, 1.0, v204\n"
"v_add_f32 v201, 1.0, v201\n"
"v_add_f32 v205, 1.0, v205\n"
"v_add_f32 v202, 1.0, v202\n"
"v_add_f32 v206, 1.0, v206\n"
"v_mfma_f32_16x16x32_bf16 v[64:67], v[156:159], a[64:67], v[64:67]\n"
"v_add_f32 v203, 1.0, v203\n"
"v_add_f32 v207, 1.0, v207\n"
"v_rcp_f32 v200, v200\n"
"v_rcp_f32 v204, v204\n"
"v_rcp_f32 v201, v201\n"
"v_mfma_f32_16x16x32_bf16 v[100:103], v[152:155], a[52:55], v[100:103]\n"
"v_rcp_f32 v205, v205\n"
"v_rcp_f32 v202, v202\n"
"v_rcp_f32 v206, v206\n"
"v_rcp_f32 v203, v203\n"
"v_mfma_f32_16x16x32_bf16 v[100:103], v[156:159], a[68:71], v[100:103]\n"
"v_rcp_f32 v207, v207\n"
"v_mul_f32 v52, v52, v200\n"
"v_mul_f32 v56, v56, v204\n"
"v_mul_f32 v53, v53, v201\n"
"v_mul_f32 v57, v57, v205\n"
"v_mul_f32 v54, v54, v202\n"
"v_mul_f32 v58, v58, v206\n"
"v_mfma_f32_16x16x32_bf16 v[136:139], v[152:155], a[56:59], v[136:139]\n"
"v_mul_f32 v55, v55, v203\n"
"v_mul_f32 v59, v59, v207\n"
"v_cvt_pk_bf16_f32 v52, v52, v53\n"
"v_cvt_pk_bf16_f32 v53, v54, v55\n"
"v_cvt_pk_bf16_f32 v54, v56, v57\n"
"v_cvt_pk_bf16_f32 v55, v58, v59\n"
"v_exp_f32 v200, -v88\n"
"v_mfma_f32_16x16x32_bf16 v[136:139], v[156:159], a[72:75], v[136:139]\n"
"s_waitcnt lgkmcnt(0)\n"
"s_barrier\n"
"s_waitcnt vmcnt(31)\n"
"s_barrier\n"
"ds_read_b64_tr_b16 v[210:211], v246 offset:36864\n"
"ds_read_b64_tr_b16 v[212:213], v246 offset:41472\n"
"ds_read_b64_tr_b16 v[214:215], v246 offset:46080\n"
"ds_read_b64_tr_b16 v[216:217], v246 offset:50688\n"
"ds_read_b64_tr_b16 v[218:219], v246 offset:36896\n"
"ds_read_b64_tr_b16 v[220:221], v246 offset:41504\n"
"ds_read_b64_tr_b16 v[222:223], v246 offset:46112\n"
"ds_read_b64_tr_b16 v[224:225], v246 offset:50720\n"
"v_mfma_f32_16x16x32_bf16 v[32:35], v[160:163], a[44:47], v[32:35]\n"
"L_tc2_3:\n"
"s_cbranch_execz L_tp2_3_1\n"
"buffer_store_dwordx4 v[52:55], v197, s[60:63], s86 offen offset:2048 sc1\n"
"v_exp_f32 v204, -v92\n"
"v_exp_f32 v201, -v89\n"
"v_exp_f32 v205, -v93\n"
"v_mfma_f32_16x16x32_bf16 v[32:35], v[164:167], a[60:63], v[32:35]\n"
"s_add_u32 m0, s71, 0\n"
"v_exp_f32 v202, -v90\n"
"v_exp_f32 v206, -v94\n"
"v_exp_f32 v203, -v91\n"
"v_exp_f32 v207, -v95\n"
"v_mfma_f32_16x16x32_bf16 v[68:71], v[160:163], a[48:51], v[68:71]\n"
"s_mov_b32 s76, 995328\n"
"v_add_f32 v200, 1.0, v200\n"
"v_add_f32 v204, 1.0, v204\n"
"v_add_f32 v201, 1.0, v201\n"
"v_add_f32 v205, 1.0, v205\n"
"v_add_f32 v202, 1.0, v202\n"
"v_add_f32 v206, 1.0, v206\n"
"v_add_f32 v203, 1.0, v203\n"
"v_add_f32 v207, 1.0, v207\n"
"v_mfma_f32_16x16x32_bf16 v[68:71], v[164:167], a[64:67], v[68:71]\n"
"buffer_load_dwordx4 v168, s[44:47], s76 offen lds\n"
"v_rcp_f32 v200, v200\n"
"v_rcp_f32 v204, v204\n"
"v_rcp_f32 v201, v201\n"
"v_mfma_f32_16x16x32_bf16 v[104:107], v[160:163], a[52:55], v[104:107]\n"
"s_add_u32 m0, s71, 4096\n"
"v_rcp_f32 v205, v205\n"
"v_rcp_f32 v202, v202\n"
"v_rcp_f32 v206, v206\n"
"v_rcp_f32 v203, v203\n"
"v_mfma_f32_16x16x32_bf16 v[104:107], v[164:167], a[68:71], v[104:107]\n"
"buffer_load_dwordx4 v169, s[44:47], s76 offen lds\n"
"v_rcp_f32 v207, v207\n"
"v_mul_f32 v88, v88, v200\n"
"v_mul_f32 v92, v92, v204\n"
"v_mul_f32 v89, v89, v201\n"
"v_mul_f32 v93, v93, v205\n"
"v_mfma_f32_16x16x32_bf16 v[140:143], v[160:163], a[56:59], v[140:143]\n"
"s_add_u32 m0, s71, 8192\n"
"v_mul_f32 v90, v90, v202\n"
"v_mul_f32 v94, v94, v206\n"
"v_mul_f32 v91, v91, v203\n"
"v_mul_f32 v95, v95, v207\n"
"v_cvt_pk_bf16_f32 v88, v88, v89\n"
"v_cvt_pk_bf16_f32 v89, v90, v91\n"
"v_cvt_pk_bf16_f32 v90, v92, v93\n"
"v_cvt_pk_bf16_f32 v91, v94, v95\n"
"v_mfma_f32_16x16x32_bf16 v[140:143], v[164:167], a[72:75], v[140:143]\n"
"buffer_load_dwordx4 v170, s[44:47], s76 offen lds\n"
"v_exp_f32 v200, -v124\n"
"v_exp_f32 v204, -v128\n"
"v_exp_f32 v201, -v125\n"
"s_waitcnt lgkmcnt(8)\n"
"s_barrier\n"
"s_waitcnt vmcnt(15) lgkmcnt(4)\n"
"v_mfma_f32_16x16x32_bf16 v[0:3], v[210:213], a[76:79], 0\n"
"buffer_store_dwordx4 v[88:91], v197, s[60:63], s87 offen offset:2048 sc1\n"
"v_exp_f32 v205, -v129\n"
"v_exp_f32 v202, -v126\n"
"v_exp_f32 v206, -v130\n"
"v_exp_f32 v203, -v127\n"
"v_exp_f32 v207, -v131\n"
"v_mfma_f32_16x16x32_bf16 v[0:3], v[214:217], a[84:87], v[0:3]\n"
"s_add_u32 s78, s82, 9216\n"
"ds_read_b64_tr_b16 v[226:227], v246 offset:36928\n"
"ds_read_b64_tr_b16 v[228:229], v246 offset:41536\n"
"v_add_f32 v200, 1.0, v200\n"
"v_add_f32 v204, 1.0, v204\n"
"v_add_f32 v201, 1.0, v201\n"
"v_add_f32 v205, 1.0, v205\n"
"v_add_f32 v202, 1.0, v202\n"
"v_add_f32 v206, 1.0, v206\n"
"v_add_f32 v203, 1.0, v203\n"
"v_add_f32 v207, 1.0, v207\n"
"v_rcp_f32 v200, v200\n"
"v_mfma_f32_16x16x32_bf16 v[72:75], v[210:213], a[80:83], 0\n"
"buffer_load_dwordx4 a[108:111], v186, s[60:63], s78 offen offset:0 sc1\n"
"v_rcp_f32 v204, v204\n"
"v_rcp_f32 v201, v201\n"
"v_rcp_f32 v205, v205\n"
"v_rcp_f32 v202, v202\n"
"v_rcp_f32 v206, v206\n"
"v_mfma_f32_16x16x32_bf16 v[72:75], v[214:217], a[88:91], v[72:75]\n"
"s_waitcnt lgkmcnt(2)\n"
"ds_read_b64_tr_b16 v[230:231], v246 offset:46144\n"
"ds_read_b64_tr_b16 v[232:233], v246 offset:50752\n"
"v_rcp_f32 v203, v203\n"
"v_rcp_f32 v207, v207\n"
"v_mul_f32 v124, v124, v200\n"
"v_mul_f32 v128, v128, v204\n"
"v_mul_f32 v125, v125, v201\n"
"v_mul_f32 v129, v129, v205\n"
"v_mul_f32 v126, v126, v202\n"
"v_mul_f32 v130, v130, v206\n"
"v_mfma_f32_16x16x32_bf16 v[4:7], v[218:221], a[76:79], 0\n"
"s_add_u32 s79, s83, 9216\n"
"ds_read_b64_tr_b16 v[234:235], v246 offset:36960\n"
"ds_read_b64_tr_b16 v[236:237], v246 offset:41568\n"
"v_mul_f32 v127, v127, v203\n"
"v_mul_f32 v131, v131, v207\n"
"v_cvt_pk_bf16_f32 v124, v124, v125\n"
"v_cvt_pk_bf16_f32 v125, v126, v127\n"
"v_cvt_pk_bf16_f32 v126, v128, v129\n"
"v_cvt_pk_bf16_f32 v127, v130, v131\n"
"v_exp_f32 v200, -v24\n"
"v_exp_f32 v204, -v28\n"
"v_mfma_f32_16x16x32_bf16 v[4:7], v[222:225], a[84:87], v[4:7]\n"
"buffer_load_dwordx4 a[112:115], v186, s[60:63], s79 offen offset:0 sc1\n"
"v_exp_f32 v201, -v25\n"
"v_exp_f32 v205, -v29\n"
"v_exp_f32 v202, -v26\n"
"v_exp_f32 v206, -v30\n"
"v_exp_f32 v203, -v27\n"
"v_mfma_f32_16x16x32_bf16 v[76:79], v[218:221], a[80:83], 0\n"
"buffer_store_dwordx4 v[124:127], v197, s[60:63], s91 offen offset:2048 sc1\n"
"v_exp_f32 v207, -v31\n"
"v_add_f32 v200, 1.0, v200\n"
"v_add_f32 v204, 1.0, v204\n"
"v_add_f32 v201, 1.0, v201\n"
"v_add_f32 v205, 1.0, v205\n"
"v_add_f32 v202, 1.0, v202\n"
"v_add_f32 v206, 1.0, v206\n"
"v_add_f32 v203, 1.0, v203\n"
"v_add_f32 v207, 1.0, v207\n"
"v_mfma_f32_16x16x32_bf16 v[76:79], v[222:225], a[88:91], v[76:79]\n"
"s_waitcnt lgkmcnt(2)\n"
"ds_read_b64_tr_b16 v[238:239], v246 offset:46176\n"
"ds_read_b64_tr_b16 v[240:241], v246 offset:50784\n"
"v_rcp_f32 v200, v200\n"
"v_rcp_f32 v204, v204\n"
"v_rcp_f32 v201, v201\n"
"v_rcp_f32 v205, v205\n"
"v_rcp_f32 v202, v202\n"
"v_mfma_f32_16x16x32_bf16 v[8:11], v[226:229], a[76:79], 0\n"
"buffer_load_dwordx4 a[116:119], v186, s[60:63], s78 offen offset:1024 sc1\n"
"v_rcp_f32 v206, v206\n"
"v_rcp_f32 v203, v203\n"
"v_rcp_f32 v207, v207\n"
"v_mul_f32 v24, v24, v200\n"
"v_mul_f32 v28, v28, v204\n"
"v_mul_f32 v25, v25, v201\n"
"v_mul_f32 v29, v29, v205\n"
"v_mfma_f32_16x16x32_bf16 v[8:11], v[230:233], a[84:87], v[8:11]\n"
"buffer_load_dwordx4 a[120:123], v186, s[60:63], s79 offen offset:1024 sc1\n"
"v_mul_f32 v26, v26, v202\n"
"v_mul_f32 v30, v30, v206\n"
"v_mul_f32 v27, v27, v203\n"
"v_mul_f32 v31, v31, v207\n"
"v_cvt_pk_bf16_f32 v24, v24, v25\n"
"v_cvt_pk_bf16_f32 v25, v26, v27\n"
"v_cvt_pk_bf16_f32 v26, v28, v29\n"
"v_cvt_pk_bf16_f32 v27, v30, v31\n"
"v_exp_f32 v200, -v60\n"
"v_mfma_f32_16x16x32_bf16 v[80:83], v[226:229], a[80:83], 0\n"
"s_nop 0\n"
"ds_read_b64_tr_b16 v[210:211], v246 offset:36992\n"
"ds_read_b64_tr_b16 v[212:213], v246 offset:41600\n"
"v_exp_f32 v204, -v64\n"
"v_exp_f32 v201, -v61\n"
"v_exp_f32 v205, -v65\n"
"v_exp_f32 v202, -v62\n"
"v_exp_f32 v206, -v66\n"
"v_mfma_f32_16x16x32_bf16 v[80:83], v[230:233], a[88:91], v[80:83]\n"
"s_waitcnt lgkmcnt(2)\n"
"ds_read_b64_tr_b16 v[214:215], v246 offset:46208\n"
"ds_read_b64_tr_b16 v[216:217], v246 offset:50816\n"
"v_exp_f32 v203, -v63\n"
"v_exp_f32 v207, -v67\n"
"v_add_f32 v200, 1.0, v200\n"
"v_add_f32 v204, 1.0, v204\n"
"v_add_f32 v201, 1.0, v201\n"
"v_add_f32 v205, 1.0, v205\n"
"v_add_f32 v202, 1.0, v202\n"
"v_add_f32 v206, 1.0, v206\n"
"v_mfma_f32_16x16x32_bf16 v[12:15], v[234:237], a[76:79], 0\n"
"buffer_store_dwordx4 v[24:27], v197, s[60:63], s92 offen offset:3072 sc1\n"
"v_add_f32 v203, 1.0, v203\n"
"v_add_f32 v207, 1.0, v207\n"
"v_rcp_f32 v200, v200\n"
"v_rcp_f32 v204, v204\n"
"v_rcp_f32 v201, v201\n"
"v_rcp_f32 v205, v205\n"
"v_mfma_f32_16x16x32_bf16 v[12:15], v[238:241], a[84:87], v[12:15]\n"
"s_add_u32 s78, s82, 14336\n"
"ds_read_b64_tr_b16 v[218:219], v246 offset:37024\n"
"ds_read_b64_tr_b16 v[220:221], v246 offset:41632\n"
"v_rcp_f32 v202, v202\n"
"v_rcp_f32 v206, v206\n"
"v_rcp_f32 v203, v203\n"
"v_rcp_f32 v207, v207\n"
"v_mul_f32 v60, v60, v200\n"
"v_mul_f32 v64, v64, v204\n"
"v_mfma_f32_16x16x32_bf16 v[84:87], v[234:237], a[80:83], 0\n"
"buffer_load_dwordx4 a[124:127], v186, s[60:63], s78 offen offset:0 sc1\n"
"v_mul_f32 v61, v61, v201\n"
"v_mul_f32 v65, v65, v205\n"
"v_mul_f32 v62, v62, v202\n"
"v_mul_f32 v66, v66, v206\n"
"v_mul_f32 v63, v63, v203\n"
"v_mul_f32 v67, v67, v207\n"
"v_cvt_pk_bf16_f32 v60, v60, v61\n"
"v_cvt_pk_bf16_f32 v61, v62, v63\n"
"v_cvt_pk_bf16_f32 v62, v64, v65\n"
"v_cvt_pk_bf16_f32 v63, v66, v67\n"
"v_mfma_f32_16x16x32_bf16 v[84:87], v[238:241], a[88:91], v[84:87]\n"
"s_waitcnt lgkmcnt(2)\n"
"ds_read_b64_tr_b16 v[222:223], v246 offset:46240\n"
"ds_read_b64_tr_b16 v[224:225], v246 offset:50848\n"
"v_exp_f32 v200, -v96\n"
"v_exp_f32 v204, -v100\n"
"v_exp_f32 v201, -v97\n"
"v_exp_f32 v205, -v101\n"
"v_exp_f32 v202, -v98\n"
"v_mfma_f32_16x16x32_bf16 v[36:39], v[210:213], a[76:79], 0\n"
"buffer_store_dwordx4 v[60:63], v197, s[60:63], s86 offen offset:3072 sc1\n"
"v_exp_f32 v206, -v102\n"
"v_exp_f32 v203, -v99\n"
"v_exp_f32 v207, -v103\n"
"v_add_f32 v200, 1.0, v200\n"
"v_add_f32 v204, 1.0, v204\n"
"v_add_f32 v201, 1.0, v201\n"
"v_add_f32 v205, 1.0, v205\n"
"v_mfma_f32_16x16x32_bf16 v[36:39], v[214:217], a[84:87], v[36:39]\n"
"s_add_u32 s79, s83, 14336\n"
"ds_read_b64_tr_b16 v[226:227], v246 offset:37056\n"
"ds_read_b64_tr_b16 v[228:229], v246 offset:41664\n"
"v_add_f32 v202, 1.0, v202\n"
"v_add_f32 v206, 1.0, v206\n"
"v_add_f32 v203, 1.0, v203\n"
"v_add_f32 v207, 1.0, v207\n"
"v_rcp_f32 v200, v200\n"
"v_rcp_f32 v204, v204\n"
"v_rcp_f32 v201, v201\n"
"v_mfma_f32_16x16x32_bf16 v[108:111], v[210:213], a[80:83], 0\n"
"buffer_load_dwordx4 a[128:131], v186, s[60:63], s79 offen offset:0 sc1\n"
"v_rcp_f32 v205, v205\n"
"v_rcp_f32 v202, v202\n"
"v_rcp_f32 v206, v206\n"
"v_rcp_f32 v203, v203\n"
"v_rcp_f32 v207, v207\n"
"v_mfma_f32_16x16x32_bf16 v[108:111], v[214:217], a[88:91], v[108:111]\n"
"s_waitcnt lgkmcnt(2)\n"
"ds_read_b64_tr_b16 v[230:231], v246 offset:46272\n"
"ds_read_b64_tr_b16 v[232:233], v246 offset:50880\n"
"v_mul_f32 v96, v96, v200\n"
"v_mul_f32 v100, v100, v204\n"
"v_mul_f32 v97, v97, v201\n"
"v_mul_f32 v101, v101, v205\n"
"v_mul_f32 v98, v98, v202\n"
"v_mul_f32 v102, v102, v206\n"
"v_mul_f32 v99, v99, v203\n"
"v_mul_f32 v103, v103, v207\n"
"v_cvt_pk_bf16_f32 v96, v96, v97\n"
"v_cvt_pk_bf16_f32 v97, v98, v99\n"
"v_mfma_f32_16x16x32_bf16 v[40:43], v[218:221], a[76:79], 0\n"
"buffer_load_dwordx4 a[132:135], v186, s[60:63], s78 offen offset:1024 sc1\n"
"v_cvt_pk_bf16_f32 v98, v100, v101\n"
"v_cvt_pk_bf16_f32 v99, v102, v103\n"
"v_exp_f32 v200, -v132\n"
"v_exp_f32 v204, -v136\n"
"v_exp_f32 v201, -v133\n"
"v_exp_f32 v205, -v137\n"
"v_mfma_f32_16x16x32_bf16 v[40:43], v[222:225], a[84:87], v[40:43]\n"
"buffer_store_dwordx4 v[96:99], v197, s[60:63], s87 offen offset:3072 sc1\n"
"v_exp_f32 v202, -v134\n"
"v_exp_f32 v206, -v138\n"
"v_exp_f32 v203, -v135\n"
"v_exp_f32 v207, -v139\n"
"v_add_f32 v200, 1.0, v200\n"
"v_add_f32 v204, 1.0, v204\n"
"v_mfma_f32_16x16x32_bf16 v[112:115], v[218:221], a[80:83], 0\n"
"s_nop 0\n"
"ds_read_b64_tr_b16 v[234:235], v246 offset:37088\n"
"ds_read_b64_tr_b16 v[236:237], v246 offset:41696\n"
"v_add_f32 v201, 1.0, v201\n"
"v_add_f32 v205, 1.0, v205\n"
"v_add_f32 v202, 1.0, v202\n"
"v_add_f32 v206, 1.0, v206\n"
"v_add_f32 v203, 1.0, v203\n"
"v_add_f32 v207, 1.0, v207\n"
"v_rcp_f32 v200, v200\n"
"v_rcp_f32 v204, v204\n"
"v_mfma_f32_16x16x32_bf16 v[112:115], v[222:225], a[88:91], v[112:115]\n"
"s_waitcnt lgkmcnt(2)\n"
"ds_read_b64_tr_b16 v[238:239], v246 offset:46304\n"
"ds_read_b64_tr_b16 v[240:241], v246 offset:50912\n"
"v_rcp_f32 v201, v201\n"
"v_rcp_f32 v205, v205\n"
"v_rcp_f32 v202, v202\n"
"v_rcp_f32 v206, v206\n"
"v_rcp_f32 v203, v203\n"
"v_mfma_f32_16x16x32_bf16 v[44:47], v[226:229], a[76:79], 0\n"
"buffer_load_dwordx4 a[136:139], v186, s[60:63], s79 offen offset:1024 sc1\n"
"v_rcp_f32 v207, v207\n"
"v_mul_f32 v132, v132, v200\n"
"v_mul_f32 v136, v136, v204\n"
"v_mul_f32 v133, v133, v201\n"
"v_mul_f32 v137, v137, v205\n"
"v_mul_f32 v134, v134, v202\n"
"v_mul_f32 v138, v138, v206\n"
"v_mul_f32 v135, v135, v203\n"
"v_mul_f32 v139, v139, v207\n"
"v_mfma_f32_16x16x32_bf16 v[44:47], v[230:233], a[84:87], v[44:47]\n"
"s_add_u32 s78, s82, 18432\n"
"ds_read_b64_tr_b16 v[210:211], v249 offset:36864\n"
"ds_read_b64_tr_b16 v[212:213], v249 offset:41472\n"
"v_cvt_pk_bf16_f32 v132, v132, v133\n"
"v_cvt_pk_bf16_f32 v133, v134, v135\n"
"v_cvt_pk_bf16_f32 v134, v136, v137\n"
"v_cvt_pk_bf16_f32 v135, v138, v139\n"
"v_exp_f32 v200, -v32\n"
"v_exp_f32 v204, -v68\n"
"v_exp_f32 v201, -v33\n"
"v_mfma_f32_16x16x32_bf16 v[116:119], v[226:229], a[80:83], 0\n"
"buffer_load_dwordx4 a[140:143], v186, s[60:63], s78 offen offset:0 sc1\n"
"v_exp_f32 v205, -v69\n"
"v_exp_f32 v202, -v34\n"
"v_exp_f32 v206, -v70\n"
"v_exp_f32 v203, -v35\n"
"v_exp_f32 v207, -v71\n"
"v_mfma_f32_16x16x32_bf16 v[116:119], v[230:233], a[88:91], v[116:119]\n"
"s_waitcnt lgkmcnt(2)\n"
"ds_read_b64_tr_b16 v[214:215], v249 offset:46080\n"
"ds_read_b64_tr_b16 v[216:217], v249 offset:50688\n"
"v_add_f32 v200, 1.0, v200\n"
"v_add_f32 v204, 1.0, v204\n"
"v_add_f32 v201, 1.0, v201\n"
"v_add_f32 v205, 1.0, v205\n"
"v_add_f32 v202, 1.0, v202\n"
"v_add_f32 v206, 1.0, v206\n"
"v_add_f32 v203, 1.0, v203\n"
"v_add_f32 v207, 1.0, v207\n"
"v_rcp_f32 v200, v200\n"
"v_mfma_f32_16x16x32_bf16 v[48:51], v[234:237], a[76:79], 0\n"
"buffer_store_dwordx4 v[132:135], v197, s[60:63], s91 offen offset:3072 sc1\n"
"v_rcp_f32 v204, v204\n"
"v_rcp_f32 v201, v201\n"
"v_rcp_f32 v205, v205\n"
"v_rcp_f32 v202, v202\n"
"v_rcp_f32 v206, v206\n"
"v_mfma_f32_16x16x32_bf16 v[48:51], v[238:241], a[84:87], v[48:51]\n"
"s_add_u32 s79, s83, 18432\n"
"ds_read_b64_tr_b16 v[218:219], v247 offset:0\n"
"ds_read_b64_tr_b16 v[220:221], v247 offset:4608\n"
"v_rcp_f32 v203, v203\n"
"v_rcp_f32 v207, v207\n"
"v_mul_f32 v32, v32, v200\n"
"v_mul_f32 v68, v68, v204\n"
"v_mul_f32 v33, v33, v201\n"
"v_mul_f32 v69, v69, v205\n"
"v_mul_f32 v34, v34, v202\n"
"v_mul_f32 v70, v70, v206\n"
"v_mfma_f32_16x16x32_bf16 v[120:123], v[234:237], a[80:83], 0\n"
"buffer_load_dwordx4 a[144:147], v186, s[60:63], s79 offen offset:0 sc1\n"
"v_mul_f32 v35, v35, v203\n"
"v_mul_f32 v71, v71, v207\n"
"v_cvt_pk_bf16_f32 v32, v32, v33\n"
"v_cvt_pk_bf16_f32 v33, v34, v35\n"
"v_cvt_pk_bf16_f32 v68, v68, v69\n"
"v_cvt_pk_bf16_f32 v69, v70, v71\n"
"v_exp_f32 v200, -v104\n"
"v_exp_f32 v204, -v140\n"
"v_mfma_f32_16x16x32_bf16 v[120:123], v[238:241], a[88:91], v[120:123]\n"
"s_waitcnt lgkmcnt(2)\n"
"ds_read_b64_tr_b16 v[222:223], v247 offset:9216\n"
"ds_read_b64_tr_b16 v[224:225], v247 offset:13824\n"
"v_exp_f32 v201, -v105\n"
"v_exp_f32 v205, -v141\n"
"v_exp_f32 v202, -v106\n"
"v_exp_f32 v206, -v142\n"
"v_exp_f32 v203, -v107\n"
"v_mfma_f32_16x16x32_bf16 v[144:147], v[210:213], a[76:79], 0\n"
"buffer_store_dwordx2 v[32:33], v198, s[60:63], s92 offen sc1\n"
"v_exp_f32 v207, -v143\n"
"v_add_f32 v200, 1.0, v200\n"
"v_add_f32 v204, 1.0, v204\n"
"v_add_f32 v201, 1.0, v201\n"
"v_add_f32 v205, 1.0, v205\n"
"v_add_f32 v202, 1.0, v202\n"
"v_add_f32 v206, 1.0, v206\n"
"v_add_f32 v203, 1.0, v203\n"
"v_add_f32 v207, 1.0, v207\n"
"v_mfma_f32_16x16x32_bf16 v[144:147], v[214:217], a[84:87], v[144:147]\n"
"buffer_store_dwordx2 v[68:69], v198, s[60:63], s86 offen sc1\n"
"v_rcp_f32 v200, v200\n"
"v_rcp_f32 v204, v204\n"
"v_rcp_f32 v201, v201\n"
"v_rcp_f32 v205, v205\n"
"v_rcp_f32 v202, v202\n"
"v_mfma_f32_16x16x32_bf16 v[148:151], v[210:213], a[80:83], 0\n"
"s_nop 0\n"
"ds_read_b64_tr_b16 v[226:227], v247 offset:32\n"
"ds_read_b64_tr_b16 v[228:229], v247 offset:4640\n"
"v_rcp_f32 v206, v206\n"
"v_rcp_f32 v203, v203\n"
"v_rcp_f32 v207, v207\n"
"v_mul_f32 v104, v104, v200\n"
"v_mul_f32 v140, v140, v204\n"
"v_mul_f32 v105, v105, v201\n"
"v_mul_f32 v141, v141, v205\n"
"v_mfma_f32_16x16x32_bf16 v[148:151], v[214:217], a[88:91], v[148:151]\n"
"s_waitcnt vmcnt(29) lgkmcnt(2)\n"
"ds_read_b64_tr_b16 v[230:231], v247 offset:9248\n"
"ds_read_b64_tr_b16 v[232:233], v247 offset:13856\n"
"v_mul_f32 v106, v106, v202\n"
"v_mul_f32 v142, v142, v206\n"
"v_mul_f32 v107, v107, v203\n"
"v_mfma_f32_16x16x32_bf16 v[0:3], v[218:221], a[92:95], v[0:3]\n"
"buffer_load_dwordx4 a[148:151], v186, s[60:63], s78 offen offset:1024 sc1\n"
"v_mfma_f32_16x16x32_bf16 v[0:3], v[222:225], a[100:103], v[0:3]\n"
"s_nop 0\n"
"ds_read_b64_tr_b16 v[234:235], v247 offset:64\n"
"ds_read_b64_tr_b16 v[236:237], v247 offset:4672\n"
"v_mfma_f32_16x16x32_bf16 v[72:75], v[218:221], a[96:99], v[72:75]\n"
"s_nop 0\n"
"ds_read_b64_tr_b16 v[238:239], v247 offset:9280\n"
"ds_read_b64_tr_b16 v[240:241], v247 offset:13888\n"
"v_mfma_f32_16x16x32_bf16 v[72:75], v[222:225], a[104:107], v[72:75]\n"
"s_waitcnt lgkmcnt(4)\n"
"v_mul_f32 v143, v143, v207\n"
"v_cvt_pk_bf16_f32 v104, v104, v105\n"
"v_mfma_f32_16x16x32_bf16 v[4:7], v[226:229], a[92:95], v[4:7]\n"
"buffer_load_dwordx4 a[152:155], v186, s[60:63], s79 offen offset:1024 sc1\n"
"v_mfma_f32_16x16x32_bf16 v[4:7], v[230:233], a[100:103], v[4:7]\n"
"s_add_u32 s78, s82, 23552\n"
"ds_read_b64_tr_b16 v[210:211], v247 offset:96\n"
"ds_read_b64_tr_b16 v[212:213], v247 offset:4704\n"
"v_mfma_f32_16x16x32_bf16 v[76:79], v[226:229], a[96:99], v[76:79]\n"
"s_nop 0\n"
"ds_read_b64_tr_b16 v[214:215], v247 offset:9312\n"
"ds_read_b64_tr_b16 v[216:217], v247 offset:13920\n"
"v_mfma_f32_16x16x32_bf16 v[76:79], v[230:233], a[104:107], v[76:79]\n"
"s_waitcnt lgkmcnt(4)\n"
"v_cvt_pk_bf16_f32 v105, v106, v107\n"
"v_cvt_pk_bf16_f32 v140, v140, v141\n"
"v_mfma_f32_16x16x32_bf16 v[8:11], v[234:237], a[92:95], v[8:11]\n"
"buffer_load_dwordx4 a[156:159], v186, s[60:63], s78 offen offset:0 sc1\n"
"v_mfma_f32_16x16x32_bf16 v[8:11], v[238:241], a[100:103], v[8:11]\n"
"s_nop 0\n"
"ds_read_b64_tr_b16 v[218:219], v247 offset:128\n"
"ds_read_b64_tr_b16 v[220:221], v247 offset:4736\n"
"v_mfma_f32_16x16x32_bf16 v[80:83], v[234:237], a[96:99], v[80:83]\n"
"s_nop 0\n"
"ds_read_b64_tr_b16 v[222:223], v247 offset:9344\n"
"ds_read_b64_tr_b16 v[224:225], v247 offset:13952\n"
"v_mfma_f32_16x16x32_bf16 v[80:83], v[238:241], a[104:107], v[80:83]\n"
"s_waitcnt lgkmcnt(4)\n"
"v_cvt_pk_bf16_f32 v141, v142, v143\n"
"s_lshl_b32 s72, s68, 6\n"
"v_mfma_f32_16x16x32_bf16 v[12:15], v[210:213], a[92:95], v[12:15]\n"
"buffer_store_dwordx2 v[104:105], v198, s[60:63], s87 offen sc1\n"
"v_mfma_f32_16x16x32_bf16 v[12:15], v[214:217], a[100:103], v[12:15]\n"
"s_nop 0\n"
"ds_read_b64_tr_b16 v[226:227], v247 offset:160\n"
"ds_read_b64_tr_b16 v[228:229], v247 offset:4768\n"
"v_mfma_f32_16x16x32_bf16 v[84:87], v[210:213], a[96:99], v[84:87]\n"
"buffer_store_dwordx2 v[140:141], v198, s[60:63], s91 offen sc1\n"
"v_mfma_f32_16x16x32_bf16 v[84:87], v[214:217], a[104:107], v[84:87]\n"
"s_waitcnt lgkmcnt(2)\n"
"ds_read_b64_tr_b16 v[230:231], v247 offset:9376\n"
"ds_read_b64_tr_b16 v[232:233], v247 offset:13984\n"
"v_mfma_f32_16x16x32_bf16 v[36:39], v[218:221], a[92:95], v[36:39]\n"
"s_add_u32 s79, s83, 23552\n"
"ds_read_b64_tr_b16 v[234:235], v247 offset:192\n"
"ds_read_b64_tr_b16 v[236:237], v247 offset:4800\n"
"v_mfma_f32_16x16x32_bf16 v[36:39], v[222:225], a[100:103], v[36:39]\n"
"buffer_load_dwordx4 a[160:163], v186, s[60:63], s79 offen offset:0 sc1\n"
"v_mfma_f32_16x16x32_bf16 v[108:111], v[218:221], a[96:99], v[108:111]\n"
"s_nop 0\n"
"ds_read_b64_tr_b16 v[238:239], v247 offset:9408\n"
"ds_read_b64_tr_b16 v[240:241], v247 offset:14016\n"
"v_mfma_f32_16x16x32_bf16 v[108:111], v[222:225], a[104:107], v[108:111]\n"
"s_waitcnt lgkmcnt(4)\n"
"v_add_u32 v153, s72, v199\n"
"s_mov_b32 s73, 1152\n"
"v_mfma_f32_16x16x32_bf16 v[40:43], v[226:229], a[92:95], v[40:43]\n"
"s_nop 0\n"
"ds_read_b64_tr_b16 v[210:211], v247 offset:224\n"
"ds_read_b64_tr_b16 v[212:213], v247 offset:4832\n"
"v_mfma_f32_16x16x32_bf16 v[40:43], v[230:233], a[100:103], v[40:43]\n"
"buffer_load_dwordx4 a[164:167], v186, s[60:63], s78 offen offset:1024 sc1\n"
"v_mfma_f32_16x16x32_bf16 v[112:115], v[226:229], a[96:99], v[112:115]\n"
"s_nop 0\n"
"ds_read_b64_tr_b16 v[214:215], v247 offset:9440\n"
"ds_read_b64_tr_b16 v[216:217], v247 offset:14048\n"
"v_mfma_f32_16x16x32_bf16 v[112:115], v[230:233], a[104:107], v[112:115]\n"
"s_waitcnt lgkmcnt(4)\n"
"v_add_u32 v154, 0, v153\n"
"v_mul_u32_u24 v155, 58255, v154\n"
"v_mfma_f32_16x16x32_bf16 v[44:47], v[234:237], a[92:95], v[44:47]\n"
"s_nop 0\n"
"ds_read_b64_tr_b16 v[218:219], v250 offset:0\n"
"ds_read_b64_tr_b16 v[220:221], v250 offset:4608\n"
"v_mfma_f32_16x16x32_bf16 v[44:47], v[238:241], a[100:103], v[44:47]\n"
"buffer_load_dwordx4 a[168:171], v186, s[60:63], s79 offen offset:1024 sc1\n"
"v_mfma_f32_16x16x32_bf16 v[116:119], v[234:237], a[96:99], v[116:119]\n"
"s_add_u32 m0, s71, 12288\n"
"ds_read_b64_tr_b16 v[222:223], v250 offset:9216\n"
"ds_read_b64_tr_b16 v[224:225], v250 offset:13824\n"
"v_mfma_f32_16x16x32_bf16 v[116:119], v[238:241], a[104:107], v[116:119]\n"
"s_waitcnt vmcnt(49) lgkmcnt(4)\n"
"s_barrier\n"
"v_mfma_f32_16x16x32_bf16 v[48:51], v[210:213], a[92:95], v[48:51]\n"
"s_nop 0\n"
"ds_read_b64_tr_b16 v[226:227], v247 offset:18432\n"
"ds_read_b64_tr_b16 v[228:229], v247 offset:23040\n"
"v_mfma_f32_16x16x32_bf16 v[48:51], v[214:217], a[100:103], v[48:51]\n"
"s_nop 0\n"
"ds_read_b64_tr_b16 v[230:231], v247 offset:27648\n"
"ds_read_b64_tr_b16 v[232:233], v247 offset:32256\n"
"v_mfma_f32_16x16x32_bf16 v[120:123], v[210:213], a[96:99], v[120:123]\n"
"buffer_load_dwordx4 v171, s[44:47], s76 offen lds\n"
"v_mfma_f32_16x16x32_bf16 v[120:123], v[214:217], a[104:107], v[120:123]\n"
"s_waitcnt lgkmcnt(4)\n"
"s_barrier\n"
"v_mfma_f32_16x16x32_bf16 v[144:147], v[218:221], a[92:95], v[144:147]\n"
"s_add_u32 m0, s71, 16384\n"
"ds_read_b64_tr_b16 v[234:235], v247 offset:18464\n"
"ds_read_b64_tr_b16 v[236:237], v247 offset:23072\n"
"v_mfma_f32_16x16x32_bf16 v[144:147], v[222:225], a[100:103], v[144:147]\n"
"s_nop 0\n"
"ds_read_b64_tr_b16 v[238:239], v247 offset:27680\n"
"ds_read_b64_tr_b16 v[240:241], v247 offset:32288\n"
"v_mfma_f32_16x16x32_bf16 v[148:151], v[218:221], a[96:99], v[148:151]\n"
"buffer_load_dwordx4 v172, s[44:47], s76 offen lds\n"
"v_mfma_f32_16x16x32_bf16 v[148:151], v[222:225], a[104:107], v[148:151]\n"
"s_waitcnt vmcnt(22) lgkmcnt(4)\n"
"v_lshrrev_b32 v155, 20, v155\n"
"v_mul_u32_u24 v156, 18, v155\n"
"v_mfma_f32_16x16x32_bf16 v[0:3], v[226:229], a[108:111], v[0:3]\n"
"s_add_u32 m0, s71, 20480\n"
"ds_read_b64_tr_b16 v[210:211], v247 offset:18496\n"
"ds_read_b64_tr_b16 v[212:213], v247 offset:23104\n"
"v_mfma_f32_16x16x32_bf16 v[0:3], v[230:233], a[116:119], v[0:3]\n"
"s_nop 0\n"
"ds_read_b64_tr_b16 v[214:215], v247 offset:27712\n"
"ds_read_b64_tr_b16 v[216:217], v247 offset:32320\n"
"v_mfma_f32_16x16x32_bf16 v[72:75], v[226:229], a[112:115], v[72:75]\n"
"v_sub_u32 v154, v154, v156\n"
"v_lshrrev_b32 v156, 5, v155\n"
"v_mfma_f32_16x16x32_bf16 v[72:75], v[230:233], a[120:123], v[72:75]\n"
"s_waitcnt lgkmcnt(4)\n"
"v_lshl_add_u32 v155, v156, 8, v155\n"
"v_mul_u32_u24 v155, s73, v155\n"
"v_mfma_f32_16x16x32_bf16 v[4:7], v[234:237], a[108:111], v[4:7]\n"
"buffer_load_dwordx4 v173, s[44:47], s76 offen lds\n"
"v_mfma_f32_16x16x32_bf16 v[4:7], v[238:241], a[116:119], v[4:7]\n"
"s_add_u32 m0, s71, 24576\n"
"ds_read_b64_tr_b16 v[218:219], v247 offset:18528\n"
"ds_read_b64_tr_b16 v[220:221], v247 offset:23136\n"
"v_mfma_f32_16x16x32_bf16 v[76:79], v[234:237], a[112:115], v[76:79]\n"
"s_nop 0\n"
"ds_read_b64_tr_b16 v[222:223], v247 offset:27744\n"
"ds_read_b64_tr_b16 v[224:225], v247 offset:32352\n"
"v_mfma_f32_16x16x32_bf16 v[76:79], v[238:241], a[120:123], v[76:79]\n"
"s_waitcnt lgkmcnt(4)\n"
"v_lshl_add_u32 v177, v154, 4, v155\n"
"v_add_u32 v154, 256, v153\n"
"v_mfma_f32_16x16x32_bf16 v[8:11], v[210:213], a[108:111], v[8:11]\n"
"buffer_load_dwordx4 v174, s[44:47], s76 offen lds\n"
"v_mfma_f32_16x16x32_bf16 v[8:11], v[214:217], a[116:119], v[8:11]\n"
"s_add_u32 m0, s71, 28672\n"
"ds_read_b64_tr_b16 v[226:227], v247 offset:18560\n"
"ds_read_b64_tr_b16 v[228:229], v247 offset:23168\n"
"v_mfma_f32_16x16x32_bf16 v[80:83], v[210:213], a[112:115], v[80:83]\n"
"s_nop 0\n"
"ds_read_b64_tr_b16 v[230:231], v247 offset:27776\n"
"ds_read_b64_tr_b16 v[232:233], v247 offset:32384\n"
"v_mfma_f32_16x16x32_bf16 v[80:83], v[214:217], a[120:123], v[80:83]\n"
"s_waitcnt lgkmcnt(4)\n"
"v_mul_u32_u24 v155, 58255, v154\n"
"v_lshrrev_b32 v155, 20, v155\n"
"v_mfma_f32_16x16x32_bf16 v[12:15], v[218:221], a[108:111], v[12:15]\n"
"buffer_load_dwordx4 v175, s[44:47], s76 offen lds\n"
"v_mfma_f32_16x16x32_bf16 v[12:15], v[222:225], a[116:119], v[12:15]\n"
"s_add_u32 m0, s71, 32768\n"
"ds_read_b64_tr_b16 v[234:235], v247 offset:18592\n"
"ds_read_b64_tr_b16 v[236:237], v247 offset:23200\n"
"v_mfma_f32_16x16x32_bf16 v[84:87], v[218:221], a[112:115], v[84:87]\n"
"s_nop 0\n"
"ds_read_b64_tr_b16 v[238:239], v247 offset:27808\n"
"ds_read_b64_tr_b16 v[240:241], v247 offset:32416\n"
"v_mfma_f32_16x16x32_bf16 v[84:87], v[222:225], a[120:123], v[84:87]\n"
"s_waitcnt lgkmcnt(4)\n"
"v_mul_u32_u24 v156, 18, v155\n"
"v_sub_u32 v154, v154, v156\n"
"v_mov_b32 v209, 256\n"
"s_waitcnt vmcnt(8)\n"
"s_mov_b64 exec, 1\n"
"buffer_atomic_add v209, off, s[60:63], s65\n"
"s_mov_b64 exec, -1\n"
"v_mfma_f32_16x16x32_bf16 v[36:39], v[226:229], a[108:111], v[36:39]\n"
"buffer_load_dwordx4 v176, s[44:47], s76 offen lds\n"
"v_mfma_f32_16x16x32_bf16 v[36:39], v[230:233], a[116:119], v[36:39]\n"
"s_add_u32 s78, s82, 27648\n"
"ds_read_b64_tr_b16 v[210:211], v247 offset:18624\n"
"ds_read_b64_tr_b16 v[212:213], v247 offset:23232\n"
"v_mfma_f32_16x16x32_bf16 v[108:111], v[226:229], a[112:115], v[108:111]\n"
"s_nop 0\n"
"ds_read_b64_tr_b16 v[214:215], v247 offset:27840\n"
"ds_read_b64_tr_b16 v[216:217], v247 offset:32448\n"
"v_mfma_f32_16x16x32_bf16 v[108:111], v[230:233], a[120:123], v[108:111]\n"
"s_waitcnt lgkmcnt(4)\n"
"v_lshrrev_b32 v156, 5, v155\n"
"v_lshl_add_u32 v155, v156, 8, v155\n"
"v_mfma_f32_16x16x32_bf16 v[40:43], v[234:237], a[108:111], v[40:43]\n"
"s_nop 0\n"
"ds_read_b64_tr_b16 v[218:219], v247 offset:18656\n"
"ds_read_b64_tr_b16 v[220:221], v247 offset:23264\n"
"v_mfma_f32_16x16x32_bf16 v[40:43], v[238:241], a[116:119], v[40:43]\n"
"buffer_load_dwordx4 a[172:175], v186, s[60:63], s78 offen offset:0 sc1\n"
"v_mfma_f32_16x16x32_bf16 v[112:115], v[234:237], a[112:115], v[112:115]\n"
"s_add_u32 s79, s83, 27648\n"
"ds_read_b64_tr_b16 v[222:223], v247 offset:27872\n"
"ds_read_b64_tr_b16 v[224:225], v247 offset:32480\n"
"v_mfma_f32_16x16x32_bf16 v[112:115], v[238:241], a[120:123], v[112:115]\n"
"s_waitcnt lgkmcnt(4)\n"
"v_mul_u32_u24 v155, s73, v155\n"
"v_lshl_add_u32 v178, v154, 4, v155\n"
"v_mfma_f32_16x16x32_bf16 v[44:47], v[210:213], a[108:111], v[44:47]\n"
"s_nop 0\n"
"ds_read_b64_tr_b16 v[226:227], v250 offset:18432\n"
"ds_read_b64_tr_b16 v[228:229], v250 offset:23040\n"
"v_mfma_f32_16x16x32_bf16 v[44:47], v[214:217], a[116:119], v[44:47]\n"
"buffer_load_dwordx4 a[176:179], v186, s[60:63], s79 offen offset:0 sc1\n"
"v_mfma_f32_16x16x32_bf16 v[116:119], v[210:213], a[112:115], v[116:119]\n"
"s_nop 0\n"
"ds_read_b64_tr_b16 v[230:231], v250 offset:27648\n"
"ds_read_b64_tr_b16 v[232:233], v250 offset:32256\n"
"v_mfma_f32_16x16x32_bf16 v[116:119], v[214:217], a[120:123], v[116:119]\n"
"s_waitcnt lgkmcnt(4)\n"
"v_add_u32 v154, 512, v153\n"
"v_mul_u32_u24 v155, 58255, v154\n"
"v_mfma_f32_16x16x32_bf16 v[48:51], v[218:221], a[108:111], v[48:51]\n"
"s_nop 0\n"
"ds_read_b64_tr_b16 v[234:235], v247 offset:36864\n"
"ds_read_b64_tr_b16 v[236:237], v247 offset:41472\n"
"v_mfma_f32_16x16x32_bf16 v[48:51], v[222:225], a[116:119], v[48:51]\n"
"s_nop 0\n"
"ds_read_b64_tr_b16 v[238:239], v247 offset:46080\n"
"ds_read_b64_tr_b16 v[240:241], v247 offset:50688\n"
"v_mfma_f32_16x16x32_bf16 v[120:123], v[218:221], a[112:115], v[120:123]\n"
"buffer_load_dwordx4 a[180:183], v186, s[60:63], s78 offen offset:1024 sc1\n"
"v_mfma_f32_16x16x32_bf16 v[120:123], v[222:225], a[120:123], v[120:123]\n"
"s_waitcnt lgkmcnt(4)\n"
"v_lshrrev_b32 v155, 20, v155\n"
"v_mul_u32_u24 v156, 18, v155\n"
"v_mfma_f32_16x16x32_bf16 v[144:147], v[226:229], a[108:111], v[144:147]\n"
"L_tp2_0_1:\n"
"s_cbranch_execz L_tb2_0\n"
"ds_read_b64_tr_b16 v[210:211], v247 offset:36896\n"
"ds_read_b64_tr_b16 v[212:213], v247 offset:41504\n"
"v_mfma_f32_16x16x32_bf16 v[144:147], v[230:233], a[116:119], v[144:147]\n"
"s_nop 0\n"
"ds_read_b64_tr_b16 v[214:215], v247 offset:46112\n"
"ds_read_b64_tr_b16 v[216:217], v247 offset:50720\n"
"v_mfma_f32_16x16x32_bf16 v[148:151], v[226:229], a[112:115], v[148:151]\n"
"buffer_load_dwordx4 a[184:187], v186, s[60:63], s79 offen offset:1024 sc1\n"
"v_mfma_f32_16x16x32_bf16 v[148:151], v[230:233], a[120:123], v[148:151]\n"
"s_waitcnt vmcnt(24) lgkmcnt(4)\n"
"v_sub_u32 v154, v154, v156\n"
"v_lshrrev_b32 v156, 5, v155\n"
"v_mfma_f32_16x16x32_bf16 v[0:3], v[234:237], a[124:127], v[0:3]\n"
"s_add_u32 m0, s71, 36864\n"
"ds_read_b64_tr_b16 v[218:219], v247 offset:36928\n"
"ds_read_b64_tr_b16 v[220:221], v247 offset:41536\n"
"v_mfma_f32_16x16x32_bf16 v[0:3], v[238:241], a[132:135], v[0:3]\n"
"s_mov_b32 s76, 73728\n"
"ds_read_b64_tr_b16 v[222:223], v247 offset:46144\n"
"ds_read_b64_tr_b16 v[224:225], v247 offset:50752\n"
"v_mfma_f32_16x16x32_bf16 v[72:75], v[234:237], a[128:131], v[72:75]\n"
"v_lshl_add_u32 v155, v156, 8, v155\n"
"v_mul_u32_u24 v155, s73, v155\n"
"v_mfma_f32_16x16x32_bf16 v[72:75], v[238:241], a[136:139], v[72:75]\n"
"s_waitcnt lgkmcnt(4)\n"
"v_lshl_add_u32 v179, v154, 4, v155\n"
"v_add_u32 v154, 768, v153\n"
"v_mfma_f32_16x16x32_bf16 v[4:7], v[210:213], a[124:127], v[4:7]\n"
"buffer_load_dwordx4 v168, s[44:47], s76 offen lds\n"
"v_mfma_f32_16x16x32_bf16 v[4:7], v[214:217], a[132:135], v[4:7]\n"
"s_add_u32 m0, s71, 40960\n"
"ds_read_b64_tr_b16 v[226:227], v247 offset:36960\n"
"ds_read_b64_tr_b16 v[228:229], v247 offset:41568\n"
"v_mfma_f32_16x16x32_bf16 v[76:79], v[210:213], a[128:131], v[76:79]\n"
"s_nop 0\n"
"ds_read_b64_tr_b16 v[230:231], v247 offset:46176\n"
"ds_read_b64_tr_b16 v[232:233], v247 offset:50784\n"
"v_mfma_f32_16x16x32_bf16 v[76:79], v[214:217], a[136:139], v[76:79]\n"
"s_waitcnt lgkmcnt(4)\n"
"v_mul_u32_u24 v155, 58255, v154\n"
"v_lshrrev_b32 v155, 20, v155\n"
"v_mfma_f32_16x16x32_bf16 v[8:11], v[218:221], a[124:127], v[8:11]\n"
"buffer_load_dwordx4 v169, s[44:47], s76 offen lds\n"
"v_mfma_f32_16x16x32_bf16 v[8:11], v[222:225], a[132:135], v[8:11]\n"
"s_add_u32 s78, s82, 32768\n"
"ds_read_b64_tr_b16 v[234:235], v247 offset:36992\n"
"ds_read_b64_tr_b16 v[236:237], v247 offset:41600\n"
"v_mfma_f32_16x16x32_bf16 v[80:83], v[218:221], a[128:131], v[80:83]\n"
"s_nop 0\n"
"ds_read_b64_tr_b16 v[238:239], v247 offset:46208\n"
"ds_read_b64_tr_b16 v[240:241], v247 offset:50816\n"
"v_mfma_f32_16x16x32_bf16 v[80:83], v[222:225], a[136:139], v[80:83]\n"
"s_waitcnt lgkmcnt(4)\n"
"v_mul_u32_u24 v156, 18, v155\n"
"v_sub_u32 v154, v154, v156\n"
"v_mfma_f32_16x16x32_bf16 v[12:15], v[226:229], a[124:127], v[12:15]\n"
"buffer_load_dwordx4 a[188:191], v186, s[60:63], s78 offen offset:0 sc1\n"
"v_mfma_f32_16x16x32_bf16 v[12:15], v[230:233], a[132:135], v[12:15]\n"
"s_add_u32 s79, s83, 32768\n"
"ds_read_b64_tr_b16 v[210:211], v247 offset:37024\n"
"ds_read_b64_tr_b16 v[212:213], v247 offset:41632\n"
"v_mfma_f32_16x16x32_bf16 v[84:87], v[226:229], a[128:131], v[84:87]\n"
"s_nop 0\n"
"ds_read_b64_tr_b16 v[214:215], v247 offset:46240\n"
"ds_read_b64_tr_b16 v[216:217], v247 offset:50848\n"
"v_mfma_f32_16x16x32_bf16 v[84:87], v[230:233], a[136:139], v[84:87]\n"
"s_waitcnt lgkmcnt(4)\n"
"v_lshrrev_b32 v156, 5, v155\n"
"v_lshl_add_u32 v155, v156, 8, v155\n"
"v_mfma_f32_16x16x32_bf16 v[36:39], v[234:237], a[124:127], v[36:39]\n"
"buffer_load_dwordx4 a[192:195], v186, s[60:63], s79 offen offset:0 sc1\n"
"v_mfma_f32_16x16x32_bf16 v[36:39], v[238:241], a[132:135], v[36:39]\n"
"s_nop 0\n"
"ds_read_b64_tr_b16 v[218:219], v247 offset:37056\n"
"ds_read_b64_tr_b16 v[220:221], v247 offset:41664\n"
"v_mfma_f32_16x16x32_bf16 v[108:111], v[234:237], a[128:131], v[108:111]\n"
"s_nop 0\n"
"ds_read_b64_tr_b16 v[222:223], v247 offset:46272\n"
"ds_read_b64_tr_b16 v[224:225], v247 offset:50880\n"
"v_mfma_f32_16x16x32_bf16 v[108:111], v[238:241], a[136:139], v[108:111]\n"
"s_waitcnt lgkmcnt(4)\n"
"v_mul_u32_u24 v155, s73, v155\n"
"v_lshl_add_u32 v180, v154, 4, v155\n"
"v_mfma_f32_16x16x32_bf16 v[40:43], v[210:213], a[124:127], v[40:43]\n"
"s_nop 0\n"
"ds_read_b64_tr_b16 v[226:227], v247 offset:37088\n"
"ds_read_b64_tr_b16 v[228:229], v247 offset:41696\n"
"v_mfma_f32_16x16x32_bf16 v[40:43], v[214:217], a[132:135], v[40:43]\n"
"buffer_load_dwordx4 a[196:199], v186, s[60:63], s78 offen offset:1024 sc1\n"
"v_mfma_f32_16x16x32_bf16 v[112:115], v[210:213], a[128:131], v[112:115]\n"
"s_nop 0\n"
"ds_read_b64_tr_b16 v[230:231], v247 offset:46304\n"
"ds_read_b64_tr_b16 v[232:233], v247 offset:50912\n"
"v_mfma_f32_16x16x32_bf16 v[112:115], v[214:217], a[136:139], v[112:115]\n"
"s_waitcnt lgkmcnt(4)\n"
"v_add_u32 v154, 1024, v153\n"
"v_mul_u32_u24 v155, 58255, v154\n"
"v_mfma_f32_16x16x32_bf16 v[44:47], v[218:221], a[124:127], v[44:47]\n"
"s_nop 0\n"
"ds_read_b64_tr_b16 v[234:235], v250 offset:36864\n"
"ds_read_b64_tr_b16 v[236:237], v250 offset:41472\n"
"v_mfma_f32_16x16x32_bf16 v[44:47], v[222:225], a[132:135], v[44:47]\n"
"buffer_load_dwordx4 a[200:203], v186, s[60:63], s79 offen offset:1024 sc1\n"
"v_mfma_f32_16x16x32_bf16 v[116:119], v[218:221], a[128:131], v[116:119]\n"
"s_add_u32 m0, s71, 45056\n"
"ds_read_b64_tr_b16 v[238:239], v250 offset:46080\n"
"ds_read_b64_tr_b16 v[240:241], v250 offset:50688\n"
"v_mfma_f32_16x16x32_bf16 v[116:119], v[222:225], a[136:139], v[116:119]\n"
"s_waitcnt vmcnt(48) lgkmcnt(4)\n"
"s_barrier\n"
"v_mfma_f32_16x16x32_bf16 v[48:51], v[226:229], a[124:127], v[48:51]\n"
"s_nop 0\n"
"ds_read_b64_tr_b16 v[210:211], v248 offset:0\n"
"ds_read_b64_tr_b16 v[212:213], v248 offset:4608\n"
"v_mfma_f32_16x16x32_bf16 v[48:51], v[230:233], a[132:135], v[48:51]\n"
"s_nop 0\n"
"ds_read_b64_tr_b16 v[214:215], v248 offset:9216\n"
"ds_read_b64_tr_b16 v[216:217], v248 offset:13824\n"
"v_mfma_f32_16x16x32_bf16 v[120:123], v[226:229], a[128:131], v[120:123]\n"
"buffer_load_dwordx4 v170, s[44:47], s76 offen lds\n"
"v_mfma_f32_16x16x32_bf16 v[120:123], v[230:233], a[136:139], v[120:123]\n"
"s_waitcnt lgkmcnt(4)\n"
"s_barrier\n"
"v_mfma_f32_16x16x32_bf16 v[144:147], v[234:237], a[124:127], v[144:147]\n"
"s_add_u32 m0, s71, 49152\n"
"ds_read_b64_tr_b16 v[218:219], v248 offset:32\n"
"ds_read_b64_tr_b16 v[220:221], v248 offset:4640\n"
"v_mfma_f32_16x16x32_bf16 v[144:147], v[238:241], a[132:135], v[144:147]\n"
"s_nop 0\n"
"ds_read_b64_tr_b16 v[222:223], v248 offset:9248\n"
"ds_read_b64_tr_b16 v[224:225], v248 offset:13856\n"
"v_mfma_f32_16x16x32_bf16 v[148:151], v[234:237], a[128:131], v[148:151]\n"
"buffer_load_dwordx4 v171, s[44:47], s76 offen lds\n"
"v_mfma_f32_16x16x32_bf16 v[148:151], v[238:241], a[136:139], v[148:151]\n"
"s_waitcnt vmcnt(25) lgkmcnt(4)\n"
"v_lshrrev_b32 v155, 20, v155\n"
"v_mul_u32_u24 v156, 18, v155\n"
"v_mfma_f32_16x16x32_bf16 v[0:3], v[210:213], a[140:143], v[0:3]\n"
"s_add_u32 m0, s71, 53248\n"
"ds_read_b64_tr_b16 v[226:227], v248 offset:64\n"
"ds_read_b64_tr_b16 v[228:229], v248 offset:4672\n"
"v_mfma_f32_16x16x32_bf16 v[0:3], v[214:217], a[148:151], v[0:3]\n"
"s_nop 0\n"
"ds_read_b64_tr_b16 v[230:231], v248 offset:9280\n"
"ds_read_b64_tr_b16 v[232:233], v248 offset:13888\n"
"v_mfma_f32_16x16x32_bf16 v[72:75], v[210:213], a[144:147], v[72:75]\n"
"v_sub_u32 v154, v154, v156\n"
"v_lshrrev_b32 v156, 5, v155\n"
"v_mfma_f32_16x16x32_bf16 v[72:75], v[214:217], a[152:155], v[72:75]\n"
"s_waitcnt lgkmcnt(4)\n"
"v_lshl_add_u32 v155, v156, 8, v155\n"
"v_mul_u32_u24 v155, s73, v155\n"
"v_mfma_f32_16x16x32_bf16 v[4:7], v[218:221], a[140:143], v[4:7]\n"
"buffer_load_dwordx4 v172, s[44:47], s76 offen lds\n"
"v_mfma_f32_16x16x32_bf16 v[4:7], v[222:225], a[148:151], v[4:7]\n"
"s_add_u32 m0, s71, 57344\n"
"ds_read_b64_tr_b16 v[234:235], v248 offset:96\n"
"ds_read_b64_tr_b16 v[236:237], v248 offset:4704\n"
"v_mfma_f32_16x16x32_bf16 v[76:79], v[218:221], a[144:147], v[76:79]\n"
"s_nop 0\n"
"ds_read_b64_tr_b16 v[238:239], v248 offset:9312\n"
"ds_read_b64_tr_b16 v[240:241], v248 offset:13920\n"
"v_mfma_f32_16x16x32_bf16 v[76:79], v[222:225], a[152:155], v[76:79]\n"
"s_waitcnt lgkmcnt(4)\n"
"v_lshl_add_u32 v181, v154, 4, v155\n"
"v_add_u32 v154, 1280, v153\n"
"v_mfma_f32_16x16x32_bf16 v[8:11], v[226:229], a[140:143], v[8:11]\n"
"buffer_load_dwordx4 v173, s[44:47], s76 offen lds\n"
"v_mfma_f32_16x16x32_bf16 v[8:11], v[230:233], a[148:151], v[8:11]\n"
"s_add_u32 m0, s71, 61440\n"
"ds_read_b64_tr_b16 v[210:211], v248 offset:128\n"
"ds_read_b64_tr_b16 v[212:213], v248 offset:4736\n"
"v_mfma_f32_16x16x32_bf16 v[80:83], v[226:229], a[144:147], v[80:83]\n"
"s_nop 0\n"
"ds_read_b64_tr_b16 v[214:215], v248 offset:9344\n"
"ds_read_b64_tr_b16 v[216:217], v248 offset:13952\n"
"v_mfma_f32_16x16x32_bf16 v[80:83], v[230:233], a[152:155], v[80:83]\n"
"s_waitcnt lgkmcnt(4)\n"
"v_mul_u32_u24 v155, 58255, v154\n"
"v_lshrrev_b32 v155, 20, v155\n"
"v_mfma_f32_16x16x32_bf16 v[12:15], v[234:237], a[140:143], v[12:15]\n"
"buffer_load_dwordx4 v174, s[44:47], s76 offen lds\n"
"v_mfma_f32_16x16x32_bf16 v[12:15], v[238:241], a[148:151], v[12:15]\n"
"s_add_u32 m0, s71, 65536\n"
"ds_read_b64_tr_b16 v[218:219], v248 offset:160\n"
"ds_read_b64_tr_b16 v[220:221], v248 offset:4768\n"
"v_mfma_f32_16x16x32_bf16 v[84:87], v[234:237], a[144:147], v[84:87]\n"
"s_nop 0\n"
"ds_read_b64_tr_b16 v[222:223], v248 offset:9376\n"
"ds_read_b64_tr_b16 v[224:225], v248 offset:13984\n"
"v_mfma_f32_16x16x32_bf16 v[84:87], v[238:241], a[152:155], v[84:87]\n"
"s_waitcnt lgkmcnt(4)\n"
"v_mul_u32_u24 v156, 18, v155\n"
"v_sub_u32 v154, v154, v156\n"
"v_mfma_f32_16x16x32_bf16 v[36:39], v[210:213], a[140:143], v[36:39]\n"
"buffer_load_dwordx4 v175, s[44:47], s76 offen lds\n"
"v_mfma_f32_16x16x32_bf16 v[36:39], v[214:217], a[148:151], v[36:39]\n"
"s_add_u32 m0, s71, 69632\n"
"ds_read_b64_tr_b16 v[226:227], v248 offset:192\n"
"ds_read_b64_tr_b16 v[228:229], v248 offset:4800\n"
"v_mfma_f32_16x16x32_bf16 v[108:111], v[210:213], a[144:147], v[108:111]\n"
"s_nop 0\n"
"ds_read_b64_tr_b16 v[230:231], v248 offset:9408\n"
"ds_read_b64_tr_b16 v[232:233], v248 offset:14016\n"
"v_mfma_f32_16x16x32_bf16 v[108:111], v[214:217], a[152:155], v[108:111]\n"
"s_waitcnt lgkmcnt(4)\n"
"v_lshrrev_b32 v156, 5, v155\n"
"v_lshl_add_u32 v155, v156, 8, v155\n"
"v_mfma_f32_16x16x32_bf16 v[40:43], v[218:221], a[140:143], v[40:43]\n"
"s_nop 0\n"
"ds_read_b64_tr_b16 v[234:235], v248 offset:224\n"
"ds_read_b64_tr_b16 v[236:237], v248 offset:4832\n"
"v_mfma_f32_16x16x32_bf16 v[40:43], v[222:225], a[148:151], v[40:43]\n"
"buffer_load_dwordx4 v176, s[44:47], s76 offen lds\n"
"v_mfma_f32_16x16x32_bf16 v[112:115], v[218:221], a[144:147], v[112:115]\n"
"s_add_u32 m0, s71, 73728\n"
"ds_read_b64_tr_b16 v[238:239], v248 offset:9440\n"
"ds_read_b64_tr_b16 v[240:241], v248 offset:14048\n"
"v_mfma_f32_16x16x32_bf16 v[112:115], v[222:225], a[152:155], v[112:115]\n"
"s_waitcnt lgkmcnt(4)\n"
"v_mul_u32_u24 v155, s73, v155\n"
"v_lshl_add_u32 v182, v154, 4, v155\n"
"v_mfma_f32_16x16x32_bf16 v[44:47], v[226:229], a[140:143], v[44:47]\n"
"s_mov_b32 s76, 405504\n"
"ds_read_b64_tr_b16 v[210:211], v251 offset:0\n"
"ds_read_b64_tr_b16 v[212:213], v251 offset:4608\n"
"v_mfma_f32_16x16x32_bf16 v[44:47], v[230:233], a[148:151], v[44:47]\n"
"buffer_load_dwordx4 v168, s[44:47], s76 offen lds\n"
"v_mfma_f32_16x16x32_bf16 v[116:119], v[226:229], a[144:147], v[116:119]\n"
"s_add_u32 m0, s71, 77824\n"
"ds_read_b64_tr_b16 v[214:215], v251 offset:9216\n"
"ds_read_b64_tr_b16 v[216:217], v251 offset:13824\n"
"v_mfma_f32_16x16x32_bf16 v[116:119], v[230:233], a[152:155], v[116:119]\n"
"s_waitcnt lgkmcnt(4)\n"
"v_add_u32 v154, 1536, v153\n"
"v_mul_u32_u24 v155, 58255, v154\n"
"v_mfma_f32_16x16x32_bf16 v[48:51], v[234:237], a[140:143], v[48:51]\n"
"s_nop 0\n"
"ds_read_b64_tr_b16 v[218:219], v248 offset:18432\n"
"ds_read_b64_tr_b16 v[220:221], v248 offset:23040\n"
"v_mfma_f32_16x16x32_bf16 v[48:51], v[238:241], a[148:151], v[48:51]\n"
"s_nop 0\n"
"ds_read_b64_tr_b16 v[222:223], v248 offset:27648\n"
"ds_read_b64_tr_b16 v[224:225], v248 offset:32256\n"
"v_mfma_f32_16x16x32_bf16 v[120:123], v[234:237], a[144:147], v[120:123]\n"
"buffer_load_dwordx4 v169, s[44:47], s76 offen lds\n"
"v_mfma_f32_16x16x32_bf16 v[120:123], v[238:241], a[152:155], v[120:123]\n"
"s_waitcnt lgkmcnt(4)\n"
"v_lshrrev_b32 v155, 20, v155\n"
"v_mul_u32_u24 v156, 18, v155\n"
"v_mfma_f32_16x16x32_bf16 v[144:147], v[210:213], a[140:143], v[144:147]\n"
"s_add_u32 m0, s71, 81920\n"
"ds_read_b64_tr_b16 v[226:227], v248 offset:18464\n"
"ds_read_b64_tr_b16 v[228:229], v248 offset:23072\n"
"v_mfma_f32_16x16x32_bf16 v[144:147], v[214:217], a[148:151], v[144:147]\n"
"s_nop 0\n"
"ds_read_b64_tr_b16 v[230:231], v248 offset:27680\n"
"ds_read_b64_tr_b16 v[232:233], v248 offset:32288\n"
"v_mfma_f32_16x16x32_bf16 v[148:151], v[210:213], a[144:147], v[148:151]\n"
"buffer_load_dwordx4 v170, s[44:47], s76 offen lds\n"
"v_mfma_f32_16x16x32_bf16 v[148:151], v[214:217], a[152:155], v[148:151]\n"
"s_waitcnt vmcnt(27) lgkmcnt(4)\n"
"v_sub_u32 v154, v154, v156\n"
"v_lshrrev_b32 v156, 5, v155\n"
"v_mfma_f32_16x16x32_bf16 v[0:3], v[218:221], a[156:159], v[0:3]\n"
"s_add_u32 m0, s71, 86016\n"
"ds_read_b64_tr_b16 v[234:235], v248 offset:18496\n"
"ds_read_b64_tr_b16 v[236:237], v248 offset:23104\n"
"v_mfma_f32_16x16x32_bf16 v[0:3], v[222:225], a[164:167], v[0:3]\n"
"s_nop 0\n"
"ds_read_b64_tr_b16 v[238:239], v248 offset:27712\n"
"ds_read_b64_tr_b16 v[240:241], v248 offset:32320\n"
"v_mfma_f32_16x16x32_bf16 v[72:75], v[218:221], a[160:163], v[72:75]\n"
"v_lshl_add_u32 v155, v156, 8, v155\n"
"v_mul_u32_u24 v155, s73, v155\n"
"v_mfma_f32_16x16x32_bf16 v[72:75], v[222:225], a[168:171], v[72:75]\n"
"s_waitcnt lgkmcnt(4)\n"
"v_lshl_add_u32 v183, v154, 4, v155\n"
"v_add_u32 v154, 1792, v153\n"
"buffer_load_dword v208, off, s[60:63], s75 sc1\n"
"v_mfma_f32_16x16x32_bf16 v[4:7], v[226:229], a[156:159], v[4:7]\n"
"buffer_load_dwordx4 v171, s[44:47], s76 offen lds\n"
"v_mfma_f32_16x16x32_bf16 v[4:7], v[230:233], a[164:167], v[4:7]\n"
"s_add_u32 m0, s71, 90112\n"
"ds_read_b64_tr_b16 v[210:211], v248 offset:18528\n"
"ds_read_b64_tr_b16 v[212:213], v248 offset:23136\n"
"v_mfma_f32_16x16x32_bf16 v[76:79], v[226:229], a[160:163], v[76:79]\n"
"s_nop 0\n"
"ds_read_b64_tr_b16 v[214:215], v248 offset:27744\n"
"ds_read_b64_tr_b16 v[216:217], v248 offset:32352\n"
"v_mfma_f32_16x16x32_bf16 v[76:79], v[230:233], a[168:171], v[76:79]\n"
"s_waitcnt lgkmcnt(4)\n"
"v_mul_u32_u24 v155, 58255, v154\n"
"v_lshrrev_b32 v155, 20, v155\n"
"v_mfma_f32_16x16x32_bf16 v[8:11], v[234:237], a[156:159], v[8:11]\n"
"buffer_load_dwordx4 v172, s[44:47], s76 offen lds\n"
"v_mfma_f32_16x16x32_bf16 v[8:11], v[238:241], a[164:167], v[8:11]\n"
"s_add_u32 m0, s71, 94208\n"
"ds_read_b64_tr_b16 v[218:219], v248 offset:18560\n"
"ds_read_b64_tr_b16 v[220:221], v248 offset:23168\n"
"v_mfma_f32_16x16x32_bf16 v[80:83], v[234:237], a[160:163], v[80:83]\n"
"s_nop 0\n"
"ds_read_b64_tr_b16 v[222:223], v248 offset:27776\n"
"ds_read_b64_tr_b16 v[224:225], v248 offset:32384\n"
"v_mfma_f32_16x16x32_bf16 v[80:83], v[238:241], a[168:171], v[80:83]\n"
"s_waitcnt lgkmcnt(4)\n"
"v_mul_u32_u24 v156, 18, v155\n"
"v_sub_u32 v154, v154, v156\n"
"v_mfma_f32_16x16x32_bf16 v[12:15], v[210:213], a[156:159], v[12:15]\n"
"buffer_load_dwordx4 v173, s[44:47], s76 offen lds\n"
"v_mfma_f32_16x16x32_bf16 v[12:15], v[214:217], a[164:167], v[12:15]\n"
"s_add_u32 m0, s71, 98304\n"
"ds_read_b64_tr_b16 v[226:227], v248 offset:18592\n"
"ds_read_b64_tr_b16 v[228:229], v248 offset:23200\n"
"v_mfma_f32_16x16x32_bf16 v[84:87], v[210:213], a[160:163], v[84:87]\n"
"s_nop 0\n"
"ds_read_b64_tr_b16 v[230:231], v248 offset:27808\n"
"ds_read_b64_tr_b16 v[232:233], v248 offset:32416\n"
"v_mfma_f32_16x16x32_bf16 v[84:87], v[214:217], a[168:171], v[84:87]\n"
"s_waitcnt lgkmcnt(4)\n"
"v_lshrrev_b32 v156, 5, v155\n"
"v_lshl_add_u32 v155, v156, 8, v155\n"
"v_mfma_f32_16x16x32_bf16 v[36:39], v[218:221], a[156:159], v[36:39]\n"
"buffer_load_dwordx4 v174, s[44:47], s76 offen lds\n"
"v_mfma_f32_16x16x32_bf16 v[36:39], v[222:225], a[164:167], v[36:39]\n"
"s_add_u32 m0, s71, 102400\n"
"ds_read_b64_tr_b16 v[234:235], v248 offset:18624\n"
"ds_read_b64_tr_b16 v[236:237], v248 offset:23232\n"
"v_mfma_f32_16x16x32_bf16 v[108:111], v[218:221], a[160:163], v[108:111]\n"
"s_nop 0\n"
"ds_read_b64_tr_b16 v[238:239], v248 offset:27840\n"
"ds_read_b64_tr_b16 v[240:241], v248 offset:32448\n"
"v_mfma_f32_16x16x32_bf16 v[108:111], v[222:225], a[168:171], v[108:111]\n"
"s_waitcnt lgkmcnt(4)\n"
"v_mul_u32_u24 v155, s73, v155\n"
"v_lshl_add_u32 v184, v154, 4, v155\n"
"v_mfma_f32_16x16x32_bf16 v[40:43], v[226:229], a[156:159], v[40:43]\n"
"s_nop 0\n"
"ds_read_b64_tr_b16 v[210:211], v248 offset:18656\n"
"ds_read_b64_tr_b16 v[212:213], v248 offset:23264\n"
"v_mfma_f32_16x16x32_bf16 v[40:43], v[230:233], a[164:167], v[40:43]\n"
"buffer_load_dwordx4 v175, s[44:47], s76 offen lds\n"
"v_mfma_f32_16x16x32_bf16 v[112:115], v[226:229], a[160:163], v[112:115]\n"
"s_add_u32 m0, s71, 106496\n"
"ds_read_b64_tr_b16 v[214:215], v248 offset:27872\n"
"ds_read_b64_tr_b16 v[216:217], v248 offset:32480\n"
"v_mfma_f32_16x16x32_bf16 v[112:115], v[230:233], a[168:171], v[112:115]\n"
"s_waitcnt lgkmcnt(4)\n"
"v_add_u32 v154, 2048, v153\n"
"v_mul_u32_u24 v155, 58255, v154\n"
"v_mfma_f32_16x16x32_bf16 v[44:47], v[234:237], a[156:159], v[44:47]\n"
"s_nop 0\n"
"ds_read_b64_tr_b16 v[218:219], v251 offset:18432\n"
"ds_read_b64_tr_b16 v[220:221], v251 offset:23040\n"
"v_mfma_f32_16x16x32_bf16 v[44:47], v[238:241], a[164:167], v[44:47]\n"
"buffer_load_dwordx4 v176, s[44:47], s76 offen lds\n"
"v_mfma_f32_16x16x32_bf16 v[116:119], v[234:237], a[160:163], v[116:119]\n"
"s_nop 0\n"
"ds_read_b64_tr_b16 v[222:223], v251 offset:27648\n"
"ds_read_b64_tr_b16 v[224:225], v251 offset:32256\n"
"v_mfma_f32_16x16x32_bf16 v[116:119], v[238:241], a[168:171], v[116:119]\n"
"s_waitcnt vmcnt(27) lgkmcnt(4)\n"
"s_barrier\n"
"v_mfma_f32_16x16x32_bf16 v[48:51], v[210:213], a[156:159], v[48:51]\n"
"s_nop 0\n"
"ds_read_b64_tr_b16 v[226:227], v246 offset:0\n"
"ds_read_b64_tr_b16 v[228:229], v246 offset:4608\n"
"v_mfma_f32_16x16x32_bf16 v[48:51], v[214:217], a[164:167], v[48:51]\n"
"s_nop 0\n"
"ds_read_b64_tr_b16 v[230:231], v246 offset:9216\n"
"ds_read_b64_tr_b16 v[232:233], v246 offset:13824\n"
"v_mfma_f32_16x16x32_bf16 v[120:123], v[210:213], a[160:163], v[120:123]\n"
"v_lshrrev_b32 v155, 20, v155\n"
"v_mul_u32_u24 v156, 18, v155\n"
"v_mfma_f32_16x16x32_bf16 v[120:123], v[214:217], a[168:171], v[120:123]\n"
"s_waitcnt lgkmcnt(4)\n"
"s_barrier\n"
"v_mfma_f32_16x16x32_bf16 v[144:147], v[218:221], a[156:159], v[144:147]\n"
"s_add_u32 m0, s71, 110592\n"
"ds_read_b64_tr_b16 v[234:235], v246 offset:32\n"
"ds_read_b64_tr_b16 v[236:237], v246 offset:4640\n"
"v_mfma_f32_16x16x32_bf16 v[144:147], v[222:225], a[164:167], v[144:147]\n"
"s_mov_b32 s76, 737280\n"
"ds_read_b64_tr_b16 v[238:239], v246 offset:9248\n"
"ds_read_b64_tr_b16 v[240:241], v246 offset:13856\n"
"v_mfma_f32_16x16x32_bf16 v[148:151], v[218:221], a[160:163], v[148:151]\n"
"buffer_load_dwordx4 v168, s[44:47], s76 offen lds\n"
"v_mfma_f32_16x16x32_bf16 v[148:151], v[222:225], a[168:171], v[148:151]\n"
"s_waitcnt vmcnt(24) lgkmcnt(4)\n"
"v_sub_u32 v154, v154, v156\n"
"v_lshrrev_b32 v156, 5, v155\n"
"s_waitcnt vmcnt(7)\n"
"v_readfirstlane_b32 s74, v208\n"
"s_nop 1\n"
"s_bfe_u32 s74, s74, 524296\n"
"s_cmp_ge_u32 s74, 8\n"
"s_cbranch_scc1 L_po_abok24\n"
"s_mov_b32 s97, 20000\n"
"L_po_poll25:\n"
"buffer_load_dword v208, off, s[60:63], s75 sc1\n"
"s_waitcnt vmcnt(0)\n"
"v_readfirstlane_b32 s74, v208\n"
"s_nop 1\n"
"s_bfe_u32 s74, s74, 524296\n"
"s_cmp_ge_u32 s74, 8\n"
"s_cbranch_scc1 L_po_polld26\n"
"s_sleep 2\n"
"s_sub_u32 s97, s97, 1\n"
"s_cbranch_scc0 L_po_poll25\n"
"L_po_polld26:\n"
"L_po_abok24:\n"
"v_mfma_f32_16x16x32_bf16 v[0:3], v[226:229], a[172:175], v[0:3]\n"
"s_add_u32 m0, s71, 114688\n"
"ds_read_b64_tr_b16 v[210:211], v246 offset:64\n"
"ds_read_b64_tr_b16 v[212:213], v246 offset:4672\n"
"v_mfma_f32_16x16x32_bf16 v[0:3], v[230:233], a[180:183], v[0:3]\n"
"buffer_load_dwordx4 v169, s[44:47], s76 offen lds\n"
"v_mfma_f32_16x16x32_bf16 v[72:75], v[226:229], a[176:179], v[72:75]\n"
"s_add_u32 s78, s82, 2048\n"
"ds_read_b64_tr_b16 v[214:215], v246 offset:9280\n"
"ds_read_b64_tr_b16 v[216:217], v246 offset:13888\n"
"v_mfma_f32_16x16x32_bf16 v[72:75], v[230:233], a[184:187], v[72:75]\n"
"s_waitcnt lgkmcnt(4)\n"
"v_lshl_add_u32 v155, v156, 8, v155\n"
"v_mul_u32_u24 v155, s73, v155\n"
"v_mfma_f32_16x16x32_bf16 v[4:7], v[234:237], a[172:175], v[4:7]\n"
"s_nop 0\n"
"ds_read_b64_tr_b16 v[218:219], v246 offset:96\n"
"ds_read_b64_tr_b16 v[220:221], v246 offset:4704\n"
"v_mfma_f32_16x16x32_bf16 v[4:7], v[238:241], a[180:183], v[4:7]\n"
"buffer_load_dwordx4 a[204:207], v186, s[60:63], s78 offen offset:0 sc1\n"
"v_mfma_f32_16x16x32_bf16 v[76:79], v[234:237], a[176:179], v[76:79]\n"
"s_add_u32 s79, s83, 2048\n"
"ds_read_b64_tr_b16 v[222:223], v246 offset:9312\n"
"ds_read_b64_tr_b16 v[224:225], v246 offset:13920\n"
"v_mfma_f32_16x16x32_bf16 v[76:79], v[238:241], a[184:187], v[76:79]\n"
"s_waitcnt lgkmcnt(4)\n"
"v_lshl_add_u32 v185, v154, 4, v155\n"
"v_mfma_f32_16x16x32_bf16 v[8:11], v[210:213], a[172:175], v[8:11]\n"
"s_nop 0\n"
"ds_read_b64_tr_b16 v[226:227], v246 offset:128\n"
"ds_read_b64_tr_b16 v[228:229], v246 offset:4736\n"
"v_mfma_f32_16x16x32_bf16 v[8:11], v[214:217], a[180:183], v[8:11]\n"
"s_nop 0\n"
"ds_read_b64_tr_b16 v[230:231], v246 offset:9344\n"
"ds_read_b64_tr_b16 v[232:233], v246 offset:13952\n"
"v_mfma_f32_16x16x32_bf16 v[80:83], v[210:213], a[176:179], v[80:83]\n"
"buffer_load_dwordx4 a[208:211], v186, s[60:63], s79 offen offset:0 sc1\n"
"v_mfma_f32_16x16x32_bf16 v[80:83], v[214:217], a[184:187], v[80:83]\n"
"s_waitcnt lgkmcnt(4)\n"
"v_mfma_f32_16x16x32_bf16 v[12:15], v[218:221], a[172:175], v[12:15]\n"
"s_nop 0\n"
"ds_read_b64_tr_b16 v[234:235], v246 offset:160\n"
"ds_read_b64_tr_b16 v[236:237], v246 offset:4768\n"
"v_mfma_f32_16x16x32_bf16 v[12:15], v[222:225], a[180:183], v[12:15]\n"
"s_nop 0\n"
"ds_read_b64_tr_b16 v[238:239], v246 offset:9376\n"
"ds_read_b64_tr_b16 v[240:241], v246 offset:13984\n"
"v_mfma_f32_16x16x32_bf16 v[84:87], v[218:221], a[176:179], v[84:87]\n"
"buffer_load_dwordx4 a[212:215], v186, s[60:63], s78 offen offset:1024 sc1\n"
"v_mfma_f32_16x16x32_bf16 v[84:87], v[222:225], a[184:187], v[84:87]\n"
"s_waitcnt lgkmcnt(4)\n"
"v_mfma_f32_16x16x32_bf16 v[36:39], v[226:229], a[172:175], v[36:39]\n"
"s_nop 0\n"
"ds_read_b64_tr_b16 v[210:211], v246 offset:192\n"
"ds_read_b64_tr_b16 v[212:213], v246 offset:4800\n"
"v_mfma_f32_16x16x32_bf16 v[36:39], v[230:233], a[180:183], v[36:39]\n"
"s_nop 0\n"
"ds_read_b64_tr_b16 v[214:215], v246 offset:9408\n"
"ds_read_b64_tr_b16 v[216:217], v246 offset:14016\n"
"v_mfma_f32_16x16x32_bf16 v[108:111], v[226:229], a[176:179], v[108:111]\n"
"v_mfma_f32_16x16x32_bf16 v[108:111], v[230:233], a[184:187], v[108:111]\n"
"s_waitcnt lgkmcnt(4)\n"
"v_mfma_f32_16x16x32_bf16 v[40:43], v[234:237], a[172:175], v[40:43]\n"
"buffer_load_dwordx4 a[216:219], v186, s[60:63], s79 offen offset:1024 sc1\n"
"v_mfma_f32_16x16x32_bf16 v[40:43], v[238:241], a[180:183], v[40:43]\n"
"s_add_u32 s78, s82, 7168\n"
"ds_read_b64_tr_b16 v[218:219], v246 offset:224\n"
"ds_read_b64_tr_b16 v[220:221], v246 offset:4832\n"
"v_mfma_f32_16x16x32_bf16 v[112:115], v[234:237], a[176:179], v[112:115]\n"
"s_nop 0\n"
"ds_read_b64_tr_b16 v[222:223], v246 offset:9440\n"
"ds_read_b64_tr_b16 v[224:225], v246 offset:14048\n"
"v_mfma_f32_16x16x32_bf16 v[112:115], v[238:241], a[184:187], v[112:115]\n"
"s_waitcnt lgkmcnt(4)\n"
"v_mfma_f32_16x16x32_bf16 v[44:47], v[210:213], a[172:175], v[44:47]\n"
"buffer_load_dwordx4 a[220:223], v186, s[60:63], s78 offen offset:0 sc1\n"
"v_mfma_f32_16x16x32_bf16 v[44:47], v[214:217], a[180:183], v[44:47]\n"
"s_add_u32 s79, s83, 7168\n"
"ds_read_b64_tr_b16 v[226:227], v249 offset:0\n"
"ds_read_b64_tr_b16 v[228:229], v249 offset:4608\n"
"v_mfma_f32_16x16x32_bf16 v[116:119], v[210:213], a[176:179], v[116:119]\n"
"s_nop 0\n"
"ds_read_b64_tr_b16 v[230:231], v249 offset:9216\n"
"ds_read_b64_tr_b16 v[232:233], v249 offset:13824\n"
"v_mfma_f32_16x16x32_bf16 v[116:119], v[214:217], a[184:187], v[116:119]\n"
"s_waitcnt lgkmcnt(4)\n"
"v_mfma_f32_16x16x32_bf16 v[48:51], v[218:221], a[172:175], v[48:51]\n"
"buffer_load_dwordx4 a[224:227], v186, s[60:63], s79 offen offset:0 sc1\n"
"v_mfma_f32_16x16x32_bf16 v[48:51], v[222:225], a[180:183], v[48:51]\n"
"s_nop 0\n"
"ds_read_b64_tr_b16 v[234:235], v246 offset:18432\n"
"ds_read_b64_tr_b16 v[236:237], v246 offset:23040\n"
"v_mfma_f32_16x16x32_bf16 v[120:123], v[218:221], a[176:179], v[120:123]\n"
"s_nop 0\n"
"ds_read_b64_tr_b16 v[238:239], v246 offset:27648\n"
"ds_read_b64_tr_b16 v[240:241], v246 offset:32256\n"
"v_mfma_f32_16x16x32_bf16 v[120:123], v[222:225], a[184:187], v[120:123]\n"
"s_waitcnt lgkmcnt(4)\n"
"v_mfma_f32_16x16x32_bf16 v[144:147], v[226:229], a[172:175], v[144:147]\n"
"buffer_load_dwordx4 a[228:231], v186, s[60:63], s78 offen offset:1024 sc1\n"
"v_mfma_f32_16x16x32_bf16 v[144:147], v[230:233], a[180:183], v[144:147]\n"
"s_nop 0\n"
"ds_read_b64_tr_b16 v[210:211], v246 offset:18464\n"
"ds_read_b64_tr_b16 v[212:213], v246 offset:23072\n"
"v_mfma_f32_16x16x32_bf16 v[148:151], v[226:229], a[176:179], v[148:151]\n"
"s_nop 0\n"
"ds_read_b64_tr_b16 v[214:215], v246 offset:27680\n"
"ds_read_b64_tr_b16 v[216:217], v246 offset:32288\n"
"v_mfma_f32_16x16x32_bf16 v[148:151], v[230:233], a[184:187], v[148:151]\n"
"s_waitcnt vmcnt(26) lgkmcnt(4)\n"
"v_mfma_f32_16x16x32_bf16 v[0:3], v[234:237], a[188:191], v[0:3]\n"
"s_nop 0\n"
"ds_read_b64_tr_b16 v[218:219], v246 offset:18496\n"
"ds_read_b64_tr_b16 v[220:221], v246 offset:23104\n"
"v_mfma_f32_16x16x32_bf16 v[0:3], v[238:241], a[196:199], v[0:3]\n"
"buffer_load_dwordx4 a[232:235], v186, s[60:63], s79 offen offset:1024 sc1\n"
"v_mfma_f32_16x16x32_bf16 v[72:75], v[234:237], a[192:195], v[72:75]\n"
"s_add_u32 s78, s82, 11264\n"
"ds_read_b64_tr_b16 v[222:223], v246 offset:27712\n"
"ds_read_b64_tr_b16 v[224:225], v246 offset:32320\n"
"v_mfma_f32_16x16x32_bf16 v[72:75], v[238:241], a[200:203], v[72:75]\n"
"s_waitcnt lgkmcnt(4)\n"
"v_mfma_f32_16x16x32_bf16 v[4:7], v[210:213], a[188:191], v[4:7]\n"
"s_nop 0\n"
"ds_read_b64_tr_b16 v[226:227], v246 offset:18528\n"
"ds_read_b64_tr_b16 v[228:229], v246 offset:23136\n"
"v_mfma_f32_16x16x32_bf16 v[4:7], v[214:217], a[196:199], v[4:7]\n"
"buffer_load_dwordx4 a[236:239], v186, s[60:63], s78 offen offset:0 sc1\n"
"v_mfma_f32_16x16x32_bf16 v[76:79], v[210:213], a[192:195], v[76:79]\n"
"s_add_u32 s79, s83, 11264\n"
"ds_read_b64_tr_b16 v[230:231], v246 offset:27744\n"
"ds_read_b64_tr_b16 v[232:233], v246 offset:32352\n"
"v_mfma_f32_16x16x32_bf16 v[76:79], v[214:217], a[200:203], v[76:79]\n"
"s_waitcnt lgkmcnt(4)\n"
"v_mfma_f32_16x16x32_bf16 v[8:11], v[218:221], a[188:191], v[8:11]\n"
"s_nop 0\n"
"ds_read_b64_tr_b16 v[234:235], v246 offset:18560\n"
"ds_read_b64_tr_b16 v[236:237], v246 offset:23168\n"
"v_mfma_f32_16x16x32_bf16 v[8:11], v[222:225], a[196:199], v[8:11]\n"
"s_nop 0\n"
"ds_read_b64_tr_b16 v[238:239], v246 offset:27776\n"
"ds_read_b64_tr_b16 v[240:241], v246 offset:32384\n"
"v_mfma_f32_16x16x32_bf16 v[80:83], v[218:221], a[192:195], v[80:83]\n"
"buffer_load_dwordx4 a[240:243], v186, s[60:63], s79 offen offset:0 sc1\n"
"v_mfma_f32_16x16x32_bf16 v[80:83], v[222:225], a[200:203], v[80:83]\n"
"s_waitcnt lgkmcnt(4)\n"
"v_mfma_f32_16x16x32_bf16 v[12:15], v[226:229], a[188:191], v[12:15]\n"
"s_nop 0\n"
"ds_read_b64_tr_b16 v[210:211], v246 offset:18592\n"
"ds_read_b64_tr_b16 v[212:213], v246 offset:23200\n"
"v_mfma_f32_16x16x32_bf16 v[12:15], v[230:233], a[196:199], v[12:15]\n"
"s_nop 0\n"
"ds_read_b64_tr_b16 v[214:215], v246 offset:27808\n"
"ds_read_b64_tr_b16 v[216:217], v246 offset:32416\n"
"v_mfma_f32_16x16x32_bf16 v[84:87], v[226:229], a[192:195], v[84:87]\n"
"buffer_load_dwordx4 a[244:247], v186, s[60:63], s78 offen offset:1024 sc1\n"
"v_mfma_f32_16x16x32_bf16 v[84:87], v[230:233], a[200:203], v[84:87]\n"
"s_waitcnt lgkmcnt(4)\n"
"v_mfma_f32_16x16x32_bf16 v[36:39], v[234:237], a[188:191], v[36:39]\n"
"s_nop 0\n"
"ds_read_b64_tr_b16 v[218:219], v246 offset:18624\n"
"ds_read_b64_tr_b16 v[220:221], v246 offset:23232\n"
"v_mfma_f32_16x16x32_bf16 v[36:39], v[238:241], a[196:199], v[36:39]\n"
"s_nop 0\n"
"ds_read_b64_tr_b16 v[222:223], v246 offset:27840\n"
"ds_read_b64_tr_b16 v[224:225], v246 offset:32448\n"
"v_mfma_f32_16x16x32_bf16 v[108:111], v[234:237], a[192:195], v[108:111]\n"
"v_mfma_f32_16x16x32_bf16 v[108:111], v[238:241], a[200:203], v[108:111]\n"
"s_waitcnt lgkmcnt(4)\n"
"v_mfma_f32_16x16x32_bf16 v[40:43], v[210:213], a[188:191], v[40:43]\n"
"buffer_load_dwordx4 a[248:251], v186, s[60:63], s79 offen offset:1024 sc1\n"
"v_mfma_f32_16x16x32_bf16 v[40:43], v[214:217], a[196:199], v[40:43]\n"
"s_add_u32 s78, s82, 16384\n"
"ds_read_b64_tr_b16 v[226:227], v246 offset:18656\n"
"ds_read_b64_tr_b16 v[228:229], v246 offset:23264\n"
"v_mfma_f32_16x16x32_bf16 v[112:115], v[210:213], a[192:195], v[112:115]\n"
"L_tp2_1_1:\n"
"s_cbranch_execz L_tb2_1\n"
"ds_read_b64_tr_b16 v[230:231], v246 offset:27872\n"
"ds_read_b64_tr_b16 v[232:233], v246 offset:32480\n"
"v_mfma_f32_16x16x32_bf16 v[112:115], v[214:217], a[200:203], v[112:115]\n"
"s_waitcnt lgkmcnt(4)\n"
"v_mfma_f32_16x16x32_bf16 v[44:47], v[218:221], a[188:191], v[44:47]\n"
"buffer_load_dwordx4 a[60:63], v186, s[60:63], s78 offen offset:0 sc1\n"
"v_mfma_f32_16x16x32_bf16 v[44:47], v[222:225], a[196:199], v[44:47]\n"
"s_add_u32 s79, s83, 16384\n"
"ds_read_b64_tr_b16 v[234:235], v249 offset:18432\n"
"ds_read_b64_tr_b16 v[236:237], v249 offset:23040\n"
"v_mfma_f32_16x16x32_bf16 v[116:119], v[218:221], a[192:195], v[116:119]\n"
"s_nop 0\n"
"ds_read_b64_tr_b16 v[238:239], v249 offset:27648\n"
"ds_read_b64_tr_b16 v[240:241], v249 offset:32256\n"
"v_mfma_f32_16x16x32_bf16 v[116:119], v[222:225], a[200:203], v[116:119]\n"
"s_waitcnt vmcnt(25) lgkmcnt(4)\n"
"s_barrier\n"
"v_mfma_f32_16x16x32_bf16 v[48:51], v[226:229], a[188:191], v[48:51]\n"
"buffer_load_dwordx4 a[64:67], v186, s[60:63], s79 offen offset:0 sc1\n"
"v_mfma_f32_16x16x32_bf16 v[48:51], v[230:233], a[196:199], v[48:51]\n"
"s_nop 0\n"
"ds_read_b64_tr_b16 v[210:211], v246 offset:36864\n"
"ds_read_b64_tr_b16 v[212:213], v246 offset:41472\n"
"v_mfma_f32_16x16x32_bf16 v[120:123], v[226:229], a[192:195], v[120:123]\n"
"s_nop 0\n"
"ds_read_b64_tr_b16 v[214:215], v246 offset:46080\n"
"ds_read_b64_tr_b16 v[216:217], v246 offset:50688\n"
"v_mfma_f32_16x16x32_bf16 v[120:123], v[230:233], a[200:203], v[120:123]\n"
"s_waitcnt lgkmcnt(4)\n"
"s_barrier\n"
"v_mfma_f32_16x16x32_bf16 v[144:147], v[234:237], a[188:191], v[144:147]\n"
"buffer_load_dwordx4 a[68:71], v186, s[60:63], s78 offen offset:1024 sc1\n"
"v_mfma_f32_16x16x32_bf16 v[144:147], v[238:241], a[196:199], v[144:147]\n"
"s_nop 0\n"
"ds_read_b64_tr_b16 v[218:219], v246 offset:36896\n"
"ds_read_b64_tr_b16 v[220:221], v246 offset:41504\n"
"v_mfma_f32_16x16x32_bf16 v[148:151], v[234:237], a[192:195], v[148:151]\n"
"s_nop 0\n"
"ds_read_b64_tr_b16 v[222:223], v246 offset:46112\n"
"ds_read_b64_tr_b16 v[224:225], v246 offset:50720\n"
"v_mfma_f32_16x16x32_bf16 v[148:151], v[238:241], a[200:203], v[148:151]\n"
"s_waitcnt vmcnt(11) lgkmcnt(4)\n"
"v_mfma_f32_16x16x32_bf16 v[0:3], v[210:213], a[204:207], v[0:3]\n"
"s_nop 0\n"
"ds_read_b64_tr_b16 v[226:227], v246 offset:36928\n"
"ds_read_b64_tr_b16 v[228:229], v246 offset:41536\n"
"v_mfma_f32_16x16x32_bf16 v[0:3], v[214:217], a[212:215], v[0:3]\n"
"buffer_load_dwordx4 a[72:75], v186, s[60:63], s79 offen offset:1024 sc1\n"
"v_mfma_f32_16x16x32_bf16 v[72:75], v[210:213], a[208:211], v[72:75]\n"
"s_add_u32 m0, s71, 118784\n"
"ds_read_b64_tr_b16 v[230:231], v246 offset:46144\n"
"ds_read_b64_tr_b16 v[232:233], v246 offset:50752\n"
"v_mfma_f32_16x16x32_bf16 v[72:75], v[214:217], a[216:219], v[72:75]\n"
"s_waitcnt lgkmcnt(4)\n"
"v_mfma_f32_16x16x32_bf16 v[4:7], v[218:221], a[204:207], v[4:7]\n"
"s_nop 0\n"
"ds_read_b64_tr_b16 v[234:235], v246 offset:36960\n"
"ds_read_b64_tr_b16 v[236:237], v246 offset:41568\n"
"v_mfma_f32_16x16x32_bf16 v[4:7], v[222:225], a[212:215], v[4:7]\n"
"buffer_load_dwordx4 v170, s[44:47], s76 offen lds\n"
"v_mfma_f32_16x16x32_bf16 v[76:79], v[218:221], a[208:211], v[76:79]\n"
"s_add_u32 m0, s71, 122880\n"
"ds_read_b64_tr_b16 v[238:239], v246 offset:46176\n"
"ds_read_b64_tr_b16 v[240:241], v246 offset:50784\n"
"v_mfma_f32_16x16x32_bf16 v[76:79], v[222:225], a[216:219], v[76:79]\n"
"s_waitcnt lgkmcnt(4)\n"
"v_mfma_f32_16x16x32_bf16 v[8:11], v[226:229], a[204:207], v[8:11]\n"
"s_nop 0\n"
"ds_read_b64_tr_b16 v[210:211], v246 offset:36992\n"
"ds_read_b64_tr_b16 v[212:213], v246 offset:41600\n"
"v_mfma_f32_16x16x32_bf16 v[8:11], v[230:233], a[212:215], v[8:11]\n"
"s_nop 0\n"
"ds_read_b64_tr_b16 v[214:215], v246 offset:46208\n"
"ds_read_b64_tr_b16 v[216:217], v246 offset:50816\n"
"v_mfma_f32_16x16x32_bf16 v[80:83], v[226:229], a[208:211], v[80:83]\n"
"buffer_load_dwordx4 v171, s[44:47], s76 offen lds\n"
"v_mfma_f32_16x16x32_bf16 v[80:83], v[230:233], a[216:219], v[80:83]\n"
"s_waitcnt lgkmcnt(4)\n"
"v_mfma_f32_16x16x32_bf16 v[12:15], v[234:237], a[204:207], v[12:15]\n"
"s_add_u32 m0, s71, 126976\n"
"ds_read_b64_tr_b16 v[218:219], v246 offset:37024\n"
"ds_read_b64_tr_b16 v[220:221], v246 offset:41632\n"
"v_mfma_f32_16x16x32_bf16 v[12:15], v[238:241], a[212:215], v[12:15]\n"
"s_nop 0\n"
"ds_read_b64_tr_b16 v[222:223], v246 offset:46240\n"
"ds_read_b64_tr_b16 v[224:225], v246 offset:50848\n"
"v_mfma_f32_16x16x32_bf16 v[84:87], v[234:237], a[208:211], v[84:87]\n"
"buffer_load_dwordx4 v172, s[44:47], s76 offen lds\n"
"v_mfma_f32_16x16x32_bf16 v[84:87], v[238:241], a[216:219], v[84:87]\n"
"s_waitcnt lgkmcnt(4)\n"
"v_mfma_f32_16x16x32_bf16 v[36:39], v[210:213], a[204:207], v[36:39]\n"
"s_add_u32 m0, s71, 131072\n"
"ds_read_b64_tr_b16 v[226:227], v246 offset:37056\n"
"ds_read_b64_tr_b16 v[228:229], v246 offset:41664\n"
"v_mfma_f32_16x16x32_bf16 v[36:39], v[214:217], a[212:215], v[36:39]\n"
"s_nop 0\n"
"ds_read_b64_tr_b16 v[230:231], v246 offset:46272\n"
"ds_read_b64_tr_b16 v[232:233], v246 offset:50880\n"
"v_mfma_f32_16x16x32_bf16 v[108:111], v[210:213], a[208:211], v[108:111]\n"
"v_mfma_f32_16x16x32_bf16 v[108:111], v[214:217], a[216:219], v[108:111]\n"
"s_waitcnt lgkmcnt(4)\n"
"v_mfma_f32_16x16x32_bf16 v[40:43], v[218:221], a[204:207], v[40:43]\n"
"buffer_load_dwordx4 v173, s[44:47], s76 offen lds\n"
"v_mfma_f32_16x16x32_bf16 v[40:43], v[222:225], a[212:215], v[40:43]\n"
"s_add_u32 m0, s71, 135168\n"
"ds_read_b64_tr_b16 v[234:235], v246 offset:37088\n"
"ds_read_b64_tr_b16 v[236:237], v246 offset:41696\n"
"v_mfma_f32_16x16x32_bf16 v[112:115], v[218:221], a[208:211], v[112:115]\n"
"s_nop 0\n"
"ds_read_b64_tr_b16 v[238:239], v246 offset:46304\n"
"ds_read_b64_tr_b16 v[240:241], v246 offset:50912\n"
"v_mfma_f32_16x16x32_bf16 v[112:115], v[222:225], a[216:219], v[112:115]\n"
"s_waitcnt lgkmcnt(4)\n"
"v_mfma_f32_16x16x32_bf16 v[44:47], v[226:229], a[204:207], v[44:47]\n"
"buffer_load_dwordx4 v174, s[44:47], s76 offen lds\n"
"v_mfma_f32_16x16x32_bf16 v[44:47], v[230:233], a[212:215], v[44:47]\n"
"s_add_u32 m0, s71, 139264\n"
"ds_read_b64_tr_b16 v[210:211], v249 offset:36864\n"
"ds_read_b64_tr_b16 v[212:213], v249 offset:41472\n"
"v_mfma_f32_16x16x32_bf16 v[116:119], v[226:229], a[208:211], v[116:119]\n"
"s_nop 0\n"
"ds_read_b64_tr_b16 v[214:215], v249 offset:46080\n"
"ds_read_b64_tr_b16 v[216:217], v249 offset:50688\n"
"v_mfma_f32_16x16x32_bf16 v[116:119], v[230:233], a[216:219], v[116:119]\n"
"s_waitcnt lgkmcnt(4)\n"
"v_mfma_f32_16x16x32_bf16 v[48:51], v[234:237], a[204:207], v[48:51]\n"
"buffer_load_dwordx4 v175, s[44:47], s76 offen lds\n"
"v_mfma_f32_16x16x32_bf16 v[48:51], v[238:241], a[212:215], v[48:51]\n"
"s_add_u32 m0, s71, 143360\n"
"ds_read_b64_tr_b16 v[218:219], v247 offset:0\n"
"ds_read_b64_tr_b16 v[220:221], v247 offset:4608\n"
"v_mfma_f32_16x16x32_bf16 v[120:123], v[234:237], a[208:211], v[120:123]\n"
"s_nop 0\n"
"ds_read_b64_tr_b16 v[222:223], v247 offset:9216\n"
"ds_read_b64_tr_b16 v[224:225], v247 offset:13824\n"
"v_mfma_f32_16x16x32_bf16 v[120:123], v[238:241], a[216:219], v[120:123]\n"
"s_waitcnt lgkmcnt(4)\n"
"v_mfma_f32_16x16x32_bf16 v[144:147], v[210:213], a[204:207], v[144:147]\n"
"buffer_load_dwordx4 v176, s[44:47], s76 offen lds\n"
"v_mfma_f32_16x16x32_bf16 v[144:147], v[214:217], a[212:215], v[144:147]\n"
"s_add_u32 s78, s82, 20480\n"
"ds_read_b64_tr_b16 v[226:227], v247 offset:32\n"
"ds_read_b64_tr_b16 v[228:229], v247 offset:4640\n"
"v_mfma_f32_16x16x32_bf16 v[148:151], v[210:213], a[208:211], v[148:151]\n"
"s_nop 0\n"
"ds_read_b64_tr_b16 v[230:231], v247 offset:9248\n"
"ds_read_b64_tr_b16 v[232:233], v247 offset:13856\n"
"v_mfma_f32_16x16x32_bf16 v[148:151], v[214:217], a[216:219], v[148:151]\n"
"s_waitcnt vmcnt(15) lgkmcnt(4)\n"
"v_mfma_f32_16x16x32_bf16 v[0:3], v[218:221], a[220:223], v[0:3]\n"
"s_nop 0\n"
"ds_read_b64_tr_b16 v[234:235], v247 offset:64\n"
"ds_read_b64_tr_b16 v[236:237], v247 offset:4672\n"
"v_mfma_f32_16x16x32_bf16 v[0:3], v[222:225], a[228:231], v[0:3]\n"
"buffer_load_dwordx4 a[76:79], v186, s[60:63], s78 offen offset:0 sc1\n"
"v_mfma_f32_16x16x32_bf16 v[72:75], v[218:221], a[224:227], v[72:75]\n"
"s_add_u32 s79, s83, 20480\n"
"ds_read_b64_tr_b16 v[238:239], v247 offset:9280\n"
"ds_read_b64_tr_b16 v[240:241], v247 offset:13888\n"
"v_mfma_f32_16x16x32_bf16 v[72:75], v[222:225], a[232:235], v[72:75]\n"
"s_waitcnt lgkmcnt(4)\n"
"v_mfma_f32_16x16x32_bf16 v[4:7], v[226:229], a[220:223], v[4:7]\n"
"s_nop 0\n"
"ds_read_b64_tr_b16 v[210:211], v247 offset:96\n"
"ds_read_b64_tr_b16 v[212:213], v247 offset:4704\n"
"v_mfma_f32_16x16x32_bf16 v[4:7], v[230:233], a[228:231], v[4:7]\n"
"buffer_load_dwordx4 a[80:83], v186, s[60:63], s79 offen offset:0 sc1\n"
"v_mfma_f32_16x16x32_bf16 v[76:79], v[226:229], a[224:227], v[76:79]\n"
"s_nop 0\n"
"ds_read_b64_tr_b16 v[214:215], v247 offset:9312\n"
"ds_read_b64_tr_b16 v[216:217], v247 offset:13920\n"
"v_mfma_f32_16x16x32_bf16 v[76:79], v[230:233], a[232:235], v[76:79]\n"
"s_waitcnt lgkmcnt(4)\n"
"v_mfma_f32_16x16x32_bf16 v[8:11], v[234:237], a[220:223], v[8:11]\n"
"s_nop 0\n"
"ds_read_b64_tr_b16 v[218:219], v247 offset:128\n"
"ds_read_b64_tr_b16 v[220:221], v247 offset:4736\n"
"v_mfma_f32_16x16x32_bf16 v[8:11], v[238:241], a[228:231], v[8:11]\n"
"s_nop 0\n"
"ds_read_b64_tr_b16 v[222:223], v247 offset:9344\n"
"ds_read_b64_tr_b16 v[224:225], v247 offset:13952\n"
"v_mfma_f32_16x16x32_bf16 v[80:83], v[234:237], a[224:227], v[80:83]\n"
"buffer_load_dwordx4 a[84:87], v186, s[60:63], s78 offen offset:1024 sc1\n"
"v_mfma_f32_16x16x32_bf16 v[80:83], v[238:241], a[232:235], v[80:83]\n"
"s_waitcnt lgkmcnt(4)\n"
"v_mfma_f32_16x16x32_bf16 v[12:15], v[210:213], a[220:223], v[12:15]\n"
"s_nop 0\n"
"ds_read_b64_tr_b16 v[226:227], v247 offset:160\n"
"ds_read_b64_tr_b16 v[228:229], v247 offset:4768\n"
"v_mfma_f32_16x16x32_bf16 v[12:15], v[214:217], a[228:231], v[12:15]\n"
"s_nop 0\n"
"ds_read_b64_tr_b16 v[230:231], v247 offset:9376\n"
"ds_read_b64_tr_b16 v[232:233], v247 offset:13984\n"
"v_mfma_f32_16x16x32_bf16 v[84:87], v[210:213], a[224:227], v[84:87]\n"
"buffer_load_dwordx4 a[88:91], v186, s[60:63], s79 offen offset:1024 sc1\n"
"v_mfma_f32_16x16x32_bf16 v[84:87], v[214:217], a[232:235], v[84:87]\n"
"s_waitcnt lgkmcnt(4)\n"
"v_mfma_f32_16x16x32_bf16 v[36:39], v[218:221], a[220:223], v[36:39]\n"
"s_add_u32 m0, s71, 0\n"
"ds_read_b64_tr_b16 v[234:235], v247 offset:192\n"
"ds_read_b64_tr_b16 v[236:237], v247 offset:4800\n"
"v_mfma_f32_16x16x32_bf16 v[36:39], v[222:225], a[228:231], v[36:39]\n"
"s_mov_b32 s76, 1069056\n"
"ds_read_b64_tr_b16 v[238:239], v247 offset:9408\n"
"ds_read_b64_tr_b16 v[240:241], v247 offset:14016\n"
"v_mfma_f32_16x16x32_bf16 v[108:111], v[218:221], a[224:227], v[108:111]\n"
"v_mfma_f32_16x16x32_bf16 v[108:111], v[222:225], a[232:235], v[108:111]\n"
"s_waitcnt lgkmcnt(4)\n"
"v_mfma_f32_16x16x32_bf16 v[40:43], v[226:229], a[220:223], v[40:43]\n"
"buffer_load_dwordx4 v168, s[44:47], s76 offen lds\n"
"v_mfma_f32_16x16x32_bf16 v[40:43], v[230:233], a[228:231], v[40:43]\n"
"s_add_u32 m0, s71, 4096\n"
"ds_read_b64_tr_b16 v[210:211], v247 offset:224\n"
"ds_read_b64_tr_b16 v[212:213], v247 offset:4832\n"
"v_mfma_f32_16x16x32_bf16 v[112:115], v[226:229], a[224:227], v[112:115]\n"
"s_nop 0\n"
"ds_read_b64_tr_b16 v[214:215], v247 offset:9440\n"
"ds_read_b64_tr_b16 v[216:217], v247 offset:14048\n"
"v_mfma_f32_16x16x32_bf16 v[112:115], v[230:233], a[232:235], v[112:115]\n"
"s_waitcnt lgkmcnt(4)\n"
"v_mfma_f32_16x16x32_bf16 v[44:47], v[234:237], a[220:223], v[44:47]\n"
"buffer_load_dwordx4 v169, s[44:47], s76 offen lds\n"
"v_mfma_f32_16x16x32_bf16 v[44:47], v[238:241], a[228:231], v[44:47]\n"
"s_add_u32 s78, s82, 25600\n"
"ds_read_b64_tr_b16 v[218:219], v250 offset:0\n"
"ds_read_b64_tr_b16 v[220:221], v250 offset:4608\n"
"v_mfma_f32_16x16x32_bf16 v[116:119], v[234:237], a[224:227], v[116:119]\n"
"s_nop 0\n"
"ds_read_b64_tr_b16 v[222:223], v250 offset:9216\n"
"ds_read_b64_tr_b16 v[224:225], v250 offset:13824\n"
"v_mfma_f32_16x16x32_bf16 v[116:119], v[238:241], a[232:235], v[116:119]\n"
"s_waitcnt vmcnt(31) lgkmcnt(4)\n"
"s_barrier\n"
"v_mfma_f32_16x16x32_bf16 v[48:51], v[210:213], a[220:223], v[48:51]\n"
"buffer_load_dwordx4 a[92:95], v186, s[60:63], s78 offen offset:0 sc1\n"
"v_mfma_f32_16x16x32_bf16 v[48:51], v[214:217], a[228:231], v[48:51]\n"
"s_add_u32 s79, s83, 25600\n"
"ds_read_b64_tr_b16 v[226:227], v247 offset:18432\n"
"ds_read_b64_tr_b16 v[228:229], v247 offset:23040\n"
"v_mfma_f32_16x16x32_bf16 v[120:123], v[210:213], a[224:227], v[120:123]\n"
"s_nop 0\n"
"ds_read_b64_tr_b16 v[230:231], v247 offset:27648\n"
"ds_read_b64_tr_b16 v[232:233], v247 offset:32256\n"
"v_mfma_f32_16x16x32_bf16 v[120:123], v[214:217], a[232:235], v[120:123]\n"
"s_waitcnt lgkmcnt(4)\n"
"s_barrier\n"
"v_mfma_f32_16x16x32_bf16 v[144:147], v[218:221], a[220:223], v[144:147]\n"
"buffer_load_dwordx4 a[96:99], v186, s[60:63], s79 offen offset:0 sc1\n"
"v_mfma_f32_16x16x32_bf16 v[144:147], v[222:225], a[228:231], v[144:147]\n"
"s_nop 0\n"
"ds_read_b64_tr_b16 v[234:235], v247 offset:18464\n"
"ds_read_b64_tr_b16 v[236:237], v247 offset:23072\n"
"v_mfma_f32_16x16x32_bf16 v[148:151], v[218:221], a[224:227], v[148:151]\n"
"s_nop 0\n"
"ds_read_b64_tr_b16 v[238:239], v247 offset:27680\n"
"ds_read_b64_tr_b16 v[240:241], v247 offset:32288\n"
"v_mfma_f32_16x16x32_bf16 v[148:151], v[222:225], a[232:235], v[148:151]\n"
"s_waitcnt vmcnt(19) lgkmcnt(4)\n"
"v_mfma_f32_16x16x32_bf16 v[0:3], v[226:229], a[236:239], v[0:3]\n"
"s_nop 0\n"
"ds_read_b64_tr_b16 v[210:211], v247 offset:18496\n"
"ds_read_b64_tr_b16 v[212:213], v247 offset:23104\n"
"v_mfma_f32_16x16x32_bf16 v[0:3], v[230:233], a[244:247], v[0:3]\n"
"buffer_load_dwordx4 a[100:103], v186, s[60:63], s78 offen offset:1024 sc1\n"
"v_mfma_f32_16x16x32_bf16 v[72:75], v[226:229], a[240:243], v[72:75]\n"
"s_nop 0\n"
"ds_read_b64_tr_b16 v[214:215], v247 offset:27712\n"
"ds_read_b64_tr_b16 v[216:217], v247 offset:32320\n"
"v_mfma_f32_16x16x32_bf16 v[72:75], v[230:233], a[248:251], v[72:75]\n"
"s_waitcnt lgkmcnt(4)\n"
"v_mfma_f32_16x16x32_bf16 v[4:7], v[234:237], a[236:239], v[4:7]\n"
"s_nop 0\n"
"ds_read_b64_tr_b16 v[218:219], v247 offset:18528\n"
"ds_read_b64_tr_b16 v[220:221], v247 offset:23136\n"
"v_mfma_f32_16x16x32_bf16 v[4:7], v[238:241], a[244:247], v[4:7]\n"
"buffer_load_dwordx4 a[104:107], v186, s[60:63], s79 offen offset:1024 sc1\n"
"v_mfma_f32_16x16x32_bf16 v[76:79], v[234:237], a[240:243], v[76:79]\n"
"s_add_u32 m0, s71, 8192\n"
"ds_read_b64_tr_b16 v[222:223], v247 offset:27744\n"
"ds_read_b64_tr_b16 v[224:225], v247 offset:32352\n"
"v_mfma_f32_16x16x32_bf16 v[76:79], v[238:241], a[248:251], v[76:79]\n"
"s_waitcnt lgkmcnt(4)\n"
"v_mfma_f32_16x16x32_bf16 v[8:11], v[210:213], a[236:239], v[8:11]\n"
"s_nop 0\n"
"ds_read_b64_tr_b16 v[226:227], v247 offset:18560\n"
"ds_read_b64_tr_b16 v[228:229], v247 offset:23168\n"
"v_mfma_f32_16x16x32_bf16 v[8:11], v[214:217], a[244:247], v[8:11]\n"
"s_nop 0\n"
"ds_read_b64_tr_b16 v[230:231], v247 offset:27776\n"
"ds_read_b64_tr_b16 v[232:233], v247 offset:32384\n"
"v_mfma_f32_16x16x32_bf16 v[80:83], v[210:213], a[240:243], v[80:83]\n"
"buffer_load_dwordx4 v170, s[44:47], s76 offen lds\n"
"v_mfma_f32_16x16x32_bf16 v[80:83], v[214:217], a[248:251], v[80:83]\n"
"s_waitcnt lgkmcnt(4)\n"
"v_mfma_f32_16x16x32_bf16 v[12:15], v[218:221], a[236:239], v[12:15]\n"
"s_add_u32 m0, s71, 12288\n"
"ds_read_b64_tr_b16 v[234:235], v247 offset:18592\n"
"ds_read_b64_tr_b16 v[236:237], v247 offset:23200\n"
"v_mfma_f32_16x16x32_bf16 v[12:15], v[222:225], a[244:247], v[12:15]\n"
"s_nop 0\n"
"ds_read_b64_tr_b16 v[238:239], v247 offset:27808\n"
"ds_read_b64_tr_b16 v[240:241], v247 offset:32416\n"
"v_mfma_f32_16x16x32_bf16 v[84:87], v[218:221], a[240:243], v[84:87]\n"
"buffer_load_dwordx4 v171, s[44:47], s76 offen lds\n"
"v_mfma_f32_16x16x32_bf16 v[84:87], v[222:225], a[248:251], v[84:87]\n"
"s_waitcnt lgkmcnt(4)\n"
"v_mfma_f32_16x16x32_bf16 v[36:39], v[226:229], a[236:239], v[36:39]\n"
"s_add_u32 m0, s71, 16384\n"
"ds_read_b64_tr_b16 v[210:211], v247 offset:18624\n"
"ds_read_b64_tr_b16 v[212:213], v247 offset:23232\n"
"v_mfma_f32_16x16x32_bf16 v[36:39], v[230:233], a[244:247], v[36:39]\n"
"s_nop 0\n"
"ds_read_b64_tr_b16 v[214:215], v247 offset:27840\n"
"ds_read_b64_tr_b16 v[216:217], v247 offset:32448\n"
"v_mfma_f32_16x16x32_bf16 v[108:111], v[226:229], a[240:243], v[108:111]\n"
"v_mfma_f32_16x16x32_bf16 v[108:111], v[230:233], a[248:251], v[108:111]\n"
"s_waitcnt lgkmcnt(4)\n"
"v_mfma_f32_16x16x32_bf16 v[40:43], v[234:237], a[236:239], v[40:43]\n"
"buffer_load_dwordx4 v172, s[44:47], s76 offen lds\n"
"v_mfma_f32_16x16x32_bf16 v[40:43], v[238:241], a[244:247], v[40:43]\n"
"s_add_u32 m0, s71, 20480\n"
"ds_read_b64_tr_b16 v[218:219], v247 offset:18656\n"
"ds_read_b64_tr_b16 v[220:221], v247 offset:23264\n"
"v_mfma_f32_16x16x32_bf16 v[112:115], v[234:237], a[240:243], v[112:115]\n"
"s_nop 0\n"
"ds_read_b64_tr_b16 v[222:223], v247 offset:27872\n"
"ds_read_b64_tr_b16 v[224:225], v247 offset:32480\n"
"v_mfma_f32_16x16x32_bf16 v[112:115], v[238:241], a[248:251], v[112:115]\n"
"s_waitcnt lgkmcnt(4)\n"
"v_mfma_f32_16x16x32_bf16 v[44:47], v[210:213], a[236:239], v[44:47]\n"
"buffer_load_dwordx4 v173, s[44:47], s76 offen lds\n"
"v_mfma_f32_16x16x32_bf16 v[44:47], v[214:217], a[244:247], v[44:47]\n"
"s_add_u32 m0, s71, 24576\n"
"ds_read_b64_tr_b16 v[226:227], v250 offset:18432\n"
"ds_read_b64_tr_b16 v[228:229], v250 offset:23040\n"
"v_mfma_f32_16x16x32_bf16 v[116:119], v[210:213], a[240:243], v[116:119]\n"
"s_nop 0\n"
"ds_read_b64_tr_b16 v[230:231], v250 offset:27648\n"
"ds_read_b64_tr_b16 v[232:233], v250 offset:32256\n"
"v_mfma_f32_16x16x32_bf16 v[116:119], v[214:217], a[248:251], v[116:119]\n"
"s_waitcnt lgkmcnt(4)\n"
"v_mfma_f32_16x16x32_bf16 v[48:51], v[218:221], a[236:239], v[48:51]\n"
"buffer_load_dwordx4 v174, s[44:47], s76 offen lds\n"
"v_mfma_f32_16x16x32_bf16 v[48:51], v[222:225], a[244:247], v[48:51]\n"
"s_add_u32 m0, s71, 28672\n"
"ds_read_b64_tr_b16 v[234:235], v247 offset:36864\n"
"ds_read_b64_tr_b16 v[236:237], v247 offset:41472\n"
"v_mfma_f32_16x16x32_bf16 v[120:123], v[218:221], a[240:243], v[120:123]\n"
"s_nop 0\n"
"ds_read_b64_tr_b16 v[238:239], v247 offset:46080\n"
"ds_read_b64_tr_b16 v[240:241], v247 offset:50688\n"
"v_mfma_f32_16x16x32_bf16 v[120:123], v[222:225], a[248:251], v[120:123]\n"
"s_waitcnt lgkmcnt(4)\n"
"v_mfma_f32_16x16x32_bf16 v[144:147], v[226:229], a[236:239], v[144:147]\n"
"buffer_load_dwordx4 v175, s[44:47], s76 offen lds\n"
"v_mfma_f32_16x16x32_bf16 v[144:147], v[230:233], a[244:247], v[144:147]\n"
"s_add_u32 m0, s71, 32768\n"
"ds_read_b64_tr_b16 v[210:211], v247 offset:36896\n"
"ds_read_b64_tr_b16 v[212:213], v247 offset:41504\n"
"v_mfma_f32_16x16x32_bf16 v[148:151], v[226:229], a[240:243], v[148:151]\n"
"s_nop 0\n"
"ds_read_b64_tr_b16 v[214:215], v247 offset:46112\n"
"ds_read_b64_tr_b16 v[216:217], v247 offset:50720\n"
"v_mfma_f32_16x16x32_bf16 v[148:151], v[230:233], a[248:251], v[148:151]\n"
"s_waitcnt vmcnt(23) lgkmcnt(4)\n"
"v_mfma_f32_16x16x32_bf16 v[0:3], v[234:237], a[60:63], v[0:3]\n"
"s_nop 0\n"
"ds_read_b64_tr_b16 v[218:219], v247 offset:36928\n"
"ds_read_b64_tr_b16 v[220:221], v247 offset:41536\n"
"v_mfma_f32_16x16x32_bf16 v[0:3], v[238:241], a[68:71], v[0:3]\n"
"buffer_load_dwordx4 v176, s[44:47], s76 offen lds\n"
"v_mfma_f32_16x16x32_bf16 v[72:75], v[234:237], a[64:67], v[72:75]\n"
"s_add_u32 m0, s71, 36864\n"
"ds_read_b64_tr_b16 v[222:223], v247 offset:46144\n"
"ds_read_b64_tr_b16 v[224:225], v247 offset:50752\n"
"v_mfma_f32_16x16x32_bf16 v[72:75], v[238:241], a[72:75], v[72:75]\n"
"s_waitcnt lgkmcnt(4)\n"
"v_mfma_f32_16x16x32_bf16 v[4:7], v[210:213], a[60:63], v[4:7]\n"
"s_mov_b32 s76, 147456\n"
"ds_read_b64_tr_b16 v[226:227], v247 offset:36960\n"
"ds_read_b64_tr_b16 v[228:229], v247 offset:41568\n"
"v_mfma_f32_16x16x32_bf16 v[4:7], v[214:217], a[68:71], v[4:7]\n"
"buffer_load_dwordx4 v177, s[44:47], s76 offen lds\n"
"v_mfma_f32_16x16x32_bf16 v[76:79], v[210:213], a[64:67], v[76:79]\n"
"s_add_u32 s78, s82, 29696\n"
"ds_read_b64_tr_b16 v[230:231], v247 offset:46176\n"
"ds_read_b64_tr_b16 v[232:233], v247 offset:50784\n"
"v_mfma_f32_16x16x32_bf16 v[76:79], v[214:217], a[72:75], v[76:79]\n"
"s_waitcnt lgkmcnt(4)\n"
"v_mfma_f32_16x16x32_bf16 v[8:11], v[218:221], a[60:63], v[8:11]\n"
"s_nop 0\n"
"ds_read_b64_tr_b16 v[234:235], v247 offset:36992\n"
"ds_read_b64_tr_b16 v[236:237], v247 offset:41600\n"
"v_mfma_f32_16x16x32_bf16 v[8:11], v[222:225], a[68:71], v[8:11]\n"
"s_nop 0\n"
"ds_read_b64_tr_b16 v[238:239], v247 offset:46208\n"
"ds_read_b64_tr_b16 v[240:241], v247 offset:50816\n"
"v_mfma_f32_16x16x32_bf16 v[80:83], v[218:221], a[64:67], v[80:83]\n"
"buffer_load_dwordx4 a[108:111], v186, s[60:63], s78 offen offset:0 sc1\n"
"v_mfma_f32_16x16x32_bf16 v[80:83], v[222:225], a[72:75], v[80:83]\n"
"s_waitcnt lgkmcnt(4)\n"
"v_mfma_f32_16x16x32_bf16 v[12:15], v[226:229], a[60:63], v[12:15]\n"
"s_add_u32 s79, s83, 29696\n"
"ds_read_b64_tr_b16 v[210:211], v247 offset:37024\n"
"ds_read_b64_tr_b16 v[212:213], v247 offset:41632\n"
"v_mfma_f32_16x16x32_bf16 v[12:15], v[230:233], a[68:71], v[12:15]\n"
"s_nop 0\n"
"ds_read_b64_tr_b16 v[214:215], v247 offset:46240\n"
"ds_read_b64_tr_b16 v[216:217], v247 offset:50848\n"
"v_mfma_f32_16x16x32_bf16 v[84:87], v[226:229], a[64:67], v[84:87]\n"
"buffer_load_dwordx4 a[112:115], v186, s[60:63], s79 offen offset:0 sc1\n"
"v_mfma_f32_16x16x32_bf16 v[84:87], v[230:233], a[72:75], v[84:87]\n"
"s_waitcnt lgkmcnt(4)\n"
"v_mfma_f32_16x16x32_bf16 v[36:39], v[234:237], a[60:63], v[36:39]\n"
"s_nop 0\n"
"ds_read_b64_tr_b16 v[218:219], v247 offset:37056\n"
"ds_read_b64_tr_b16 v[220:221], v247 offset:41664\n"
"v_mfma_f32_16x16x32_bf16 v[36:39], v[238:241], a[68:71], v[36:39]\n"
"s_nop 0\n"
"ds_read_b64_tr_b16 v[222:223], v247 offset:46272\n"
"ds_read_b64_tr_b16 v[224:225], v247 offset:50880\n"
"v_mfma_f32_16x16x32_bf16 v[108:111], v[234:237], a[64:67], v[108:111]\n"
"v_mfma_f32_16x16x32_bf16 v[108:111], v[238:241], a[72:75], v[108:111]\n"
"s_waitcnt lgkmcnt(4)\n"
"v_mfma_f32_16x16x32_bf16 v[40:43], v[210:213], a[60:63], v[40:43]\n"
"buffer_load_dwordx4 a[116:119], v186, s[60:63], s78 offen offset:1024 sc1\n"
"v_mfma_f32_16x16x32_bf16 v[40:43], v[214:217], a[68:71], v[40:43]\n"
"s_nop 0\n"
"ds_read_b64_tr_b16 v[226:227], v247 offset:37088\n"
"ds_read_b64_tr_b16 v[228:229], v247 offset:41696\n"
"v_mfma_f32_16x16x32_bf16 v[112:115], v[210:213], a[64:67], v[112:115]\n"
"s_nop 0\n"
"ds_read_b64_tr_b16 v[230:231], v247 offset:46304\n"
"ds_read_b64_tr_b16 v[232:233], v247 offset:50912\n"
"v_mfma_f32_16x16x32_bf16 v[112:115], v[214:217], a[72:75], v[112:115]\n"
"s_waitcnt lgkmcnt(4)\n"
"v_mfma_f32_16x16x32_bf16 v[44:47], v[218:221], a[60:63], v[44:47]\n"
"buffer_load_dwordx4 a[120:123], v186, s[60:63], s79 offen offset:1024 sc1\n"
"v_mfma_f32_16x16x32_bf16 v[44:47], v[222:225], a[68:71], v[44:47]\n"
"s_add_u32 m0, s71, 40960\n"
"ds_read_b64_tr_b16 v[234:235], v250 offset:36864\n"
"ds_read_b64_tr_b16 v[236:237], v250 offset:41472\n"
"v_mfma_f32_16x16x32_bf16 v[116:119], v[218:221], a[64:67], v[116:119]\n"
"s_nop 0\n"
"ds_read_b64_tr_b16 v[238:239], v250 offset:46080\n"
"ds_read_b64_tr_b16 v[240:241], v250 offset:50688\n"
"v_mfma_f32_16x16x32_bf16 v[116:119], v[222:225], a[72:75], v[116:119]\n"
"s_waitcnt vmcnt(22) lgkmcnt(4)\n"
"s_barrier\n"
"v_mfma_f32_16x16x32_bf16 v[48:51], v[226:229], a[60:63], v[48:51]\n"
"buffer_load_dwordx4 v178, s[44:47], s76 offen lds\n"
"v_mfma_f32_16x16x32_bf16 v[48:51], v[230:233], a[68:71], v[48:51]\n"
"s_add_u32 m0, s71, 45056\n"
"ds_read_b64_tr_b16 v[210:211], v248 offset:0\n"
"ds_read_b64_tr_b16 v[212:213], v248 offset:4608\n"
"v_mfma_f32_16x16x32_bf16 v[120:123], v[226:229], a[64:67], v[120:123]\n"
"s_nop 0\n"
"ds_read_b64_tr_b16 v[214:215], v248 offset:9216\n"
"ds_read_b64_tr_b16 v[216:217], v248 offset:13824\n"
"v_mfma_f32_16x16x32_bf16 v[120:123], v[230:233], a[72:75], v[120:123]\n"
"s_waitcnt lgkmcnt(4)\n"
"v_mfma_f32_16x16x32_bf16 v[144:147], v[234:237], a[60:63], v[144:147]\n"
"buffer_load_dwordx4 v179, s[44:47], s76 offen lds\n"
"v_mfma_f32_16x16x32_bf16 v[144:147], v[238:241], a[68:71], v[144:147]\n"
"s_add_u32 m0, s71, 49152\n"
"ds_read_b64_tr_b16 v[218:219], v248 offset:32\n"
"ds_read_b64_tr_b16 v[220:221], v248 offset:4640\n"
"v_mfma_f32_16x16x32_bf16 v[148:151], v[234:237], a[64:67], v[148:151]\n"
"s_nop 0\n"
"ds_read_b64_tr_b16 v[222:223], v248 offset:9248\n"
"ds_read_b64_tr_b16 v[224:225], v248 offset:13856\n"
"v_mfma_f32_16x16x32_bf16 v[148:151], v[238:241], a[72:75], v[148:151]\n"
"s_waitcnt vmcnt(20) lgkmcnt(4)\n"
"v_mfma_f32_16x16x32_bf16 v[0:3], v[210:213], a[76:79], v[0:3]\n"
"s_nop 0\n"
"ds_read_b64_tr_b16 v[226:227], v248 offset:64\n"
"ds_read_b64_tr_b16 v[228:229], v248 offset:4672\n"
"v_mfma_f32_16x16x32_bf16 v[0:3], v[214:217], a[84:87], v[0:3]\n"
"buffer_load_dwordx4 v180, s[44:47], s76 offen lds\n"
"v_mfma_f32_16x16x32_bf16 v[72:75], v[210:213], a[80:83], v[72:75]\n"
"s_add_u32 m0, s71, 53248\n"
"ds_read_b64_tr_b16 v[230:231], v248 offset:9280\n"
"ds_read_b64_tr_b16 v[232:233], v248 offset:13888\n"
"v_mfma_f32_16x16x32_bf16 v[72:75], v[214:217], a[88:91], v[72:75]\n"
"s_waitcnt lgkmcnt(4)\n"
"v_mfma_f32_16x16x32_bf16 v[4:7], v[218:221], a[76:79], v[4:7]\n"
"s_nop 0\n"
"ds_read_b64_tr_b16 v[234:235], v248 offset:96\n"
"ds_read_b64_tr_b16 v[236:237], v248 offset:4704\n"
"v_mfma_f32_16x16x32_bf16 v[4:7], v[222:225], a[84:87], v[4:7]\n"
"buffer_load_dwordx4 v181, s[44:47], s76 offen lds\n"
"v_mfma_f32_16x16x32_bf16 v[76:79], v[218:221], a[80:83], v[76:79]\n"
"s_add_u32 m0, s71, 57344\n"
"ds_read_b64_tr_b16 v[238:239], v248 offset:9312\n"
"ds_read_b64_tr_b16 v[240:241], v248 offset:13920\n"
"v_mfma_f32_16x16x32_bf16 v[76:79], v[222:225], a[88:91], v[76:79]\n"
"s_waitcnt lgkmcnt(4)\n"
"v_mfma_f32_16x16x32_bf16 v[8:11], v[226:229], a[76:79], v[8:11]\n"
"s_nop 0\n"
"ds_read_b64_tr_b16 v[210:211], v248 offset:128\n"
"ds_read_b64_tr_b16 v[212:213], v248 offset:4736\n"
"v_mfma_f32_16x16x32_bf16 v[8:11], v[230:233], a[84:87], v[8:11]\n"
"s_nop 0\n"
"ds_read_b64_tr_b16 v[214:215], v248 offset:9344\n"
"ds_read_b64_tr_b16 v[216:217], v248 offset:13952\n"
"v_mfma_f32_16x16x32_bf16 v[80:83], v[226:229], a[80:83], v[80:83]\n"
"buffer_load_dwordx4 v182, s[44:47], s76 offen lds\n"
"v_mfma_f32_16x16x32_bf16 v[80:83], v[230:233], a[88:91], v[80:83]\n"
"s_waitcnt lgkmcnt(4)\n"
"v_mfma_f32_16x16x32_bf16 v[12:15], v[234:237], a[76:79], v[12:15]\n"
"s_add_u32 m0, s71, 61440\n"
"ds_read_b64_tr_b16 v[218:219], v248 offset:160\n"
"ds_read_b64_tr_b16 v[220:221], v248 offset:4768\n"
"v_mfma_f32_16x16x32_bf16 v[12:15], v[238:241], a[84:87], v[12:15]\n"
"s_nop 0\n"
"ds_read_b64_tr_b16 v[222:223], v248 offset:9376\n"
"ds_read_b64_tr_b16 v[224:225], v248 offset:13984\n"
"v_mfma_f32_16x16x32_bf16 v[84:87], v[234:237], a[80:83], v[84:87]\n"
"buffer_load_dwordx4 v183, s[44:47], s76 offen lds\n"
"v_mfma_f32_16x16x32_bf16 v[84:87], v[238:241], a[88:91], v[84:87]\n"
"s_waitcnt lgkmcnt(4)\n"
"v_mfma_f32_16x16x32_bf16 v[36:39], v[210:213], a[76:79], v[36:39]\n"
"s_add_u32 m0, s71, 65536\n"
"ds_read_b64_tr_b16 v[226:227], v248 offset:192\n"
"ds_read_b64_tr_b16 v[228:229], v248 offset:4800\n"
"v_mfma_f32_16x16x32_bf16 v[36:39], v[214:217], a[84:87], v[36:39]\n"
"s_nop 0\n"
"ds_read_b64_tr_b16 v[230:231], v248 offset:9408\n"
"ds_read_b64_tr_b16 v[232:233], v248 offset:14016\n"
"v_mfma_f32_16x16x32_bf16 v[108:111], v[210:213], a[80:83], v[108:111]\n"
"v_mfma_f32_16x16x32_bf16 v[108:111], v[214:217], a[88:91], v[108:111]\n"
"s_waitcnt lgkmcnt(4)\n"
"v_mfma_f32_16x16x32_bf16 v[40:43], v[218:221], a[76:79], v[40:43]\n"
"buffer_load_dwordx4 v184, s[44:47], s76 offen lds\n"
"v_mfma_f32_16x16x32_bf16 v[40:43], v[222:225], a[84:87], v[40:43]\n"
"s_add_u32 m0, s71, 69632\n"
"ds_read_b64_tr_b16 v[234:235], v248 offset:224\n"
"ds_read_b64_tr_b16 v[236:237], v248 offset:4832\n"
"v_mfma_f32_16x16x32_bf16 v[112:115], v[218:221], a[80:83], v[112:115]\n"
"s_nop 0\n"
"ds_read_b64_tr_b16 v[238:239], v248 offset:9440\n"
"ds_read_b64_tr_b16 v[240:241], v248 offset:14048\n"
"v_mfma_f32_16x16x32_bf16 v[112:115], v[222:225], a[88:91], v[112:115]\n"
"s_waitcnt lgkmcnt(4)\n"
"v_mfma_f32_16x16x32_bf16 v[44:47], v[226:229], a[76:79], v[44:47]\n"
"buffer_load_dwordx4 v185, s[44:47], s76 offen lds\n"
"v_mfma_f32_16x16x32_bf16 v[44:47], v[230:233], a[84:87], v[44:47]\n"
"s_add_u32 s78, s82, 34816\n"
"ds_read_b64_tr_b16 v[210:211], v251 offset:0\n"
"ds_read_b64_tr_b16 v[212:213], v251 offset:4608\n"
"v_mfma_f32_16x16x32_bf16 v[116:119], v[226:229], a[80:83], v[116:119]\n"
"s_nop 0\n"
"ds_read_b64_tr_b16 v[214:215], v251 offset:9216\n"
"ds_read_b64_tr_b16 v[216:217], v251 offset:13824\n"
"v_mfma_f32_16x16x32_bf16 v[116:119], v[230:233], a[88:91], v[116:119]\n"
"s_waitcnt lgkmcnt(4)\n"
"v_mfma_f32_16x16x32_bf16 v[48:51], v[234:237], a[76:79], v[48:51]\n"
"buffer_load_dwordx4 a[124:127], v186, s[60:63], s78 offen offset:0 sc1\n"
"v_mfma_f32_16x16x32_bf16 v[48:51], v[238:241], a[84:87], v[48:51]\n"
"s_add_u32 s79, s83, 34816\n"
"ds_read_b64_tr_b16 v[218:219], v248 offset:18432\n"
"ds_read_b64_tr_b16 v[220:221], v248 offset:23040\n"
"v_mfma_f32_16x16x32_bf16 v[120:123], v[234:237], a[80:83], v[120:123]\n"
"L_tp2_2_1:\n"
"s_cbranch_execz L_tb2_2\n"
"ds_read_b64_tr_b16 v[222:223], v248 offset:27648\n"
"ds_read_b64_tr_b16 v[224:225], v248 offset:32256\n"
"v_mfma_f32_16x16x32_bf16 v[120:123], v[238:241], a[88:91], v[120:123]\n"
"s_waitcnt lgkmcnt(4)\n"
"v_mfma_f32_16x16x32_bf16 v[144:147], v[210:213], a[76:79], v[144:147]\n"
"buffer_load_dwordx4 a[128:131], v186, s[60:63], s79 offen offset:0 sc1\n"
"v_mfma_f32_16x16x32_bf16 v[144:147], v[214:217], a[84:87], v[144:147]\n"
"s_nop 0\n"
"ds_read_b64_tr_b16 v[226:227], v248 offset:18464\n"
"ds_read_b64_tr_b16 v[228:229], v248 offset:23072\n"
"v_mfma_f32_16x16x32_bf16 v[148:151], v[210:213], a[80:83], v[148:151]\n"
"s_nop 0\n"
"ds_read_b64_tr_b16 v[230:231], v248 offset:27680\n"
"ds_read_b64_tr_b16 v[232:233], v248 offset:32288\n"
"v_mfma_f32_16x16x32_bf16 v[148:151], v[214:217], a[88:91], v[148:151]\n"
"s_waitcnt vmcnt(22) lgkmcnt(4)\n"
"v_mfma_f32_16x16x32_bf16 v[0:3], v[218:221], a[92:95], v[0:3]\n"
"s_nop 0\n"
"ds_read_b64_tr_b16 v[234:235], v248 offset:18496\n"
"ds_read_b64_tr_b16 v[236:237], v248 offset:23104\n"
"v_mfma_f32_16x16x32_bf16 v[0:3], v[222:225], a[100:103], v[0:3]\n"
"buffer_load_dwordx4 a[132:135], v186, s[60:63], s78 offen offset:1024 sc1\n"
"v_mfma_f32_16x16x32_bf16 v[72:75], v[218:221], a[96:99], v[72:75]\n"
"s_nop 0\n"
"ds_read_b64_tr_b16 v[238:239], v248 offset:27712\n"
"ds_read_b64_tr_b16 v[240:241], v248 offset:32320\n"
"v_mfma_f32_16x16x32_bf16 v[72:75], v[222:225], a[104:107], v[72:75]\n"
"s_waitcnt lgkmcnt(4)\n"
"v_mfma_f32_16x16x32_bf16 v[4:7], v[226:229], a[92:95], v[4:7]\n"
"s_nop 0\n"
"ds_read_b64_tr_b16 v[210:211], v248 offset:18528\n"
"ds_read_b64_tr_b16 v[212:213], v248 offset:23136\n"
"v_mfma_f32_16x16x32_bf16 v[4:7], v[230:233], a[100:103], v[4:7]\n"
"buffer_load_dwordx4 a[136:139], v186, s[60:63], s79 offen offset:1024 sc1\n"
"v_mfma_f32_16x16x32_bf16 v[76:79], v[226:229], a[96:99], v[76:79]\n"
"s_add_u32 s78, s82, 4096\n"
"ds_read_b64_tr_b16 v[214:215], v248 offset:27744\n"
"ds_read_b64_tr_b16 v[216:217], v248 offset:32352\n"
"v_mfma_f32_16x16x32_bf16 v[76:79], v[230:233], a[104:107], v[76:79]\n"
"s_waitcnt lgkmcnt(4)\n"
"v_mfma_f32_16x16x32_bf16 v[8:11], v[234:237], a[92:95], v[8:11]\n"
"s_nop 0\n"
"ds_read_b64_tr_b16 v[218:219], v248 offset:18560\n"
"ds_read_b64_tr_b16 v[220:221], v248 offset:23168\n"
"v_mfma_f32_16x16x32_bf16 v[8:11], v[238:241], a[100:103], v[8:11]\n"
"s_nop 0\n"
"ds_read_b64_tr_b16 v[222:223], v248 offset:27776\n"
"ds_read_b64_tr_b16 v[224:225], v248 offset:32384\n"
"v_mfma_f32_16x16x32_bf16 v[80:83], v[234:237], a[96:99], v[80:83]\n"
"buffer_load_dwordx2 a[140:141], v187, s[60:63], s78 offen sc1\n"
"v_mfma_f32_16x16x32_bf16 v[80:83], v[238:241], a[104:107], v[80:83]\n"
"s_waitcnt lgkmcnt(4)\n"
"v_mfma_f32_16x16x32_bf16 v[12:15], v[210:213], a[92:95], v[12:15]\n"
"buffer_load_dwordx2 a[142:143], v187, s[60:63], s78 offen offset:512 sc1\n"
"v_mfma_f32_16x16x32_bf16 v[12:15], v[214:217], a[100:103], v[12:15]\n"
"s_add_u32 s79, s83, 4096\n"
"ds_read_b64_tr_b16 v[226:227], v248 offset:18592\n"
"ds_read_b64_tr_b16 v[228:229], v248 offset:23200\n"
"v_mfma_f32_16x16x32_bf16 v[84:87], v[210:213], a[96:99], v[84:87]\n"
"buffer_load_dwordx2 a[144:145], v187, s[60:63], s79 offen sc1\n"
"v_mfma_f32_16x16x32_bf16 v[84:87], v[214:217], a[104:107], v[84:87]\n"
"s_waitcnt lgkmcnt(2)\n"
"ds_read_b64_tr_b16 v[230:231], v248 offset:27808\n"
"ds_read_b64_tr_b16 v[232:233], v248 offset:32416\n"
"v_mfma_f32_16x16x32_bf16 v[36:39], v[218:221], a[92:95], v[36:39]\n"
"s_nop 0\n"
"ds_read_b64_tr_b16 v[234:235], v248 offset:18624\n"
"ds_read_b64_tr_b16 v[236:237], v248 offset:23232\n"
"v_mfma_f32_16x16x32_bf16 v[36:39], v[222:225], a[100:103], v[36:39]\n"
"buffer_load_dwordx2 a[146:147], v187, s[60:63], s79 offen offset:512 sc1\n"
"v_mfma_f32_16x16x32_bf16 v[108:111], v[218:221], a[96:99], v[108:111]\n"
"s_add_u32 s78, s82, 13312\n"
"ds_read_b64_tr_b16 v[238:239], v248 offset:27840\n"
"ds_read_b64_tr_b16 v[240:241], v248 offset:32448\n"
"v_mfma_f32_16x16x32_bf16 v[108:111], v[222:225], a[104:107], v[108:111]\n"
"s_waitcnt lgkmcnt(4)\n"
"v_mfma_f32_16x16x32_bf16 v[40:43], v[226:229], a[92:95], v[40:43]\n"
"buffer_load_dwordx2 a[148:149], v187, s[60:63], s78 offen sc1\n"
"v_mfma_f32_16x16x32_bf16 v[40:43], v[230:233], a[100:103], v[40:43]\n"
"buffer_load_dwordx2 a[150:151], v187, s[60:63], s78 offen offset:512 sc1\n"
"v_mfma_f32_16x16x32_bf16 v[112:115], v[226:229], a[96:99], v[112:115]\n"
"s_add_u32 s79, s83, 13312\n"
"ds_read_b64_tr_b16 v[210:211], v248 offset:18656\n"
"ds_read_b64_tr_b16 v[212:213], v248 offset:23264\n"
"v_mfma_f32_16x16x32_bf16 v[112:115], v[230:233], a[104:107], v[112:115]\n"
"s_waitcnt lgkmcnt(2)\n"
"ds_read_b64_tr_b16 v[214:215], v248 offset:27872\n"
"ds_read_b64_tr_b16 v[216:217], v248 offset:32480\n"
"v_mfma_f32_16x16x32_bf16 v[44:47], v[234:237], a[92:95], v[44:47]\n"
"buffer_load_dwordx2 a[152:153], v187, s[60:63], s79 offen sc1\n"
"v_mfma_f32_16x16x32_bf16 v[44:47], v[238:241], a[100:103], v[44:47]\n"
"s_nop 0\n"
"ds_read_b64_tr_b16 v[218:219], v251 offset:18432\n"
"ds_read_b64_tr_b16 v[220:221], v251 offset:23040\n"
"v_mfma_f32_16x16x32_bf16 v[116:119], v[234:237], a[96:99], v[116:119]\n"
"s_nop 0\n"
"ds_read_b64_tr_b16 v[222:223], v251 offset:27648\n"
"ds_read_b64_tr_b16 v[224:225], v251 offset:32256\n"
"v_mfma_f32_16x16x32_bf16 v[116:119], v[238:241], a[104:107], v[116:119]\n"
"s_waitcnt vmcnt(24) lgkmcnt(4)\n"
"s_barrier\n"
"v_mfma_f32_16x16x32_bf16 v[48:51], v[210:213], a[92:95], v[48:51]\n"
"buffer_load_dwordx2 a[154:155], v187, s[60:63], s79 offen offset:512 sc1\n"
"v_mfma_f32_16x16x32_bf16 v[48:51], v[214:217], a[100:103], v[48:51]\n"
"s_add_u32 s78, s82, 22528\n"
"ds_read_b64_tr_b16 v[226:227], v246 offset:0\n"
"ds_read_b64_tr_b16 v[228:229], v246 offset:4608\n"
"v_mfma_f32_16x16x32_bf16 v[120:123], v[210:213], a[96:99], v[120:123]\n"
"buffer_load_dwordx2 a[156:157], v187, s[60:63], s78 offen sc1\n"
"v_mfma_f32_16x16x32_bf16 v[120:123], v[214:217], a[104:107], v[120:123]\n"
"s_waitcnt lgkmcnt(2)\n"
"ds_read_b64_tr_b16 v[230:231], v246 offset:9216\n"
"ds_read_b64_tr_b16 v[232:233], v246 offset:13824\n"
"v_mfma_f32_16x16x32_bf16 v[144:147], v[218:221], a[92:95], v[144:147]\n"
"buffer_load_dwordx2 a[158:159], v187, s[60:63], s78 offen offset:512 sc1\n"
"v_mfma_f32_16x16x32_bf16 v[144:147], v[222:225], a[100:103], v[144:147]\n"
"s_add_u32 s79, s83, 22528\n"
"ds_read_b64_tr_b16 v[234:235], v246 offset:32\n"
"ds_read_b64_tr_b16 v[236:237], v246 offset:4640\n"
"v_mfma_f32_16x16x32_bf16 v[148:151], v[218:221], a[96:99], v[148:151]\n"
"buffer_load_dwordx2 a[160:161], v187, s[60:63], s79 offen sc1\n"
"v_mfma_f32_16x16x32_bf16 v[148:151], v[222:225], a[104:107], v[148:151]\n"
"s_waitcnt vmcnt(23) lgkmcnt(2)\n"
"ds_read_b64_tr_b16 v[238:239], v246 offset:9248\n"
"ds_read_b64_tr_b16 v[240:241], v246 offset:13856\n"
"v_mfma_f32_16x16x32_bf16 v[0:3], v[226:229], a[108:111], v[0:3]\n"
"buffer_load_dwordx2 a[162:163], v187, s[60:63], s79 offen offset:512 sc1\n"
"v_mfma_f32_16x16x32_bf16 v[0:3], v[230:233], a[116:119], v[0:3]\n"
"s_add_u32 s78, s82, 31744\n"
"ds_read_b64_tr_b16 v[210:211], v246 offset:64\n"
"ds_read_b64_tr_b16 v[212:213], v246 offset:4672\n"
"v_mfma_f32_16x16x32_bf16 v[72:75], v[226:229], a[112:115], v[72:75]\n"
"buffer_load_dwordx2 a[164:165], v187, s[60:63], s78 offen sc1\n"
"v_mfma_f32_16x16x32_bf16 v[72:75], v[230:233], a[120:123], v[72:75]\n"
"s_waitcnt lgkmcnt(2)\n"
"ds_read_b64_tr_b16 v[214:215], v246 offset:9280\n"
"ds_read_b64_tr_b16 v[216:217], v246 offset:13888\n"
"v_mfma_f32_16x16x32_bf16 v[4:7], v[234:237], a[108:111], v[4:7]\n"
"buffer_load_dwordx2 a[166:167], v187, s[60:63], s78 offen offset:512 sc1\n"
"v_mfma_f32_16x16x32_bf16 v[4:7], v[238:241], a[116:119], v[4:7]\n"
"s_add_u32 s79, s83, 31744\n"
"ds_read_b64_tr_b16 v[218:219], v246 offset:96\n"
"ds_read_b64_tr_b16 v[220:221], v246 offset:4704\n"
"v_mfma_f32_16x16x32_bf16 v[76:79], v[234:237], a[112:115], v[76:79]\n"
"buffer_load_dwordx2 a[168:169], v187, s[60:63], s79 offen sc1\n"
"v_mfma_f32_16x16x32_bf16 v[76:79], v[238:241], a[120:123], v[76:79]\n"
"s_waitcnt lgkmcnt(2)\n"
"ds_read_b64_tr_b16 v[222:223], v246 offset:9312\n"
"ds_read_b64_tr_b16 v[224:225], v246 offset:13920\n"
"v_mfma_f32_16x16x32_bf16 v[8:11], v[210:213], a[108:111], v[8:11]\n"
"buffer_load_dwordx2 a[170:171], v187, s[60:63], s79 offen offset:512 sc1\n"
"v_mfma_f32_16x16x32_bf16 v[8:11], v[214:217], a[116:119], v[8:11]\n"
"s_nop 0\n"
"ds_read_b64_tr_b16 v[226:227], v246 offset:128\n"
"ds_read_b64_tr_b16 v[228:229], v246 offset:4736\n"
"v_mfma_f32_16x16x32_bf16 v[80:83], v[210:213], a[112:115], v[80:83]\n"
"s_nop 0\n"
"ds_read_b64_tr_b16 v[230:231], v246 offset:9344\n"
"ds_read_b64_tr_b16 v[232:233], v246 offset:13952\n"
"v_mfma_f32_16x16x32_bf16 v[80:83], v[214:217], a[120:123], v[80:83]\n"
"s_waitcnt lgkmcnt(4)\n"
"v_mfma_f32_16x16x32_bf16 v[12:15], v[218:221], a[108:111], v[12:15]\n"
"s_nop 0\n"
"ds_read_b64_tr_b16 v[234:235], v246 offset:160\n"
"ds_read_b64_tr_b16 v[236:237], v246 offset:4768\n"
"v_mfma_f32_16x16x32_bf16 v[12:15], v[222:225], a[116:119], v[12:15]\n"
"s_nop 0\n"
"ds_read_b64_tr_b16 v[238:239], v246 offset:9376\n"
"ds_read_b64_tr_b16 v[240:241], v246 offset:13984\n"
"v_mfma_f32_16x16x32_bf16 v[84:87], v[218:221], a[112:115], v[84:87]\n"
"v_mfma_f32_16x16x32_bf16 v[84:87], v[222:225], a[120:123], v[84:87]\n"
"s_waitcnt lgkmcnt(4)\n"
"v_mfma_f32_16x16x32_bf16 v[36:39], v[226:229], a[108:111], v[36:39]\n"
"s_nop 0\n"
"ds_read_b64_tr_b16 v[210:211], v246 offset:192\n"
"ds_read_b64_tr_b16 v[212:213], v246 offset:4800\n"
"v_mfma_f32_16x16x32_bf16 v[36:39], v[230:233], a[116:119], v[36:39]\n"
"s_nop 0\n"
"ds_read_b64_tr_b16 v[214:215], v246 offset:9408\n"
"ds_read_b64_tr_b16 v[216:217], v246 offset:14016\n"
"v_mfma_f32_16x16x32_bf16 v[108:111], v[226:229], a[112:115], v[108:111]\n"
"v_mfma_f32_16x16x32_bf16 v[108:111], v[230:233], a[120:123], v[108:111]\n"
"s_waitcnt lgkmcnt(4)\n"
"v_mfma_f32_16x16x32_bf16 v[40:43], v[234:237], a[108:111], v[40:43]\n"
"s_nop 0\n"
"ds_read_b64_tr_b16 v[218:219], v246 offset:224\n"
"ds_read_b64_tr_b16 v[220:221], v246 offset:4832\n"
"v_mfma_f32_16x16x32_bf16 v[40:43], v[238:241], a[116:119], v[40:43]\n"
"s_nop 0\n"
"ds_read_b64_tr_b16 v[222:223], v246 offset:9440\n"
"ds_read_b64_tr_b16 v[224:225], v246 offset:14048\n"
"v_mfma_f32_16x16x32_bf16 v[112:115], v[234:237], a[112:115], v[112:115]\n"
"v_mfma_f32_16x16x32_bf16 v[112:115], v[238:241], a[120:123], v[112:115]\n"
"s_waitcnt lgkmcnt(4)\n"
"v_mfma_f32_16x16x32_bf16 v[44:47], v[210:213], a[108:111], v[44:47]\n"
"s_nop 0\n"
"ds_read_b64_tr_b16 v[226:227], v249 offset:0\n"
"ds_read_b64_tr_b16 v[228:229], v249 offset:4608\n"
"v_mfma_f32_16x16x32_bf16 v[44:47], v[214:217], a[116:119], v[44:47]\n"
"s_nop 0\n"
"ds_read_b64_tr_b16 v[230:231], v249 offset:9216\n"
"ds_read_b64_tr_b16 v[232:233], v249 offset:13824\n"
"v_mfma_f32_16x16x32_bf16 v[116:119], v[210:213], a[112:115], v[116:119]\n"
"v_mfma_f32_16x16x32_bf16 v[116:119], v[214:217], a[120:123], v[116:119]\n"
"s_waitcnt lgkmcnt(4)\n"
"v_mfma_f32_16x16x32_bf16 v[48:51], v[218:221], a[108:111], v[48:51]\n"
"s_nop 0\n"
"ds_read_b64_tr_b16 v[234:235], v246 offset:18432\n"
"ds_read_b64_tr_b16 v[236:237], v246 offset:23040\n"
"v_mfma_f32_16x16x32_bf16 v[48:51], v[222:225], a[116:119], v[48:51]\n"
"s_nop 0\n"
"ds_read_b64_tr_b16 v[238:239], v246 offset:27648\n"
"ds_read_b64_tr_b16 v[240:241], v246 offset:32256\n"
"v_mfma_f32_16x16x32_bf16 v[120:123], v[218:221], a[112:115], v[120:123]\n"
"v_mfma_f32_16x16x32_bf16 v[120:123], v[222:225], a[120:123], v[120:123]\n"
"s_waitcnt vmcnt(20) lgkmcnt(4)\n"
"s_barrier\n"
"v_mfma_f32_16x16x32_bf16 v[144:147], v[226:229], a[108:111], v[144:147]\n"
"s_nop 0\n"
"ds_read_b64_tr_b16 v[210:211], v246 offset:36864\n"
"ds_read_b64_tr_b16 v[212:213], v246 offset:41472\n"
"v_mfma_f32_16x16x32_bf16 v[144:147], v[230:233], a[116:119], v[144:147]\n"
"s_nop 0\n"
"ds_read_b64_tr_b16 v[214:215], v246 offset:46080\n"
"ds_read_b64_tr_b16 v[216:217], v246 offset:50688\n"
"v_mfma_f32_16x16x32_bf16 v[148:151], v[226:229], a[112:115], v[148:151]\n"
"v_mfma_f32_16x16x32_bf16 v[148:151], v[230:233], a[120:123], v[148:151]\n"
"s_waitcnt vmcnt(16) lgkmcnt(4)\n"
"v_mfma_f32_16x16x32_bf16 v[0:3], v[234:237], a[124:127], v[0:3]\n"
"s_nop 0\n"
"ds_read_b64_tr_b16 v[218:219], v247 offset:0\n"
"ds_read_b64_tr_b16 v[220:221], v247 offset:4608\n"
"v_mfma_f32_16x16x32_bf16 v[0:3], v[238:241], a[132:135], v[0:3]\n"
"s_nop 0\n"
"ds_read_b64_tr_b16 v[222:223], v247 offset:9216\n"
"ds_read_b64_tr_b16 v[224:225], v247 offset:13824\n"
"v_mfma_f32_16x16x32_bf16 v[72:75], v[234:237], a[128:131], v[72:75]\n"
"v_mfma_f32_16x16x32_bf16 v[72:75], v[238:241], a[136:139], v[72:75]\n"
"s_waitcnt vmcnt(8) lgkmcnt(4)\n"
"v_mfma_f32_16x16x32_bf16 v[0:3], v[210:213], a[140:143], v[0:3]\n"
"s_nop 0\n"
"ds_read_b64_tr_b16 v[226:227], v246 offset:18464\n"
"ds_read_b64_tr_b16 v[228:229], v246 offset:23072\n"
"v_mfma_f32_16x16x32_bf16 v[0:3], v[214:217], a[148:151], v[0:3]\n"
"s_nop 0\n"
"ds_read_b64_tr_b16 v[230:231], v246 offset:27680\n"
"ds_read_b64_tr_b16 v[232:233], v246 offset:32288\n"
"v_mfma_f32_16x16x32_bf16 v[72:75], v[210:213], a[144:147], v[72:75]\n"
"v_mfma_f32_16x16x32_bf16 v[72:75], v[214:217], a[152:155], v[72:75]\n"
"s_waitcnt vmcnt(0) lgkmcnt(4)\n"
"v_mfma_f32_16x16x32_bf16 v[0:3], v[218:221], a[156:159], v[0:3]\n"
"s_nop 0\n"
"ds_read_b64_tr_b16 v[234:235], v246 offset:36896\n"
"ds_read_b64_tr_b16 v[236:237], v246 offset:41504\n"
"v_mfma_f32_16x16x32_bf16 v[0:3], v[222:225], a[164:167], v[0:3]\n"
"s_nop 0\n"
"ds_read_b64_tr_b16 v[238:239], v246 offset:46112\n"
"ds_read_b64_tr_b16 v[240:241], v246 offset:50720\n"
"v_mfma_f32_16x16x32_bf16 v[72:75], v[218:221], a[160:163], v[72:75]\n"
"v_mfma_f32_16x16x32_bf16 v[72:75], v[222:225], a[168:171], v[72:75]\n"
"s_waitcnt lgkmcnt(4)\n"
"v_mfma_f32_16x16x32_bf16 v[4:7], v[226:229], a[124:127], v[4:7]\n"
"s_nop 0\n"
"ds_read_b64_tr_b16 v[210:211], v247 offset:32\n"
"ds_read_b64_tr_b16 v[212:213], v247 offset:4640\n"
"v_mfma_f32_16x16x32_bf16 v[4:7], v[230:233], a[132:135], v[4:7]\n"
"s_nop 0\n"
"ds_read_b64_tr_b16 v[214:215], v247 offset:9248\n"
"ds_read_b64_tr_b16 v[216:217], v247 offset:13856\n"
"v_mfma_f32_16x16x32_bf16 v[76:79], v[226:229], a[128:131], v[76:79]\n"
"v_mfma_f32_16x16x32_bf16 v[76:79], v[230:233], a[136:139], v[76:79]\n"
"s_waitcnt lgkmcnt(4)\n"
"v_mfma_f32_16x16x32_bf16 v[4:7], v[234:237], a[140:143], v[4:7]\n"
"s_nop 0\n"
"ds_read_b64_tr_b16 v[218:219], v246 offset:18496\n"
"ds_read_b64_tr_b16 v[220:221], v246 offset:23104\n"
"v_mfma_f32_16x16x32_bf16 v[4:7], v[238:241], a[148:151], v[4:7]\n"
"s_nop 0\n"
"ds_read_b64_tr_b16 v[222:223], v246 offset:27712\n"
"ds_read_b64_tr_b16 v[224:225], v246 offset:32320\n"
"v_mfma_f32_16x16x32_bf16 v[76:79], v[234:237], a[144:147], v[76:79]\n"
"v_cvt_pk_bf16_f32 v0, v0, v1\n"
"v_cvt_pk_bf16_f32 v1, v2, v3\n"
"v_mfma_f32_16x16x32_bf16 v[76:79], v[238:241], a[152:155], v[76:79]\n"
"s_waitcnt lgkmcnt(4)\n"
"v_cvt_pk_bf16_f32 v2, v72, v73\n"
"v_cvt_pk_bf16_f32 v3, v74, v75\n"
"v_mfma_f32_16x16x32_bf16 v[4:7], v[210:213], a[156:159], v[4:7]\n"
"s_add_u32 s67, s89, 100663296\n"
"ds_read_b64_tr_b16 v[226:227], v246 offset:36928\n"
"ds_read_b64_tr_b16 v[228:229], v246 offset:41536\n"
"v_mfma_f32_16x16x32_bf16 v[4:7], v[214:217], a[164:167], v[4:7]\n"
"buffer_store_dwordx4 v[0:3], v196, s[60:63], s67 offen offset:0 sc1\n"
"v_mfma_f32_16x16x32_bf16 v[76:79], v[210:213], a[160:163], v[76:79]\n"
"s_nop 0\n"
"ds_read_b64_tr_b16 v[230:231], v246 offset:46144\n"
"ds_read_b64_tr_b16 v[232:233], v246 offset:50752\n"
"v_mfma_f32_16x16x32_bf16 v[76:79], v[214:217], a[168:171], v[76:79]\n"
"s_waitcnt lgkmcnt(4)\n"
"v_mfma_f32_16x16x32_bf16 v[8:11], v[218:221], a[124:127], v[8:11]\n"
"s_nop 0\n"
"ds_read_b64_tr_b16 v[234:235], v247 offset:64\n"
"ds_read_b64_tr_b16 v[236:237], v247 offset:4672\n"
"v_mfma_f32_16x16x32_bf16 v[8:11], v[222:225], a[132:135], v[8:11]\n"
"s_nop 0\n"
"ds_read_b64_tr_b16 v[238:239], v247 offset:9280\n"
"ds_read_b64_tr_b16 v[240:241], v247 offset:13888\n"
"v_mfma_f32_16x16x32_bf16 v[80:83], v[218:221], a[128:131], v[80:83]\n"
"v_mfma_f32_16x16x32_bf16 v[80:83], v[222:225], a[136:139], v[80:83]\n"
"s_waitcnt lgkmcnt(4)\n"
"v_mfma_f32_16x16x32_bf16 v[8:11], v[226:229], a[140:143], v[8:11]\n"
"s_nop 0\n"
"ds_read_b64_tr_b16 v[210:211], v246 offset:18528\n"
"ds_read_b64_tr_b16 v[212:213], v246 offset:23136\n"
"v_mfma_f32_16x16x32_bf16 v[8:11], v[230:233], a[148:151], v[8:11]\n"
"s_nop 0\n"
"ds_read_b64_tr_b16 v[214:215], v246 offset:27744\n"
"ds_read_b64_tr_b16 v[216:217], v246 offset:32352\n"
"v_mfma_f32_16x16x32_bf16 v[80:83], v[226:229], a[144:147], v[80:83]\n"
"v_cvt_pk_bf16_f32 v4, v4, v5\n"
"v_cvt_pk_bf16_f32 v5, v6, v7\n"
"v_mfma_f32_16x16x32_bf16 v[80:83], v[230:233], a[152:155], v[80:83]\n"
"s_waitcnt lgkmcnt(4)\n"
"v_cvt_pk_bf16_f32 v6, v76, v77\n"
"v_cvt_pk_bf16_f32 v7, v78, v79\n"
"v_mfma_f32_16x16x32_bf16 v[8:11], v[234:237], a[156:159], v[8:11]\n"
"buffer_store_dwordx4 v[4:7], v196, s[60:63], s67 offen offset:256 sc1\n"
"v_mfma_f32_16x16x32_bf16 v[8:11], v[238:241], a[164:167], v[8:11]\n"
"s_nop 0\n"
"ds_read_b64_tr_b16 v[218:219], v246 offset:36960\n"
"ds_read_b64_tr_b16 v[220:221], v246 offset:41568\n"
"v_mfma_f32_16x16x32_bf16 v[80:83], v[234:237], a[160:163], v[80:83]\n"
"s_nop 0\n"
"ds_read_b64_tr_b16 v[222:223], v246 offset:46176\n"
"ds_read_b64_tr_b16 v[224:225], v246 offset:50784\n"
"v_mfma_f32_16x16x32_bf16 v[80:83], v[238:241], a[168:171], v[80:83]\n"
"s_waitcnt lgkmcnt(4)\n"
"v_mfma_f32_16x16x32_bf16 v[12:15], v[210:213], a[124:127], v[12:15]\n"
"s_nop 0\n"
"ds_read_b64_tr_b16 v[226:227], v247 offset:96\n"
"ds_read_b64_tr_b16 v[228:229], v247 offset:4704\n"
"v_mfma_f32_16x16x32_bf16 v[12:15], v[214:217], a[132:135], v[12:15]\n"
"s_nop 0\n"
"ds_read_b64_tr_b16 v[230:231], v247 offset:9312\n"
"ds_read_b64_tr_b16 v[232:233], v247 offset:13920\n"
"v_mfma_f32_16x16x32_bf16 v[84:87], v[210:213], a[128:131], v[84:87]\n"
"v_mfma_f32_16x16x32_bf16 v[84:87], v[214:217], a[136:139], v[84:87]\n"
"s_waitcnt lgkmcnt(4)\n"
"v_mfma_f32_16x16x32_bf16 v[12:15], v[218:221], a[140:143], v[12:15]\n"
"s_nop 0\n"
"ds_read_b64_tr_b16 v[234:235], v246 offset:18560\n"
"ds_read_b64_tr_b16 v[236:237], v246 offset:23168\n"
"v_mfma_f32_16x16x32_bf16 v[12:15], v[222:225], a[148:151], v[12:15]\n"
"s_nop 0\n"
"ds_read_b64_tr_b16 v[238:239], v246 offset:27776\n"
"ds_read_b64_tr_b16 v[240:241], v246 offset:32384\n"
"v_mfma_f32_16x16x32_bf16 v[84:87], v[218:221], a[144:147], v[84:87]\n"
"v_cvt_pk_bf16_f32 v8, v8, v9\n"
"v_cvt_pk_bf16_f32 v9, v10, v11\n"
"v_mfma_f32_16x16x32_bf16 v[84:87], v[222:225], a[152:155], v[84:87]\n"
"s_waitcnt lgkmcnt(4)\n"
"v_cvt_pk_bf16_f32 v10, v80, v81\n"
"v_cvt_pk_bf16_f32 v11, v82, v83\n"
"v_mfma_f32_16x16x32_bf16 v[12:15], v[226:229], a[156:159], v[12:15]\n"
"buffer_store_dwordx4 v[8:11], v196, s[60:63], s67 offen offset:512 sc1\n"
"v_mfma_f32_16x16x32_bf16 v[12:15], v[230:233], a[164:167], v[12:15]\n"
"s_nop 0\n"
"ds_read_b64_tr_b16 v[210:211], v246 offset:36992\n"
"ds_read_b64_tr_b16 v[212:213], v246 offset:41600\n"
"v_mfma_f32_16x16x32_bf16 v[84:87], v[226:229], a[160:163], v[84:87]\n"
"s_nop 0\n"
"ds_read_b64_tr_b16 v[214:215], v246 offset:46208\n"
"ds_read_b64_tr_b16 v[216:217], v246 offset:50816\n"
"v_mfma_f32_16x16x32_bf16 v[84:87], v[230:233], a[168:171], v[84:87]\n"
"s_waitcnt lgkmcnt(4)\n"
"v_mfma_f32_16x16x32_bf16 v[36:39], v[234:237], a[124:127], v[36:39]\n"
"s_nop 0\n"
"ds_read_b64_tr_b16 v[218:219], v247 offset:128\n"
"ds_read_b64_tr_b16 v[220:221], v247 offset:4736\n"
"v_mfma_f32_16x16x32_bf16 v[36:39], v[238:241], a[132:135], v[36:39]\n"
"s_nop 0\n"
"ds_read_b64_tr_b16 v[222:223], v247 offset:9344\n"
"ds_read_b64_tr_b16 v[224:225], v247 offset:13952\n"
"v_mfma_f32_16x16x32_bf16 v[108:111], v[234:237], a[128:131], v[108:111]\n"
"v_mfma_f32_16x16x32_bf16 v[108:111], v[238:241], a[136:139], v[108:111]\n"
"s_waitcnt lgkmcnt(4)\n"
"v_mfma_f32_16x16x32_bf16 v[36:39], v[210:213], a[140:143], v[36:39]\n"
"s_nop 0\n"
"ds_read_b64_tr_b16 v[226:227], v246 offset:18592\n"
"ds_read_b64_tr_b16 v[228:229], v246 offset:23200\n"
"v_mfma_f32_16x16x32_bf16 v[36:39], v[214:217], a[148:151], v[36:39]\n"
"s_nop 0\n"
"ds_read_b64_tr_b16 v[230:231], v246 offset:27808\n"
"ds_read_b64_tr_b16 v[232:233], v246 offset:32416\n"
"v_mfma_f32_16x16x32_bf16 v[108:111], v[210:213], a[144:147], v[108:111]\n"
"v_cvt_pk_bf16_f32 v12, v12, v13\n"
"v_cvt_pk_bf16_f32 v13, v14, v15\n"
"v_mfma_f32_16x16x32_bf16 v[108:111], v[214:217], a[152:155], v[108:111]\n"
"s_waitcnt lgkmcnt(4)\n"
"v_cvt_pk_bf16_f32 v14, v84, v85\n"
"v_cvt_pk_bf16_f32 v15, v86, v87\n"
"v_mfma_f32_16x16x32_bf16 v[36:39], v[218:221], a[156:159], v[36:39]\n"
"buffer_store_dwordx4 v[12:15], v196, s[60:63], s67 offen offset:768 sc1\n"
"v_mfma_f32_16x16x32_bf16 v[36:39], v[222:225], a[164:167], v[36:39]\n"
"s_nop 0\n"
"ds_read_b64_tr_b16 v[234:235], v246 offset:37024\n"
"ds_read_b64_tr_b16 v[236:237], v246 offset:41632\n"
"v_mfma_f32_16x16x32_bf16 v[108:111], v[218:221], a[160:163], v[108:111]\n"
"s_nop 0\n"
"ds_read_b64_tr_b16 v[238:239], v246 offset:46240\n"
"ds_read_b64_tr_b16 v[240:241], v246 offset:50848\n"
"v_mfma_f32_16x16x32_bf16 v[108:111], v[222:225], a[168:171], v[108:111]\n"
"s_waitcnt lgkmcnt(4)\n"
"v_mfma_f32_16x16x32_bf16 v[40:43], v[226:229], a[124:127], v[40:43]\n"
"s_nop 0\n"
"ds_read_b64_tr_b16 v[210:211], v247 offset:160\n"
"ds_read_b64_tr_b16 v[212:213], v247 offset:4768\n"
"v_mfma_f32_16x16x32_bf16 v[40:43], v[230:233], a[132:135], v[40:43]\n"
"s_nop 0\n"
"ds_read_b64_tr_b16 v[214:215], v247 offset:9376\n"
"ds_read_b64_tr_b16 v[216:217], v247 offset:13984\n"
"v_mfma_f32_16x16x32_bf16 v[112:115], v[226:229], a[128:131], v[112:115]\n"
"v_mfma_f32_16x16x32_bf16 v[112:115], v[230:233], a[136:139], v[112:115]\n"
"s_waitcnt lgkmcnt(4)\n"
"v_mfma_f32_16x16x32_bf16 v[40:43], v[234:237], a[140:143], v[40:43]\n"
"s_nop 0\n"
"ds_read_b64_tr_b16 v[218:219], v246 offset:18624\n"
"ds_read_b64_tr_b16 v[220:221], v246 offset:23232\n"
"v_mfma_f32_16x16x32_bf16 v[40:43], v[238:241], a[148:151], v[40:43]\n"
"s_nop 0\n"
"ds_read_b64_tr_b16 v[222:223], v246 offset:27840\n"
"ds_read_b64_tr_b16 v[224:225], v246 offset:32448\n"
"v_mfma_f32_16x16x32_bf16 v[112:115], v[234:237], a[144:147], v[112:115]\n"
"v_cvt_pk_bf16_f32 v36, v36, v37\n"
"v_cvt_pk_bf16_f32 v37, v38, v39\n"
"v_mfma_f32_16x16x32_bf16 v[112:115], v[238:241], a[152:155], v[112:115]\n"
"s_waitcnt lgkmcnt(4)\n"
"v_cvt_pk_bf16_f32 v38, v108, v109\n"
"v_cvt_pk_bf16_f32 v39, v110, v111\n"
"v_mfma_f32_16x16x32_bf16 v[40:43], v[210:213], a[156:159], v[40:43]\n"
"buffer_store_dwordx4 v[36:39], v196, s[60:63], s67 offen offset:1024 sc1\n"
"v_mfma_f32_16x16x32_bf16 v[40:43], v[214:217], a[164:167], v[40:43]\n"
"s_nop 0\n"
"ds_read_b64_tr_b16 v[226:227], v246 offset:37056\n"
"ds_read_b64_tr_b16 v[228:229], v246 offset:41664\n"
"v_mfma_f32_16x16x32_bf16 v[112:115], v[210:213], a[160:163], v[112:115]\n"
"s_nop 0\n"
"ds_read_b64_tr_b16 v[230:231], v246 offset:46272\n"
"ds_read_b64_tr_b16 v[232:233], v246 offset:50880\n"
"v_mfma_f32_16x16x32_bf16 v[112:115], v[214:217], a[168:171], v[112:115]\n"
"s_waitcnt lgkmcnt(4)\n"
"v_mfma_f32_16x16x32_bf16 v[44:47], v[218:221], a[124:127], v[44:47]\n"
"s_nop 0\n"
"ds_read_b64_tr_b16 v[234:235], v247 offset:192\n"
"ds_read_b64_tr_b16 v[236:237], v247 offset:4800\n"
"v_mfma_f32_16x16x32_bf16 v[44:47], v[222:225], a[132:135], v[44:47]\n"
"s_nop 0\n"
"ds_read_b64_tr_b16 v[238:239], v247 offset:9408\n"
"ds_read_b64_tr_b16 v[240:241], v247 offset:14016\n"
"v_mfma_f32_16x16x32_bf16 v[116:119], v[218:221], a[128:131], v[116:119]\n"
"v_mfma_f32_16x16x32_bf16 v[116:119], v[222:225], a[136:139], v[116:119]\n"
"s_waitcnt lgkmcnt(4)\n"
"v_mfma_f32_16x16x32_bf16 v[44:47], v[226:229], a[140:143], v[44:47]\n"
"s_nop 0\n"
"ds_read_b64_tr_b16 v[210:211], v246 offset:18656\n"
"ds_read_b64_tr_b16 v[212:213], v246 offset:23264\n"
"v_mfma_f32_16x16x32_bf16 v[44:47], v[230:233], a[148:151], v[44:47]\n"
"s_nop 0\n"
"ds_read_b64_tr_b16 v[214:215], v246 offset:27872\n"
"ds_read_b64_tr_b16 v[216:217], v246 offset:32480\n"
"v_mfma_f32_16x16x32_bf16 v[116:119], v[226:229], a[144:147], v[116:119]\n"
"v_cvt_pk_bf16_f32 v40, v40, v41\n"
"v_cvt_pk_bf16_f32 v41, v42, v43\n"
"v_mfma_f32_16x16x32_bf16 v[116:119], v[230:233], a[152:155], v[116:119]\n"
"s_waitcnt lgkmcnt(4)\n"
"v_cvt_pk_bf16_f32 v42, v112, v113\n"
"v_cvt_pk_bf16_f32 v43, v114, v115\n"
"v_mfma_f32_16x16x32_bf16 v[44:47], v[234:237], a[156:159], v[44:47]\n"
"buffer_store_dwordx4 v[40:43], v196, s[60:63], s67 offen offset:1280 sc1\n"
"v_mfma_f32_16x16x32_bf16 v[44:47], v[238:241], a[164:167], v[44:47]\n"
"s_nop 0\n"
"ds_read_b64_tr_b16 v[218:219], v246 offset:37088\n"
"ds_read_b64_tr_b16 v[220:221], v246 offset:41696\n"
"v_mfma_f32_16x16x32_bf16 v[116:119], v[234:237], a[160:163], v[116:119]\n"
"s_nop 0\n"
"ds_read_b64_tr_b16 v[222:223], v246 offset:46304\n"
"ds_read_b64_tr_b16 v[224:225], v246 offset:50912\n"
"v_mfma_f32_16x16x32_bf16 v[116:119], v[238:241], a[168:171], v[116:119]\n"
"s_waitcnt lgkmcnt(4)\n"
"v_mfma_f32_16x16x32_bf16 v[48:51], v[210:213], a[124:127], v[48:51]\n"
"s_nop 0\n"
"ds_read_b64_tr_b16 v[226:227], v247 offset:224\n"
"ds_read_b64_tr_b16 v[228:229], v247 offset:4832\n"
"v_mfma_f32_16x16x32_bf16 v[48:51], v[214:217], a[132:135], v[48:51]\n"
"s_nop 0\n"
"ds_read_b64_tr_b16 v[230:231], v247 offset:9440\n"
"ds_read_b64_tr_b16 v[232:233], v247 offset:14048\n"
"v_mfma_f32_16x16x32_bf16 v[120:123], v[210:213], a[128:131], v[120:123]\n"
"v_mfma_f32_16x16x32_bf16 v[120:123], v[214:217], a[136:139], v[120:123]\n"
"s_waitcnt lgkmcnt(4)\n"
"v_mfma_f32_16x16x32_bf16 v[48:51], v[218:221], a[140:143], v[48:51]\n"
"s_nop 0\n"
"ds_read_b64_tr_b16 v[234:235], v249 offset:18432\n"
"ds_read_b64_tr_b16 v[236:237], v249 offset:23040\n"
"v_mfma_f32_16x16x32_bf16 v[48:51], v[222:225], a[148:151], v[48:51]\n"
"s_nop 0\n"
"ds_read_b64_tr_b16 v[238:239], v249 offset:27648\n"
"ds_read_b64_tr_b16 v[240:241], v249 offset:32256\n"
"v_mfma_f32_16x16x32_bf16 v[120:123], v[218:221], a[144:147], v[120:123]\n"
"v_cvt_pk_bf16_f32 v44, v44, v45\n"
"v_cvt_pk_bf16_f32 v45, v46, v47\n"
"v_mfma_f32_16x16x32_bf16 v[120:123], v[222:225], a[152:155], v[120:123]\n"
"s_waitcnt lgkmcnt(4)\n"
"v_cvt_pk_bf16_f32 v46, v116, v117\n"
"v_cvt_pk_bf16_f32 v47, v118, v119\n"
"v_mfma_f32_16x16x32_bf16 v[48:51], v[226:229], a[156:159], v[48:51]\n"
"buffer_store_dwordx4 v[44:47], v196, s[60:63], s67 offen offset:1536 sc1\n"
"v_mfma_f32_16x16x32_bf16 v[48:51], v[230:233], a[164:167], v[48:51]\n"
"s_nop 0\n"
"ds_read_b64_tr_b16 v[210:211], v249 offset:36864\n"
"ds_read_b64_tr_b16 v[212:213], v249 offset:41472\n"
"v_mfma_f32_16x16x32_bf16 v[120:123], v[226:229], a[160:163], v[120:123]\n"
"s_nop 0\n"
"ds_read_b64_tr_b16 v[214:215], v249 offset:46080\n"
"ds_read_b64_tr_b16 v[216:217], v249 offset:50688\n"
"v_mfma_f32_16x16x32_bf16 v[120:123], v[230:233], a[168:171], v[120:123]\n"
"s_waitcnt lgkmcnt(4)\n"
"v_mfma_f32_16x16x32_bf16 v[144:147], v[234:237], a[124:127], v[144:147]\n"
"s_nop 0\n"
"ds_read_b64_tr_b16 v[218:219], v250 offset:0\n"
"ds_read_b64_tr_b16 v[220:221], v250 offset:4608\n"
"v_mfma_f32_16x16x32_bf16 v[144:147], v[238:241], a[132:135], v[144:147]\n"
"s_nop 0\n"
"ds_read_b64_tr_b16 v[222:223], v250 offset:9216\n"
"ds_read_b64_tr_b16 v[224:225], v250 offset:13824\n"
"v_mfma_f32_16x16x32_bf16 v[148:151], v[234:237], a[128:131], v[148:151]\n"
"v_mfma_f32_16x16x32_bf16 v[148:151], v[238:241], a[136:139], v[148:151]\n"
"s_waitcnt lgkmcnt(4)\n"
"v_mfma_f32_16x16x32_bf16 v[144:147], v[210:213], a[140:143], v[144:147]\n"
"v_cvt_pk_bf16_f32 v48, v48, v49\n"
"v_cvt_pk_bf16_f32 v49, v50, v51\n"
"v_mfma_f32_16x16x32_bf16 v[144:147], v[214:217], a[148:151], v[144:147]\n"
"v_cvt_pk_bf16_f32 v50, v120, v121\n"
"v_cvt_pk_bf16_f32 v51, v122, v123\n"
"v_mfma_f32_16x16x32_bf16 v[148:151], v[210:213], a[144:147], v[148:151]\n"
"buffer_store_dwordx4 v[48:51], v196, s[60:63], s67 offen offset:1792 sc1\n"
"v_mfma_f32_16x16x32_bf16 v[148:151], v[214:217], a[152:155], v[148:151]\n"
"s_waitcnt lgkmcnt(0)\n"
"v_mfma_f32_16x16x32_bf16 v[144:147], v[218:221], a[156:159], v[144:147]\n"
"v_mfma_f32_16x16x32_bf16 v[144:147], v[222:225], a[164:167], v[144:147]\n"
"v_mfma_f32_16x16x32_bf16 v[148:151], v[218:221], a[160:163], v[148:151]\n"
"v_mfma_f32_16x16x32_bf16 v[148:151], v[222:225], a[168:171], v[148:151]\n"
"s_nop 7\n"
"v_cvt_pk_bf16_f32 v144, v144, v145\n"
"v_cvt_pk_bf16_f32 v145, v146, v147\n"
"v_cvt_pk_bf16_f32 v146, v148, v149\n"
"v_cvt_pk_bf16_f32 v147, v150, v151\n"
"buffer_store_dwordx4 v[144:147], v196, s[60:63], s67 offen offset:2048 sc1\n"
"s_lshl2_add_u32 s74, s94, s68\n"
"s_lshl_b32 s72, s74, 8\n"
"s_add_u32 s72, s72, 409600\n"
"v_mov_b32 v200, 1\n"
"s_waitcnt vmcnt(0)\n"
"s_mov_b64 exec, 1\n"
"buffer_atomic_add v200, off, s[60:63], s72\n"
"s_mov_b64 exec, -1\n"
"s_add_u32 s74, s69, 150016\n"
"v_add_u32 v203, s74, v243\n"
"ds_read_b128 v[60:63], v203 offset:6912\n"
"ds_read_b128 v[64:67], v203 offset:7936\n"
"ds_read_b128 v[68:71], v203 offset:8960\n"
"s_waitcnt lgkmcnt(0)\n"
"v_mul_f32 v60, 0x3f317218, v60\n"
"v_mul_f32 v61, 0x3f317218, v61\n"
"v_mul_f32 v62, 0x3f317218, v62\n"
"v_mul_f32 v63, 0x3f317218, v63\n"
"L_tp2_3_1:\n"
"s_cbranch_execz L_tb2_3\n"
"v_mul_f32 v64, 0x3f317218, v64\n"
"v_mul_f32 v65, 0x3f317218, v65\n"
"v_mul_f32 v66, 0x3f317218, v66\n"
"v_mul_f32 v67, 0x3f317218, v67\n"
"v_mul_f32 v68, 0x3f317218, v68\n"
"v_mul_f32 v69, 0x3f317218, v69\n"
"v_mul_f32 v70, 0x3f317218, v70\n"
"v_mul_f32 v71, 0x3f317218, v71\n"
"v_mov_b32 v142, 0x3f80\n"
"v_mov_b32 v143, 0x3f800000\n"
"v_accvgpr_read_b32 v48, a0\n"
"v_accvgpr_read_b32 v49, a1\n"
"v_accvgpr_read_b32 v50, a2\n"
"v_accvgpr_read_b32 v51, a3\n"
"v_accvgpr_read_b32 v52, a4\n"
"v_accvgpr_read_b32 v53, a5\n"
"v_accvgpr_read_b32 v54, a6\n"
"v_accvgpr_read_b32 v55, a7\n"
"v_accvgpr_read_b32 v56, a8\n"
"v_accvgpr_read_b32 v57, a9\n"
"v_accvgpr_read_b32 v58, a10\n"
"v_accvgpr_read_b32 v59, a11\n"
"v_lshlrev_b32 v72, 16, v48\n"
"v_and_b32 v73, 0xffff0000, v48\n"
"v_lshlrev_b32 v74, 16, v49\n"
"v_and_b32 v75, 0xffff0000, v49\n"
"v_lshlrev_b32 v76, 16, v50\n"
"v_and_b32 v77, 0xffff0000, v50\n"
"v_lshlrev_b32 v78, 16, v51\n"
"v_and_b32 v79, 0xffff0000, v51\n"
"v_lshlrev_b32 v80, 16, v52\n"
"v_and_b32 v81, 0xffff0000, v52\n"
"v_lshlrev_b32 v82, 16, v53\n"
"v_and_b32 v83, 0xffff0000, v53\n"
"v_lshlrev_b32 v84, 16, v54\n"
"v_and_b32 v85, 0xffff0000, v54\n"
"v_lshlrev_b32 v86, 16, v55\n"
"v_and_b32 v87, 0xffff0000, v55\n"
"v_lshlrev_b32 v88, 16, v56\n"
"v_and_b32 v89, 0xffff0000, v56\n"
"v_lshlrev_b32 v90, 16, v57\n"
"v_and_b32 v91, 0xffff0000, v57\n"
"v_lshlrev_b32 v92, 16, v58\n"
"v_and_b32 v93, 0xffff0000, v58\n"
"v_lshlrev_b32 v94, 16, v59\n"
"v_and_b32 v95, 0xffff0000, v59\n"
"s_lshl2_add_u32 s74, s94, %11\n"
"s_cmp_eq_u32 s68, 0\n"
"s_cbranch_scc0 L_po_w027\n"
"s_lshl_b32 s72, s74, 8\n"
"s_add_u32 s72, s72, 409600\n"
"s_mov_b32 s97, 20000\n"
"L_po_poll28:\n"
"buffer_load_dword v208, off, s[60:63], s72 sc1\n"
"s_waitcnt vmcnt(0)\n"
"v_readfirstlane_b32 s73, v208\n"
"s_nop 1\n"
"s_cmp_ge_u32 s73, 16\n"
"s_cbranch_scc1 L_po_polld29\n"
"s_sleep 2\n"
"s_sub_u32 s97, s97, 1\n"
"s_cbranch_scc0 L_po_poll28\n"
"L_po_polld29:\n"
"L_po_w027:\n"
"s_barrier\n"
"s_lshl_b32 s72, s68, 10\n"
"s_add_u32 s72, s72, s90\n"
"s_add_u32 s72, s72, 100663296\n"
"s_add_u32 s73, s72, 0\n"
"buffer_load_dwordx4 v[0:3], v186, s[60:63], s73 offen sc1\n"
"s_add_u32 s73, s72, 9216\n"
"buffer_load_dwordx4 v[4:7], v186, s[60:63], s73 offen sc1\n"
"s_add_u32 s73, s72, 18432\n"
"buffer_load_dwordx4 v[8:11], v186, s[60:63], s73 offen sc1\n"
"s_add_u32 s73, s72, 27648\n"
"buffer_load_dwordx4 v[12:15], v186, s[60:63], s73 offen sc1\n"
"s_add_u32 s73, s72, 4096\n"
"buffer_load_dwordx4 v[16:19], v186, s[60:63], s73 offen sc1\n"
"s_add_u32 s73, s72, 13312\n"
"buffer_load_dwordx4 v[20:23], v186, s[60:63], s73 offen sc1\n"
"s_add_u32 s73, s72, 22528\n"
"buffer_load_dwordx4 v[24:27], v186, s[60:63], s73 offen sc1\n"
"s_add_u32 s73, s72, 31744\n"
"buffer_load_dwordx4 v[28:31], v186, s[60:63], s73 offen sc1\n"
"s_cmp_eq_u32 s68, 0\n"
"s_cbranch_scc0 L_po_w030\n"
"s_add_u32 s73, s72, 8192\n"
"buffer_load_dwordx4 v[32:35], v186, s[60:63], s73 offen sc1\n"
"s_add_u32 s73, s72, 17408\n"
"buffer_load_dwordx4 v[36:39], v186, s[60:63], s73 offen sc1\n"
"s_add_u32 s73, s72, 26624\n"
"buffer_load_dwordx4 v[40:43], v186, s[60:63], s73 offen sc1\n"
"s_add_u32 s73, s72, 35840\n"
"buffer_load_dwordx4 v[44:47], v186, s[60:63], s73 offen sc1\n"
"L_po_w030:\n"
"s_waitcnt vmcnt(4)\n"
"v_dot2_f32_bf16 v96, v0, v142, 0\n"
"v_dot2_f32_bf16 v97, v0, v143, 0\n"
"v_dot2_f32_bf16 v98, v1, v142, 0\n"
"v_dot2_f32_bf16 v99, v1, v143, 0\n"
"v_dot2_f32_bf16 v100, v2, v142, 0\n"
"v_dot2_f32_bf16 v101, v2, v143, 0\n"
"v_dot2_f32_bf16 v102, v3, v142, 0\n"
"v_dot2_f32_bf16 v103, v3, v143, 0\n"
"v_dot2_f32_bf16 v96, v4, v142, v96\n"
"v_dot2_f32_bf16 v97, v4, v143, v97\n"
"v_dot2_f32_bf16 v98, v5, v142, v98\n"
"v_dot2_f32_bf16 v99, v5, v143, v99\n"
"v_dot2_f32_bf16 v100, v6, v142, v100\n"
"v_dot2_f32_bf16 v101, v6, v143, v101\n"
"v_dot2_f32_bf16 v102, v7, v142, v102\n"
"v_dot2_f32_bf16 v103, v7, v143, v103\n"
"v_dot2_f32_bf16 v96, v8, v142, v96\n"
"v_dot2_f32_bf16 v97, v8, v143, v97\n"
"v_dot2_f32_bf16 v98, v9, v142, v98\n"
"v_dot2_f32_bf16 v99, v9, v143, v99\n"
"v_dot2_f32_bf16 v100, v10, v142, v100\n"
"v_dot2_f32_bf16 v101, v10, v143, v101\n"
"v_dot2_f32_bf16 v102, v11, v142, v102\n"
"v_dot2_f32_bf16 v103, v11, v143, v103\n"
"v_dot2_f32_bf16 v96, v12, v142, v96\n"
"v_dot2_f32_bf16 v97, v12, v143, v97\n"
"v_dot2_f32_bf16 v98, v13, v142, v98\n"
"v_dot2_f32_bf16 v99, v13, v143, v99\n"
"v_dot2_f32_bf16 v100, v14, v142, v100\n"
"v_dot2_f32_bf16 v101, v14, v143, v101\n"
"v_dot2_f32_bf16 v102, v15, v142, v102\n"
"v_dot2_f32_bf16 v103, v15, v143, v103\n"
"s_nop 2\n"
"v_pk_fma_f32 v[96:97], v[96:97], v[60:61], v[72:73]\n"
"v_pk_fma_f32 v[98:99], v[98:99], v[62:63], v[74:75]\n"
"v_cvt_pk_bf16_f32 v48, v96, v97\n"
"v_cvt_pk_bf16_f32 v49, v98, v99\n"
"s_add_u32 s72, s93, 0\n"
"buffer_store_dwordx2 v[48:49], v244, s[56:59], s72 offen offset:0 sc1\n"
"v_pk_fma_f32 v[100:101], v[100:101], v[60:61], v[76:77]\n"
"v_pk_fma_f32 v[102:103], v[102:103], v[62:63], v[78:79]\n"
"v_cvt_pk_bf16_f32 v50, v100, v101\n"
"v_cvt_pk_bf16_f32 v51, v102, v103\n"
"s_add_u32 s72, s93, 18432\n"
"buffer_store_dwordx2 v[50:51], v244, s[56:59], s72 offen offset:0 sc1\n"
"s_waitcnt vmcnt(2)\n"
"v_dot2_f32_bf16 v104, v16, v142, 0\n"
"v_dot2_f32_bf16 v105, v16, v143, 0\n"
"v_dot2_f32_bf16 v106, v17, v142, 0\n"
"v_dot2_f32_bf16 v107, v17, v143, 0\n"
"v_dot2_f32_bf16 v108, v18, v142, 0\n"
"v_dot2_f32_bf16 v109, v18, v143, 0\n"
"v_dot2_f32_bf16 v110, v19, v142, 0\n"
"v_dot2_f32_bf16 v111, v19, v143, 0\n"
"v_dot2_f32_bf16 v104, v20, v142, v104\n"
"v_dot2_f32_bf16 v105, v20, v143, v105\n"
"v_dot2_f32_bf16 v106, v21, v142, v106\n"
"v_dot2_f32_bf16 v107, v21, v143, v107\n"
"v_dot2_f32_bf16 v108, v22, v142, v108\n"
"v_dot2_f32_bf16 v109, v22, v143, v109\n"
"v_dot2_f32_bf16 v110, v23, v142, v110\n"
"v_dot2_f32_bf16 v111, v23, v143, v111\n"
"v_dot2_f32_bf16 v104, v24, v142, v104\n"
"v_dot2_f32_bf16 v105, v24, v143, v105\n"
"v_dot2_f32_bf16 v106, v25, v142, v106\n"
"v_dot2_f32_bf16 v107, v25, v143, v107\n"
"v_dot2_f32_bf16 v108, v26, v142, v108\n"
"v_dot2_f32_bf16 v109, v26, v143, v109\n"
"v_dot2_f32_bf16 v110, v27, v142, v110\n"
"v_dot2_f32_bf16 v111, v27, v143, v111\n"
"v_dot2_f32_bf16 v104, v28, v142, v104\n"
"v_dot2_f32_bf16 v105, v28, v143, v105\n"
"v_dot2_f32_bf16 v106, v29, v142, v106\n"
"v_dot2_f32_bf16 v107, v29, v143, v107\n"
"v_dot2_f32_bf16 v108, v30, v142, v108\n"
"v_dot2_f32_bf16 v109, v30, v143, v109\n"
"v_dot2_f32_bf16 v110, v31, v142, v110\n"
"v_dot2_f32_bf16 v111, v31, v143, v111\n"
"s_nop 2\n"
"v_pk_fma_f32 v[104:105], v[104:105], v[64:65], v[80:81]\n"
"v_pk_fma_f32 v[106:107], v[106:107], v[66:67], v[82:83]\n"
"v_cvt_pk_bf16_f32 v52, v104, v105\n"
"v_cvt_pk_bf16_f32 v53, v106, v107\n"
"s_add_u32 s72, s93, 0\n"
"buffer_store_dwordx2 v[52:53], v244, s[56:59], s72 offen offset:512 sc1\n"
"v_pk_fma_f32 v[108:109], v[108:109], v[64:65], v[84:85]\n"
"v_pk_fma_f32 v[110:111], v[110:111], v[66:67], v[86:87]\n"
"v_cvt_pk_bf16_f32 v54, v108, v109\n"
"v_cvt_pk_bf16_f32 v55, v110, v111\n"
"s_add_u32 s72, s93, 18432\n"
"buffer_store_dwordx2 v[54:55], v244, s[56:59], s72 offen offset:512 sc1\n"
"s_cmp_eq_u32 s68, 0\n"
"s_cbranch_scc0 L_po_w031\n"
"v_dot2_f32_bf16 v112, v32, v142, 0\n"
"v_dot2_f32_bf16 v113, v32, v143, 0\n"
"v_dot2_f32_bf16 v114, v33, v142, 0\n"
"v_dot2_f32_bf16 v115, v33, v143, 0\n"
"v_dot2_f32_bf16 v116, v34, v142, 0\n"
"v_dot2_f32_bf16 v117, v34, v143, 0\n"
"v_dot2_f32_bf16 v118, v35, v142, 0\n"
"v_dot2_f32_bf16 v119, v35, v143, 0\n"
"v_dot2_f32_bf16 v112, v36, v142, v112\n"
"v_dot2_f32_bf16 v113, v36, v143, v113\n"
"v_dot2_f32_bf16 v114, v37, v142, v114\n"
"v_dot2_f32_bf16 v115, v37, v143, v115\n"
"v_dot2_f32_bf16 v116, v38, v142, v116\n"
"v_dot2_f32_bf16 v117, v38, v143, v117\n"
"v_dot2_f32_bf16 v118, v39, v142, v118\n"
"v_dot2_f32_bf16 v119, v39, v143, v119\n"
"v_dot2_f32_bf16 v112, v40, v142, v112\n"
"v_dot2_f32_bf16 v113, v40, v143, v113\n"
"v_dot2_f32_bf16 v114, v41, v142, v114\n"
"v_dot2_f32_bf16 v115, v41, v143, v115\n"
"v_dot2_f32_bf16 v116, v42, v142, v116\n"
"v_dot2_f32_bf16 v117, v42, v143, v117\n"
"v_dot2_f32_bf16 v118, v43, v142, v118\n"
"v_dot2_f32_bf16 v119, v43, v143, v119\n"
"v_dot2_f32_bf16 v112, v44, v142, v112\n"
"v_dot2_f32_bf16 v113, v44, v143, v113\n"
"v_dot2_f32_bf16 v114, v45, v142, v114\n"
"v_dot2_f32_bf16 v115, v45, v143, v115\n"
"v_dot2_f32_bf16 v116, v46, v142, v116\n"
"v_dot2_f32_bf16 v117, v46, v143, v117\n"
"v_dot2_f32_bf16 v118, v47, v142, v118\n"
"v_dot2_f32_bf16 v119, v47, v143, v119\n"
"s_nop 2\n"
"v_pk_fma_f32 v[112:113], v[112:113], v[68:69], v[88:89]\n"
"v_pk_fma_f32 v[114:115], v[114:115], v[70:71], v[90:91]\n"
"v_cvt_pk_bf16_f32 v56, v112, v113\n"
"v_cvt_pk_bf16_f32 v57, v114, v115\n"
"s_add_u32 s72, s93, 0\n"
"buffer_store_dwordx2 v[56:57], v244, s[56:59], s72 offen offset:1024 sc1\n"
"v_pk_fma_f32 v[116:117], v[116:117], v[68:69], v[92:93]\n"
"v_pk_fma_f32 v[118:119], v[118:119], v[70:71], v[94:95]\n"
"v_cvt_pk_bf16_f32 v58, v116, v117\n"
"v_cvt_pk_bf16_f32 v59, v118, v119\n"
"s_add_u32 s72, s93, 18432\n"
"buffer_store_dwordx2 v[58:59], v244, s[56:59], s72 offen offset:1024 sc1\n"
"L_po_w031:\n"
"s_branch L_at_end\n"
"L_at_slow:\n"
"s_waitcnt vmcnt(0) lgkmcnt(0)\n"
"v_lshrrev_b32 v239, 4, v230\n"
"v_and_b32 v237, 15, v239\n"
"v_lshlrev_b32 v237, 8, v237\n"
"v_lshrrev_b32 v240, 4, v239\n"
"v_lshl_add_u32 v237, v240, 5, v237\n"
"s_lshl_b32 s79, s68, 12\n"
"s_add_u32 s79, s79, s69\n"
"s_add_u32 s79, s79, 147456\n"
"v_lshrrev_b32 v238, 2, v239\n"
"v_lshlrev_b32 v238, 8, v238\n"
"v_and_b32 v240, 3, v239\n"
"v_lshl_add_u32 v238, v240, 3, v238\n"
"v_add_u32 v238, s79, v238\n"
"v_mov_b32 v224, 0xff800000\n"
"v_mov_b32 v225, 0xff800000\n"
"v_mov_b32 v226, 0xff800000\n"
"v_mov_b32 v227, 0xff800000\n"
"s_mov_b32 s78, 0\n"
"s_mov_b32 s80, 63\n"
"L_at_s1:\n"
"s_add_u32 s72, s78, 4096\n"
"buffer_load_dwordx4 a[64:67], v237, s[44:47], s78 offen offset:0\n"
"buffer_load_dwordx4 a[68:71], v237, s[44:47], s78 offen offset:16\n"
"buffer_load_dwordx4 a[72:75], v237, s[44:47], s78 offen offset:128\n"
"buffer_load_dwordx4 a[76:79], v237, s[44:47], s78 offen offset:144\n"
"buffer_load_dwordx4 a[80:83], v237, s[44:47], s72 offen offset:0\n"
"buffer_load_dwordx4 a[84:87], v237, s[44:47], s72 offen offset:16\n"
"buffer_load_dwordx4 a[88:91], v237, s[44:47], s72 offen offset:128\n"
"buffer_load_dwordx4 a[92:95], v237, s[44:47], s72 offen offset:144\n"
"s_waitcnt vmcnt(0)\n"
"v_mfma_f32_16x16x32_bf16 v[128:131], a[64:67], a[192:195], 0\n"
"v_mfma_f32_16x16x32_bf16 v[128:131], a[68:71], a[196:199], v[128:131]\n"
"v_mfma_f32_16x16x32_bf16 v[128:131], a[72:75], a[200:203], v[128:131]\n"
"v_mfma_f32_16x16x32_bf16 v[128:131], a[76:79], a[204:207], v[128:131]\n"
"v_mfma_f32_16x16x32_bf16 v[132:135], a[80:83], a[192:195], 0\n"
"v_mfma_f32_16x16x32_bf16 v[132:135], a[84:87], a[196:199], v[132:135]\n"
"v_mfma_f32_16x16x32_bf16 v[132:135], a[88:91], a[200:203], v[132:135]\n"
"v_mfma_f32_16x16x32_bf16 v[132:135], a[92:95], a[204:207], v[132:135]\n"
"v_mfma_f32_16x16x32_bf16 v[136:139], a[64:67], a[208:211], 0\n"
"v_mfma_f32_16x16x32_bf16 v[136:139], a[68:71], a[212:215], v[136:139]\n"
"v_mfma_f32_16x16x32_bf16 v[136:139], a[72:75], a[216:219], v[136:139]\n"
"v_mfma_f32_16x16x32_bf16 v[136:139], a[76:79], a[220:223], v[136:139]\n"
"v_mfma_f32_16x16x32_bf16 v[140:143], a[80:83], a[208:211], 0\n"
"v_mfma_f32_16x16x32_bf16 v[140:143], a[84:87], a[212:215], v[140:143]\n"
"v_mfma_f32_16x16x32_bf16 v[140:143], a[88:91], a[216:219], v[140:143]\n"
"v_mfma_f32_16x16x32_bf16 v[140:143], a[92:95], a[220:223], v[140:143]\n"
"v_mfma_f32_16x16x32_bf16 v[144:147], a[64:67], a[224:227], 0\n"
"v_mfma_f32_16x16x32_bf16 v[144:147], a[68:71], a[228:231], v[144:147]\n"
"v_mfma_f32_16x16x32_bf16 v[144:147], a[72:75], a[232:235], v[144:147]\n"
"v_mfma_f32_16x16x32_bf16 v[144:147], a[76:79], a[236:239], v[144:147]\n"
"v_mfma_f32_16x16x32_bf16 v[148:151], a[80:83], a[224:227], 0\n"
"v_mfma_f32_16x16x32_bf16 v[148:151], a[84:87], a[228:231], v[148:151]\n"
"v_mfma_f32_16x16x32_bf16 v[148:151], a[88:91], a[232:235], v[148:151]\n"
"v_mfma_f32_16x16x32_bf16 v[148:151], a[92:95], a[236:239], v[148:151]\n"
"v_mfma_f32_16x16x32_bf16 v[152:155], a[64:67], a[240:243], 0\n"
"v_mfma_f32_16x16x32_bf16 v[152:155], a[68:71], a[244:247], v[152:155]\n"
"v_mfma_f32_16x16x32_bf16 v[152:155], a[72:75], a[248:251], v[152:155]\n"
"v_mfma_f32_16x16x32_bf16 v[152:155], a[76:79], a[252:255], v[152:155]\n"
"v_mfma_f32_16x16x32_bf16 v[156:159], a[80:83], a[240:243], 0\n"
"v_mfma_f32_16x16x32_bf16 v[156:159], a[84:87], a[244:247], v[156:159]\n"
"v_mfma_f32_16x16x32_bf16 v[156:159], a[88:91], a[248:251], v[156:159]\n"
"v_mfma_f32_16x16x32_bf16 v[156:159], a[92:95], a[252:255], v[156:159]\n"
"s_nop 7\n"
"v_max3_f32 v224, v224, v128, v129\n"
"v_max3_f32 v224, v224, v130, v131\n"
"v_max3_f32 v224, v224, v132, v133\n"
"v_max3_f32 v224, v224, v134, v135\n"
"v_max3_f32 v225, v225, v136, v137\n"
"v_max3_f32 v225, v225, v138, v139\n"
"v_max3_f32 v225, v225, v140, v141\n"
"v_max3_f32 v225, v225, v142, v143\n"
"v_max3_f32 v226, v226, v144, v145\n"
"v_max3_f32 v226, v226, v146, v147\n"
"v_max3_f32 v226, v226, v148, v149\n"
"v_max3_f32 v226, v226, v150, v151\n"
"v_max3_f32 v227, v227, v152, v153\n"
"v_max3_f32 v227, v227, v154, v155\n"
"v_max3_f32 v227, v227, v156, v157\n"
"v_max3_f32 v227, v227, v158, v159\n"
"s_add_u32 s78, s78, 8192\n"
"s_sub_u32 s80, s80, 1\n"
"s_cbranch_scc0 L_at_s1\n"
"v_mov_b32 v210, v224\n"
"v_mov_b32 v214, v225\n"
"v_mov_b32 v218, v226\n"
"v_mov_b32 v222, v227\n"
"s_nop 1\n"
"v_permlane16_swap_b32 v210, v224\n"
"v_permlane16_swap_b32 v214, v225\n"
"v_permlane16_swap_b32 v218, v226\n"
"v_permlane16_swap_b32 v222, v227\n"
"s_nop 1\n"
"v_max_f32 v224, v224, v210\n"
"v_max_f32 v225, v225, v214\n"
"v_max_f32 v226, v226, v218\n"
"v_max_f32 v227, v227, v222\n"
"v_mov_b32 v210, v224\n"
"v_mov_b32 v214, v225\n"
"v_mov_b32 v218, v226\n"
"v_mov_b32 v222, v227\n"
"s_nop 1\n"
"v_permlane32_swap_b32 v210, v224\n"
"v_permlane32_swap_b32 v214, v225\n"
"v_permlane32_swap_b32 v218, v226\n"
"v_permlane32_swap_b32 v222, v227\n"
"s_nop 1\n"
"v_max_f32 v224, v224, v210\n"
"v_max_f32 v225, v225, v214\n"
"v_max_f32 v226, v226, v218\n"
"v_max_f32 v227, v227, v222\n"
"v_sub_f32 v192, 0, v224\n"
"v_sub_f32 v193, 0, v224\n"
"v_sub_f32 v194, 0, v224\n"
"v_sub_f32 v195, 0, v224\n"
"v_mov_b32 v208, 0\n"
"v_mov_b32 v209, 0\n"
"v_mov_b32 v210, 0\n"
"v_mov_b32 v211, 0\n"
"v_sub_f32 v196, 0, v225\n"
"v_sub_f32 v197, 0, v225\n"
"v_sub_f32 v198, 0, v225\n"
"v_sub_f32 v199, 0, v225\n"
"v_mov_b32 v212, 0\n"
"v_mov_b32 v213, 0\n"
"v_mov_b32 v214, 0\n"
"v_mov_b32 v215, 0\n"
"v_sub_f32 v200, 0, v226\n"
"v_sub_f32 v201, 0, v226\n"
"v_sub_f32 v202, 0, v226\n"
"v_sub_f32 v203, 0, v226\n"
"v_mov_b32 v216, 0\n"
"v_mov_b32 v217, 0\n"
"v_mov_b32 v218, 0\n"
"v_mov_b32 v219, 0\n"
"v_sub_f32 v204, 0, v227\n"
"v_sub_f32 v205, 0, v227\n"
"v_sub_f32 v206, 0, v227\n"
"v_sub_f32 v207, 0, v227\n"
"v_mov_b32 v220, 0\n"
"v_mov_b32 v221, 0\n"
"v_mov_b32 v222, 0\n"
"v_mov_b32 v223, 0\n"
"v_mov_b32 v0, 0\n"
"v_mov_b32 v1, 0\n"
"v_mov_b32 v2, 0\n"
"v_mov_b32 v3, 0\n"
"v_mov_b32 v4, 0\n"
"v_mov_b32 v5, 0\n"
"v_mov_b32 v6, 0\n"
"v_mov_b32 v7, 0\n"
"v_mov_b32 v8, 0\n"
"v_mov_b32 v9, 0\n"
"v_mov_b32 v10, 0\n"
"v_mov_b32 v11, 0\n"
"v_mov_b32 v12, 0\n"
"v_mov_b32 v13, 0\n"
"v_mov_b32 v14, 0\n"
"v_mov_b32 v15, 0\n"
"v_mov_b32 v16, 0\n"
"v_mov_b32 v17, 0\n"
"v_mov_b32 v18, 0\n"
"v_mov_b32 v19, 0\n"
"v_mov_b32 v20, 0\n"
"v_mov_b32 v21, 0\n"
"v_mov_b32 v22, 0\n"
"v_mov_b32 v23, 0\n"
"v_mov_b32 v24, 0\n"
"v_mov_b32 v25, 0\n"
"v_mov_b32 v26, 0\n"
"v_mov_b32 v27, 0\n"
"v_mov_b32 v28, 0\n"
"v_mov_b32 v29, 0\n"
"v_mov_b32 v30, 0\n"
"v_mov_b32 v31, 0\n"
"v_mov_b32 v32, 0\n"
"v_mov_b32 v33, 0\n"
"v_mov_b32 v34, 0\n"
"v_mov_b32 v35, 0\n"
"v_mov_b32 v36, 0\n"
"v_mov_b32 v37, 0\n"
"v_mov_b32 v38, 0\n"
"v_mov_b32 v39, 0\n"
"v_mov_b32 v40, 0\n"
"v_mov_b32 v41, 0\n"
"v_mov_b32 v42, 0\n"
"v_mov_b32 v43, 0\n"
"v_mov_b32 v44, 0\n"
"v_mov_b32 v45, 0\n"
"v_mov_b32 v46, 0\n"
"v_mov_b32 v47, 0\n"
"v_mov_b32 v48, 0\n"
"v_mov_b32 v49, 0\n"
"v_mov_b32 v50, 0\n"
"v_mov_b32 v51, 0\n"
"v_mov_b32 v52, 0\n"
"v_mov_b32 v53, 0\n"
"v_mov_b32 v54, 0\n"
"v_mov_b32 v55, 0\n"
"v_mov_b32 v56, 0\n"
"v_mov_b32 v57, 0\n"
"v_mov_b32 v58, 0\n"
"v_mov_b32 v59, 0\n"
"v_mov_b32 v60, 0\n"
"v_mov_b32 v61, 0\n"
"v_mov_b32 v62, 0\n"
"v_mov_b32 v63, 0\n"
"v_mov_b32 v64, 0\n"
"v_mov_b32 v65, 0\n"
"v_mov_b32 v66, 0\n"
"v_mov_b32 v67, 0\n"
"v_mov_b32 v68, 0\n"
"v_mov_b32 v69, 0\n"
"v_mov_b32 v70, 0\n"
"v_mov_b32 v71, 0\n"
"v_mov_b32 v72, 0\n"
"v_mov_b32 v73, 0\n"
"v_mov_b32 v74, 0\n"
"v_mov_b32 v75, 0\n"
"v_mov_b32 v76, 0\n"
"v_mov_b32 v77, 0\n"
"v_mov_b32 v78, 0\n"
"v_mov_b32 v79, 0\n"
"v_mov_b32 v80, 0\n"
"v_mov_b32 v81, 0\n"
"v_mov_b32 v82, 0\n"
"v_mov_b32 v83, 0\n"
"v_mov_b32 v84, 0\n"
"v_mov_b32 v85, 0\n"
"v_mov_b32 v86, 0\n"
"v_mov_b32 v87, 0\n"
"v_mov_b32 v88, 0\n"
"v_mov_b32 v89, 0\n"
"v_mov_b32 v90, 0\n"
"v_mov_b32 v91, 0\n"
"v_mov_b32 v92, 0\n"
"v_mov_b32 v93, 0\n"
"v_mov_b32 v94, 0\n"
"v_mov_b32 v95, 0\n"
"v_mov_b32 v96, 0\n"
"v_mov_b32 v97, 0\n"
"v_mov_b32 v98, 0\n"
"v_mov_b32 v99, 0\n"
"v_mov_b32 v100, 0\n"
"v_mov_b32 v101, 0\n"
"v_mov_b32 v102, 0\n"
"v_mov_b32 v103, 0\n"
"v_mov_b32 v104, 0\n"
"v_mov_b32 v105, 0\n"
"v_mov_b32 v106, 0\n"
"v_mov_b32 v107, 0\n"
"v_mov_b32 v108, 0\n"
"v_mov_b32 v109, 0\n"
"v_mov_b32 v110, 0\n"
"v_mov_b32 v111, 0\n"
"v_mov_b32 v112, 0\n"
"v_mov_b32 v113, 0\n"
"v_mov_b32 v114, 0\n"
"v_mov_b32 v115, 0\n"
"v_mov_b32 v116, 0\n"
"v_mov_b32 v117, 0\n"
"v_mov_b32 v118, 0\n"
"v_mov_b32 v119, 0\n"
"v_mov_b32 v120, 0\n"
"v_mov_b32 v121, 0\n"
"v_mov_b32 v122, 0\n"
"v_mov_b32 v123, 0\n"
"v_mov_b32 v124, 0\n"
"v_mov_b32 v125, 0\n"
"v_mov_b32 v126, 0\n"
"v_mov_b32 v127, 0\n"
"s_mov_b32 s78, 0\n"
"s_mov_b32 s80, 63\n"
"L_at_s2:\n"
"s_mov_b32 m0, s79\n"
"s_add_u32 s72, s78, 4096\n"
"buffer_load_dwordx4 a[64:67], v237, s[44:47], s78 offen offset:0\n"
"buffer_load_dwordx4 a[68:71], v237, s[44:47], s78 offen offset:16\n"
"buffer_load_dwordx4 a[72:75], v237, s[44:47], s78 offen offset:128\n"
"buffer_load_dwordx4 a[76:79], v237, s[44:47], s78 offen offset:144\n"
"buffer_load_dwordx4 a[80:83], v237, s[44:47], s72 offen offset:0\n"
"buffer_load_dwordx4 a[84:87], v237, s[44:47], s72 offen offset:16\n"
"buffer_load_dwordx4 a[88:91], v237, s[44:47], s72 offen offset:128\n"
"buffer_load_dwordx4 a[92:95], v237, s[44:47], s72 offen offset:144\n"
"buffer_load_dwordx4 v230, s[48:51], s78 offen offset:0 lds\n"
"buffer_load_dwordx4 v230, s[48:51], s78 offen offset:1024 lds\n"
"buffer_load_dwordx4 v230, s[48:51], s78 offen offset:2048 lds\n"
"buffer_load_dwordx4 v230, s[48:51], s78 offen offset:3072 lds\n"
"s_waitcnt vmcnt(0)\n"
"ds_read_b64_tr_b16 a[128:129], v238 offset:0\n"
"ds_read_b64_tr_b16 a[132:133], v238 offset:32\n"
"ds_read_b64_tr_b16 a[136:137], v238 offset:64\n"
"ds_read_b64_tr_b16 a[140:141], v238 offset:96\n"
"ds_read_b64_tr_b16 a[144:145], v238 offset:128\n"
"ds_read_b64_tr_b16 a[148:149], v238 offset:160\n"
"ds_read_b64_tr_b16 a[152:153], v238 offset:192\n"
"ds_read_b64_tr_b16 a[156:157], v238 offset:224\n"
"s_waitcnt lgkmcnt(0)\n"
"buffer_load_dwordx4 v230, s[48:51], s72 offen offset:0 lds\n"
"buffer_load_dwordx4 v230, s[48:51], s72 offen offset:1024 lds\n"
"buffer_load_dwordx4 v230, s[48:51], s72 offen offset:2048 lds\n"
"buffer_load_dwordx4 v230, s[48:51], s72 offen offset:3072 lds\n"
"s_waitcnt vmcnt(0)\n"
"ds_read_b64_tr_b16 a[130:131], v238 offset:0\n"
"ds_read_b64_tr_b16 a[134:135], v238 offset:32\n"
"ds_read_b64_tr_b16 a[138:139], v238 offset:64\n"
"ds_read_b64_tr_b16 a[142:143], v238 offset:96\n"
"ds_read_b64_tr_b16 a[146:147], v238 offset:128\n"
"ds_read_b64_tr_b16 a[150:151], v238 offset:160\n"
"ds_read_b64_tr_b16 a[154:155], v238 offset:192\n"
"ds_read_b64_tr_b16 a[158:159], v238 offset:224\n"
"s_waitcnt lgkmcnt(0)\n"
"v_mfma_f32_16x16x32_bf16 v[128:131], a[64:67], a[192:195], v[192:195]\n"
"v_mfma_f32_16x16x32_bf16 v[128:131], a[68:71], a[196:199], v[128:131]\n"
"v_mfma_f32_16x16x32_bf16 v[128:131], a[72:75], a[200:203], v[128:131]\n"
"v_mfma_f32_16x16x32_bf16 v[128:131], a[76:79], a[204:207], v[128:131]\n"
"v_mfma_f32_16x16x32_bf16 v[132:135], a[80:83], a[192:195], v[192:195]\n"
"v_mfma_f32_16x16x32_bf16 v[132:135], a[84:87], a[196:199], v[132:135]\n"
"v_mfma_f32_16x16x32_bf16 v[132:135], a[88:91], a[200:203], v[132:135]\n"
"v_mfma_f32_16x16x32_bf16 v[132:135], a[92:95], a[204:207], v[132:135]\n"
"v_mfma_f32_16x16x32_bf16 v[136:139], a[64:67], a[208:211], v[196:199]\n"
"v_mfma_f32_16x16x32_bf16 v[136:139], a[68:71], a[212:215], v[136:139]\n"
"v_mfma_f32_16x16x32_bf16 v[136:139], a[72:75], a[216:219], v[136:139]\n"
"v_mfma_f32_16x16x32_bf16 v[136:139], a[76:79], a[220:223], v[136:139]\n"
"v_mfma_f32_16x16x32_bf16 v[140:143], a[80:83], a[208:211], v[196:199]\n"
"v_mfma_f32_16x16x32_bf16 v[140:143], a[84:87], a[212:215], v[140:143]\n"
"v_mfma_f32_16x16x32_bf16 v[140:143], a[88:91], a[216:219], v[140:143]\n"
"v_mfma_f32_16x16x32_bf16 v[140:143], a[92:95], a[220:223], v[140:143]\n"
"v_mfma_f32_16x16x32_bf16 v[144:147], a[64:67], a[224:227], v[200:203]\n"
"v_mfma_f32_16x16x32_bf16 v[144:147], a[68:71], a[228:231], v[144:147]\n"
"v_mfma_f32_16x16x32_bf16 v[144:147], a[72:75], a[232:235], v[144:147]\n"
"v_mfma_f32_16x16x32_bf16 v[144:147], a[76:79], a[236:239], v[144:147]\n"
"v_mfma_f32_16x16x32_bf16 v[148:151], a[80:83], a[224:227], v[200:203]\n"
"v_mfma_f32_16x16x32_bf16 v[148:151], a[84:87], a[228:231], v[148:151]\n"
"v_mfma_f32_16x16x32_bf16 v[148:151], a[88:91], a[232:235], v[148:151]\n"
"v_mfma_f32_16x16x32_bf16 v[148:151], a[92:95], a[236:239], v[148:151]\n"
"v_mfma_f32_16x16x32_bf16 v[152:155], a[64:67], a[240:243], v[204:207]\n"
"v_mfma_f32_16x16x32_bf16 v[152:155], a[68:71], a[244:247], v[152:155]\n"
"v_mfma_f32_16x16x32_bf16 v[152:155], a[72:75], a[248:251], v[152:155]\n"
"v_mfma_f32_16x16x32_bf16 v[152:155], a[76:79], a[252:255], v[152:155]\n"
"v_mfma_f32_16x16x32_bf16 v[156:159], a[80:83], a[240:243], v[204:207]\n"
"v_mfma_f32_16x16x32_bf16 v[156:159], a[84:87], a[244:247], v[156:159]\n"
"v_mfma_f32_16x16x32_bf16 v[156:159], a[88:91], a[248:251], v[156:159]\n"
"v_mfma_f32_16x16x32_bf16 v[156:159], a[92:95], a[252:255], v[156:159]\n"
"s_nop 7\n"
"v_exp_f32 v128, v128\n"
"v_exp_f32 v129, v129\n"
"v_exp_f32 v130, v130\n"
"v_exp_f32 v131, v131\n"
"v_exp_f32 v132, v132\n"
"v_exp_f32 v133, v133\n"
"v_exp_f32 v134, v134\n"
"v_exp_f32 v135, v135\n"
"v_exp_f32 v136, v136\n"
"v_exp_f32 v137, v137\n"
"v_exp_f32 v138, v138\n"
"v_exp_f32 v139, v139\n"
"v_exp_f32 v140, v140\n"
"v_exp_f32 v141, v141\n"
"v_exp_f32 v142, v142\n"
"v_exp_f32 v143, v143\n"
"v_exp_f32 v144, v144\n"
"v_exp_f32 v145, v145\n"
"v_exp_f32 v146, v146\n"
"v_exp_f32 v147, v147\n"
"v_exp_f32 v148, v148\n"
"v_exp_f32 v149, v149\n"
"v_exp_f32 v150, v150\n"
"v_exp_f32 v151, v151\n"
"v_exp_f32 v152, v152\n"
"v_exp_f32 v153, v153\n"
"v_exp_f32 v154, v154\n"
"v_exp_f32 v155, v155\n"
"v_exp_f32 v156, v156\n"
"v_exp_f32 v157, v157\n"
"v_exp_f32 v158, v158\n"
"v_exp_f32 v159, v159\n"
"s_nop 0\n"
"v_cvt_pk_bf16_f32 v128, v128, v129\n"
"v_cvt_pk_bf16_f32 v129, v130, v131\n"
"v_cvt_pk_bf16_f32 v130, v132, v133\n"
"v_cvt_pk_bf16_f32 v131, v134, v135\n"
"v_cvt_pk_bf16_f32 v136, v136, v137\n"
"v_cvt_pk_bf16_f32 v137, v138, v139\n"
"v_cvt_pk_bf16_f32 v138, v140, v141\n"
"v_cvt_pk_bf16_f32 v139, v142, v143\n"
"v_cvt_pk_bf16_f32 v144, v144, v145\n"
"v_cvt_pk_bf16_f32 v145, v146, v147\n"
"v_cvt_pk_bf16_f32 v146, v148, v149\n"
"v_cvt_pk_bf16_f32 v147, v150, v151\n"
"v_cvt_pk_bf16_f32 v152, v152, v153\n"
"v_cvt_pk_bf16_f32 v153, v154, v155\n"
"v_cvt_pk_bf16_f32 v154, v156, v157\n"
"v_cvt_pk_bf16_f32 v155, v158, v159\n"
"s_nop 1\n"
"v_mfma_f32_16x16x32_bf16 v[208:211], a[160:163], v[128:131], v[208:211]\n"
"v_mfma_f32_16x16x32_bf16 v[212:215], a[160:163], v[136:139], v[212:215]\n"
"v_mfma_f32_16x16x32_bf16 v[216:219], a[160:163], v[144:147], v[216:219]\n"
"v_mfma_f32_16x16x32_bf16 v[220:223], a[160:163], v[152:155], v[220:223]\n"
"v_mfma_f32_16x16x32_bf16 v[0:3], a[128:131], v[128:131], v[0:3]\n"
"v_mfma_f32_16x16x32_bf16 v[32:35], a[128:131], v[136:139], v[32:35]\n"
"v_mfma_f32_16x16x32_bf16 v[64:67], a[128:131], v[144:147], v[64:67]\n"
"v_mfma_f32_16x16x32_bf16 v[96:99], a[128:131], v[152:155], v[96:99]\n"
"v_mfma_f32_16x16x32_bf16 v[4:7], a[132:135], v[128:131], v[4:7]\n"
"v_mfma_f32_16x16x32_bf16 v[36:39], a[132:135], v[136:139], v[36:39]\n"
"v_mfma_f32_16x16x32_bf16 v[68:71], a[132:135], v[144:147], v[68:71]\n"
"v_mfma_f32_16x16x32_bf16 v[100:103], a[132:135], v[152:155], v[100:103]\n"
"v_mfma_f32_16x16x32_bf16 v[8:11], a[136:139], v[128:131], v[8:11]\n"
"v_mfma_f32_16x16x32_bf16 v[40:43], a[136:139], v[136:139], v[40:43]\n"
"v_mfma_f32_16x16x32_bf16 v[72:75], a[136:139], v[144:147], v[72:75]\n"
"v_mfma_f32_16x16x32_bf16 v[104:107], a[136:139], v[152:155], v[104:107]\n"
"v_mfma_f32_16x16x32_bf16 v[12:15], a[140:143], v[128:131], v[12:15]\n"
"v_mfma_f32_16x16x32_bf16 v[44:47], a[140:143], v[136:139], v[44:47]\n"
"v_mfma_f32_16x16x32_bf16 v[76:79], a[140:143], v[144:147], v[76:79]\n"
"v_mfma_f32_16x16x32_bf16 v[108:111], a[140:143], v[152:155], v[108:111]\n"
"v_mfma_f32_16x16x32_bf16 v[16:19], a[144:147], v[128:131], v[16:19]\n"
"v_mfma_f32_16x16x32_bf16 v[48:51], a[144:147], v[136:139], v[48:51]\n"
"v_mfma_f32_16x16x32_bf16 v[80:83], a[144:147], v[144:147], v[80:83]\n"
"v_mfma_f32_16x16x32_bf16 v[112:115], a[144:147], v[152:155], v[112:115]\n"
"v_mfma_f32_16x16x32_bf16 v[20:23], a[148:151], v[128:131], v[20:23]\n"
"v_mfma_f32_16x16x32_bf16 v[52:55], a[148:151], v[136:139], v[52:55]\n"
"v_mfma_f32_16x16x32_bf16 v[84:87], a[148:151], v[144:147], v[84:87]\n"
"v_mfma_f32_16x16x32_bf16 v[116:119], a[148:151], v[152:155], v[116:119]\n"
"v_mfma_f32_16x16x32_bf16 v[24:27], a[152:155], v[128:131], v[24:27]\n"
"v_mfma_f32_16x16x32_bf16 v[56:59], a[152:155], v[136:139], v[56:59]\n"
"v_mfma_f32_16x16x32_bf16 v[88:91], a[152:155], v[144:147], v[88:91]\n"
"v_mfma_f32_16x16x32_bf16 v[120:123], a[152:155], v[152:155], v[120:123]\n"
"v_mfma_f32_16x16x32_bf16 v[28:31], a[156:159], v[128:131], v[28:31]\n"
"v_mfma_f32_16x16x32_bf16 v[60:63], a[156:159], v[136:139], v[60:63]\n"
"v_mfma_f32_16x16x32_bf16 v[92:95], a[156:159], v[144:147], v[92:95]\n"
"v_mfma_f32_16x16x32_bf16 v[124:127], a[156:159], v[152:155], v[124:127]\n"
"s_add_u32 s78, s78, 8192\n"
"s_sub_u32 s80, s80, 1\n"
"s_cbranch_scc0 L_at_s2\n"
"s_nop 7\n"
"v_rcp_f32 v192, v208\n"
"s_nop 0\n"
"v_pk_mul_f32 v[0:1], v[0:1], v[192:193] op_sel_hi:[1,0]\n"
"v_pk_mul_f32 v[2:3], v[2:3], v[192:193] op_sel_hi:[1,0]\n"
"v_pk_mul_f32 v[4:5], v[4:5], v[192:193] op_sel_hi:[1,0]\n"
"v_pk_mul_f32 v[6:7], v[6:7], v[192:193] op_sel_hi:[1,0]\n"
"v_cvt_pk_bf16_f32 v0, v0, v1\n"
"v_cvt_pk_bf16_f32 v1, v2, v3\n"
"v_cvt_pk_bf16_f32 v2, v4, v5\n"
"v_cvt_pk_bf16_f32 v3, v6, v7\n"
"buffer_store_dwordx4 v[0:3], v230, s[60:63], s88 offen offset:0 sc1\n"
"v_pk_mul_f32 v[8:9], v[8:9], v[192:193] op_sel_hi:[1,0]\n"
"v_pk_mul_f32 v[10:11], v[10:11], v[192:193] op_sel_hi:[1,0]\n"
"v_pk_mul_f32 v[12:13], v[12:13], v[192:193] op_sel_hi:[1,0]\n"
"v_pk_mul_f32 v[14:15], v[14:15], v[192:193] op_sel_hi:[1,0]\n"
"v_cvt_pk_bf16_f32 v8, v8, v9\n"
"v_cvt_pk_bf16_f32 v9, v10, v11\n"
"v_cvt_pk_bf16_f32 v10, v12, v13\n"
"v_cvt_pk_bf16_f32 v11, v14, v15\n"
"buffer_store_dwordx4 v[8:11], v230, s[60:63], s88 offen offset:1024 sc1\n"
"v_pk_mul_f32 v[16:17], v[16:17], v[192:193] op_sel_hi:[1,0]\n"
"v_pk_mul_f32 v[18:19], v[18:19], v[192:193] op_sel_hi:[1,0]\n"
"v_pk_mul_f32 v[20:21], v[20:21], v[192:193] op_sel_hi:[1,0]\n"
"v_pk_mul_f32 v[22:23], v[22:23], v[192:193] op_sel_hi:[1,0]\n"
"v_cvt_pk_bf16_f32 v16, v16, v17\n"
"v_cvt_pk_bf16_f32 v17, v18, v19\n"
"v_cvt_pk_bf16_f32 v18, v20, v21\n"
"v_cvt_pk_bf16_f32 v19, v22, v23\n"
"buffer_store_dwordx4 v[16:19], v230, s[60:63], s88 offen offset:2048 sc1\n"
"v_pk_mul_f32 v[24:25], v[24:25], v[192:193] op_sel_hi:[1,0]\n"
"v_pk_mul_f32 v[26:27], v[26:27], v[192:193] op_sel_hi:[1,0]\n"
"v_pk_mul_f32 v[28:29], v[28:29], v[192:193] op_sel_hi:[1,0]\n"
"v_pk_mul_f32 v[30:31], v[30:31], v[192:193] op_sel_hi:[1,0]\n"
"v_cvt_pk_bf16_f32 v24, v24, v25\n"
"v_cvt_pk_bf16_f32 v25, v26, v27\n"
"v_cvt_pk_bf16_f32 v26, v28, v29\n"
"v_cvt_pk_bf16_f32 v27, v30, v31\n"
"buffer_store_dwordx4 v[24:27], v230, s[60:63], s88 offen offset:3072 sc1\n"
"v_rcp_f32 v196, v212\n"
"s_nop 0\n"
"v_pk_mul_f32 v[32:33], v[32:33], v[196:197] op_sel_hi:[1,0]\n"
"v_pk_mul_f32 v[34:35], v[34:35], v[196:197] op_sel_hi:[1,0]\n"
"v_pk_mul_f32 v[36:37], v[36:37], v[196:197] op_sel_hi:[1,0]\n"
"v_pk_mul_f32 v[38:39], v[38:39], v[196:197] op_sel_hi:[1,0]\n"
"v_cvt_pk_bf16_f32 v32, v32, v33\n"
"v_cvt_pk_bf16_f32 v33, v34, v35\n"
"v_cvt_pk_bf16_f32 v34, v36, v37\n"
"v_cvt_pk_bf16_f32 v35, v38, v39\n"
"buffer_store_dwordx4 v[32:35], v230, s[60:63], s89 offen offset:0 sc1\n"
"v_pk_mul_f32 v[40:41], v[40:41], v[196:197] op_sel_hi:[1,0]\n"
"v_pk_mul_f32 v[42:43], v[42:43], v[196:197] op_sel_hi:[1,0]\n"
"v_pk_mul_f32 v[44:45], v[44:45], v[196:197] op_sel_hi:[1,0]\n"
"v_pk_mul_f32 v[46:47], v[46:47], v[196:197] op_sel_hi:[1,0]\n"
"v_cvt_pk_bf16_f32 v40, v40, v41\n"
"v_cvt_pk_bf16_f32 v41, v42, v43\n"
"v_cvt_pk_bf16_f32 v42, v44, v45\n"
"v_cvt_pk_bf16_f32 v43, v46, v47\n"
"buffer_store_dwordx4 v[40:43], v230, s[60:63], s89 offen offset:1024 sc1\n"
"v_pk_mul_f32 v[48:49], v[48:49], v[196:197] op_sel_hi:[1,0]\n"
"v_pk_mul_f32 v[50:51], v[50:51], v[196:197] op_sel_hi:[1,0]\n"
"v_pk_mul_f32 v[52:53], v[52:53], v[196:197] op_sel_hi:[1,0]\n"
"v_pk_mul_f32 v[54:55], v[54:55], v[196:197] op_sel_hi:[1,0]\n"
"v_cvt_pk_bf16_f32 v48, v48, v49\n"
"v_cvt_pk_bf16_f32 v49, v50, v51\n"
"v_cvt_pk_bf16_f32 v50, v52, v53\n"
"v_cvt_pk_bf16_f32 v51, v54, v55\n"
"buffer_store_dwordx4 v[48:51], v230, s[60:63], s89 offen offset:2048 sc1\n"
"v_pk_mul_f32 v[56:57], v[56:57], v[196:197] op_sel_hi:[1,0]\n"
"v_pk_mul_f32 v[58:59], v[58:59], v[196:197] op_sel_hi:[1,0]\n"
"v_pk_mul_f32 v[60:61], v[60:61], v[196:197] op_sel_hi:[1,0]\n"
"v_pk_mul_f32 v[62:63], v[62:63], v[196:197] op_sel_hi:[1,0]\n"
"v_cvt_pk_bf16_f32 v56, v56, v57\n"
"v_cvt_pk_bf16_f32 v57, v58, v59\n"
"v_cvt_pk_bf16_f32 v58, v60, v61\n"
"v_cvt_pk_bf16_f32 v59, v62, v63\n"
"buffer_store_dwordx4 v[56:59], v230, s[60:63], s89 offen offset:3072 sc1\n"
"v_rcp_f32 v200, v216\n"
"s_nop 0\n"
"v_pk_mul_f32 v[64:65], v[64:65], v[200:201] op_sel_hi:[1,0]\n"
"v_pk_mul_f32 v[66:67], v[66:67], v[200:201] op_sel_hi:[1,0]\n"
"v_pk_mul_f32 v[68:69], v[68:69], v[200:201] op_sel_hi:[1,0]\n"
"v_pk_mul_f32 v[70:71], v[70:71], v[200:201] op_sel_hi:[1,0]\n"
"v_cvt_pk_bf16_f32 v64, v64, v65\n"
"v_cvt_pk_bf16_f32 v65, v66, v67\n"
"v_cvt_pk_bf16_f32 v66, v68, v69\n"
"v_cvt_pk_bf16_f32 v67, v70, v71\n"
"buffer_store_dwordx4 v[64:67], v230, s[60:63], s90 offen offset:0 sc1\n"
"v_pk_mul_f32 v[72:73], v[72:73], v[200:201] op_sel_hi:[1,0]\n"
"v_pk_mul_f32 v[74:75], v[74:75], v[200:201] op_sel_hi:[1,0]\n"
"v_pk_mul_f32 v[76:77], v[76:77], v[200:201] op_sel_hi:[1,0]\n"
"v_pk_mul_f32 v[78:79], v[78:79], v[200:201] op_sel_hi:[1,0]\n"
"v_cvt_pk_bf16_f32 v72, v72, v73\n"
"v_cvt_pk_bf16_f32 v73, v74, v75\n"
"v_cvt_pk_bf16_f32 v74, v76, v77\n"
"v_cvt_pk_bf16_f32 v75, v78, v79\n"
"buffer_store_dwordx4 v[72:75], v230, s[60:63], s90 offen offset:1024 sc1\n"
"v_pk_mul_f32 v[80:81], v[80:81], v[200:201] op_sel_hi:[1,0]\n"
"v_pk_mul_f32 v[82:83], v[82:83], v[200:201] op_sel_hi:[1,0]\n"
"v_pk_mul_f32 v[84:85], v[84:85], v[200:201] op_sel_hi:[1,0]\n"
"v_pk_mul_f32 v[86:87], v[86:87], v[200:201] op_sel_hi:[1,0]\n"
"v_cvt_pk_bf16_f32 v80, v80, v81\n"
"v_cvt_pk_bf16_f32 v81, v82, v83\n"
"v_cvt_pk_bf16_f32 v82, v84, v85\n"
"v_cvt_pk_bf16_f32 v83, v86, v87\n"
"buffer_store_dwordx4 v[80:83], v230, s[60:63], s90 offen offset:2048 sc1\n"
"v_pk_mul_f32 v[88:89], v[88:89], v[200:201] op_sel_hi:[1,0]\n"
"v_pk_mul_f32 v[90:91], v[90:91], v[200:201] op_sel_hi:[1,0]\n"
"v_pk_mul_f32 v[92:93], v[92:93], v[200:201] op_sel_hi:[1,0]\n"
"v_pk_mul_f32 v[94:95], v[94:95], v[200:201] op_sel_hi:[1,0]\n"
"v_cvt_pk_bf16_f32 v88, v88, v89\n"
"v_cvt_pk_bf16_f32 v89, v90, v91\n"
"v_cvt_pk_bf16_f32 v90, v92, v93\n"
"v_cvt_pk_bf16_f32 v91, v94, v95\n"
"buffer_store_dwordx4 v[88:91], v230, s[60:63], s90 offen offset:3072 sc1\n"
"v_rcp_f32 v204, v220\n"
"s_nop 0\n"
"v_pk_mul_f32 v[96:97], v[96:97], v[204:205] op_sel_hi:[1,0]\n"
"v_pk_mul_f32 v[98:99], v[98:99], v[204:205] op_sel_hi:[1,0]\n"
"v_pk_mul_f32 v[100:101], v[100:101], v[204:205] op_sel_hi:[1,0]\n"
"v_pk_mul_f32 v[102:103], v[102:103], v[204:205] op_sel_hi:[1,0]\n"
"v_cvt_pk_bf16_f32 v96, v96, v97\n"
"v_cvt_pk_bf16_f32 v97, v98, v99\n"
"v_cvt_pk_bf16_f32 v98, v100, v101\n"
"v_cvt_pk_bf16_f32 v99, v102, v103\n"
"buffer_store_dwordx4 v[96:99], v230, s[60:63], s92 offen offset:0 sc1\n"
"v_pk_mul_f32 v[104:105], v[104:105], v[204:205] op_sel_hi:[1,0]\n"
"v_pk_mul_f32 v[106:107], v[106:107], v[204:205] op_sel_hi:[1,0]\n"
"v_pk_mul_f32 v[108:109], v[108:109], v[204:205] op_sel_hi:[1,0]\n"
"v_pk_mul_f32 v[110:111], v[110:111], v[204:205] op_sel_hi:[1,0]\n"
"v_cvt_pk_bf16_f32 v104, v104, v105\n"
"v_cvt_pk_bf16_f32 v105, v106, v107\n"
"v_cvt_pk_bf16_f32 v106, v108, v109\n"
"v_cvt_pk_bf16_f32 v107, v110, v111\n"
"buffer_store_dwordx4 v[104:107], v230, s[60:63], s92 offen offset:1024 sc1\n"
"v_pk_mul_f32 v[112:113], v[112:113], v[204:205] op_sel_hi:[1,0]\n"
"v_pk_mul_f32 v[114:115], v[114:115], v[204:205] op_sel_hi:[1,0]\n"
"v_pk_mul_f32 v[116:117], v[116:117], v[204:205] op_sel_hi:[1,0]\n"
"v_pk_mul_f32 v[118:119], v[118:119], v[204:205] op_sel_hi:[1,0]\n"
"v_cvt_pk_bf16_f32 v112, v112, v113\n"
"v_cvt_pk_bf16_f32 v113, v114, v115\n"
"v_cvt_pk_bf16_f32 v114, v116, v117\n"
"v_cvt_pk_bf16_f32 v115, v118, v119\n"
"buffer_store_dwordx4 v[112:115], v230, s[60:63], s92 offen offset:2048 sc1\n"
"v_pk_mul_f32 v[120:121], v[120:121], v[204:205] op_sel_hi:[1,0]\n"
"v_pk_mul_f32 v[122:123], v[122:123], v[204:205] op_sel_hi:[1,0]\n"
"v_pk_mul_f32 v[124:125], v[124:125], v[204:205] op_sel_hi:[1,0]\n"
"v_pk_mul_f32 v[126:127], v[126:127], v[204:205] op_sel_hi:[1,0]\n"
"v_cvt_pk_bf16_f32 v120, v120, v121\n"
"v_cvt_pk_bf16_f32 v121, v122, v123\n"
"v_cvt_pk_bf16_f32 v122, v124, v125\n"
"v_cvt_pk_bf16_f32 v123, v126, v127\n"
"buffer_store_dwordx4 v[120:123], v230, s[60:63], s92 offen offset:3072 sc1\n"
"s_waitcnt vmcnt(0)\n"
"s_branch L_at_back\n"
"L_at_end:\n"
    :: "s"(ws), "s"(kc), "s"(vc), "s"(wq), "s"(wo), "s"(w1), "s"(w2), "s"(x), "s"(out), "s"(wid), "s"(ldsb), "s"(split), "s"(rb), "s"(ct), "s"(rbw), "s"(c), "s"(bid), "s"(cvec), "s"(wmod)
    :
"v0","v1","v2","v3","v4","v5","v6","v7","v8","v9","v10","v11","v12","v13","v14","v15","v16","v17","v18","v19","v20","v21","v22","v23","v24","v25","v26","v27","v28","v29","v30","v31","v32","v33","v34","v35","v36","v37","v38","v39","v40","v41","v42","v43","v44","v45","v46","v47","v48","v49","v50","v51","v52","v53","v54","v55","v56","v57","v58","v59","v60","v61","v62","v63","v64","v65","v66","v67","v68","v69","v70","v71","v72","v73","v74","v75","v76","v77","v78","v79","v80","v81","v82","v83","v84","v85","v86","v87","v88","v89","v90","v91","v92","v93","v94","v95","v96","v97","v98","v99","v100","v101","v102","v103","v104","v105","v106","v107","v108","v109","v110","v111","v112","v113","v114","v115","v116","v117","v118","v119","v120","v121","v122","v123","v124","v125","v126","v127","v128","v129","v130","v131","v132","v133","v134","v135","v136","v137","v138","v139","v140","v141","v142","v143","v144","v145","v146","v147","v148","v149","v150","v151","v152","v153","v154","v155","v156","v157","v158","v159","v160","v161","v162","v163","v164","v165","v166","v167","v168","v169","v170","v171","v172","v173","v174","v175","v176","v177","v178","v179","v180","v181","v182","v183","v184","v185","v186","v187","v188","v189","v190","v191","v192","v193","v194","v195","v196","v197","v198","v199","v200","v201","v202","v203","v204","v205","v206","v207","v208","v209","v210","v211","v212","v213","v214","v215","v216","v217","v218","v219","v220","v221","v222","v223","v224","v225","v226","v227","v228","v229","v230","v231","v232","v233","v234","v235","v236","v237","v238","v239","v240","v241","v242","v243","v244","v245","v246","v247","v248","v249","v250","v251","v252","v253","v254","v255","a0","a1","a2","a3","a4","a5","a6","a7","a8","a9","a10","a11","a12","a13","a14","a15","a16","a17","a18","a19","a20","a21","a22","a23","a24","a25","a26","a27","a28","a29","a30","a31","a32","a33","a34","a35","a36","a37","a38","a39","a40","a41","a42","a43","a44","a45","a46","a47","a48","a49","a50","a51","a52","a53","a54","a55","a56","a57","a58","a59","a60","a61","a62","a63","a64","a65","a66","a67","a68","a69","a70","a71","a72","a73","a74","a75","a76","a77","a78","a79","a80","a81","a82","a83","a84","a85","a86","a87","a88","a89","a90","a91","a92","a93","a94","a95","a96","a97","a98","a99","a100","a101","a102","a103","a104","a105","a106","a107","a108","a109","a110","a111","a112","a113","a114","a115","a116","a117","a118","a119","a120","a121","a122","a123","a124","a125","a126","a127","a128","a129","a130","a131","a132","a133","a134","a135","a136","a137","a138","a139","a140","a141","a142","a143","a144","a145","a146","a147","a148","a149","a150","a151","a152","a153","a154","a155","a156","a157","a158","a159","a160","a161","a162","a163","a164","a165","a166","a167","a168","a169","a170","a171","a172","a173","a174","a175","a176","a177","a178","a179","a180","a181","a182","a183","a184","a185","a186","a187","a188","a189","a190","a191","a192","a193","a194","a195","a196","a197","a198","a199","a200","a201","a202","a203","a204","a205","a206","a207","a208","a209","a210","a211","a212","a213","a214","a215","a216","a217","a218","a219","a220","a221","a222","a223","a224","a225","a226","a227","a228","a229","a230","a231","a232","a233","a234","a235","a236","a237","a238","a239","a240","a241","a242","a243","a244","a245","a246","a247","a248","a249","a250","a251","a252","a253","a254","a255","s44","s45","s46","s47","s48","s49","s50","s51","s52","s53","s54","s55","s56","s57","s58","s59","s60","s61","s62","s63","s64","s65","s66","s67","s68","s69","s70","s71","s72","s73","s74","s75","s76","s77","s78","s79","s80","s81","s82","s83","s84","s85","s86","s87","s88","s89","s90","s91","s92","s93","s94","s95","s96","s97","s98","s99","vcc","scc","memory"
  );
}

extern "C" void kernel_launch(void* const* d_in, const int* in_sizes, int n_in, void* d_out, int out_size, void* d_ws, size_t ws_size, hipStream_t stream){
  const u16* x=(const u16*)d_in[0]; const u16* kc=(const u16*)d_in[1]; const u16* vc=(const u16*)d_in[2];
  const u16* wq=(const u16*)d_in[3]; const u16* wo=(const u16*)d_in[4]; const u16* w1=(const u16*)d_in[6]; const u16* w2=(const u16*)d_in[7];
  const u16* c=(const u16*)d_in[8]; const u16* wmod=(const u16*)d_in[9];
  (void)hipFuncSetAttribute((const void*)k_mega, hipFuncAttributeMaxDynamicSharedMemorySize, 163840);
  k_mega<<<256,256,163840,stream>>>((char*)d_ws,kc,vc,wq,wo,w1,w2,x,(u16*)d_out,c,wmod);
}
```
